```python
import torch

def _softmax(x, axis=-1):
    return torch.softmax(x, dim=axis)

def _gelu(x):
    return torch.nn.functional.gelu(x, approximate='tanh')

def _silu(x):
    return torch.nn.functional.silu(x)

def _sigmoid(x):
    return torch.sigmoid(x)

def _layernorm(x, axis=-1, eps=1e-05):
    m = x.mean(dim=axis, keepdim=True)
    v = x.var(dim=axis, keepdim=True, unbiased=False)
    return (x - m) / torch.sqrt(v + eps)

def _scatter_add(tgt, idx, src, axis=0):
    return tgt.clone().index_add_(axis, idx, src)

def _segment_sum(src, seg, n):
    out = torch.zeros((n,) + src.shape[1:], dtype=src.dtype)
    return out.index_add_(0, seg, src)

def _pad(x, pw):
    flat = tuple((v for pair in reversed(pw) for v in pair))
    return torch.nn.functional.pad(x, flat)
kernel_name = 'adaLN_cond__mha_block_cached__glu_gated_swiglu__prompt'

def setup_inputs():

    _w = lambda a: (lambda t: t.to(torch.bfloat16) if t.is_floating_point() else t)(torch.as_tensor(a))

    def _A_setup():
        B, Sq, D = (1, 2048, 512)
        H, Sk, Dh = (32, 2048, 128)
        return {'x': _w((lambda *s: torch.randn(*s, dtype=torch.bfloat16) * 0.1)(B, Sq, D)), 'k_cache': _w((lambda *s: torch.randn(*s, dtype=torch.bfloat16) * 0.8)(B, H, Sk, Dh)), 'v_cache': _w((lambda *s: torch.randn(*s, dtype=torch.bfloat16) * 3.2)(B, H, Sk, Dh)), 'wq': _w((lambda *s: torch.randn(*s, dtype=torch.bfloat16) * 0.8)(D, H * Dh) * D ** (-0.5)), 'wo': _w((lambda *s: torch.randn(*s, dtype=torch.bfloat16) * 3.2)(H * Dh, D) * (H * Dh) ** (-0.5))}
    _A_in = _A_setup()

    def _B_setup():
        B, S, D, F = (1, 2048, 512, 1792)
        return {'x': _w((lambda *s: torch.randn(*s, dtype=torch.bfloat16) * 0.1)(B, S, D)), 'w1': _w((lambda *s: torch.randn(*s, dtype=torch.bfloat16) * 3.2)(D, F) * (2.0 / D) ** 0.5), 'w3': _w((lambda *s: torch.randn(*s, dtype=torch.bfloat16) * 3.2)(D, F) * (2.0 / D) ** 0.5), 'w2': _w((lambda *s: torch.randn(*s, dtype=torch.bfloat16) * 3.2)(F, D) * (2.0 / F) ** 0.5)}
    _B_in = _B_setup()
    return {'A_x': _A_in['x'], 'A_k_cache': _A_in['k_cache'], 'A_v_cache': _A_in['v_cache'], 'A_wq': _A_in['wq'], 'A_wo': _A_in['wo'], 'B_x': _B_in['x'], 'B_w1': _B_in['w1'], 'B_w3': _B_in['w3'], 'B_w2': _B_in['w2'], 'c': _w((lambda *s: torch.randn(*s, dtype=torch.bfloat16) * 0.8)(1, 512) * 8.0), 'w_mod': _w((lambda *s: torch.randn(*s, dtype=torch.bfloat16) * 0.8)(512, 6 * 512) * 512 ** (-0.5) * 8.0)}

def reference(A_x, A_k_cache, A_v_cache, A_wq, A_wo, B_x, B_w1, B_w3, B_w2, c, w_mod):

    def _A_ref(x, k_cache, v_cache, wq, wo):
        B, Sq, D = x.shape
        H, Dh = (32, 128)
        q = (lambda x, *d: x.permute(*d))((x @ wq).reshape(B, Sq, H, Dh), 0, 2, 1, 3)
        scores = torch.einsum('bhqd,bhkd->bhqk', q, k_cache) * Dh ** (-0.5)
        p = _softmax(scores, axis=-1)
        o = torch.einsum('bhqk,bhkd->bhqd', p, v_cache)
        return (lambda x, *d: x.permute(*d))(o, 0, 2, 1, 3).reshape(B, Sq, H * Dh) @ wo

    def _B_ref(x, w1, w3, w2):
        gate = torch.einsum('bsd,df->bsf', x, w1)
        up = torch.einsum('bsd,df->bsf', x, w3)
        h = _silu(gate) * up
        out = torch.einsum('bsf,fd->bsd', h, w2)
        return out
    x = A_x
    mod = (_silu(c) @ w_mod).reshape(c.shape[0], 6, 1, -1)
    sa, ca, ga, sb, cb, gb = (mod[:, 0].clone(), mod[:, 1].clone(), mod[:, 2].clone(), mod[:, 3].clone(), mod[:, 4].clone(), mod[:, 5].clone())
    h = _layernorm(x, axis=-1) * (1 + ca) + sa
    x = x + ga * _A_ref(h, A_k_cache, A_v_cache, A_wq, A_wo)
    h = _layernorm(x, axis=-1) * (1 + cb) + sb
    x = x + gb * _B_ref(h, B_w1, B_w3, B_w2)
    return x
```

```cpp
#include <hip/hip_runtime.h>
#include <hip/hip_bf16.h>
#include <stdint.h>
typedef unsigned short u16;
typedef float f32x4 __attribute__((ext_vector_type(4)));
typedef float f32x2 __attribute__((ext_vector_type(2)));
typedef unsigned u32x4 __attribute__((ext_vector_type(4)));
typedef unsigned u32x2 __attribute__((ext_vector_type(2)));
typedef __bf16 bf16x2_t __attribute__((ext_vector_type(2)));
#define DI __device__ __forceinline__
DI float bf2f(unsigned h) { return __uint_as_float(h << 16); }
DI float bflo(unsigned w) { return __uint_as_float(w << 16); }
DI float bfhi(unsigned w) { return __uint_as_float(w & 0xffff0000u); }
DI unsigned pk2(float lo, float hi) { f32x2 v = {lo, hi}; bf16x2_t b = __builtin_convertvector(v, bf16x2_t); return __builtin_bit_cast(unsigned, b); }
DI float rbf(float x) { return bflo(pk2(x, 0.f)); }

constexpr size_t MB = 1u << 20;
constexpr size_t WS_MODP = 0;
constexpr size_t WS_H1 = 1 * MB;
constexpr size_t WS_O = 4 * MB;
constexpr size_t WS_PART = 20 * MB;
constexpr size_t WS_X1 = 40 * MB;
constexpr size_t WS_H2 = 44 * MB;
constexpr size_t WS_ACT = 46 * MB;

__global__ __launch_bounds__(256) void k_mod(const u16* __restrict__ c, const u16* __restrict__ wmod, float* __restrict__ modp) {
  __shared__ float red[4][64];
  const int t = threadIdx.x, cg = blockIdx.x >> 2, kq = blockIdx.x & 3;
  const int ch = t & 7, r0 = t >> 3;
  u32x4 w[4]; float cv[4];
#pragma unroll
  for (int i = 0; i < 4; ++i) { const int k = kq * 128 + r0 + 32 * i; w[i] = *(const u32x4*)(wmod + (size_t)k * 3072 + cg * 64 + ch * 8); cv[i] = bf2f(c[k]); }
  float acc[8] = {};
#pragma unroll
  for (int i = 0; i < 4; ++i) {
    const float s = rbf(cv[i] / (1.f + __expf(-cv[i])));
#pragma unroll
    for (int e = 0; e < 4; ++e) { acc[2 * e] += s * bflo(w[i][e]); acc[2 * e + 1] += s * bfhi(w[i][e]); }
  }
#pragma unroll
  for (int e = 0; e < 8; ++e) { float v = acc[e]; v += __shfl_xor(v, 8); v += __shfl_xor(v, 16); v += __shfl_xor(v, 32); acc[e] = v; }
  if ((t & 63) < 8) {
#pragma unroll
    for (int e = 0; e < 8; ++e) red[t >> 6][ch * 8 + e] = acc[e];
  }
  __syncthreads();
  if (t < 64) modp[kq * 3072 + cg * 64 + t] = red[0][t] + red[1][t] + red[2][t] + red[3][t];
}

template <int NBW, int NW> DI void ln_stats(const float (&v)[NBW][4], float* red  , int w, int lane, float& mean, float& rstd) {
  float s = 0.f;
#pragma unroll
  for (int i = 0; i < NBW; ++i) s += (v[i][0] + v[i][1]) + (v[i][2] + v[i][3]);
  s += __shfl_xor(s, 16); s += __shfl_xor(s, 32);
  if (lane < 16) red[w * 16 + lane] = s;
  __syncthreads();
  float tot = 0.f;
#pragma unroll
  for (int i = 0; i < NW; ++i) tot += red[i * 16 + (lane & 15)];
  mean = tot * (1.f / 512.f);
  float qd = 0.f;
#pragma unroll
  for (int i = 0; i < NBW; ++i) {
#pragma unroll
    for (int r = 0; r < 4; ++r) { const float dlt = v[i][r] - mean; qd += dlt * dlt; } }
  qd += __shfl_xor(qd, 16); qd += __shfl_xor(qd, 32);
  if (lane < 16) red[NW * 16 + w * 16 + lane] = qd;
  __syncthreads();
  float tq = 0.f;
#pragma unroll
  for (int i = 0; i < NW; ++i) tq += red[NW * 16 + i * 16 + (lane & 15)];
  rstd = 1.f / sqrtf(tq * (1.f / 512.f) + 1e-5f);
}

__global__ __launch_bounds__(256) void k_ln1(const u16* __restrict__ x, const float* __restrict__ modp, u16* __restrict__ h1) {
  __shared__ float sm[1024]; __shared__ float red[2 * 4 * 16];
  const int t = threadIdx.x, w = t >> 6, lane = t & 63, q = lane & 15, g = lane >> 4, rb = blockIdx.x;
  u32x2 xr[8];
#pragma unroll
  for (int i = 0; i < 8; ++i) xr[i] = *(const u32x2*)(x + (size_t)(rb * 16 + q) * 512 + (8 * w + i) * 16 + 4 * g);
  { f32x4 a = *(const f32x4*)(modp + 4 * t), b = *(const f32x4*)(modp + 3072 + 4 * t), c2 = *(const f32x4*)(modp + 6144 + 4 * t), d2 = *(const f32x4*)(modp + 9216 + 4 * t);
#pragma unroll
    for (int e = 0; e < 4; ++e) sm[4 * t + e] = rbf((a[e] + b[e]) + (c2[e] + d2[e])); }
  float v[8][4];
#pragma unroll
  for (int i = 0; i < 8; ++i) { v[i][0] = bflo(xr[i][0]); v[i][1] = bfhi(xr[i][0]); v[i][2] = bflo(xr[i][1]); v[i][3] = bfhi(xr[i][1]); }
  float mean, rstd; ln_stats<8, 4>(v, red, w, lane, mean, rstd);
#pragma unroll
  for (int s2 = 0; s2 < 4; ++s2) {
    u32x4 o;
#pragma unroll
    for (int b = 0; b < 2; ++b) {
      const int i = 2 * s2 + b, n = (8 * w + i) * 16 + 4 * g;
      const f32x4 sa = *(const f32x4*)(sm + n), ca = *(const f32x4*)(sm + 512 + n);
      float h[4];
#pragma unroll
      for (int r = 0; r < 4; ++r) h[r] = (v[i][r] - mean) * rstd * (1.f + ca[r]) + sa[r];
      o[2 * b] = pk2(h[0], h[1]); o[2 * b + 1] = pk2(h[2], h[3]);
    }
    *(u32x4*)(h1 + ((size_t)(rb * 16 + 4 * w + s2) * 64 + lane) * 8) = o;
  }
}

__global__ __launch_bounds__(512) void k_ln2(const u16* __restrict__ x, const float* __restrict__ modp, const float* __restrict__ part, float* __restrict__ x1, u16* __restrict__ h2) {
  __shared__ float sm[1536]; __shared__ float red[2 * 8 * 16];
  const int t = threadIdx.x, w = t >> 6, lane = t & 63, q = lane & 15, g = lane >> 4, rb = blockIdx.x;
  f32x4 p[4][4]; u32x2 xr[4];
#pragma unroll
  for (int i = 0; i < 4; ++i) {
#pragma unroll
    for (int sp = 0; sp < 4; ++sp) p[i][sp] = *(const f32x4*)(part + ((((size_t)sp * 128 + rb) * 32 + 4 * w + i) * 64 + lane) * 4);
    xr[i] = *(const u32x2*)(x + (size_t)(rb * 16 + q) * 512 + (4 * w + i) * 16 + 4 * g);
  }
  if (t < 384) { const float* mp = modp + 1024 + 4 * t;
    f32x4 a = *(const f32x4*)(mp), b = *(const f32x4*)(mp + 3072), c2 = *(const f32x4*)(mp + 6144), d2 = *(const f32x4*)(mp + 9216);
#pragma unroll
    for (int e = 0; e < 4; ++e) sm[4 * t + e] = rbf((a[e] + b[e]) + (c2[e] + d2[e])); }
  __syncthreads();
  float v[4][4];
#pragma unroll
  for (int i = 0; i < 4; ++i) {
    const int n = (4 * w + i) * 16 + 4 * g; const f32x4 ga = *(const f32x4*)(sm + n);
    const f32x4 a = (p[i][0] + p[i][1]) + (p[i][2] + p[i][3]);
    v[i][0] = bflo(xr[i][0]) + ga[0] * a[0]; v[i][1] = bfhi(xr[i][0]) + ga[1] * a[1]; v[i][2] = bflo(xr[i][1]) + ga[2] * a[2]; v[i][3] = bfhi(xr[i][1]) + ga[3] * a[3];
    *(f32x4*)(x1 + (((size_t)rb * 32 + 4 * w + i) * 64 + lane) * 4) = (f32x4){v[i][0], v[i][1], v[i][2], v[i][3]};
  }
  float mean, rstd; ln_stats<4, 8>(v, red, w, lane, mean, rstd);
#pragma unroll
  for (int s2 = 0; s2 < 2; ++s2) {
    u32x4 o;
#pragma unroll
    for (int b = 0; b < 2; ++b) {
      const int i = 2 * s2 + b, n = (4 * w + i) * 16 + 4 * g;
      const f32x4 sb = *(const f32x4*)(sm + 512 + n), cb = *(const f32x4*)(sm + 1024 + n);
      float h[4];
#pragma unroll
      for (int r = 0; r < 4; ++r) h[r] = (v[i][r] - mean) * rstd * (1.f + cb[r]) + sb[r];
      o[2 * b] = pk2(h[0], h[1]); o[2 * b + 1] = pk2(h[2], h[3]);
    }
    *(u32x4*)(h2 + ((size_t)(rb * 16 + 2 * w + s2) * 64 + lane) * 8) = o;
  }
}
DI size_t ft_idx(int row, int k, int K) { const int rb = row >> 4, q = row & 15, s = k >> 5, jh = (k >> 4) & 1, g = (k >> 2) & 3, jl = k & 3; return (((size_t)rb * (K / 32) + s) * 64 + g * 16 + q) * 8 + jh * 4 + jl; }
DI size_t acc_idx(int row, int n, int N) { const int rb = row >> 4, q = row & 15, nb = n >> 4, g = (n >> 2) & 3, r = n & 3; return (((size_t)rb * (N / 16) + nb) * 64 + g * 16 + q) * 4 + r; }
__global__ void n_attn(const u16* h1, const u16* wq, const u16* K, const u16* V, u16* o) {
  __shared__ float q[128]; __shared__ float sc[2048]; __shared__ float red[64];
  const int row = blockIdx.x & 2047, h = blockIdx.x >> 11, t = threadIdx.x;
  for (int d = t; d < 128; d += 64) { float a = 0; for (int k = 0; k < 512; ++k) a += bf2f(h1[ft_idx(row, k, 512)]) * bf2f(wq[(size_t)k * 4096 + h * 128 + d]); q[d] = rbf(a * 0.12751743082f); }
  __syncthreads();
  float mx = -1e30f;
  for (int j = t; j < 2048; j += 64) { float a = 0; const u16* kr = K + ((size_t)h * 2048 + j) * 128; for (int d = 0; d < 128; ++d) a += q[d] * bf2f(kr[d]); sc[j] = a; mx = fmaxf(mx, a); }
  for (int o2 = 1; o2 < 64; o2 <<= 1) mx = fmaxf(mx, __shfl_xor(mx, o2));
  float l = 0; for (int j = t; j < 2048; j += 64) { float p = rbf(exp2f(sc[j] - mx)); sc[j] = p; l += p; }
  for (int o2 = 1; o2 < 64; o2 <<= 1) l += __shfl_xor(l, o2);
  __syncthreads();
  for (int d = t; d < 128; d += 64) { float a = 0; for (int j = 0; j < 2048; ++j) a += sc[j] * bf2f(V[((size_t)h * 2048 + j) * 128 + d]); o[ft_idx(row, h * 128 + d, 4096)] = (u16)(pk2(a / l, 0.f) & 0xffff); }
}
__global__ void n_oproj(const u16* o, const u16* wo, float* part) {
  const int i = blockIdx.x * 256 + threadIdx.x; const int n = i & 511, row = (i >> 9) & 2047, sp = i >> 20;
  float a = 0; for (int k = sp * 1024; k < sp * 1024 + 1024; ++k) a += bf2f(o[ft_idx(row, k, 4096)]) * bf2f(wo[(size_t)k * 512 + n]);
  part[(size_t)sp * 2048 * 512 + acc_idx(row, n, 512)] = a;
}
__global__ void n_up(const u16* h2, const u16* w1, const u16* w3, u16* act) {
  const int i = blockIdx.x * 256 + threadIdx.x; const int f = i % 1792, row = i / 1792;
  float a = 0, b = 0; for (int k = 0; k < 512; ++k) { const float hv = bf2f(h2[ft_idx(row, k, 512)]); a += hv * bf2f(w1[(size_t)k * 1792 + f]); b += hv * bf2f(w3[(size_t)k * 1792 + f]); }
  act[ft_idx(row, f, 1792)] = (u16)(pk2(a / (1.f + __expf(-a)) * b, 0.f) & 0xffff);
}
__global__ void n_down(const u16* act, const u16* w2, const float* x1, const float* modp, u16* out) {
  const int i = blockIdx.x * 256 + threadIdx.x; const int n = i & 511, row = i >> 9;
  float a = 0; for (int k = 0; k < 1792; ++k) a += bf2f(act[ft_idx(row, k, 1792)]) * bf2f(w2[(size_t)k * 512 + n]);
  const float gb = rbf(modp[2560 + n] + modp[3072 + 2560 + n] + modp[6144 + 2560 + n] + modp[9216 + 2560 + n]);
  out[(size_t)row * 512 + n] = (u16)(pk2(x1[acc_idx(row, n, 512)] + gb * a, 0.f) & 0xffff);
}
extern "C" void kernel_launch(void* const* d_in, const int* in_sizes, int n_in, void* d_out, int out_size, void* d_ws, size_t ws_size, hipStream_t stream) {
  const u16* x = (const u16*)d_in[0]; const u16* kc = (const u16*)d_in[1]; const u16* vc = (const u16*)d_in[2]; const u16* wq = (const u16*)d_in[3]; const u16* wo = (const u16*)d_in[4];
  const u16* w1 = (const u16*)d_in[6]; const u16* w3 = (const u16*)d_in[7]; const u16* w2 = (const u16*)d_in[8]; const u16* c = (const u16*)d_in[9]; const u16* wmod = (const u16*)d_in[10];
  char* ws = (char*)d_ws;
  float* modp = (float*)(ws + WS_MODP); u16* h1 = (u16*)(ws + WS_H1); u16* o = (u16*)(ws + WS_O); float* part = (float*)(ws + WS_PART); float* x1 = (float*)(ws + WS_X1); u16* h2 = (u16*)(ws + WS_H2); u16* act = (u16*)(ws + WS_ACT);
  k_mod<<<192, 256, 0, stream>>>(c, wmod, modp);
  k_ln1<<<128, 256, 0, stream>>>(x, modp, h1);
  n_attn<<<2048 * 32, 64, 0, stream>>>(h1, wq, kc, vc, o);
  n_oproj<<<4 * 2048 * 512 / 256, 256, 0, stream>>>(o, wo, part);
  k_ln2<<<128, 512, 0, stream>>>(x, modp, part, x1, h2);
  n_up<<<2048 * 1792 / 256, 256, 0, stream>>>(h2, w1, w3, act);
  n_down<<<2048 * 512 / 256, 256, 0, stream>>>(act, w2, x1, modp, (u16*)d_out);
}
```

```cpp
#define CODEPF
#define PF_WQ 1
#define PF_KV 0
#include <hip/hip_runtime.h>
#include <hip/hip_bf16.h>
#include <stdint.h>
typedef unsigned short u16;
typedef float f32x4 __attribute__((ext_vector_type(4)));
typedef float f32x2 __attribute__((ext_vector_type(2)));
typedef unsigned u32x4 __attribute__((ext_vector_type(4)));
typedef unsigned u32x2 __attribute__((ext_vector_type(2)));
typedef __bf16 bf16x2_t __attribute__((ext_vector_type(2)));
#define DI __device__ __forceinline__
DI float bf2f(unsigned h) { return __uint_as_float(h << 16); }
DI float bflo(unsigned w) { return __uint_as_float(w << 16); }
DI float bfhi(unsigned w) { return __uint_as_float(w & 0xffff0000u); }
DI unsigned pk2(float lo, float hi) { f32x2 v = {lo, hi}; bf16x2_t b = __builtin_convertvector(v, bf16x2_t); return __builtin_bit_cast(unsigned, b); }
typedef _Float16 f16x2_t __attribute__((ext_vector_type(2)));
DI unsigned pkh2(float lo, float hi) { f16x2_t h = {(_Float16)lo, (_Float16)hi}; return __builtin_bit_cast(unsigned, h); }
DI float rbf(float x) { return bflo(pk2(x, 0.f)); }

constexpr size_t MB = 1u << 20;
constexpr size_t WS_MODP = 0;
constexpr size_t WS_H1 = 1 * MB;
constexpr size_t WS_O = 4 * MB;
constexpr size_t WS_PART = 20 * MB;
constexpr size_t WS_X1 = 40 * MB;
constexpr size_t WS_H2 = 44 * MB;
constexpr size_t WS_ACT = 46 * MB;

template <int NBW, int NW> DI void ln_stats(const float (&v)[NBW][4], float* red  , int w, int lane, float& mean, float& rstd) {
  float s = 0.f;
#pragma unroll
  for (int i = 0; i < NBW; ++i) s += (v[i][0] + v[i][1]) + (v[i][2] + v[i][3]);
  s += __shfl_xor(s, 16); s += __shfl_xor(s, 32);
  if (lane < 16) red[w * 16 + lane] = s;
  __syncthreads();
  float tot = 0.f;
#pragma unroll
  for (int i = 0; i < NW; ++i) tot += red[i * 16 + (lane & 15)];
  mean = tot * (1.f / 512.f);
  float qd = 0.f;
#pragma unroll
  for (int i = 0; i < NBW; ++i) {
#pragma unroll
    for (int r = 0; r < 4; ++r) { const float dlt = v[i][r] - mean; qd += dlt * dlt; } }
  qd += __shfl_xor(qd, 16); qd += __shfl_xor(qd, 32);
  if (lane < 16) red[NW * 16 + w * 16 + lane] = qd;
  __syncthreads();
  float tq = 0.f;
#pragma unroll
  for (int i = 0; i < NW; ++i) tq += red[NW * 16 + i * 16 + (lane & 15)];
  rstd = 1.f / sqrtf(tq * (1.f / 512.f) + 1e-5f);
}

__global__ __launch_bounds__(256) void k_pre(const u16* __restrict__ c, const u16* __restrict__ wmod, float* __restrict__ modp, const u16* __restrict__ x, u16* __restrict__ xh,
                                             const char* __restrict__ wq, const char* __restrict__ kc, const char* __restrict__ vc, unsigned* __restrict__ flags) {
  __shared__ float red[4 * 64];
  const int t = threadIdx.x;
#define PSTAMP()
  if (blockIdx.x >= 320) {
    if (blockIdx.x == 320 && t < 208) flags[t * 64] = 0u;
    const unsigned pb = blockIdx.x - 320;
    const char* p0 = wq + (size_t)pb * 65536 + t * 16;
    const char* p1 = (pb & 1 ? vc : kc) + (size_t)(pb >> 1) * 524288 + t * 16;
    u32x4 a[16], b[12];
#pragma unroll
    for (int i = 0; i < 16; ++i) a[i] = PF_WQ ? *(const u32x4*)(p0 + i * 4096) : u32x4{};
#pragma unroll
    for (int i = 0; i < 12; ++i) b[i] = PF_KV ? *(const u32x4*)(p1 + i * 4096) : u32x4{};
    {
      const unsigned wv = __builtin_amdgcn_readfirstlane(((blockIdx.x - 320) >> 3) * 4 + (t >> 6));
      asm volatile("s_getpc_b64 s[40:41]\nLpre_here:\ns_add_u32 s40, s40, Lcode_begin-Lpre_here\ns_addc_u32 s41, s41, 0\ns_and_b32 s41, s41, 0xffff\ns_mov_b32 s42, Lcode_end-Lcode_begin\ns_mov_b32 s43, 0x20000\n"
                   "s_mul_i32 s44, %0, 3072\nbuffer_load_dwordx4 v[40:43], %1, s[40:43], s44 offen\nbuffer_load_dwordx4 v[44:47], %1, s[40:43], s44 offen offset:1024\nbuffer_load_dwordx4 v[48:51], %1, s[40:43], s44 offen offset:2048\ns_waitcnt vmcnt(0)\n"
                   :: "s"(wv), "v"((t & 63) * 16) : "s40", "s41", "s42", "s43", "s44", "v40", "v41", "v42", "v43", "v44", "v45", "v46", "v47", "v48", "v49", "v50", "v51", "scc", "memory");
    }
#pragma unroll
    for (int i = 0; i < 16; ++i) asm volatile("" :: "v"(a[i]));
#pragma unroll
    for (int i = 0; i < 12; ++i) asm volatile("" :: "v"(b[i]));
    PSTAMP();
    return;
  }
  if (blockIdx.x >= 192) {
    const int w = t >> 6, lane = t & 63, q = lane & 15, g = lane >> 4, rb = blockIdx.x - 192;
    u32x2 xr[8];
#pragma unroll
    for (int i = 0; i < 8; ++i) xr[i] = *(const u32x2*)(x + (size_t)(rb * 16 + q) * 512 + (8 * w + i) * 16 + 4 * g);
    float v[8][4];
#pragma unroll
    for (int i = 0; i < 8; ++i) { v[i][0] = bflo(xr[i][0]); v[i][1] = bfhi(xr[i][0]); v[i][2] = bflo(xr[i][1]); v[i][3] = bfhi(xr[i][1]); }
    float mean, rstd; ln_stats<8, 4>(v, red, w, lane, mean, rstd);
#pragma unroll
    for (int s2 = 0; s2 < 4; ++s2) {
      u32x4 o;
#pragma unroll
      for (int b = 0; b < 2; ++b) { const int i = 2 * s2 + b;
        o[2 * b] = pkh2((v[i][0] - mean) * rstd, (v[i][1] - mean) * rstd); o[2 * b + 1] = pkh2((v[i][2] - mean) * rstd, (v[i][3] - mean) * rstd); }
      *(u32x4*)(xh + ((size_t)(rb * 16 + 4 * w + s2) * 64 + lane) * 8) = o;
    }
    PSTAMP();
    return;
  }
  const int cg = blockIdx.x >> 2, kq = blockIdx.x & 3;
  const int ch = t & 7, r0 = t >> 3;
  u32x4 w[4]; float cv[4];
#pragma unroll
  for (int i = 0; i < 4; ++i) { const int k = kq * 128 + r0 + 32 * i; w[i] = *(const u32x4*)(wmod + (size_t)k * 3072 + cg * 64 + ch * 8); cv[i] = bf2f(c[k]); }
  float acc[8] = {};
#pragma unroll
  for (int i = 0; i < 4; ++i) {
    const float s = rbf(cv[i] / (1.f + __expf(-cv[i])));
#pragma unroll
    for (int e = 0; e < 4; ++e) { acc[2 * e] += s * bflo(w[i][e]); acc[2 * e + 1] += s * bfhi(w[i][e]); }
  }
#pragma unroll
  for (int e = 0; e < 8; ++e) { float v = acc[e]; v += __shfl_xor(v, 8); v += __shfl_xor(v, 16); v += __shfl_xor(v, 32); acc[e] = v; }
  if ((t & 63) < 8) {
#pragma unroll
    for (int e = 0; e < 8; ++e) red[(t >> 6) * 64 + ch * 8 + e] = acc[e];
  }
  __syncthreads();
  if (t < 64) modp[kq * 3072 + cg * 64 + t] = red[t] + red[64 + t] + red[128 + t] + red[192 + t];
  PSTAMP();
}

#define GAS __attribute__((address_space(1)))
#define LAS __attribute__((address_space(3)))
constexpr size_t WS_FLAGS = 56 * MB;
constexpr int NFLAGS = 208;
constexpr unsigned LDS_TKT = 0x24800;
DI unsigned lane_id() { unsigned l; asm volatile("v_mbcnt_lo_u32_b32 %0, -1, 0\n\tv_mbcnt_hi_u32_b32 %0, -1, %0" : "=v"(l)); return l; }
DI void wg_barrier() { asm volatile("s_waitcnt vmcnt(0) lgkmcnt(0)\n\ts_barrier" ::: "memory"); }
DI void publish(unsigned* flags, int idx, unsigned wid) {
  wg_barrier();
  if (wid == 0) { if (lane_id() == 0) __hip_atomic_fetch_add((GAS unsigned*)(flags + idx * 64), 1u, __ATOMIC_RELAXED, __HIP_MEMORY_SCOPE_AGENT); }
}
DI void signal(unsigned* flags, int idx, unsigned wid) {
  if (wid == 0) { if (lane_id() == 0) __hip_atomic_fetch_add((GAS unsigned*)(flags + idx * 64), 1u, __ATOMIC_RELAXED, __HIP_MEMORY_SCOPE_AGENT); }
}
DI unsigned ticket(unsigned* flags, int idx, char* lds, unsigned wid) {
  LAS unsigned* w = (LAS unsigned*)(LAS char*)(lds + LDS_TKT);
  if (wid == 0) { if (lane_id() == 0) *w = __hip_atomic_fetch_add((GAS unsigned*)(flags + idx * 64), 1u, __ATOMIC_RELAXED, __HIP_MEMORY_SCOPE_AGENT); }
  asm volatile("s_waitcnt lgkmcnt(0)\n\ts_barrier" ::: "memory");
  return __builtin_amdgcn_readfirstlane(*w);
}
DI void wait_for(unsigned* flags, int idx, unsigned target, unsigned wid) {
  if (wid == 0) {
    GAS unsigned* w = (GAS unsigned*)(flags + idx * 64); GAS unsigned* tmo = (GAS unsigned*)(flags + 120 * 64);
    for (unsigned spins = 0;;) {
      if ((unsigned)__builtin_amdgcn_readfirstlane(__hip_atomic_load(w, __ATOMIC_RELAXED, __HIP_MEMORY_SCOPE_AGENT)) >= target) break;
      __builtin_amdgcn_s_sleep(1);
      if (++spins > (1u << 16)) { __hip_atomic_store(tmo, 1u, __ATOMIC_RELAXED, __HIP_MEMORY_SCOPE_AGENT); break; }
      if ((spins & 255u) == 0u && __builtin_amdgcn_readfirstlane(__hip_atomic_load(tmo, __ATOMIC_RELAXED, __HIP_MEMORY_SCOPE_AGENT))) break;
    }
  }
  asm volatile("s_waitcnt lgkmcnt(0)\n\ts_barrier" ::: "memory");
}
DI void wait_for2(unsigned* flags, int ia, unsigned ta, int ib, unsigned tb, unsigned wid) {
  if (wid == 0) {
    GAS unsigned* wa = (GAS unsigned*)(flags + ia * 64); GAS unsigned* wb = (GAS unsigned*)(flags + ib * 64); GAS unsigned* tmo = (GAS unsigned*)(flags + 120 * 64);
    for (unsigned spins = 0;;) {
      const unsigned va = __hip_atomic_load(wa, __ATOMIC_RELAXED, __HIP_MEMORY_SCOPE_AGENT), vb = __hip_atomic_load(wb, __ATOMIC_RELAXED, __HIP_MEMORY_SCOPE_AGENT);
      if ((unsigned)__builtin_amdgcn_readfirstlane(va) >= ta && (unsigned)__builtin_amdgcn_readfirstlane(vb) >= tb) break;
      __builtin_amdgcn_s_sleep(1);
      if (++spins > (1u << 16)) { __hip_atomic_store(tmo, 1u, __ATOMIC_RELAXED, __HIP_MEMORY_SCOPE_AGENT); break; }
      if ((spins & 255u) == 0u && __builtin_amdgcn_readfirstlane(__hip_atomic_load(tmo, __ATOMIC_RELAXED, __HIP_MEMORY_SCOPE_AGENT))) break;
    }
  }
  asm volatile("s_waitcnt lgkmcnt(0)\n\ts_barrier" ::: "memory");
}
constexpr unsigned LDS_MOD = 0x25000;
constexpr unsigned LDS_RED = 0x24000;
DI void mod_to_lds(const float* __restrict__ modp, char* lds, unsigned wid) {
  const unsigned t = wid * 64 + lane_id();
  f32x4 a[3][4];
#pragma unroll
  for (int i = 0; i < 3; ++i)
#pragma unroll
    for (int k = 0; k < 4; ++k) a[i][k] = *(const f32x4*)(modp + k * 3072 + (i * 256 + t) * 4);
  asm volatile("" ::: "memory"); __builtin_amdgcn_sched_barrier(0);
#pragma unroll
  for (int i = 0; i < 3; ++i) {
    f32x4 r;
#pragma unroll
    for (int k = 0; k < 4; ++k) r[k] = rbf((a[i][0][k] + a[i][1][k]) + (a[i][2][k] + a[i][3][k]));
    *(LAS f32x4*)(LAS char*)(lds + LDS_MOD + (i * 256 + t) * 16) = r;
  }
}
template <int CTRL> DI float dpp_add(float v) { return v + __builtin_bit_cast(float, __builtin_amdgcn_update_dpp(0, __builtin_bit_cast(int, v), CTRL, 0xf, 0xf, true)); }
DI float sum_q8(float v) {
  v = dpp_add<0x128>(v);
  { auto r = __builtin_amdgcn_permlane16_swap(__float_as_uint(v), __float_as_uint(v), false, false); v = __uint_as_float(r[0]) + __uint_as_float(r[1]); }
  { auto r = __builtin_amdgcn_permlane32_swap(__float_as_uint(v), __float_as_uint(v), false, false); v = __uint_as_float(r[0]) + __uint_as_float(r[1]); }
  return v;
}
struct LnX { u32x2 xr[2][2]; };
DI LnX ln2_load_x(const u16* __restrict__ x, unsigned wid, unsigned rb, unsigned half) {
  const unsigned l = lane_id(), fr = l >> 5, g = (l >> 3) & 3, q = 8 * half + (l & 7);
  LnX r;
#pragma unroll
  for (int i = 0; i < 2; ++i)
#pragma unroll
    for (int jh = 0; jh < 2; ++jh) r.xr[i][jh] = *(const u32x2*)(x + (size_t)(rb * 16 + q) * 512 + 32 * (4 * wid + 2 * i + fr) + 16 * jh + 4 * g);
  return r;
}
template <class F> DI void ln2_phase(F&& after_h2, const LnX& X, const u16* __restrict__ part, u16* __restrict__ x1, u16* __restrict__ h2, char* lds, unsigned wid, unsigned rb, unsigned half) {
  const unsigned l = lane_id(), fr = l >> 5, g = (l >> 3) & 3, q8 = l & 7, q = 8 * half + q8;
  const __amdgpu_buffer_rsrc_t rp = __builtin_amdgcn_make_buffer_rsrc((void*)part, 0, 16u << 20, 0x00020000);
  const __amdgpu_buffer_rsrc_t rh = __builtin_amdgcn_make_buffer_rsrc((void*)h2, 0, 2u << 20, 0x00020000);
  const __amdgpu_buffer_rsrc_t rx = __builtin_amdgcn_make_buffer_rsrc((void*)x1, 0, 2u << 20, 0x00020000);
  u32x4 p[2][8];
  const unsigned fo = (g * 16 + q) * 16;
#pragma unroll
  for (int i = 0; i < 2; ++i)
#pragma unroll
    for (int sp = 0; sp < 8; ++sp) p[i][sp] = __builtin_amdgcn_raw_buffer_load_b128(rp, ((sp * 128 + rb) * 16 + 4 * wid + 2 * i + fr) * 1024 + fo, 0, 16);
  asm volatile("" ::: "memory"); __builtin_amdgcn_sched_barrier(0);
  LAS float* md = (LAS float*)(LAS char*)(lds + LDS_MOD); LAS f32x2* red = (LAS f32x2*)(LAS char*)(lds + LDS_RED);
  float v[2][2][4];
#pragma unroll
  for (int i = 0; i < 2; ++i) {
    const unsigned s = 4 * wid + 2 * i + fr;
#pragma unroll
    for (int jh = 0; jh < 2; ++jh) {
      float a[4] = {0.f, 0.f, 0.f, 0.f};
#pragma unroll
      for (int sp = 0; sp < 8; ++sp) { const unsigned d0 = p[i][sp][2 * jh], d1 = p[i][sp][2 * jh + 1]; a[0] += bflo(d0); a[1] += bfhi(d0); a[2] += bflo(d1); a[3] += bfhi(d1); }
      const f32x4 ga = *(LAS f32x4*)(md + 1024 + 32 * s + 16 * jh + 4 * g);
      v[i][jh][0] = bflo(X.xr[i][jh][0]) + ga[0] * a[0]; v[i][jh][1] = bfhi(X.xr[i][jh][0]) + ga[1] * a[1]; v[i][jh][2] = bflo(X.xr[i][jh][1]) + ga[2] * a[2]; v[i][jh][3] = bfhi(X.xr[i][jh][1]) + ga[3] * a[3];
    }
  }
  float sum = 0.f;
#pragma unroll
  for (int i = 0; i < 2; ++i)
#pragma unroll
    for (int jh = 0; jh < 2; ++jh) sum += (v[i][jh][0] + v[i][jh][1]) + (v[i][jh][2] + v[i][jh][3]);
  const float mw = sum_q8(sum) * (1.f / 128.f);
  float qd = 0.f;
#pragma unroll
  for (int i = 0; i < 2; ++i)
#pragma unroll
    for (int jh = 0; jh < 2; ++jh)
#pragma unroll
      for (int r = 0; r < 4; ++r) { const float d = v[i][jh][r] - mw; qd += d * d; }
  qd = sum_q8(qd);
  if (l < 8) red[wid * 8 + l] = (f32x2){mw, qd};
  asm volatile("s_waitcnt lgkmcnt(0)\n\ts_barrier" ::: "memory");
  const f32x2 r0 = red[q8], r1 = red[8 + q8], r2 = red[16 + q8], r3 = red[24 + q8];
  const float mean = ((r0[0] + r1[0]) + (r2[0] + r3[0])) * 0.25f;
  const float d0 = r0[0] - mean, d1 = r1[0] - mean, d2 = r2[0] - mean, d3 = r3[0] - mean;
  const float m2 = ((r0[1] + r1[1]) + (r2[1] + r3[1])) + 128.f * ((d0 * d0 + d1 * d1) + (d2 * d2 + d3 * d3));
  const float rstd = 1.f / sqrtf(m2 * (1.f / 512.f) + 1e-5f);
#pragma unroll
  for (int i = 0; i < 2; ++i) {
    const unsigned s = 4 * wid + 2 * i + fr;
    u32x4 o;
#pragma unroll
    for (int jh = 0; jh < 2; ++jh) {
      const f32x4 sb = *(LAS f32x4*)(md + 1536 + 32 * s + 16 * jh + 4 * g), cb = *(LAS f32x4*)(md + 2048 + 32 * s + 16 * jh + 4 * g);
      float h[4];
#pragma unroll
      for (int r = 0; r < 4; ++r) h[r] = (v[i][jh][r] - mean) * rstd * (1.f + cb[r]) + sb[r];
      o[2 * jh] = pk2(h[0], h[1]); o[2 * jh + 1] = pk2(h[2], h[3]);
    }
    __builtin_amdgcn_raw_buffer_store_b128(o, rh, (rb * 16 + s) * 1024 + fo, 0, 16);
  }
  after_h2();
#pragma unroll
  for (int i = 0; i < 2; ++i) {
    const u32x4 o = {pk2(v[i][0][0], v[i][0][1]), pk2(v[i][0][2], v[i][0][3]), pk2(v[i][1][0], v[i][1][1]), pk2(v[i][1][2], v[i][1][3])};
    __builtin_amdgcn_raw_buffer_store_b128(o, rx, (rb * 16 + 4 * wid + 2 * i + fr) * 1024 + fo, 0, 16);
  }
}
#define MAPD 0
#define RFL(x) __builtin_amdgcn_readfirstlane((unsigned)(x))
#define STAMP(i)

struct MainArgs { const u16 *x, *wq, *kc, *vc, *wo, *w1, *w3, *w2; u16* out; char* ws; };
typedef __attribute__((address_space(4))) const MainArgs* KArgs;
#define ARGS() asm volatile("" : "+s"(ap)); const u16 *x = ap->x, *wq = ap->wq, *kc = ap->kc, *vc = ap->vc, *wo = ap->wo, *w1 = ap->w1, *w3 = ap->w3, *w2 = ap->w2; u16* out = ap->out; char* ws = ap->ws; \
  const float* modp = (const float*)(ws + WS_MODP); u16 *h1 = (u16*)(ws + WS_H1), *o = (u16*)(ws + WS_O), *part = (u16*)(ws + WS_PART), *h2 = (u16*)(ws + WS_H2), *act = (u16*)(ws + WS_ACT); \
  u16* x1 = (u16*)(ws + WS_X1); unsigned* flags = (unsigned*)(ws + WS_FLAGS); char* dbg = ws + 60 * MB; const size_t dbgb = (size_t)dbg + MB + (size_t)(b * 4 + wid) * 256; \
  (void)x; (void)wq; (void)kc; (void)vc; (void)wo; (void)w1; (void)w3; (void)w2; (void)out; (void)modp; (void)h1; (void)o; (void)part; (void)h2; (void)act; (void)x1; (void)flags; (void)dbgb;
extern "C" __global__ __launch_bounds__(256, 1) void k_main(MainArgs args_) {
  extern __shared__ __attribute__((aligned(16))) char lds[];
  KArgs ap = (KArgs)__builtin_amdgcn_kernarg_segment_ptr();
  const unsigned b = blockIdx.x, wid = __builtin_amdgcn_readfirstlane(threadIdx.x >> 6);
  const unsigned sp = b & 7, nt = (b >> 3) & 3, pan = b >> 5, j = b & 31, head = sp * 4 + nt;
  {
    ARGS();
    const size_t kb = (size_t)kc + (size_t)head * 524288, vb = (size_t)vc + (size_t)head * 524288 - 896, wb = (size_t)wq + head * 256 - 896;
    const size_t hb = (size_t)h1 + (size_t)(pan * 16 + wid * 4) * 16384, ob = (size_t)o + ((size_t)(pan * 16 + wid * 4) * 128 + head * 4) * 1024;
    const size_t db = (size_t)dbg + (size_t)(b * 4 + wid) * 128, wob = (size_t)wo + (size_t)sp * 512 * 1024 + nt * 256 - 896;
    const unsigned gw = b * 4 + wid; size_t pfb; unsigned pfs = 1024;
    if (gw < 128) pfb = (size_t)w1 + (size_t)gw * 14336; else if (gw < 256) pfb = (size_t)w3 + (size_t)(gw - 128) * 14336; else if (gw < 384) pfb = (size_t)w2 + (size_t)(gw - 256) * 14336;
    else if (gw < 676) pfb = (size_t)wo + (size_t)(gw - 384) * 14336; else if (gw == 676) pfb = (size_t)wo + 4194304 - 14336; else { pfb = (size_t)wo; pfs = 0; }
  asm volatile(
"s_mov_b32 s36, %0\n"
"s_and_b32 s37, %1, 0xffff\n"
"s_mov_b32 s38, 524288\n"
"s_mov_b32 s39, 0x20000\n"
"s_mov_b32 s40, %2\n"
"s_and_b32 s41, %3, 0xffff\n"
"s_mov_b32 s42, 525184\n"
"s_mov_b32 s43, 0x20000\n"
"s_mov_b32 s44, %4\n"
"s_and_b32 s45, %5, 0xffff\n"
"s_mov_b32 s46, 4195200\n"
"s_mov_b32 s47, 0x20000\n"
"s_mov_b32 s48, %6\n"
"s_and_b32 s49, %7, 0xffff\n"
"s_mov_b32 s50, 65536\n"
"s_mov_b32 s51, 0x20000\n"
"s_mov_b32 s52, %8\n"
"s_and_b32 s53, %9, 0xffff\n"
"s_mov_b32 s54, 397312\n"
"s_mov_b32 s55, 0x20000\n"
"s_mov_b32 s56, %10\n"
"s_mov_b32 s66, %11\n"
"s_mov_b32 s67, %12\n"
"s_getpc_b64 s[72:73]\n"
"Lcode_begin:\n"
"s_and_b32 s73, s73, 0xffff\n"
"s_mov_b32 s74, Lcode_end-Lcode_begin\n"
"s_mov_b32 s75, 0x20000\n"
"v_mbcnt_lo_u32_b32 v252, -1, 0\n"
"v_mbcnt_hi_u32_b32 v252, -1, v252\n"
"v_lshlrev_b32 v252, 4, v252\n"
"s_lshl_b32 s76, %20, 10\n"
"buffer_load_dwordx4 a[252:255], v252, s[72:75], s76 offen\n"
"v_mbcnt_lo_u32_b32 v224, -1, 0\n"
"v_mbcnt_hi_u32_b32 v224, -1, v224\n"
"v_lshrrev_b32 v225, 4, v224\n"
"v_and_b32 v226, 15, v224\n"
"v_lshlrev_b32 v252, 4, v224\n"
"v_and_b32 v227, 1, v225\n"
"v_lshlrev_b32 v227, 1, v227\n"
"v_and_b32 v228, 3, v224\n"
"v_xor_b32 v227, v227, v228\n"
"v_lshlrev_b32 v227, 4, v227\n"
"v_lshrrev_b32 v228, 5, v224\n"
"v_lshl_add_u32 v227, v228, 6, v227\n"
"v_bfe_u32 v228, v224, 2, 3\n"
"v_lshl_add_u32 v217, v228, 13, v227\n"
"v_add_u32 v216, 896, v217\n"
"v_lshl_add_u32 v251, v228, 8, v227\n"
"v_add_u32 v250, 896, v251\n"
"v_xor_b32 v227, v226, v225\n"
"v_xor_b32 v228, 0, v227\n"
"v_lshlrev_b32 v228, 4, v228\n"
"v_lshl_add_u32 v246, v225, 8, v228\n"
"v_xor_b32 v228, 4, v227\n"
"v_lshlrev_b32 v228, 4, v228\n"
"v_lshl_add_u32 v247, v225, 8, v228\n"
"v_xor_b32 v228, 8, v227\n"
"v_lshlrev_b32 v228, 4, v228\n"
"v_lshl_add_u32 v248, v225, 8, v228\n"
"v_xor_b32 v228, 12, v227\n"
"v_lshlrev_b32 v228, 4, v228\n"
"v_lshl_add_u32 v249, v225, 8, v228\n"
"v_xor_b32 v228, 0, v227\n"
"v_lshlrev_b32 v228, 4, v228\n"
"v_lshl_add_u32 v240, v226, 8, v228\n"
"v_add_u32 v220, 131072, v240\n"
"v_xor_b32 v228, 4, v227\n"
"v_lshlrev_b32 v228, 4, v228\n"
"v_lshl_add_u32 v241, v226, 8, v228\n"
"v_add_u32 v221, 131072, v241\n"
"v_xor_b32 v228, 8, v227\n"
"v_lshlrev_b32 v228, 4, v228\n"
"v_lshl_add_u32 v242, v226, 8, v228\n"
"v_add_u32 v222, 131072, v242\n"
"v_xor_b32 v228, 12, v227\n"
"v_lshlrev_b32 v228, 4, v228\n"
"v_lshl_add_u32 v243, v226, 8, v228\n"
"v_add_u32 v223, 131072, v243\n"
"v_lshrrev_b32 v227, 1, v225\n"
"v_lshlrev_b32 v227, 11, v227\n"
"v_and_b32 v228, 1, v225\n"
"v_lshl_add_u32 v227, v228, 8, v227\n"
"v_bfe_u32 v253, v224, 2, 2\n"
"v_lshl_add_u32 v227, v253, 6, v227\n"
"v_and_b32 v253, 3, v224\n"
"v_lshl_add_u32 v254, v253, 3, v227\n"
"v_lshl_add_u32 v244, v228, 5, v254\n"
"v_xor_b32 v255, 1, v228\n"
"v_lshl_add_u32 v245, v255, 5, v254\n"
"v_lshlrev_b32 v255, 1, v228\n"
"v_xor_b32 v255, v255, v253\n"
"v_lshl_add_u32 v218, v255, 4, v227\n"
"v_mov_b32 v253, 0x3f803f80\n"
"s_mov_b32 s72, %13\n"
"s_mov_b32 s73, %14\n"
"v_lshlrev_b32 v227, 3, v224\n"
"s_lshl_b32 s62, s56, 9\n"
"v_add_u32 v227, s62, v227\n"
"v_add_u32 v228, 0, v227\n"
"global_load_dwordx2 v[192:193], v228, s[72:73]\n"
"global_load_dwordx2 v[200:201], v228, s[72:73] offset:2048\n"
"v_add_u32 v228, 12288, v227\n"
"global_load_dwordx2 v[194:195], v228, s[72:73]\n"
"global_load_dwordx2 v[202:203], v228, s[72:73] offset:2048\n"
"v_add_u32 v228, 24576, v227\n"
"global_load_dwordx2 v[196:197], v228, s[72:73]\n"
"global_load_dwordx2 v[204:205], v228, s[72:73] offset:2048\n"
"v_add_u32 v228, 36864, v227\n"
"global_load_dwordx2 v[198:199], v228, s[72:73]\n"
"global_load_dwordx2 v[206:207], v228, s[72:73] offset:2048\n"
"v_add_u32 v219, 0x10000, v218\n"
"s_mul_i32 s70, s56, 2048\n"
"s_mul_i32 s71, s56, 65536\n"
"s_add_u32 m0, s70, 0\n"
"s_add_u32 s62, s71, 0\n"
"buffer_load_dwordx4 v216, s[44:47], s62 offen lds\n"
"buffer_load_dwordx4 v217, s[44:47], s62 offen offset:1024 lds\n"
"s_add_u32 m0, s70, 8192\n"
"s_add_u32 s62, s71, 262144\n"
"buffer_load_dwordx4 v216, s[44:47], s62 offen lds\n"
"buffer_load_dwordx4 v217, s[44:47], s62 offen offset:1024 lds\n"
"s_mov_b32 s63, 0\n"
"buffer_load_dwordx4 v[128:131], v252, s[48:51], s63 offen offset:0\n"
"s_mov_b32 s63, 16384\n"
"buffer_load_dwordx4 v[132:135], v252, s[48:51], s63 offen offset:0\n"
"s_mov_b32 s63, 32768\n"
"buffer_load_dwordx4 v[136:139], v252, s[48:51], s63 offen offset:0\n"
"s_mov_b32 s63, 49152\n"
"buffer_load_dwordx4 v[140:143], v252, s[48:51], s63 offen offset:0\n"
"s_mov_b32 s63, 0\n"
"buffer_load_dwordx4 v[144:147], v252, s[48:51], s63 offen offset:1024\n"
"s_mov_b32 s63, 16384\n"
"buffer_load_dwordx4 v[148:151], v252, s[48:51], s63 offen offset:1024\n"
"s_mov_b32 s63, 32768\n"
"buffer_load_dwordx4 v[152:155], v252, s[48:51], s63 offen offset:1024\n"
"s_mov_b32 s63, 49152\n"
"buffer_load_dwordx4 v[156:159], v252, s[48:51], s63 offen offset:1024\n"
"s_add_u32 m0, s70, 16384\n"
"s_add_u32 s62, s71, 524288\n"
"buffer_load_dwordx4 v216, s[44:47], s62 offen lds\n"
"buffer_load_dwordx4 v217, s[44:47], s62 offen offset:1024 lds\n"
"s_add_u32 m0, s70, 24576\n"
"s_add_u32 s62, s71, 786432\n"
"buffer_load_dwordx4 v216, s[44:47], s62 offen lds\n"
"buffer_load_dwordx4 v217, s[44:47], s62 offen offset:1024 lds\n"
"s_mov_b32 s63, 0\n"
"buffer_load_dwordx4 v[160:163], v252, s[48:51], s63 offen offset:2048\n"
"s_mov_b32 s63, 16384\n"
"buffer_load_dwordx4 v[164:167], v252, s[48:51], s63 offen offset:2048\n"
"s_mov_b32 s63, 32768\n"
"buffer_load_dwordx4 v[168:171], v252, s[48:51], s63 offen offset:2048\n"
"s_mov_b32 s63, 49152\n"
"buffer_load_dwordx4 v[172:175], v252, s[48:51], s63 offen offset:2048\n"
"s_waitcnt vmcnt(20)\n"
"v_add_f32 v192, v192, v194\n"
"v_add_f32 v196, v196, v198\n"
"v_add_f32 v192, v192, v196\n"
"v_add_f32 v193, v193, v195\n"
"v_add_f32 v197, v197, v199\n"
"v_add_f32 v193, v193, v197\n"
"v_cvt_pk_bf16_f32 v192, v192, v193\n"
"v_and_b32 v193, 0xffff0000, v192\n"
"v_lshlrev_b32 v192, 16, v192\n"
"v_add_f32 v200, v200, v202\n"
"v_add_f32 v204, v204, v206\n"
"v_add_f32 v200, v200, v204\n"
"v_add_f32 v201, v201, v203\n"
"v_add_f32 v205, v205, v207\n"
"v_add_f32 v201, v201, v205\n"
"v_cvt_pk_bf16_f32 v200, v200, v201\n"
"v_and_b32 v201, 0xffff0000, v200\n"
"v_lshlrev_b32 v200, 16, v200\n"
"v_mov_b32 v228, 0x3e0293ee\n"
"v_add_f32 v200, 1.0, v200\n"
"v_add_f32 v201, 1.0, v201\n"
"v_mul_f32 v200, v200, v228\n"
"v_mul_f32 v201, v201, v228\n"
"v_mul_f32 v192, v192, v228\n"
"v_mul_f32 v193, v193, v228\n"
"v_add_u32 v227, 147456, v227\n"
"ds_write_b64 v227, v[200:201]\n"
"ds_write_b64 v227, v[192:193] offset:2048\n"
"s_waitcnt lgkmcnt(0)\n"
"v_lshlrev_b32 v229, 4, v225\n"
"v_add_u32 v229, 147456, v229\n"
"s_waitcnt vmcnt(16)\n"
"s_barrier\n"
"ds_read_b64_tr_b16 a[64:65], v218 offset:0\n"
"ds_read_b64_tr_b16 a[66:67], v218 offset:4096\n"
"ds_read_b64_tr_b16 a[68:69], v218 offset:8\n"
"ds_read_b64_tr_b16 a[70:71], v218 offset:4104\n"
"ds_read_b64_tr_b16 a[72:73], v218 offset:512\n"
"ds_read_b64_tr_b16 a[74:75], v218 offset:4608\n"
"ds_read_b64_tr_b16 a[76:77], v218 offset:520\n"
"ds_read_b64_tr_b16 a[78:79], v218 offset:4616\n"
"ds_read_b64_tr_b16 a[80:81], v218 offset:1024\n"
"ds_read_b64_tr_b16 a[82:83], v218 offset:5120\n"
"ds_read_b64_tr_b16 a[84:85], v218 offset:1032\n"
"ds_read_b64_tr_b16 a[86:87], v218 offset:5128\n"
"ds_read_b64_tr_b16 a[88:89], v218 offset:1536\n"
"ds_read_b64_tr_b16 a[90:91], v218 offset:5632\n"
"ds_read_b64_tr_b16 a[92:93], v218 offset:1544\n"
"ds_read_b64_tr_b16 a[94:95], v218 offset:5640\n"
"s_waitcnt vmcnt(12)\n"
"ds_read_b128 v[192:195], v229 offset:0\n"
"ds_read_b128 v[196:199], v229 offset:64\n"
"ds_read_b128 v[200:203], v229 offset:2048\n"
"ds_read_b128 v[204:207], v229 offset:2112\n"
"ds_read_b128 v[230:233], v229 offset:128\n"
"ds_read_b128 v[234:237], v229 offset:192\n"
"ds_read_b128 v[238:241], v229 offset:2176\n"
"ds_read_b128 v[242:245], v229 offset:2240\n"
"s_waitcnt lgkmcnt(0)\n"
"v_fma_mix_f32 v208, v128, v192, v200 op_sel:[0,0,0] op_sel_hi:[1,0,0]\n"
"v_fma_mix_f32 v209, v128, v193, v201 op_sel:[1,0,0] op_sel_hi:[1,0,0]\n"
"v_fma_mix_f32 v210, v129, v194, v202 op_sel:[0,0,0] op_sel_hi:[1,0,0]\n"
"v_fma_mix_f32 v211, v129, v195, v203 op_sel:[1,0,0] op_sel_hi:[1,0,0]\n"
"v_cvt_pk_bf16_f32 v128, v208, v209\n"
"v_fma_mix_f32 v212, v130, v196, v204 op_sel:[0,0,0] op_sel_hi:[1,0,0]\n"
"v_fma_mix_f32 v213, v130, v197, v205 op_sel:[1,0,0] op_sel_hi:[1,0,0]\n"
"v_cvt_pk_bf16_f32 v129, v210, v211\n"
"v_fma_mix_f32 v214, v131, v198, v206 op_sel:[0,0,0] op_sel_hi:[1,0,0]\n"
"v_fma_mix_f32 v215, v131, v199, v207 op_sel:[1,0,0] op_sel_hi:[1,0,0]\n"
"v_cvt_pk_bf16_f32 v130, v212, v213\n"
"v_fma_mix_f32 v208, v132, v192, v200 op_sel:[0,0,0] op_sel_hi:[1,0,0]\n"
"v_fma_mix_f32 v209, v132, v193, v201 op_sel:[1,0,0] op_sel_hi:[1,0,0]\n"
"v_cvt_pk_bf16_f32 v131, v214, v215\n"
"v_fma_mix_f32 v210, v133, v194, v202 op_sel:[0,0,0] op_sel_hi:[1,0,0]\n"
"v_fma_mix_f32 v211, v133, v195, v203 op_sel:[1,0,0] op_sel_hi:[1,0,0]\n"
"v_cvt_pk_bf16_f32 v132, v208, v209\n"
"v_fma_mix_f32 v212, v134, v196, v204 op_sel:[0,0,0] op_sel_hi:[1,0,0]\n"
"v_fma_mix_f32 v213, v134, v197, v205 op_sel:[1,0,0] op_sel_hi:[1,0,0]\n"
"v_cvt_pk_bf16_f32 v133, v210, v211\n"
"v_fma_mix_f32 v214, v135, v198, v206 op_sel:[0,0,0] op_sel_hi:[1,0,0]\n"
"v_fma_mix_f32 v215, v135, v199, v207 op_sel:[1,0,0] op_sel_hi:[1,0,0]\n"
"v_cvt_pk_bf16_f32 v134, v212, v213\n"
"v_fma_mix_f32 v208, v136, v192, v200 op_sel:[0,0,0] op_sel_hi:[1,0,0]\n"
"v_fma_mix_f32 v209, v136, v193, v201 op_sel:[1,0,0] op_sel_hi:[1,0,0]\n"
"v_cvt_pk_bf16_f32 v135, v214, v215\n"
"v_fma_mix_f32 v210, v137, v194, v202 op_sel:[0,0,0] op_sel_hi:[1,0,0]\n"
"v_fma_mix_f32 v211, v137, v195, v203 op_sel:[1,0,0] op_sel_hi:[1,0,0]\n"
"v_cvt_pk_bf16_f32 v136, v208, v209\n"
"v_fma_mix_f32 v212, v138, v196, v204 op_sel:[0,0,0] op_sel_hi:[1,0,0]\n"
"v_fma_mix_f32 v213, v138, v197, v205 op_sel:[1,0,0] op_sel_hi:[1,0,0]\n"
"v_cvt_pk_bf16_f32 v137, v210, v211\n"
"v_fma_mix_f32 v214, v139, v198, v206 op_sel:[0,0,0] op_sel_hi:[1,0,0]\n"
"v_fma_mix_f32 v215, v139, v199, v207 op_sel:[1,0,0] op_sel_hi:[1,0,0]\n"
"v_cvt_pk_bf16_f32 v138, v212, v213\n"
"v_fma_mix_f32 v208, v140, v192, v200 op_sel:[0,0,0] op_sel_hi:[1,0,0]\n"
"v_fma_mix_f32 v209, v140, v193, v201 op_sel:[1,0,0] op_sel_hi:[1,0,0]\n"
"v_cvt_pk_bf16_f32 v139, v214, v215\n"
"v_fma_mix_f32 v210, v141, v194, v202 op_sel:[0,0,0] op_sel_hi:[1,0,0]\n"
"v_fma_mix_f32 v211, v141, v195, v203 op_sel:[1,0,0] op_sel_hi:[1,0,0]\n"
"v_cvt_pk_bf16_f32 v140, v208, v209\n"
"v_fma_mix_f32 v212, v142, v196, v204 op_sel:[0,0,0] op_sel_hi:[1,0,0]\n"
"v_fma_mix_f32 v213, v142, v197, v205 op_sel:[1,0,0] op_sel_hi:[1,0,0]\n"
"v_cvt_pk_bf16_f32 v141, v210, v211\n"
"v_fma_mix_f32 v214, v143, v198, v206 op_sel:[0,0,0] op_sel_hi:[1,0,0]\n"
"v_fma_mix_f32 v215, v143, v199, v207 op_sel:[1,0,0] op_sel_hi:[1,0,0]\n"
"v_cvt_pk_bf16_f32 v142, v212, v213\n"
"v_cvt_pk_bf16_f32 v143, v214, v215\n"
"s_waitcnt vmcnt(8) lgkmcnt(0)\n"
"v_mfma_f32_16x16x32_bf16 v[0:3], a[64:67], v[128:131], 0\n"
"ds_read_b64_tr_b16 a[96:97], v218 offset:8192\n"
"s_mov_b32 s63, 0\n"
"buffer_load_dwordx4 v[176:179], v252, s[48:51], s63 offen offset:3072\n"
"v_mfma_f32_16x16x32_bf16 v[4:7], a[64:67], v[132:135], 0\n"
"ds_read_b64_tr_b16 a[98:99], v218 offset:12288\n"
"v_fma_mix_f32 v208, v144, v230, v238 op_sel:[0,0,0] op_sel_hi:[1,0,0]\n"
"v_fma_mix_f32 v209, v144, v231, v239 op_sel:[1,0,0] op_sel_hi:[1,0,0]\n"
"v_mfma_f32_16x16x32_bf16 v[8:11], a[64:67], v[136:139], 0\n"
"ds_read_b64_tr_b16 a[100:101], v218 offset:8200\n"
"s_mov_b32 s63, 16384\n"
"buffer_load_dwordx4 v[180:183], v252, s[48:51], s63 offen offset:3072\n"
"v_mfma_f32_16x16x32_bf16 v[12:15], a[64:67], v[140:143], 0\n"
"ds_read_b64_tr_b16 a[102:103], v218 offset:12296\n"
"v_fma_mix_f32 v210, v145, v232, v240 op_sel:[0,0,0] op_sel_hi:[1,0,0]\n"
"v_fma_mix_f32 v211, v145, v233, v241 op_sel:[1,0,0] op_sel_hi:[1,0,0]\n"
"v_mfma_f32_16x16x32_bf16 v[16:19], a[68:71], v[128:131], 0\n"
"ds_read_b64_tr_b16 a[104:105], v218 offset:8704\n"
"s_mov_b32 s63, 32768\n"
"buffer_load_dwordx4 v[184:187], v252, s[48:51], s63 offen offset:3072\n"
"v_mfma_f32_16x16x32_bf16 v[20:23], a[68:71], v[132:135], 0\n"
"ds_read_b64_tr_b16 a[106:107], v218 offset:12800\n"
"v_cvt_pk_bf16_f32 v144, v208, v209\n"
"v_fma_mix_f32 v212, v146, v234, v242 op_sel:[0,0,0] op_sel_hi:[1,0,0]\n"
"v_mfma_f32_16x16x32_bf16 v[24:27], a[68:71], v[136:139], 0\n"
"ds_read_b64_tr_b16 a[108:109], v218 offset:8712\n"
"s_mov_b32 s63, 49152\n"
"buffer_load_dwordx4 v[188:191], v252, s[48:51], s63 offen offset:3072\n"
"v_mfma_f32_16x16x32_bf16 v[28:31], a[68:71], v[140:143], 0\n"
"ds_read_b64_tr_b16 a[110:111], v218 offset:12808\n"
"v_fma_mix_f32 v213, v146, v235, v243 op_sel:[1,0,0] op_sel_hi:[1,0,0]\n"
"v_mfma_f32_16x16x32_bf16 v[32:35], a[72:75], v[128:131], 0\n"
"ds_read_b64_tr_b16 a[112:113], v218 offset:9216\n"
"v_cvt_pk_bf16_f32 v145, v210, v211\n"
"v_fma_mix_f32 v214, v147, v236, v244 op_sel:[0,0,0] op_sel_hi:[1,0,0]\n"
"v_mfma_f32_16x16x32_bf16 v[36:39], a[72:75], v[132:135], 0\n"
"ds_read_b64_tr_b16 a[114:115], v218 offset:13312\n"
"v_fma_mix_f32 v215, v147, v237, v245 op_sel:[1,0,0] op_sel_hi:[1,0,0]\n"
"v_cvt_pk_bf16_f32 v146, v212, v213\n"
"v_mfma_f32_16x16x32_bf16 v[40:43], a[72:75], v[136:139], 0\n"
"ds_read_b64_tr_b16 a[116:117], v218 offset:9224\n"
"v_fma_mix_f32 v208, v148, v230, v238 op_sel:[0,0,0] op_sel_hi:[1,0,0]\n"
"v_mfma_f32_16x16x32_bf16 v[44:47], a[72:75], v[140:143], 0\n"
"ds_read_b64_tr_b16 a[118:119], v218 offset:13320\n"
"v_fma_mix_f32 v209, v148, v231, v239 op_sel:[1,0,0] op_sel_hi:[1,0,0]\n"
"v_cvt_pk_bf16_f32 v147, v214, v215\n"
"v_mfma_f32_16x16x32_bf16 v[48:51], a[76:79], v[128:131], 0\n"
"ds_read_b64_tr_b16 a[120:121], v218 offset:9728\n"
"v_fma_mix_f32 v210, v149, v232, v240 op_sel:[0,0,0] op_sel_hi:[1,0,0]\n"
"v_fma_mix_f32 v211, v149, v233, v241 op_sel:[1,0,0] op_sel_hi:[1,0,0]\n"
"v_mfma_f32_16x16x32_bf16 v[52:55], a[76:79], v[132:135], 0\n"
"ds_read_b64_tr_b16 a[122:123], v218 offset:13824\n"
"v_cvt_pk_bf16_f32 v148, v208, v209\n"
"v_fma_mix_f32 v212, v150, v234, v242 op_sel:[0,0,0] op_sel_hi:[1,0,0]\n"
"v_mfma_f32_16x16x32_bf16 v[56:59], a[76:79], v[136:139], 0\n"
"ds_read_b64_tr_b16 a[124:125], v218 offset:9736\n"
"v_fma_mix_f32 v213, v150, v235, v243 op_sel:[1,0,0] op_sel_hi:[1,0,0]\n"
"v_mfma_f32_16x16x32_bf16 v[60:63], a[76:79], v[140:143], 0\n"
"ds_read_b64_tr_b16 a[126:127], v218 offset:13832\n"
"v_cvt_pk_bf16_f32 v149, v210, v211\n"
"v_fma_mix_f32 v214, v151, v236, v244 op_sel:[0,0,0] op_sel_hi:[1,0,0]\n"
"v_mfma_f32_16x16x32_bf16 v[64:67], a[80:83], v[128:131], 0\n"
"ds_read_b128 v[192:195], v229 offset:256\n"
"v_fma_mix_f32 v215, v151, v237, v245 op_sel:[1,0,0] op_sel_hi:[1,0,0]\n"
"v_cvt_pk_bf16_f32 v150, v212, v213\n"
"v_mfma_f32_16x16x32_bf16 v[68:71], a[80:83], v[132:135], 0\n"
"ds_read_b128 v[196:199], v229 offset:320\n"
"v_fma_mix_f32 v208, v152, v230, v238 op_sel:[0,0,0] op_sel_hi:[1,0,0]\n"
"v_mfma_f32_16x16x32_bf16 v[72:75], a[80:83], v[136:139], 0\n"
"ds_read_b128 v[200:203], v229 offset:2304\n"
"v_fma_mix_f32 v209, v152, v231, v239 op_sel:[1,0,0] op_sel_hi:[1,0,0]\n"
"v_cvt_pk_bf16_f32 v151, v214, v215\n"
"v_mfma_f32_16x16x32_bf16 v[76:79], a[80:83], v[140:143], 0\n"
"ds_read_b128 v[204:207], v229 offset:2368\n"
"v_fma_mix_f32 v210, v153, v232, v240 op_sel:[0,0,0] op_sel_hi:[1,0,0]\n"
"v_fma_mix_f32 v211, v153, v233, v241 op_sel:[1,0,0] op_sel_hi:[1,0,0]\n"
"v_mfma_f32_16x16x32_bf16 v[80:83], a[84:87], v[128:131], 0\n"
"s_add_u32 m0, s70, 32768\n"
"s_add_u32 s62, s71, 1048576\n"
"buffer_load_dwordx4 v216, s[44:47], s62 offen lds\n"
"v_mfma_f32_16x16x32_bf16 v[84:87], a[84:87], v[132:135], 0\n"
"v_cvt_pk_bf16_f32 v152, v208, v209\n"
"v_fma_mix_f32 v212, v154, v234, v242 op_sel:[0,0,0] op_sel_hi:[1,0,0]\n"
"v_mfma_f32_16x16x32_bf16 v[88:91], a[84:87], v[136:139], 0\n"
"v_fma_mix_f32 v213, v154, v235, v243 op_sel:[1,0,0] op_sel_hi:[1,0,0]\n"
"v_cvt_pk_bf16_f32 v153, v210, v211\n"
"v_mfma_f32_16x16x32_bf16 v[92:95], a[84:87], v[140:143], 0\n"
"buffer_load_dwordx4 v217, s[44:47], s62 offen offset:1024 lds\n"
"v_mfma_f32_16x16x32_bf16 v[96:99], a[88:91], v[128:131], 0\n"
"v_fma_mix_f32 v214, v155, v236, v244 op_sel:[0,0,0] op_sel_hi:[1,0,0]\n"
"v_fma_mix_f32 v215, v155, v237, v245 op_sel:[1,0,0] op_sel_hi:[1,0,0]\n"
"v_cvt_pk_bf16_f32 v154, v212, v213\n"
"v_mfma_f32_16x16x32_bf16 v[100:103], a[88:91], v[132:135], 0\n"
"v_fma_mix_f32 v208, v156, v230, v238 op_sel:[0,0,0] op_sel_hi:[1,0,0]\n"
"v_fma_mix_f32 v209, v156, v231, v239 op_sel:[1,0,0] op_sel_hi:[1,0,0]\n"
"v_cvt_pk_bf16_f32 v155, v214, v215\n"
"v_mfma_f32_16x16x32_bf16 v[104:107], a[88:91], v[136:139], 0\n"
"s_add_u32 m0, s70, 40960\n"
"s_add_u32 s62, s71, 1310720\n"
"buffer_load_dwordx4 v216, s[44:47], s62 offen lds\n"
"v_mfma_f32_16x16x32_bf16 v[108:111], a[88:91], v[140:143], 0\n"
"v_fma_mix_f32 v210, v157, v232, v240 op_sel:[0,0,0] op_sel_hi:[1,0,0]\n"
"v_fma_mix_f32 v211, v157, v233, v241 op_sel:[1,0,0] op_sel_hi:[1,0,0]\n"
"v_mfma_f32_16x16x32_bf16 v[112:115], a[92:95], v[128:131], 0\n"
"v_cvt_pk_bf16_f32 v156, v208, v209\n"
"v_fma_mix_f32 v212, v158, v234, v242 op_sel:[0,0,0] op_sel_hi:[1,0,0]\n"
"v_fma_mix_f32 v213, v158, v235, v243 op_sel:[1,0,0] op_sel_hi:[1,0,0]\n"
"v_mfma_f32_16x16x32_bf16 v[116:119], a[92:95], v[132:135], 0\n"
"buffer_load_dwordx4 v217, s[44:47], s62 offen offset:1024 lds\n"
"v_mfma_f32_16x16x32_bf16 v[120:123], a[92:95], v[136:139], 0\n"
"v_cvt_pk_bf16_f32 v157, v210, v211\n"
"v_fma_mix_f32 v214, v159, v236, v244 op_sel:[0,0,0] op_sel_hi:[1,0,0]\n"
"v_fma_mix_f32 v215, v159, v237, v245 op_sel:[1,0,0] op_sel_hi:[1,0,0]\n"
"v_mfma_f32_16x16x32_bf16 v[124:127], a[92:95], v[140:143], 0\n"
"v_cvt_pk_bf16_f32 v158, v212, v213\n"
"v_cvt_pk_bf16_f32 v159, v214, v215\n"
"s_waitcnt vmcnt(8) lgkmcnt(0)\n"
"s_barrier\n"
"v_mfma_f32_16x16x32_bf16 v[0:3], a[96:99], v[144:147], v[0:3]\n"
"ds_read_b64_tr_b16 a[64:65], v218 offset:16384\n"
"s_mov_b32 s63, 4096\n"
"buffer_load_dwordx4 v[128:131], v252, s[48:51], s63 offen offset:0\n"
"v_mfma_f32_16x16x32_bf16 v[4:7], a[96:99], v[148:151], v[4:7]\n"
"ds_read_b64_tr_b16 a[66:67], v218 offset:20480\n"
"v_fma_mix_f32 v208, v160, v192, v200 op_sel:[0,0,0] op_sel_hi:[1,0,0]\n"
"v_fma_mix_f32 v209, v160, v193, v201 op_sel:[1,0,0] op_sel_hi:[1,0,0]\n"
"v_mfma_f32_16x16x32_bf16 v[8:11], a[96:99], v[152:155], v[8:11]\n"
"ds_read_b64_tr_b16 a[68:69], v218 offset:16392\n"
"s_mov_b32 s63, 20480\n"
"buffer_load_dwordx4 v[132:135], v252, s[48:51], s63 offen offset:0\n"
"v_mfma_f32_16x16x32_bf16 v[12:15], a[96:99], v[156:159], v[12:15]\n"
"ds_read_b64_tr_b16 a[70:71], v218 offset:20488\n"
"v_fma_mix_f32 v210, v161, v194, v202 op_sel:[0,0,0] op_sel_hi:[1,0,0]\n"
"v_fma_mix_f32 v211, v161, v195, v203 op_sel:[1,0,0] op_sel_hi:[1,0,0]\n"
"v_mfma_f32_16x16x32_bf16 v[16:19], a[100:103], v[144:147], v[16:19]\n"
"ds_read_b64_tr_b16 a[72:73], v218 offset:16896\n"
"s_mov_b32 s63, 36864\n"
"buffer_load_dwordx4 v[136:139], v252, s[48:51], s63 offen offset:0\n"
"v_mfma_f32_16x16x32_bf16 v[20:23], a[100:103], v[148:151], v[20:23]\n"
"ds_read_b64_tr_b16 a[74:75], v218 offset:20992\n"
"v_cvt_pk_bf16_f32 v160, v208, v209\n"
"v_fma_mix_f32 v212, v162, v196, v204 op_sel:[0,0,0] op_sel_hi:[1,0,0]\n"
"v_mfma_f32_16x16x32_bf16 v[24:27], a[100:103], v[152:155], v[24:27]\n"
"ds_read_b64_tr_b16 a[76:77], v218 offset:16904\n"
"s_mov_b32 s63, 53248\n"
"buffer_load_dwordx4 v[140:143], v252, s[48:51], s63 offen offset:0\n"
"v_mfma_f32_16x16x32_bf16 v[28:31], a[100:103], v[156:159], v[28:31]\n"
"ds_read_b64_tr_b16 a[78:79], v218 offset:21000\n"
"v_fma_mix_f32 v213, v162, v197, v205 op_sel:[1,0,0] op_sel_hi:[1,0,0]\n"
"v_mfma_f32_16x16x32_bf16 v[32:35], a[104:107], v[144:147], v[32:35]\n"
"ds_read_b64_tr_b16 a[80:81], v218 offset:17408\n"
"v_cvt_pk_bf16_f32 v161, v210, v211\n"
"v_fma_mix_f32 v214, v163, v198, v206 op_sel:[0,0,0] op_sel_hi:[1,0,0]\n"
"v_mfma_f32_16x16x32_bf16 v[36:39], a[104:107], v[148:151], v[36:39]\n"
"ds_read_b64_tr_b16 a[82:83], v218 offset:21504\n"
"v_fma_mix_f32 v215, v163, v199, v207 op_sel:[1,0,0] op_sel_hi:[1,0,0]\n"
"v_cvt_pk_bf16_f32 v162, v212, v213\n"
"v_mfma_f32_16x16x32_bf16 v[40:43], a[104:107], v[152:155], v[40:43]\n"
"ds_read_b64_tr_b16 a[84:85], v218 offset:17416\n"
"v_fma_mix_f32 v208, v164, v192, v200 op_sel:[0,0,0] op_sel_hi:[1,0,0]\n"
"v_mfma_f32_16x16x32_bf16 v[44:47], a[104:107], v[156:159], v[44:47]\n"
"ds_read_b64_tr_b16 a[86:87], v218 offset:21512\n"
"v_fma_mix_f32 v209, v164, v193, v201 op_sel:[1,0,0] op_sel_hi:[1,0,0]\n"
"v_cvt_pk_bf16_f32 v163, v214, v215\n"
"v_mfma_f32_16x16x32_bf16 v[48:51], a[108:111], v[144:147], v[48:51]\n"
"ds_read_b64_tr_b16 a[88:89], v218 offset:17920\n"
"v_fma_mix_f32 v210, v165, v194, v202 op_sel:[0,0,0] op_sel_hi:[1,0,0]\n"
"v_fma_mix_f32 v211, v165, v195, v203 op_sel:[1,0,0] op_sel_hi:[1,0,0]\n"
"v_mfma_f32_16x16x32_bf16 v[52:55], a[108:111], v[148:151], v[52:55]\n"
"ds_read_b64_tr_b16 a[90:91], v218 offset:22016\n"
"v_cvt_pk_bf16_f32 v164, v208, v209\n"
"v_fma_mix_f32 v212, v166, v196, v204 op_sel:[0,0,0] op_sel_hi:[1,0,0]\n"
"v_mfma_f32_16x16x32_bf16 v[56:59], a[108:111], v[152:155], v[56:59]\n"
"ds_read_b64_tr_b16 a[92:93], v218 offset:17928\n"
"v_fma_mix_f32 v213, v166, v197, v205 op_sel:[1,0,0] op_sel_hi:[1,0,0]\n"
"v_mfma_f32_16x16x32_bf16 v[60:63], a[108:111], v[156:159], v[60:63]\n"
"ds_read_b64_tr_b16 a[94:95], v218 offset:22024\n"
"v_cvt_pk_bf16_f32 v165, v210, v211\n"
"v_fma_mix_f32 v214, v167, v198, v206 op_sel:[0,0,0] op_sel_hi:[1,0,0]\n"
"v_mfma_f32_16x16x32_bf16 v[64:67], a[112:115], v[144:147], v[64:67]\n"
"ds_read_b128 v[230:233], v229 offset:384\n"
"v_fma_mix_f32 v215, v167, v199, v207 op_sel:[1,0,0] op_sel_hi:[1,0,0]\n"
"v_cvt_pk_bf16_f32 v166, v212, v213\n"
"v_mfma_f32_16x16x32_bf16 v[68:71], a[112:115], v[148:151], v[68:71]\n"
"ds_read_b128 v[234:237], v229 offset:448\n"
"v_fma_mix_f32 v208, v168, v192, v200 op_sel:[0,0,0] op_sel_hi:[1,0,0]\n"
"v_mfma_f32_16x16x32_bf16 v[72:75], a[112:115], v[152:155], v[72:75]\n"
"ds_read_b128 v[238:241], v229 offset:2432\n"
"v_fma_mix_f32 v209, v168, v193, v201 op_sel:[1,0,0] op_sel_hi:[1,0,0]\n"
"v_cvt_pk_bf16_f32 v167, v214, v215\n"
"v_mfma_f32_16x16x32_bf16 v[76:79], a[112:115], v[156:159], v[76:79]\n"
"ds_read_b128 v[242:245], v229 offset:2496\n"
"v_fma_mix_f32 v210, v169, v194, v202 op_sel:[0,0,0] op_sel_hi:[1,0,0]\n"
"v_fma_mix_f32 v211, v169, v195, v203 op_sel:[1,0,0] op_sel_hi:[1,0,0]\n"
"v_mfma_f32_16x16x32_bf16 v[80:83], a[116:119], v[144:147], v[80:83]\n"
"s_add_u32 m0, s70, 49152\n"
"s_add_u32 s62, s71, 1572864\n"
"buffer_load_dwordx4 v216, s[44:47], s62 offen lds\n"
"v_mfma_f32_16x16x32_bf16 v[84:87], a[116:119], v[148:151], v[84:87]\n"
"v_cvt_pk_bf16_f32 v168, v208, v209\n"
"v_fma_mix_f32 v212, v170, v196, v204 op_sel:[0,0,0] op_sel_hi:[1,0,0]\n"
"v_mfma_f32_16x16x32_bf16 v[88:91], a[116:119], v[152:155], v[88:91]\n"
"v_fma_mix_f32 v213, v170, v197, v205 op_sel:[1,0,0] op_sel_hi:[1,0,0]\n"
"v_cvt_pk_bf16_f32 v169, v210, v211\n"
"v_mfma_f32_16x16x32_bf16 v[92:95], a[116:119], v[156:159], v[92:95]\n"
"buffer_load_dwordx4 v217, s[44:47], s62 offen offset:1024 lds\n"
"v_mfma_f32_16x16x32_bf16 v[96:99], a[120:123], v[144:147], v[96:99]\n"
"v_fma_mix_f32 v214, v171, v198, v206 op_sel:[0,0,0] op_sel_hi:[1,0,0]\n"
"v_fma_mix_f32 v215, v171, v199, v207 op_sel:[1,0,0] op_sel_hi:[1,0,0]\n"
"v_cvt_pk_bf16_f32 v170, v212, v213\n"
"v_mfma_f32_16x16x32_bf16 v[100:103], a[120:123], v[148:151], v[100:103]\n"
"v_fma_mix_f32 v208, v172, v192, v200 op_sel:[0,0,0] op_sel_hi:[1,0,0]\n"
"v_fma_mix_f32 v209, v172, v193, v201 op_sel:[1,0,0] op_sel_hi:[1,0,0]\n"
"v_cvt_pk_bf16_f32 v171, v214, v215\n"
"v_mfma_f32_16x16x32_bf16 v[104:107], a[120:123], v[152:155], v[104:107]\n"
"s_add_u32 m0, s70, 57344\n"
"s_add_u32 s62, s71, 1835008\n"
"buffer_load_dwordx4 v216, s[44:47], s62 offen lds\n"
"v_mfma_f32_16x16x32_bf16 v[108:111], a[120:123], v[156:159], v[108:111]\n"
"v_fma_mix_f32 v210, v173, v194, v202 op_sel:[0,0,0] op_sel_hi:[1,0,0]\n"
"v_fma_mix_f32 v211, v173, v195, v203 op_sel:[1,0,0] op_sel_hi:[1,0,0]\n"
"v_mfma_f32_16x16x32_bf16 v[112:115], a[124:127], v[144:147], v[112:115]\n"
"v_cvt_pk_bf16_f32 v172, v208, v209\n"
"v_fma_mix_f32 v212, v174, v196, v204 op_sel:[0,0,0] op_sel_hi:[1,0,0]\n"
"v_fma_mix_f32 v213, v174, v197, v205 op_sel:[1,0,0] op_sel_hi:[1,0,0]\n"
"v_mfma_f32_16x16x32_bf16 v[116:119], a[124:127], v[148:151], v[116:119]\n"
"buffer_load_dwordx4 v217, s[44:47], s62 offen offset:1024 lds\n"
"v_mfma_f32_16x16x32_bf16 v[120:123], a[124:127], v[152:155], v[120:123]\n"
"v_cvt_pk_bf16_f32 v173, v210, v211\n"
"v_fma_mix_f32 v214, v175, v198, v206 op_sel:[0,0,0] op_sel_hi:[1,0,0]\n"
"v_fma_mix_f32 v215, v175, v199, v207 op_sel:[1,0,0] op_sel_hi:[1,0,0]\n"
"v_mfma_f32_16x16x32_bf16 v[124:127], a[124:127], v[156:159], v[124:127]\n"
"v_cvt_pk_bf16_f32 v174, v212, v213\n"
"v_cvt_pk_bf16_f32 v175, v214, v215\n"
"s_waitcnt vmcnt(12) lgkmcnt(0)\n"
"v_mfma_f32_16x16x32_bf16 v[0:3], a[64:67], v[160:163], v[0:3]\n"
"ds_read_b64_tr_b16 a[96:97], v218 offset:24576\n"
"s_mov_b32 s63, 4096\n"
"buffer_load_dwordx4 v[144:147], v252, s[48:51], s63 offen offset:1024\n"
"v_mfma_f32_16x16x32_bf16 v[4:7], a[64:67], v[164:167], v[4:7]\n"
"ds_read_b64_tr_b16 a[98:99], v218 offset:28672\n"
"v_fma_mix_f32 v208, v176, v230, v238 op_sel:[0,0,0] op_sel_hi:[1,0,0]\n"
"v_fma_mix_f32 v209, v176, v231, v239 op_sel:[1,0,0] op_sel_hi:[1,0,0]\n"
"v_mfma_f32_16x16x32_bf16 v[8:11], a[64:67], v[168:171], v[8:11]\n"
"ds_read_b64_tr_b16 a[100:101], v218 offset:24584\n"
"s_mov_b32 s63, 20480\n"
"buffer_load_dwordx4 v[148:151], v252, s[48:51], s63 offen offset:1024\n"
"v_mfma_f32_16x16x32_bf16 v[12:15], a[64:67], v[172:175], v[12:15]\n"
"ds_read_b64_tr_b16 a[102:103], v218 offset:28680\n"
"v_fma_mix_f32 v210, v177, v232, v240 op_sel:[0,0,0] op_sel_hi:[1,0,0]\n"
"v_mfma_f32_16x16x32_bf16 v[16:19], a[68:71], v[160:163], v[16:19]\n"
"ds_read_b64_tr_b16 a[104:105], v218 offset:25088\n"
"s_mov_b32 s63, 36864\n"
"buffer_load_dwordx4 v[152:155], v252, s[48:51], s63 offen offset:1024\n"
"v_mfma_f32_16x16x32_bf16 v[20:23], a[68:71], v[164:167], v[20:23]\n"
"ds_read_b64_tr_b16 a[106:107], v218 offset:29184\n"
"v_fma_mix_f32 v211, v177, v233, v241 op_sel:[1,0,0] op_sel_hi:[1,0,0]\n"
"v_cvt_pk_bf16_f32 v176, v208, v209\n"
"v_mfma_f32_16x16x32_bf16 v[24:27], a[68:71], v[168:171], v[24:27]\n"
"ds_read_b64_tr_b16 a[108:109], v218 offset:25096\n"
"s_mov_b32 s63, 53248\n"
"buffer_load_dwordx4 v[156:159], v252, s[48:51], s63 offen offset:1024\n"
"v_mfma_f32_16x16x32_bf16 v[28:31], a[68:71], v[172:175], v[28:31]\n"
"ds_read_b64_tr_b16 a[110:111], v218 offset:29192\n"
"v_fma_mix_f32 v212, v178, v234, v242 op_sel:[0,0,0] op_sel_hi:[1,0,0]\n"
"v_mfma_f32_16x16x32_bf16 v[32:35], a[72:75], v[160:163], v[32:35]\n"
"ds_read_b64_tr_b16 a[112:113], v218 offset:25600\n"
"v_fma_mix_f32 v213, v178, v235, v243 op_sel:[1,0,0] op_sel_hi:[1,0,0]\n"
"v_cvt_pk_bf16_f32 v177, v210, v211\n"
"v_mfma_f32_16x16x32_bf16 v[36:39], a[72:75], v[164:167], v[36:39]\n"
"ds_read_b64_tr_b16 a[114:115], v218 offset:29696\n"
"v_fma_mix_f32 v214, v179, v236, v244 op_sel:[0,0,0] op_sel_hi:[1,0,0]\n"
"v_mfma_f32_16x16x32_bf16 v[40:43], a[72:75], v[168:171], v[40:43]\n"
"ds_read_b64_tr_b16 a[116:117], v218 offset:25608\n"
"v_fma_mix_f32 v215, v179, v237, v245 op_sel:[1,0,0] op_sel_hi:[1,0,0]\n"
"v_mfma_f32_16x16x32_bf16 v[44:47], a[72:75], v[172:175], v[44:47]\n"
"ds_read_b64_tr_b16 a[118:119], v218 offset:29704\n"
"v_cvt_pk_bf16_f32 v178, v212, v213\n"
"v_fma_mix_f32 v208, v180, v230, v238 op_sel:[0,0,0] op_sel_hi:[1,0,0]\n"
"v_mfma_f32_16x16x32_bf16 v[48:51], a[76:79], v[160:163], v[48:51]\n"
"ds_read_b64_tr_b16 a[120:121], v218 offset:26112\n"
"v_fma_mix_f32 v209, v180, v231, v239 op_sel:[1,0,0] op_sel_hi:[1,0,0]\n"
"v_mfma_f32_16x16x32_bf16 v[52:55], a[76:79], v[164:167], v[52:55]\n"
"ds_read_b64_tr_b16 a[122:123], v218 offset:30208\n"
"v_cvt_pk_bf16_f32 v179, v214, v215\n"
"v_fma_mix_f32 v210, v181, v232, v240 op_sel:[0,0,0] op_sel_hi:[1,0,0]\n"
"v_mfma_f32_16x16x32_bf16 v[56:59], a[76:79], v[168:171], v[56:59]\n"
"ds_read_b64_tr_b16 a[124:125], v218 offset:26120\n"
"v_fma_mix_f32 v211, v181, v233, v241 op_sel:[1,0,0] op_sel_hi:[1,0,0]\n"
"v_mfma_f32_16x16x32_bf16 v[60:63], a[76:79], v[172:175], v[60:63]\n"
"ds_read_b64_tr_b16 a[126:127], v218 offset:30216\n"
"v_cvt_pk_bf16_f32 v180, v208, v209\n"
"v_fma_mix_f32 v212, v182, v234, v242 op_sel:[0,0,0] op_sel_hi:[1,0,0]\n"
"v_mfma_f32_16x16x32_bf16 v[64:67], a[80:83], v[160:163], v[64:67]\n"
"ds_read_b128 v[192:195], v229 offset:512\n"
"v_fma_mix_f32 v213, v182, v235, v243 op_sel:[1,0,0] op_sel_hi:[1,0,0]\n"
"v_mfma_f32_16x16x32_bf16 v[68:71], a[80:83], v[164:167], v[68:71]\n"
"ds_read_b128 v[196:199], v229 offset:576\n"
"v_cvt_pk_bf16_f32 v181, v210, v211\n"
"v_mfma_f32_16x16x32_bf16 v[72:75], a[80:83], v[168:171], v[72:75]\n"
"ds_read_b128 v[200:203], v229 offset:2560\n"
"v_fma_mix_f32 v214, v183, v236, v244 op_sel:[0,0,0] op_sel_hi:[1,0,0]\n"
"v_fma_mix_f32 v215, v183, v237, v245 op_sel:[1,0,0] op_sel_hi:[1,0,0]\n"
"v_mfma_f32_16x16x32_bf16 v[76:79], a[80:83], v[172:175], v[76:79]\n"
"ds_read_b128 v[204:207], v229 offset:2624\n"
"v_cvt_pk_bf16_f32 v182, v212, v213\n"
"v_mfma_f32_16x16x32_bf16 v[80:83], a[84:87], v[160:163], v[80:83]\n"
"s_add_u32 m0, s70, 65536\n"
"s_add_u32 s62, s71, 2097152\n"
"buffer_load_dwordx4 v216, s[44:47], s62 offen lds\n"
"v_mfma_f32_16x16x32_bf16 v[84:87], a[84:87], v[164:167], v[84:87]\n"
"v_fma_mix_f32 v208, v184, v230, v238 op_sel:[0,0,0] op_sel_hi:[1,0,0]\n"
"v_fma_mix_f32 v209, v184, v231, v239 op_sel:[1,0,0] op_sel_hi:[1,0,0]\n"
"v_cvt_pk_bf16_f32 v183, v214, v215\n"
"v_mfma_f32_16x16x32_bf16 v[88:91], a[84:87], v[168:171], v[88:91]\n"
"v_fma_mix_f32 v210, v185, v232, v240 op_sel:[0,0,0] op_sel_hi:[1,0,0]\n"
"v_fma_mix_f32 v211, v185, v233, v241 op_sel:[1,0,0] op_sel_hi:[1,0,0]\n"
"v_mfma_f32_16x16x32_bf16 v[92:95], a[84:87], v[172:175], v[92:95]\n"
"v_cvt_pk_bf16_f32 v184, v208, v209\n"
"v_fma_mix_f32 v212, v186, v234, v242 op_sel:[0,0,0] op_sel_hi:[1,0,0]\n"
"v_fma_mix_f32 v213, v186, v235, v243 op_sel:[1,0,0] op_sel_hi:[1,0,0]\n"
"v_mfma_f32_16x16x32_bf16 v[96:99], a[88:91], v[160:163], v[96:99]\n"
"v_cvt_pk_bf16_f32 v185, v210, v211\n"
"v_fma_mix_f32 v214, v187, v236, v244 op_sel:[0,0,0] op_sel_hi:[1,0,0]\n"
"v_mfma_f32_16x16x32_bf16 v[100:103], a[88:91], v[164:167], v[100:103]\n"
"v_fma_mix_f32 v215, v187, v237, v245 op_sel:[1,0,0] op_sel_hi:[1,0,0]\n"
"v_cvt_pk_bf16_f32 v186, v212, v213\n"
"v_mfma_f32_16x16x32_bf16 v[104:107], a[88:91], v[168:171], v[104:107]\n"
"buffer_load_dwordx4 v217, s[44:47], s62 offen offset:1024 lds\n"
"v_mfma_f32_16x16x32_bf16 v[108:111], a[88:91], v[172:175], v[108:111]\n"
"v_fma_mix_f32 v208, v188, v230, v238 op_sel:[0,0,0] op_sel_hi:[1,0,0]\n"
"v_fma_mix_f32 v209, v188, v231, v239 op_sel:[1,0,0] op_sel_hi:[1,0,0]\n"
"v_cvt_pk_bf16_f32 v187, v214, v215\n"
"v_mfma_f32_16x16x32_bf16 v[112:115], a[92:95], v[160:163], v[112:115]\n"
"v_fma_mix_f32 v210, v189, v232, v240 op_sel:[0,0,0] op_sel_hi:[1,0,0]\n"
"v_fma_mix_f32 v211, v189, v233, v241 op_sel:[1,0,0] op_sel_hi:[1,0,0]\n"
"v_mfma_f32_16x16x32_bf16 v[116:119], a[92:95], v[164:167], v[116:119]\n"
"v_cvt_pk_bf16_f32 v188, v208, v209\n"
"v_fma_mix_f32 v212, v190, v234, v242 op_sel:[0,0,0] op_sel_hi:[1,0,0]\n"
"v_fma_mix_f32 v213, v190, v235, v243 op_sel:[1,0,0] op_sel_hi:[1,0,0]\n"
"v_mfma_f32_16x16x32_bf16 v[120:123], a[92:95], v[168:171], v[120:123]\n"
"v_cvt_pk_bf16_f32 v189, v210, v211\n"
"v_fma_mix_f32 v214, v191, v236, v244 op_sel:[0,0,0] op_sel_hi:[1,0,0]\n"
"v_mfma_f32_16x16x32_bf16 v[124:127], a[92:95], v[172:175], v[124:127]\n"
"v_fma_mix_f32 v215, v191, v237, v245 op_sel:[1,0,0] op_sel_hi:[1,0,0]\n"
"v_cvt_pk_bf16_f32 v190, v212, v213\n"
"v_cvt_pk_bf16_f32 v191, v214, v215\n"
"s_waitcnt vmcnt(6) lgkmcnt(0)\n"
"s_barrier\n"
"v_mfma_f32_16x16x32_bf16 v[0:3], a[96:99], v[176:179], v[0:3]\n"
"ds_read_b64_tr_b16 a[64:65], v218 offset:32768\n"
"s_mov_b32 s63, 4096\n"
"buffer_load_dwordx4 v[160:163], v252, s[48:51], s63 offen offset:2048\n"
"v_mfma_f32_16x16x32_bf16 v[4:7], a[96:99], v[180:183], v[4:7]\n"
"ds_read_b64_tr_b16 a[66:67], v218 offset:36864\n"
"v_fma_mix_f32 v208, v128, v192, v200 op_sel:[0,0,0] op_sel_hi:[1,0,0]\n"
"v_fma_mix_f32 v209, v128, v193, v201 op_sel:[1,0,0] op_sel_hi:[1,0,0]\n"
"v_mfma_f32_16x16x32_bf16 v[8:11], a[96:99], v[184:187], v[8:11]\n"
"ds_read_b64_tr_b16 a[68:69], v218 offset:32776\n"
"s_mov_b32 s63, 20480\n"
"buffer_load_dwordx4 v[164:167], v252, s[48:51], s63 offen offset:2048\n"
"v_mfma_f32_16x16x32_bf16 v[12:15], a[96:99], v[188:191], v[12:15]\n"
"ds_read_b64_tr_b16 a[70:71], v218 offset:36872\n"
"v_fma_mix_f32 v210, v129, v194, v202 op_sel:[0,0,0] op_sel_hi:[1,0,0]\n"
"v_mfma_f32_16x16x32_bf16 v[16:19], a[100:103], v[176:179], v[16:19]\n"
"ds_read_b64_tr_b16 a[72:73], v218 offset:33280\n"
"s_mov_b32 s63, 36864\n"
"buffer_load_dwordx4 v[168:171], v252, s[48:51], s63 offen offset:2048\n"
"v_mfma_f32_16x16x32_bf16 v[20:23], a[100:103], v[180:183], v[20:23]\n"
"ds_read_b64_tr_b16 a[74:75], v218 offset:37376\n"
"v_fma_mix_f32 v211, v129, v195, v203 op_sel:[1,0,0] op_sel_hi:[1,0,0]\n"
"v_cvt_pk_bf16_f32 v128, v208, v209\n"
"v_mfma_f32_16x16x32_bf16 v[24:27], a[100:103], v[184:187], v[24:27]\n"
"ds_read_b64_tr_b16 a[76:77], v218 offset:33288\n"
"s_mov_b32 s63, 53248\n"
"buffer_load_dwordx4 v[172:175], v252, s[48:51], s63 offen offset:2048\n"
"v_mfma_f32_16x16x32_bf16 v[28:31], a[100:103], v[188:191], v[28:31]\n"
"ds_read_b64_tr_b16 a[78:79], v218 offset:37384\n"
"v_fma_mix_f32 v212, v130, v196, v204 op_sel:[0,0,0] op_sel_hi:[1,0,0]\n"
"v_mfma_f32_16x16x32_bf16 v[32:35], a[104:107], v[176:179], v[32:35]\n"
"ds_read_b64_tr_b16 a[80:81], v218 offset:33792\n"
"v_fma_mix_f32 v213, v130, v197, v205 op_sel:[1,0,0] op_sel_hi:[1,0,0]\n"
"v_cvt_pk_bf16_f32 v129, v210, v211\n"
"v_mfma_f32_16x16x32_bf16 v[36:39], a[104:107], v[180:183], v[36:39]\n"
"ds_read_b64_tr_b16 a[82:83], v218 offset:37888\n"
"v_fma_mix_f32 v214, v131, v198, v206 op_sel:[0,0,0] op_sel_hi:[1,0,0]\n"
"v_mfma_f32_16x16x32_bf16 v[40:43], a[104:107], v[184:187], v[40:43]\n"
"ds_read_b64_tr_b16 a[84:85], v218 offset:33800\n"
"v_fma_mix_f32 v215, v131, v199, v207 op_sel:[1,0,0] op_sel_hi:[1,0,0]\n"
"v_mfma_f32_16x16x32_bf16 v[44:47], a[104:107], v[188:191], v[44:47]\n"
"ds_read_b64_tr_b16 a[86:87], v218 offset:37896\n"
"v_cvt_pk_bf16_f32 v130, v212, v213\n"
"v_fma_mix_f32 v208, v132, v192, v200 op_sel:[0,0,0] op_sel_hi:[1,0,0]\n"
"v_mfma_f32_16x16x32_bf16 v[48:51], a[108:111], v[176:179], v[48:51]\n"
"ds_read_b64_tr_b16 a[88:89], v218 offset:34304\n"
"v_fma_mix_f32 v209, v132, v193, v201 op_sel:[1,0,0] op_sel_hi:[1,0,0]\n"
"v_mfma_f32_16x16x32_bf16 v[52:55], a[108:111], v[180:183], v[52:55]\n"
"ds_read_b64_tr_b16 a[90:91], v218 offset:38400\n"
"v_cvt_pk_bf16_f32 v131, v214, v215\n"
"v_fma_mix_f32 v210, v133, v194, v202 op_sel:[0,0,0] op_sel_hi:[1,0,0]\n"
"v_mfma_f32_16x16x32_bf16 v[56:59], a[108:111], v[184:187], v[56:59]\n"
"ds_read_b64_tr_b16 a[92:93], v218 offset:34312\n"
"v_fma_mix_f32 v211, v133, v195, v203 op_sel:[1,0,0] op_sel_hi:[1,0,0]\n"
"v_mfma_f32_16x16x32_bf16 v[60:63], a[108:111], v[188:191], v[60:63]\n"
"ds_read_b64_tr_b16 a[94:95], v218 offset:38408\n"
"v_cvt_pk_bf16_f32 v132, v208, v209\n"
"v_fma_mix_f32 v212, v134, v196, v204 op_sel:[0,0,0] op_sel_hi:[1,0,0]\n"
"v_mfma_f32_16x16x32_bf16 v[64:67], a[112:115], v[176:179], v[64:67]\n"
"ds_read_b128 v[230:233], v229 offset:640\n"
"v_fma_mix_f32 v213, v134, v197, v205 op_sel:[1,0,0] op_sel_hi:[1,0,0]\n"
"v_mfma_f32_16x16x32_bf16 v[68:71], a[112:115], v[180:183], v[68:71]\n"
"ds_read_b128 v[234:237], v229 offset:704\n"
"v_cvt_pk_bf16_f32 v133, v210, v211\n"
"v_mfma_f32_16x16x32_bf16 v[72:75], a[112:115], v[184:187], v[72:75]\n"
"ds_read_b128 v[238:241], v229 offset:2688\n"
"v_fma_mix_f32 v214, v135, v198, v206 op_sel:[0,0,0] op_sel_hi:[1,0,0]\n"
"v_fma_mix_f32 v215, v135, v199, v207 op_sel:[1,0,0] op_sel_hi:[1,0,0]\n"
"v_mfma_f32_16x16x32_bf16 v[76:79], a[112:115], v[188:191], v[76:79]\n"
"ds_read_b128 v[242:245], v229 offset:2752\n"
"v_cvt_pk_bf16_f32 v134, v212, v213\n"
"v_mfma_f32_16x16x32_bf16 v[80:83], a[116:119], v[176:179], v[80:83]\n"
"s_add_u32 m0, s70, 73728\n"
"s_add_u32 s62, s71, 2359296\n"
"buffer_load_dwordx4 v216, s[44:47], s62 offen lds\n"
"v_mfma_f32_16x16x32_bf16 v[84:87], a[116:119], v[180:183], v[84:87]\n"
"v_fma_mix_f32 v208, v136, v192, v200 op_sel:[0,0,0] op_sel_hi:[1,0,0]\n"
"v_fma_mix_f32 v209, v136, v193, v201 op_sel:[1,0,0] op_sel_hi:[1,0,0]\n"
"v_cvt_pk_bf16_f32 v135, v214, v215\n"
"v_mfma_f32_16x16x32_bf16 v[88:91], a[116:119], v[184:187], v[88:91]\n"
"v_fma_mix_f32 v210, v137, v194, v202 op_sel:[0,0,0] op_sel_hi:[1,0,0]\n"
"v_fma_mix_f32 v211, v137, v195, v203 op_sel:[1,0,0] op_sel_hi:[1,0,0]\n"
"v_mfma_f32_16x16x32_bf16 v[92:95], a[116:119], v[188:191], v[92:95]\n"
"v_cvt_pk_bf16_f32 v136, v208, v209\n"
"v_fma_mix_f32 v212, v138, v196, v204 op_sel:[0,0,0] op_sel_hi:[1,0,0]\n"
"v_fma_mix_f32 v213, v138, v197, v205 op_sel:[1,0,0] op_sel_hi:[1,0,0]\n"
"v_mfma_f32_16x16x32_bf16 v[96:99], a[120:123], v[176:179], v[96:99]\n"
"v_cvt_pk_bf16_f32 v137, v210, v211\n"
"v_fma_mix_f32 v214, v139, v198, v206 op_sel:[0,0,0] op_sel_hi:[1,0,0]\n"
"v_mfma_f32_16x16x32_bf16 v[100:103], a[120:123], v[180:183], v[100:103]\n"
"v_fma_mix_f32 v215, v139, v199, v207 op_sel:[1,0,0] op_sel_hi:[1,0,0]\n"
"v_cvt_pk_bf16_f32 v138, v212, v213\n"
"v_mfma_f32_16x16x32_bf16 v[104:107], a[120:123], v[184:187], v[104:107]\n"
"buffer_load_dwordx4 v217, s[44:47], s62 offen offset:1024 lds\n"
"v_mfma_f32_16x16x32_bf16 v[108:111], a[120:123], v[188:191], v[108:111]\n"
"v_fma_mix_f32 v208, v140, v192, v200 op_sel:[0,0,0] op_sel_hi:[1,0,0]\n"
"v_fma_mix_f32 v209, v140, v193, v201 op_sel:[1,0,0] op_sel_hi:[1,0,0]\n"
"v_cvt_pk_bf16_f32 v139, v214, v215\n"
"v_mfma_f32_16x16x32_bf16 v[112:115], a[124:127], v[176:179], v[112:115]\n"
"v_fma_mix_f32 v210, v141, v194, v202 op_sel:[0,0,0] op_sel_hi:[1,0,0]\n"
"v_fma_mix_f32 v211, v141, v195, v203 op_sel:[1,0,0] op_sel_hi:[1,0,0]\n"
"v_mfma_f32_16x16x32_bf16 v[116:119], a[124:127], v[180:183], v[116:119]\n"
"v_cvt_pk_bf16_f32 v140, v208, v209\n"
"v_fma_mix_f32 v212, v142, v196, v204 op_sel:[0,0,0] op_sel_hi:[1,0,0]\n"
"v_fma_mix_f32 v213, v142, v197, v205 op_sel:[1,0,0] op_sel_hi:[1,0,0]\n"
"v_mfma_f32_16x16x32_bf16 v[120:123], a[124:127], v[184:187], v[120:123]\n"
"v_cvt_pk_bf16_f32 v141, v210, v211\n"
"v_fma_mix_f32 v214, v143, v198, v206 op_sel:[0,0,0] op_sel_hi:[1,0,0]\n"
"v_mfma_f32_16x16x32_bf16 v[124:127], a[124:127], v[188:191], v[124:127]\n"
"v_fma_mix_f32 v215, v143, v199, v207 op_sel:[1,0,0] op_sel_hi:[1,0,0]\n"
"v_cvt_pk_bf16_f32 v142, v212, v213\n"
"v_cvt_pk_bf16_f32 v143, v214, v215\n"
"s_waitcnt vmcnt(8) lgkmcnt(0)\n"
"v_mfma_f32_16x16x32_bf16 v[0:3], a[64:67], v[128:131], v[0:3]\n"
"ds_read_b64_tr_b16 a[96:97], v218 offset:40960\n"
"s_mov_b32 s63, 4096\n"
"buffer_load_dwordx4 v[176:179], v252, s[48:51], s63 offen offset:3072\n"
"v_mfma_f32_16x16x32_bf16 v[4:7], a[64:67], v[132:135], v[4:7]\n"
"ds_read_b64_tr_b16 a[98:99], v218 offset:45056\n"
"v_fma_mix_f32 v208, v144, v230, v238 op_sel:[0,0,0] op_sel_hi:[1,0,0]\n"
"v_fma_mix_f32 v209, v144, v231, v239 op_sel:[1,0,0] op_sel_hi:[1,0,0]\n"
"v_mfma_f32_16x16x32_bf16 v[8:11], a[64:67], v[136:139], v[8:11]\n"
"ds_read_b64_tr_b16 a[100:101], v218 offset:40968\n"
"s_mov_b32 s63, 20480\n"
"buffer_load_dwordx4 v[180:183], v252, s[48:51], s63 offen offset:3072\n"
"v_mfma_f32_16x16x32_bf16 v[12:15], a[64:67], v[140:143], v[12:15]\n"
"ds_read_b64_tr_b16 a[102:103], v218 offset:45064\n"
"v_fma_mix_f32 v210, v145, v232, v240 op_sel:[0,0,0] op_sel_hi:[1,0,0]\n"
"v_mfma_f32_16x16x32_bf16 v[16:19], a[68:71], v[128:131], v[16:19]\n"
"ds_read_b64_tr_b16 a[104:105], v218 offset:41472\n"
"s_mov_b32 s63, 36864\n"
"buffer_load_dwordx4 v[184:187], v252, s[48:51], s63 offen offset:3072\n"
"v_mfma_f32_16x16x32_bf16 v[20:23], a[68:71], v[132:135], v[20:23]\n"
"ds_read_b64_tr_b16 a[106:107], v218 offset:45568\n"
"v_fma_mix_f32 v211, v145, v233, v241 op_sel:[1,0,0] op_sel_hi:[1,0,0]\n"
"v_cvt_pk_bf16_f32 v144, v208, v209\n"
"v_mfma_f32_16x16x32_bf16 v[24:27], a[68:71], v[136:139], v[24:27]\n"
"ds_read_b64_tr_b16 a[108:109], v218 offset:41480\n"
"s_mov_b32 s63, 53248\n"
"buffer_load_dwordx4 v[188:191], v252, s[48:51], s63 offen offset:3072\n"
"v_mfma_f32_16x16x32_bf16 v[28:31], a[68:71], v[140:143], v[28:31]\n"
"ds_read_b64_tr_b16 a[110:111], v218 offset:45576\n"
"v_fma_mix_f32 v212, v146, v234, v242 op_sel:[0,0,0] op_sel_hi:[1,0,0]\n"
"v_mfma_f32_16x16x32_bf16 v[32:35], a[72:75], v[128:131], v[32:35]\n"
"ds_read_b64_tr_b16 a[112:113], v218 offset:41984\n"
"v_fma_mix_f32 v213, v146, v235, v243 op_sel:[1,0,0] op_sel_hi:[1,0,0]\n"
"v_cvt_pk_bf16_f32 v145, v210, v211\n"
"v_mfma_f32_16x16x32_bf16 v[36:39], a[72:75], v[132:135], v[36:39]\n"
"ds_read_b64_tr_b16 a[114:115], v218 offset:46080\n"
"v_fma_mix_f32 v214, v147, v236, v244 op_sel:[0,0,0] op_sel_hi:[1,0,0]\n"
"v_mfma_f32_16x16x32_bf16 v[40:43], a[72:75], v[136:139], v[40:43]\n"
"ds_read_b64_tr_b16 a[116:117], v218 offset:41992\n"
"v_fma_mix_f32 v215, v147, v237, v245 op_sel:[1,0,0] op_sel_hi:[1,0,0]\n"
"v_mfma_f32_16x16x32_bf16 v[44:47], a[72:75], v[140:143], v[44:47]\n"
"ds_read_b64_tr_b16 a[118:119], v218 offset:46088\n"
"v_cvt_pk_bf16_f32 v146, v212, v213\n"
"v_fma_mix_f32 v208, v148, v230, v238 op_sel:[0,0,0] op_sel_hi:[1,0,0]\n"
"v_mfma_f32_16x16x32_bf16 v[48:51], a[76:79], v[128:131], v[48:51]\n"
"ds_read_b64_tr_b16 a[120:121], v218 offset:42496\n"
"v_fma_mix_f32 v209, v148, v231, v239 op_sel:[1,0,0] op_sel_hi:[1,0,0]\n"
"v_mfma_f32_16x16x32_bf16 v[52:55], a[76:79], v[132:135], v[52:55]\n"
"ds_read_b64_tr_b16 a[122:123], v218 offset:46592\n"
"v_cvt_pk_bf16_f32 v147, v214, v215\n"
"v_fma_mix_f32 v210, v149, v232, v240 op_sel:[0,0,0] op_sel_hi:[1,0,0]\n"
"v_mfma_f32_16x16x32_bf16 v[56:59], a[76:79], v[136:139], v[56:59]\n"
"ds_read_b64_tr_b16 a[124:125], v218 offset:42504\n"
"v_fma_mix_f32 v211, v149, v233, v241 op_sel:[1,0,0] op_sel_hi:[1,0,0]\n"
"v_mfma_f32_16x16x32_bf16 v[60:63], a[76:79], v[140:143], v[60:63]\n"
"ds_read_b64_tr_b16 a[126:127], v218 offset:46600\n"
"v_cvt_pk_bf16_f32 v148, v208, v209\n"
"v_fma_mix_f32 v212, v150, v234, v242 op_sel:[0,0,0] op_sel_hi:[1,0,0]\n"
"v_mfma_f32_16x16x32_bf16 v[64:67], a[80:83], v[128:131], v[64:67]\n"
"ds_read_b128 v[192:195], v229 offset:768\n"
"v_fma_mix_f32 v213, v150, v235, v243 op_sel:[1,0,0] op_sel_hi:[1,0,0]\n"
"v_mfma_f32_16x16x32_bf16 v[68:71], a[80:83], v[132:135], v[68:71]\n"
"ds_read_b128 v[196:199], v229 offset:832\n"
"v_cvt_pk_bf16_f32 v149, v210, v211\n"
"v_mfma_f32_16x16x32_bf16 v[72:75], a[80:83], v[136:139], v[72:75]\n"
"ds_read_b128 v[200:203], v229 offset:2816\n"
"v_fma_mix_f32 v214, v151, v236, v244 op_sel:[0,0,0] op_sel_hi:[1,0,0]\n"
"v_fma_mix_f32 v215, v151, v237, v245 op_sel:[1,0,0] op_sel_hi:[1,0,0]\n"
"v_mfma_f32_16x16x32_bf16 v[76:79], a[80:83], v[140:143], v[76:79]\n"
"ds_read_b128 v[204:207], v229 offset:2880\n"
"v_cvt_pk_bf16_f32 v150, v212, v213\n"
"v_mfma_f32_16x16x32_bf16 v[80:83], a[84:87], v[128:131], v[80:83]\n"
"s_add_u32 m0, s70, 81920\n"
"s_add_u32 s62, s71, 2621440\n"
"buffer_load_dwordx4 v216, s[44:47], s62 offen lds\n"
"v_mfma_f32_16x16x32_bf16 v[84:87], a[84:87], v[132:135], v[84:87]\n"
"v_fma_mix_f32 v208, v152, v230, v238 op_sel:[0,0,0] op_sel_hi:[1,0,0]\n"
"v_fma_mix_f32 v209, v152, v231, v239 op_sel:[1,0,0] op_sel_hi:[1,0,0]\n"
"v_cvt_pk_bf16_f32 v151, v214, v215\n"
"v_mfma_f32_16x16x32_bf16 v[88:91], a[84:87], v[136:139], v[88:91]\n"
"v_fma_mix_f32 v210, v153, v232, v240 op_sel:[0,0,0] op_sel_hi:[1,0,0]\n"
"v_fma_mix_f32 v211, v153, v233, v241 op_sel:[1,0,0] op_sel_hi:[1,0,0]\n"
"v_mfma_f32_16x16x32_bf16 v[92:95], a[84:87], v[140:143], v[92:95]\n"
"v_cvt_pk_bf16_f32 v152, v208, v209\n"
"v_fma_mix_f32 v212, v154, v234, v242 op_sel:[0,0,0] op_sel_hi:[1,0,0]\n"
"v_fma_mix_f32 v213, v154, v235, v243 op_sel:[1,0,0] op_sel_hi:[1,0,0]\n"
"v_mfma_f32_16x16x32_bf16 v[96:99], a[88:91], v[128:131], v[96:99]\n"
"v_cvt_pk_bf16_f32 v153, v210, v211\n"
"v_fma_mix_f32 v214, v155, v236, v244 op_sel:[0,0,0] op_sel_hi:[1,0,0]\n"
"v_mfma_f32_16x16x32_bf16 v[100:103], a[88:91], v[132:135], v[100:103]\n"
"v_fma_mix_f32 v215, v155, v237, v245 op_sel:[1,0,0] op_sel_hi:[1,0,0]\n"
"v_cvt_pk_bf16_f32 v154, v212, v213\n"
"v_mfma_f32_16x16x32_bf16 v[104:107], a[88:91], v[136:139], v[104:107]\n"
"buffer_load_dwordx4 v217, s[44:47], s62 offen offset:1024 lds\n"
"v_mfma_f32_16x16x32_bf16 v[108:111], a[88:91], v[140:143], v[108:111]\n"
"v_fma_mix_f32 v208, v156, v230, v238 op_sel:[0,0,0] op_sel_hi:[1,0,0]\n"
"v_fma_mix_f32 v209, v156, v231, v239 op_sel:[1,0,0] op_sel_hi:[1,0,0]\n"
"v_cvt_pk_bf16_f32 v155, v214, v215\n"
"v_mfma_f32_16x16x32_bf16 v[112:115], a[92:95], v[128:131], v[112:115]\n"
"v_fma_mix_f32 v210, v157, v232, v240 op_sel:[0,0,0] op_sel_hi:[1,0,0]\n"
"v_fma_mix_f32 v211, v157, v233, v241 op_sel:[1,0,0] op_sel_hi:[1,0,0]\n"
"v_mfma_f32_16x16x32_bf16 v[116:119], a[92:95], v[132:135], v[116:119]\n"
"v_cvt_pk_bf16_f32 v156, v208, v209\n"
"v_fma_mix_f32 v212, v158, v234, v242 op_sel:[0,0,0] op_sel_hi:[1,0,0]\n"
"v_fma_mix_f32 v213, v158, v235, v243 op_sel:[1,0,0] op_sel_hi:[1,0,0]\n"
"v_mfma_f32_16x16x32_bf16 v[120:123], a[92:95], v[136:139], v[120:123]\n"
"v_cvt_pk_bf16_f32 v157, v210, v211\n"
"v_fma_mix_f32 v214, v159, v236, v244 op_sel:[0,0,0] op_sel_hi:[1,0,0]\n"
"v_mfma_f32_16x16x32_bf16 v[124:127], a[92:95], v[140:143], v[124:127]\n"
"v_fma_mix_f32 v215, v159, v237, v245 op_sel:[1,0,0] op_sel_hi:[1,0,0]\n"
"v_cvt_pk_bf16_f32 v158, v212, v213\n"
"v_cvt_pk_bf16_f32 v159, v214, v215\n"
"s_waitcnt vmcnt(8) lgkmcnt(0)\n"
"v_mfma_f32_16x16x32_bf16 v[0:3], a[96:99], v[144:147], v[0:3]\n"
"ds_read_b64_tr_b16 a[64:65], v218 offset:49152\n"
"s_mov_b32 s63, 8192\n"
"buffer_load_dwordx4 v[128:131], v252, s[48:51], s63 offen offset:0\n"
"v_mfma_f32_16x16x32_bf16 v[4:7], a[96:99], v[148:151], v[4:7]\n"
"ds_read_b64_tr_b16 a[66:67], v218 offset:53248\n"
"v_fma_mix_f32 v208, v160, v192, v200 op_sel:[0,0,0] op_sel_hi:[1,0,0]\n"
"v_fma_mix_f32 v209, v160, v193, v201 op_sel:[1,0,0] op_sel_hi:[1,0,0]\n"
"v_mfma_f32_16x16x32_bf16 v[8:11], a[96:99], v[152:155], v[8:11]\n"
"ds_read_b64_tr_b16 a[68:69], v218 offset:49160\n"
"s_mov_b32 s63, 24576\n"
"buffer_load_dwordx4 v[132:135], v252, s[48:51], s63 offen offset:0\n"
"v_mfma_f32_16x16x32_bf16 v[12:15], a[96:99], v[156:159], v[12:15]\n"
"ds_read_b64_tr_b16 a[70:71], v218 offset:53256\n"
"v_fma_mix_f32 v210, v161, v194, v202 op_sel:[0,0,0] op_sel_hi:[1,0,0]\n"
"v_fma_mix_f32 v211, v161, v195, v203 op_sel:[1,0,0] op_sel_hi:[1,0,0]\n"
"v_mfma_f32_16x16x32_bf16 v[16:19], a[100:103], v[144:147], v[16:19]\n"
"ds_read_b64_tr_b16 a[72:73], v218 offset:49664\n"
"s_mov_b32 s63, 40960\n"
"buffer_load_dwordx4 v[136:139], v252, s[48:51], s63 offen offset:0\n"
"v_mfma_f32_16x16x32_bf16 v[20:23], a[100:103], v[148:151], v[20:23]\n"
"ds_read_b64_tr_b16 a[74:75], v218 offset:53760\n"
"v_cvt_pk_bf16_f32 v160, v208, v209\n"
"v_fma_mix_f32 v212, v162, v196, v204 op_sel:[0,0,0] op_sel_hi:[1,0,0]\n"
"v_mfma_f32_16x16x32_bf16 v[24:27], a[100:103], v[152:155], v[24:27]\n"
"ds_read_b64_tr_b16 a[76:77], v218 offset:49672\n"
"s_mov_b32 s63, 57344\n"
"buffer_load_dwordx4 v[140:143], v252, s[48:51], s63 offen offset:0\n"
"v_mfma_f32_16x16x32_bf16 v[28:31], a[100:103], v[156:159], v[28:31]\n"
"ds_read_b64_tr_b16 a[78:79], v218 offset:53768\n"
"v_fma_mix_f32 v213, v162, v197, v205 op_sel:[1,0,0] op_sel_hi:[1,0,0]\n"
"v_mfma_f32_16x16x32_bf16 v[32:35], a[104:107], v[144:147], v[32:35]\n"
"ds_read_b64_tr_b16 a[80:81], v218 offset:50176\n"
"v_cvt_pk_bf16_f32 v161, v210, v211\n"
"v_fma_mix_f32 v214, v163, v198, v206 op_sel:[0,0,0] op_sel_hi:[1,0,0]\n"
"v_mfma_f32_16x16x32_bf16 v[36:39], a[104:107], v[148:151], v[36:39]\n"
"ds_read_b64_tr_b16 a[82:83], v218 offset:54272\n"
"v_fma_mix_f32 v215, v163, v199, v207 op_sel:[1,0,0] op_sel_hi:[1,0,0]\n"
"v_cvt_pk_bf16_f32 v162, v212, v213\n"
"v_mfma_f32_16x16x32_bf16 v[40:43], a[104:107], v[152:155], v[40:43]\n"
"ds_read_b64_tr_b16 a[84:85], v218 offset:50184\n"
"v_fma_mix_f32 v208, v164, v192, v200 op_sel:[0,0,0] op_sel_hi:[1,0,0]\n"
"v_mfma_f32_16x16x32_bf16 v[44:47], a[104:107], v[156:159], v[44:47]\n"
"ds_read_b64_tr_b16 a[86:87], v218 offset:54280\n"
"v_fma_mix_f32 v209, v164, v193, v201 op_sel:[1,0,0] op_sel_hi:[1,0,0]\n"
"v_cvt_pk_bf16_f32 v163, v214, v215\n"
"v_mfma_f32_16x16x32_bf16 v[48:51], a[108:111], v[144:147], v[48:51]\n"
"ds_read_b64_tr_b16 a[88:89], v218 offset:50688\n"
"v_fma_mix_f32 v210, v165, v194, v202 op_sel:[0,0,0] op_sel_hi:[1,0,0]\n"
"v_fma_mix_f32 v211, v165, v195, v203 op_sel:[1,0,0] op_sel_hi:[1,0,0]\n"
"v_mfma_f32_16x16x32_bf16 v[52:55], a[108:111], v[148:151], v[52:55]\n"
"ds_read_b64_tr_b16 a[90:91], v218 offset:54784\n"
"v_cvt_pk_bf16_f32 v164, v208, v209\n"
"v_fma_mix_f32 v212, v166, v196, v204 op_sel:[0,0,0] op_sel_hi:[1,0,0]\n"
"v_mfma_f32_16x16x32_bf16 v[56:59], a[108:111], v[152:155], v[56:59]\n"
"ds_read_b64_tr_b16 a[92:93], v218 offset:50696\n"
"v_fma_mix_f32 v213, v166, v197, v205 op_sel:[1,0,0] op_sel_hi:[1,0,0]\n"
"v_mfma_f32_16x16x32_bf16 v[60:63], a[108:111], v[156:159], v[60:63]\n"
"ds_read_b64_tr_b16 a[94:95], v218 offset:54792\n"
"v_cvt_pk_bf16_f32 v165, v210, v211\n"
"v_fma_mix_f32 v214, v167, v198, v206 op_sel:[0,0,0] op_sel_hi:[1,0,0]\n"
"v_mfma_f32_16x16x32_bf16 v[64:67], a[112:115], v[144:147], v[64:67]\n"
"ds_read_b128 v[230:233], v229 offset:896\n"
"v_fma_mix_f32 v215, v167, v199, v207 op_sel:[1,0,0] op_sel_hi:[1,0,0]\n"
"v_cvt_pk_bf16_f32 v166, v212, v213\n"
"v_mfma_f32_16x16x32_bf16 v[68:71], a[112:115], v[148:151], v[68:71]\n"
"ds_read_b128 v[234:237], v229 offset:960\n"
"v_fma_mix_f32 v208, v168, v192, v200 op_sel:[0,0,0] op_sel_hi:[1,0,0]\n"
"v_mfma_f32_16x16x32_bf16 v[72:75], a[112:115], v[152:155], v[72:75]\n"
"ds_read_b128 v[238:241], v229 offset:2944\n"
"v_fma_mix_f32 v209, v168, v193, v201 op_sel:[1,0,0] op_sel_hi:[1,0,0]\n"
"v_cvt_pk_bf16_f32 v167, v214, v215\n"
"v_mfma_f32_16x16x32_bf16 v[76:79], a[112:115], v[156:159], v[76:79]\n"
"ds_read_b128 v[242:245], v229 offset:3008\n"
"v_fma_mix_f32 v210, v169, v194, v202 op_sel:[0,0,0] op_sel_hi:[1,0,0]\n"
"v_fma_mix_f32 v211, v169, v195, v203 op_sel:[1,0,0] op_sel_hi:[1,0,0]\n"
"v_mfma_f32_16x16x32_bf16 v[80:83], a[116:119], v[144:147], v[80:83]\n"
"s_add_u32 m0, s70, 90112\n"
"s_add_u32 s62, s71, 2883584\n"
"buffer_load_dwordx4 v216, s[44:47], s62 offen lds\n"
"v_mfma_f32_16x16x32_bf16 v[84:87], a[116:119], v[148:151], v[84:87]\n"
"v_cvt_pk_bf16_f32 v168, v208, v209\n"
"v_fma_mix_f32 v212, v170, v196, v204 op_sel:[0,0,0] op_sel_hi:[1,0,0]\n"
"v_mfma_f32_16x16x32_bf16 v[88:91], a[116:119], v[152:155], v[88:91]\n"
"v_fma_mix_f32 v213, v170, v197, v205 op_sel:[1,0,0] op_sel_hi:[1,0,0]\n"
"v_cvt_pk_bf16_f32 v169, v210, v211\n"
"v_mfma_f32_16x16x32_bf16 v[92:95], a[116:119], v[156:159], v[92:95]\n"
"buffer_load_dwordx4 v217, s[44:47], s62 offen offset:1024 lds\n"
"v_mfma_f32_16x16x32_bf16 v[96:99], a[120:123], v[144:147], v[96:99]\n"
"v_fma_mix_f32 v214, v171, v198, v206 op_sel:[0,0,0] op_sel_hi:[1,0,0]\n"
"v_fma_mix_f32 v215, v171, v199, v207 op_sel:[1,0,0] op_sel_hi:[1,0,0]\n"
"v_cvt_pk_bf16_f32 v170, v212, v213\n"
"v_mfma_f32_16x16x32_bf16 v[100:103], a[120:123], v[148:151], v[100:103]\n"
"v_fma_mix_f32 v208, v172, v192, v200 op_sel:[0,0,0] op_sel_hi:[1,0,0]\n"
"v_fma_mix_f32 v209, v172, v193, v201 op_sel:[1,0,0] op_sel_hi:[1,0,0]\n"
"v_cvt_pk_bf16_f32 v171, v214, v215\n"
"v_mfma_f32_16x16x32_bf16 v[104:107], a[120:123], v[152:155], v[104:107]\n"
"s_add_u32 m0, s70, 98304\n"
"s_add_u32 s62, s71, 3145728\n"
"buffer_load_dwordx4 v216, s[44:47], s62 offen lds\n"
"v_mfma_f32_16x16x32_bf16 v[108:111], a[120:123], v[156:159], v[108:111]\n"
"v_fma_mix_f32 v210, v173, v194, v202 op_sel:[0,0,0] op_sel_hi:[1,0,0]\n"
"v_fma_mix_f32 v211, v173, v195, v203 op_sel:[1,0,0] op_sel_hi:[1,0,0]\n"
"v_mfma_f32_16x16x32_bf16 v[112:115], a[124:127], v[144:147], v[112:115]\n"
"v_cvt_pk_bf16_f32 v172, v208, v209\n"
"v_fma_mix_f32 v212, v174, v196, v204 op_sel:[0,0,0] op_sel_hi:[1,0,0]\n"
"v_fma_mix_f32 v213, v174, v197, v205 op_sel:[1,0,0] op_sel_hi:[1,0,0]\n"
"v_mfma_f32_16x16x32_bf16 v[116:119], a[124:127], v[148:151], v[116:119]\n"
"buffer_load_dwordx4 v217, s[44:47], s62 offen offset:1024 lds\n"
"v_mfma_f32_16x16x32_bf16 v[120:123], a[124:127], v[152:155], v[120:123]\n"
"v_cvt_pk_bf16_f32 v173, v210, v211\n"
"v_fma_mix_f32 v214, v175, v198, v206 op_sel:[0,0,0] op_sel_hi:[1,0,0]\n"
"v_fma_mix_f32 v215, v175, v199, v207 op_sel:[1,0,0] op_sel_hi:[1,0,0]\n"
"v_mfma_f32_16x16x32_bf16 v[124:127], a[124:127], v[156:159], v[124:127]\n"
"v_cvt_pk_bf16_f32 v174, v212, v213\n"
"v_cvt_pk_bf16_f32 v175, v214, v215\n"
"s_waitcnt vmcnt(10) lgkmcnt(0)\n"
"v_mfma_f32_16x16x32_bf16 v[0:3], a[64:67], v[160:163], v[0:3]\n"
"ds_read_b64_tr_b16 a[96:97], v218 offset:57344\n"
"s_mov_b32 s63, 8192\n"
"buffer_load_dwordx4 v[144:147], v252, s[48:51], s63 offen offset:1024\n"
"v_mfma_f32_16x16x32_bf16 v[4:7], a[64:67], v[164:167], v[4:7]\n"
"ds_read_b64_tr_b16 a[98:99], v218 offset:61440\n"
"v_fma_mix_f32 v208, v176, v230, v238 op_sel:[0,0,0] op_sel_hi:[1,0,0]\n"
"v_fma_mix_f32 v209, v176, v231, v239 op_sel:[1,0,0] op_sel_hi:[1,0,0]\n"
"v_mfma_f32_16x16x32_bf16 v[8:11], a[64:67], v[168:171], v[8:11]\n"
"ds_read_b64_tr_b16 a[100:101], v218 offset:57352\n"
"s_mov_b32 s63, 24576\n"
"buffer_load_dwordx4 v[148:151], v252, s[48:51], s63 offen offset:1024\n"
"v_mfma_f32_16x16x32_bf16 v[12:15], a[64:67], v[172:175], v[12:15]\n"
"ds_read_b64_tr_b16 a[102:103], v218 offset:61448\n"
"v_fma_mix_f32 v210, v177, v232, v240 op_sel:[0,0,0] op_sel_hi:[1,0,0]\n"
"v_mfma_f32_16x16x32_bf16 v[16:19], a[68:71], v[160:163], v[16:19]\n"
"ds_read_b64_tr_b16 a[104:105], v218 offset:57856\n"
"s_mov_b32 s63, 40960\n"
"buffer_load_dwordx4 v[152:155], v252, s[48:51], s63 offen offset:1024\n"
"v_mfma_f32_16x16x32_bf16 v[20:23], a[68:71], v[164:167], v[20:23]\n"
"ds_read_b64_tr_b16 a[106:107], v218 offset:61952\n"
"v_fma_mix_f32 v211, v177, v233, v241 op_sel:[1,0,0] op_sel_hi:[1,0,0]\n"
"v_cvt_pk_bf16_f32 v176, v208, v209\n"
"v_mfma_f32_16x16x32_bf16 v[24:27], a[68:71], v[168:171], v[24:27]\n"
"ds_read_b64_tr_b16 a[108:109], v218 offset:57864\n"
"s_mov_b32 s63, 57344\n"
"buffer_load_dwordx4 v[156:159], v252, s[48:51], s63 offen offset:1024\n"
"v_mfma_f32_16x16x32_bf16 v[28:31], a[68:71], v[172:175], v[28:31]\n"
"ds_read_b64_tr_b16 a[110:111], v218 offset:61960\n"
"v_fma_mix_f32 v212, v178, v234, v242 op_sel:[0,0,0] op_sel_hi:[1,0,0]\n"
"v_mfma_f32_16x16x32_bf16 v[32:35], a[72:75], v[160:163], v[32:35]\n"
"ds_read_b64_tr_b16 a[112:113], v218 offset:58368\n"
"v_fma_mix_f32 v213, v178, v235, v243 op_sel:[1,0,0] op_sel_hi:[1,0,0]\n"
"v_cvt_pk_bf16_f32 v177, v210, v211\n"
"v_mfma_f32_16x16x32_bf16 v[36:39], a[72:75], v[164:167], v[36:39]\n"
"ds_read_b64_tr_b16 a[114:115], v218 offset:62464\n"
"v_fma_mix_f32 v214, v179, v236, v244 op_sel:[0,0,0] op_sel_hi:[1,0,0]\n"
"v_mfma_f32_16x16x32_bf16 v[40:43], a[72:75], v[168:171], v[40:43]\n"
"ds_read_b64_tr_b16 a[116:117], v218 offset:58376\n"
"v_fma_mix_f32 v215, v179, v237, v245 op_sel:[1,0,0] op_sel_hi:[1,0,0]\n"
"v_mfma_f32_16x16x32_bf16 v[44:47], a[72:75], v[172:175], v[44:47]\n"
"ds_read_b64_tr_b16 a[118:119], v218 offset:62472\n"
"v_cvt_pk_bf16_f32 v178, v212, v213\n"
"v_fma_mix_f32 v208, v180, v230, v238 op_sel:[0,0,0] op_sel_hi:[1,0,0]\n"
"v_mfma_f32_16x16x32_bf16 v[48:51], a[76:79], v[160:163], v[48:51]\n"
"ds_read_b64_tr_b16 a[120:121], v218 offset:58880\n"
"v_fma_mix_f32 v209, v180, v231, v239 op_sel:[1,0,0] op_sel_hi:[1,0,0]\n"
"v_mfma_f32_16x16x32_bf16 v[52:55], a[76:79], v[164:167], v[52:55]\n"
"ds_read_b64_tr_b16 a[122:123], v218 offset:62976\n"
"v_cvt_pk_bf16_f32 v179, v214, v215\n"
"v_fma_mix_f32 v210, v181, v232, v240 op_sel:[0,0,0] op_sel_hi:[1,0,0]\n"
"v_mfma_f32_16x16x32_bf16 v[56:59], a[76:79], v[168:171], v[56:59]\n"
"ds_read_b64_tr_b16 a[124:125], v218 offset:58888\n"
"v_fma_mix_f32 v211, v181, v233, v241 op_sel:[1,0,0] op_sel_hi:[1,0,0]\n"
"v_mfma_f32_16x16x32_bf16 v[60:63], a[76:79], v[172:175], v[60:63]\n"
"ds_read_b64_tr_b16 a[126:127], v218 offset:62984\n"
"v_cvt_pk_bf16_f32 v180, v208, v209\n"
"v_fma_mix_f32 v212, v182, v234, v242 op_sel:[0,0,0] op_sel_hi:[1,0,0]\n"
"v_mfma_f32_16x16x32_bf16 v[64:67], a[80:83], v[160:163], v[64:67]\n"
"ds_read_b128 v[192:195], v229 offset:1024\n"
"v_fma_mix_f32 v213, v182, v235, v243 op_sel:[1,0,0] op_sel_hi:[1,0,0]\n"
"v_mfma_f32_16x16x32_bf16 v[68:71], a[80:83], v[164:167], v[68:71]\n"
"ds_read_b128 v[196:199], v229 offset:1088\n"
"v_cvt_pk_bf16_f32 v181, v210, v211\n"
"v_mfma_f32_16x16x32_bf16 v[72:75], a[80:83], v[168:171], v[72:75]\n"
"ds_read_b128 v[200:203], v229 offset:3072\n"
"v_fma_mix_f32 v214, v183, v236, v244 op_sel:[0,0,0] op_sel_hi:[1,0,0]\n"
"v_fma_mix_f32 v215, v183, v237, v245 op_sel:[1,0,0] op_sel_hi:[1,0,0]\n"
"v_mfma_f32_16x16x32_bf16 v[76:79], a[80:83], v[172:175], v[76:79]\n"
"ds_read_b128 v[204:207], v229 offset:3136\n"
"v_cvt_pk_bf16_f32 v182, v212, v213\n"
"v_mfma_f32_16x16x32_bf16 v[80:83], a[84:87], v[160:163], v[80:83]\n"
"s_add_u32 m0, s70, 106496\n"
"s_add_u32 s62, s71, 3407872\n"
"buffer_load_dwordx4 v216, s[44:47], s62 offen lds\n"
"v_mfma_f32_16x16x32_bf16 v[84:87], a[84:87], v[164:167], v[84:87]\n"
"v_fma_mix_f32 v208, v184, v230, v238 op_sel:[0,0,0] op_sel_hi:[1,0,0]\n"
"v_fma_mix_f32 v209, v184, v231, v239 op_sel:[1,0,0] op_sel_hi:[1,0,0]\n"
"v_cvt_pk_bf16_f32 v183, v214, v215\n"
"v_mfma_f32_16x16x32_bf16 v[88:91], a[84:87], v[168:171], v[88:91]\n"
"v_fma_mix_f32 v210, v185, v232, v240 op_sel:[0,0,0] op_sel_hi:[1,0,0]\n"
"v_fma_mix_f32 v211, v185, v233, v241 op_sel:[1,0,0] op_sel_hi:[1,0,0]\n"
"v_mfma_f32_16x16x32_bf16 v[92:95], a[84:87], v[172:175], v[92:95]\n"
"v_cvt_pk_bf16_f32 v184, v208, v209\n"
"v_fma_mix_f32 v212, v186, v234, v242 op_sel:[0,0,0] op_sel_hi:[1,0,0]\n"
"v_fma_mix_f32 v213, v186, v235, v243 op_sel:[1,0,0] op_sel_hi:[1,0,0]\n"
"v_mfma_f32_16x16x32_bf16 v[96:99], a[88:91], v[160:163], v[96:99]\n"
"v_cvt_pk_bf16_f32 v185, v210, v211\n"
"v_fma_mix_f32 v214, v187, v236, v244 op_sel:[0,0,0] op_sel_hi:[1,0,0]\n"
"v_mfma_f32_16x16x32_bf16 v[100:103], a[88:91], v[164:167], v[100:103]\n"
"v_fma_mix_f32 v215, v187, v237, v245 op_sel:[1,0,0] op_sel_hi:[1,0,0]\n"
"v_cvt_pk_bf16_f32 v186, v212, v213\n"
"v_mfma_f32_16x16x32_bf16 v[104:107], a[88:91], v[168:171], v[104:107]\n"
"buffer_load_dwordx4 v217, s[44:47], s62 offen offset:1024 lds\n"
"v_mfma_f32_16x16x32_bf16 v[108:111], a[88:91], v[172:175], v[108:111]\n"
"v_fma_mix_f32 v208, v188, v230, v238 op_sel:[0,0,0] op_sel_hi:[1,0,0]\n"
"v_fma_mix_f32 v209, v188, v231, v239 op_sel:[1,0,0] op_sel_hi:[1,0,0]\n"
"v_cvt_pk_bf16_f32 v187, v214, v215\n"
"v_mfma_f32_16x16x32_bf16 v[112:115], a[92:95], v[160:163], v[112:115]\n"
"v_fma_mix_f32 v210, v189, v232, v240 op_sel:[0,0,0] op_sel_hi:[1,0,0]\n"
"v_fma_mix_f32 v211, v189, v233, v241 op_sel:[1,0,0] op_sel_hi:[1,0,0]\n"
"v_mfma_f32_16x16x32_bf16 v[116:119], a[92:95], v[164:167], v[116:119]\n"
"v_cvt_pk_bf16_f32 v188, v208, v209\n"
"v_fma_mix_f32 v212, v190, v234, v242 op_sel:[0,0,0] op_sel_hi:[1,0,0]\n"
"v_fma_mix_f32 v213, v190, v235, v243 op_sel:[1,0,0] op_sel_hi:[1,0,0]\n"
"v_mfma_f32_16x16x32_bf16 v[120:123], a[92:95], v[168:171], v[120:123]\n"
"v_cvt_pk_bf16_f32 v189, v210, v211\n"
"v_fma_mix_f32 v214, v191, v236, v244 op_sel:[0,0,0] op_sel_hi:[1,0,0]\n"
"v_mfma_f32_16x16x32_bf16 v[124:127], a[92:95], v[172:175], v[124:127]\n"
"v_fma_mix_f32 v215, v191, v237, v245 op_sel:[1,0,0] op_sel_hi:[1,0,0]\n"
"v_cvt_pk_bf16_f32 v190, v212, v213\n"
"v_cvt_pk_bf16_f32 v191, v214, v215\n"
"s_waitcnt vmcnt(8) lgkmcnt(0)\n"
"s_barrier\n"
"s_lshl_b32 s64, s56, 13\n"
"s_mov_b32 s59, s64\n"
"s_add_u32 s60, s64, 32768\n"
"s_add_u32 s57, s64, 16384\n"
"s_mov_b32 s58, s64\n"
"v_mfma_f32_16x16x32_bf16 v[0:3], a[96:99], v[176:179], v[0:3]\n"
"ds_read_b64_tr_b16 a[64:65], v219 offset:0\n"
"s_mov_b32 s63, 8192\n"
"buffer_load_dwordx4 v[160:163], v252, s[48:51], s63 offen offset:2048\n"
"v_mfma_f32_16x16x32_bf16 v[4:7], a[96:99], v[180:183], v[4:7]\n"
"ds_read_b64_tr_b16 a[66:67], v219 offset:4096\n"
"v_fma_mix_f32 v208, v128, v192, v200 op_sel:[0,0,0] op_sel_hi:[1,0,0]\n"
"v_fma_mix_f32 v209, v128, v193, v201 op_sel:[1,0,0] op_sel_hi:[1,0,0]\n"
"v_mfma_f32_16x16x32_bf16 v[8:11], a[96:99], v[184:187], v[8:11]\n"
"ds_read_b64_tr_b16 a[68:69], v219 offset:8\n"
"s_mov_b32 s63, 24576\n"
"buffer_load_dwordx4 v[164:167], v252, s[48:51], s63 offen offset:2048\n"
"v_mfma_f32_16x16x32_bf16 v[12:15], a[96:99], v[188:191], v[12:15]\n"
"ds_read_b64_tr_b16 a[70:71], v219 offset:4104\n"
"v_fma_mix_f32 v210, v129, v194, v202 op_sel:[0,0,0] op_sel_hi:[1,0,0]\n"
"v_mfma_f32_16x16x32_bf16 v[16:19], a[100:103], v[176:179], v[16:19]\n"
"ds_read_b64_tr_b16 a[72:73], v219 offset:512\n"
"s_mov_b32 s63, 40960\n"
"buffer_load_dwordx4 v[168:171], v252, s[48:51], s63 offen offset:2048\n"
"v_mfma_f32_16x16x32_bf16 v[20:23], a[100:103], v[180:183], v[20:23]\n"
"ds_read_b64_tr_b16 a[74:75], v219 offset:4608\n"
"v_fma_mix_f32 v211, v129, v195, v203 op_sel:[1,0,0] op_sel_hi:[1,0,0]\n"
"v_cvt_pk_bf16_f32 v128, v208, v209\n"
"v_mfma_f32_16x16x32_bf16 v[24:27], a[100:103], v[184:187], v[24:27]\n"
"ds_read_b64_tr_b16 a[76:77], v219 offset:520\n"
"s_mov_b32 s63, 57344\n"
"buffer_load_dwordx4 v[172:175], v252, s[48:51], s63 offen offset:2048\n"
"v_mfma_f32_16x16x32_bf16 v[28:31], a[100:103], v[188:191], v[28:31]\n"
"ds_read_b64_tr_b16 a[78:79], v219 offset:4616\n"
"v_fma_mix_f32 v212, v130, v196, v204 op_sel:[0,0,0] op_sel_hi:[1,0,0]\n"
"v_mfma_f32_16x16x32_bf16 v[32:35], a[104:107], v[176:179], v[32:35]\n"
"ds_read_b64_tr_b16 a[80:81], v219 offset:1024\n"
"v_fma_mix_f32 v213, v130, v197, v205 op_sel:[1,0,0] op_sel_hi:[1,0,0]\n"
"v_cvt_pk_bf16_f32 v129, v210, v211\n"
"v_mfma_f32_16x16x32_bf16 v[36:39], a[104:107], v[180:183], v[36:39]\n"
"ds_read_b64_tr_b16 a[82:83], v219 offset:5120\n"
"v_fma_mix_f32 v214, v131, v198, v206 op_sel:[0,0,0] op_sel_hi:[1,0,0]\n"
"v_mfma_f32_16x16x32_bf16 v[40:43], a[104:107], v[184:187], v[40:43]\n"
"ds_read_b64_tr_b16 a[84:85], v219 offset:1032\n"
"v_fma_mix_f32 v215, v131, v199, v207 op_sel:[1,0,0] op_sel_hi:[1,0,0]\n"
"v_mfma_f32_16x16x32_bf16 v[44:47], a[104:107], v[188:191], v[44:47]\n"
"ds_read_b64_tr_b16 a[86:87], v219 offset:5128\n"
"v_cvt_pk_bf16_f32 v130, v212, v213\n"
"v_fma_mix_f32 v208, v132, v192, v200 op_sel:[0,0,0] op_sel_hi:[1,0,0]\n"
"v_mfma_f32_16x16x32_bf16 v[48:51], a[108:111], v[176:179], v[48:51]\n"
"ds_read_b64_tr_b16 a[88:89], v219 offset:1536\n"
"v_fma_mix_f32 v209, v132, v193, v201 op_sel:[1,0,0] op_sel_hi:[1,0,0]\n"
"v_mfma_f32_16x16x32_bf16 v[52:55], a[108:111], v[180:183], v[52:55]\n"
"ds_read_b64_tr_b16 a[90:91], v219 offset:5632\n"
"v_cvt_pk_bf16_f32 v131, v214, v215\n"
"v_fma_mix_f32 v210, v133, v194, v202 op_sel:[0,0,0] op_sel_hi:[1,0,0]\n"
"v_mfma_f32_16x16x32_bf16 v[56:59], a[108:111], v[184:187], v[56:59]\n"
"ds_read_b64_tr_b16 a[92:93], v219 offset:1544\n"
"v_fma_mix_f32 v211, v133, v195, v203 op_sel:[1,0,0] op_sel_hi:[1,0,0]\n"
"v_mfma_f32_16x16x32_bf16 v[60:63], a[108:111], v[188:191], v[60:63]\n"
"ds_read_b64_tr_b16 a[94:95], v219 offset:5640\n"
"v_cvt_pk_bf16_f32 v132, v208, v209\n"
"v_fma_mix_f32 v212, v134, v196, v204 op_sel:[0,0,0] op_sel_hi:[1,0,0]\n"
"v_mfma_f32_16x16x32_bf16 v[64:67], a[112:115], v[176:179], v[64:67]\n"
"ds_read_b128 v[230:233], v229 offset:1152\n"
"v_fma_mix_f32 v213, v134, v197, v205 op_sel:[1,0,0] op_sel_hi:[1,0,0]\n"
"v_mfma_f32_16x16x32_bf16 v[68:71], a[112:115], v[180:183], v[68:71]\n"
"ds_read_b128 v[234:237], v229 offset:1216\n"
"v_cvt_pk_bf16_f32 v133, v210, v211\n"
"v_mfma_f32_16x16x32_bf16 v[72:75], a[112:115], v[184:187], v[72:75]\n"
"ds_read_b128 v[238:241], v229 offset:3200\n"
"v_fma_mix_f32 v214, v135, v198, v206 op_sel:[0,0,0] op_sel_hi:[1,0,0]\n"
"v_fma_mix_f32 v215, v135, v199, v207 op_sel:[1,0,0] op_sel_hi:[1,0,0]\n"
"v_mfma_f32_16x16x32_bf16 v[76:79], a[112:115], v[188:191], v[76:79]\n"
"ds_read_b128 v[242:245], v229 offset:3264\n"
"v_cvt_pk_bf16_f32 v134, v212, v213\n"
"v_mfma_f32_16x16x32_bf16 v[80:83], a[116:119], v[176:179], v[80:83]\n"
"s_add_u32 m0, s70, 114688\n"
"s_add_u32 s62, s71, 3670016\n"
"buffer_load_dwordx4 v216, s[44:47], s62 offen lds\n"
"v_mfma_f32_16x16x32_bf16 v[84:87], a[116:119], v[180:183], v[84:87]\n"
"v_fma_mix_f32 v208, v136, v192, v200 op_sel:[0,0,0] op_sel_hi:[1,0,0]\n"
"v_fma_mix_f32 v209, v136, v193, v201 op_sel:[1,0,0] op_sel_hi:[1,0,0]\n"
"v_cvt_pk_bf16_f32 v135, v214, v215\n"
"v_mfma_f32_16x16x32_bf16 v[88:91], a[116:119], v[184:187], v[88:91]\n"
"v_fma_mix_f32 v210, v137, v194, v202 op_sel:[0,0,0] op_sel_hi:[1,0,0]\n"
"v_fma_mix_f32 v211, v137, v195, v203 op_sel:[1,0,0] op_sel_hi:[1,0,0]\n"
"v_mfma_f32_16x16x32_bf16 v[92:95], a[116:119], v[188:191], v[92:95]\n"
"v_cvt_pk_bf16_f32 v136, v208, v209\n"
"v_fma_mix_f32 v212, v138, v196, v204 op_sel:[0,0,0] op_sel_hi:[1,0,0]\n"
"v_fma_mix_f32 v213, v138, v197, v205 op_sel:[1,0,0] op_sel_hi:[1,0,0]\n"
"v_mfma_f32_16x16x32_bf16 v[96:99], a[120:123], v[176:179], v[96:99]\n"
"v_cvt_pk_bf16_f32 v137, v210, v211\n"
"v_fma_mix_f32 v214, v139, v198, v206 op_sel:[0,0,0] op_sel_hi:[1,0,0]\n"
"v_mfma_f32_16x16x32_bf16 v[100:103], a[120:123], v[180:183], v[100:103]\n"
"v_fma_mix_f32 v215, v139, v199, v207 op_sel:[1,0,0] op_sel_hi:[1,0,0]\n"
"v_cvt_pk_bf16_f32 v138, v212, v213\n"
"v_mfma_f32_16x16x32_bf16 v[104:107], a[120:123], v[184:187], v[104:107]\n"
"buffer_load_dwordx4 v217, s[44:47], s62 offen offset:1024 lds\n"
"v_mfma_f32_16x16x32_bf16 v[108:111], a[120:123], v[188:191], v[108:111]\n"
"v_fma_mix_f32 v208, v140, v192, v200 op_sel:[0,0,0] op_sel_hi:[1,0,0]\n"
"v_fma_mix_f32 v209, v140, v193, v201 op_sel:[1,0,0] op_sel_hi:[1,0,0]\n"
"v_cvt_pk_bf16_f32 v139, v214, v215\n"
"v_mfma_f32_16x16x32_bf16 v[112:115], a[124:127], v[176:179], v[112:115]\n"
"v_fma_mix_f32 v210, v141, v194, v202 op_sel:[0,0,0] op_sel_hi:[1,0,0]\n"
"v_fma_mix_f32 v211, v141, v195, v203 op_sel:[1,0,0] op_sel_hi:[1,0,0]\n"
"v_mfma_f32_16x16x32_bf16 v[116:119], a[124:127], v[180:183], v[116:119]\n"
"v_cvt_pk_bf16_f32 v140, v208, v209\n"
"v_fma_mix_f32 v212, v142, v196, v204 op_sel:[0,0,0] op_sel_hi:[1,0,0]\n"
"v_fma_mix_f32 v213, v142, v197, v205 op_sel:[1,0,0] op_sel_hi:[1,0,0]\n"
"v_mfma_f32_16x16x32_bf16 v[120:123], a[124:127], v[184:187], v[120:123]\n"
"v_cvt_pk_bf16_f32 v141, v210, v211\n"
"v_fma_mix_f32 v214, v143, v198, v206 op_sel:[0,0,0] op_sel_hi:[1,0,0]\n"
"v_mfma_f32_16x16x32_bf16 v[124:127], a[124:127], v[188:191], v[124:127]\n"
"v_fma_mix_f32 v215, v143, v199, v207 op_sel:[1,0,0] op_sel_hi:[1,0,0]\n"
"v_cvt_pk_bf16_f32 v142, v212, v213\n"
"v_cvt_pk_bf16_f32 v143, v214, v215\n"
"s_waitcnt vmcnt(8) lgkmcnt(0)\n"
"v_mfma_f32_16x16x32_bf16 v[0:3], a[64:67], v[128:131], v[0:3]\n"
"ds_read_b64_tr_b16 a[96:97], v219 offset:8192\n"
"s_mov_b32 s63, 8192\n"
"buffer_load_dwordx4 v[176:179], v252, s[48:51], s63 offen offset:3072\n"
"v_mfma_f32_16x16x32_bf16 v[4:7], a[64:67], v[132:135], v[4:7]\n"
"ds_read_b64_tr_b16 a[98:99], v219 offset:12288\n"
"v_fma_mix_f32 v208, v144, v230, v238 op_sel:[0,0,0] op_sel_hi:[1,0,0]\n"
"v_fma_mix_f32 v209, v144, v231, v239 op_sel:[1,0,0] op_sel_hi:[1,0,0]\n"
"v_mfma_f32_16x16x32_bf16 v[8:11], a[64:67], v[136:139], v[8:11]\n"
"ds_read_b64_tr_b16 a[100:101], v219 offset:8200\n"
"s_mov_b32 s63, 24576\n"
"buffer_load_dwordx4 v[180:183], v252, s[48:51], s63 offen offset:3072\n"
"v_mfma_f32_16x16x32_bf16 v[12:15], a[64:67], v[140:143], v[12:15]\n"
"ds_read_b64_tr_b16 a[102:103], v219 offset:12296\n"
"v_fma_mix_f32 v210, v145, v232, v240 op_sel:[0,0,0] op_sel_hi:[1,0,0]\n"
"v_mfma_f32_16x16x32_bf16 v[16:19], a[68:71], v[128:131], v[16:19]\n"
"ds_read_b64_tr_b16 a[104:105], v219 offset:8704\n"
"s_mov_b32 s63, 40960\n"
"buffer_load_dwordx4 v[184:187], v252, s[48:51], s63 offen offset:3072\n"
"v_mfma_f32_16x16x32_bf16 v[20:23], a[68:71], v[132:135], v[20:23]\n"
"ds_read_b64_tr_b16 a[106:107], v219 offset:12800\n"
"v_fma_mix_f32 v211, v145, v233, v241 op_sel:[1,0,0] op_sel_hi:[1,0,0]\n"
"v_cvt_pk_bf16_f32 v144, v208, v209\n"
"v_mfma_f32_16x16x32_bf16 v[24:27], a[68:71], v[136:139], v[24:27]\n"
"ds_read_b64_tr_b16 a[108:109], v219 offset:8712\n"
"s_mov_b32 s63, 57344\n"
"buffer_load_dwordx4 v[188:191], v252, s[48:51], s63 offen offset:3072\n"
"v_mfma_f32_16x16x32_bf16 v[28:31], a[68:71], v[140:143], v[28:31]\n"
"ds_read_b64_tr_b16 a[110:111], v219 offset:12808\n"
"v_fma_mix_f32 v212, v146, v234, v242 op_sel:[0,0,0] op_sel_hi:[1,0,0]\n"
"v_mfma_f32_16x16x32_bf16 v[32:35], a[72:75], v[128:131], v[32:35]\n"
"ds_read_b64_tr_b16 a[112:113], v219 offset:9216\n"
"v_fma_mix_f32 v213, v146, v235, v243 op_sel:[1,0,0] op_sel_hi:[1,0,0]\n"
"v_cvt_pk_bf16_f32 v145, v210, v211\n"
"v_mfma_f32_16x16x32_bf16 v[36:39], a[72:75], v[132:135], v[36:39]\n"
"ds_read_b64_tr_b16 a[114:115], v219 offset:13312\n"
"v_fma_mix_f32 v214, v147, v236, v244 op_sel:[0,0,0] op_sel_hi:[1,0,0]\n"
"v_mfma_f32_16x16x32_bf16 v[40:43], a[72:75], v[136:139], v[40:43]\n"
"ds_read_b64_tr_b16 a[116:117], v219 offset:9224\n"
"v_fma_mix_f32 v215, v147, v237, v245 op_sel:[1,0,0] op_sel_hi:[1,0,0]\n"
"v_mfma_f32_16x16x32_bf16 v[44:47], a[72:75], v[140:143], v[44:47]\n"
"ds_read_b64_tr_b16 a[118:119], v219 offset:13320\n"
"v_cvt_pk_bf16_f32 v146, v212, v213\n"
"v_fma_mix_f32 v208, v148, v230, v238 op_sel:[0,0,0] op_sel_hi:[1,0,0]\n"
"v_mfma_f32_16x16x32_bf16 v[48:51], a[76:79], v[128:131], v[48:51]\n"
"ds_read_b64_tr_b16 a[120:121], v219 offset:9728\n"
"v_fma_mix_f32 v209, v148, v231, v239 op_sel:[1,0,0] op_sel_hi:[1,0,0]\n"
"v_mfma_f32_16x16x32_bf16 v[52:55], a[76:79], v[132:135], v[52:55]\n"
"ds_read_b64_tr_b16 a[122:123], v219 offset:13824\n"
"v_cvt_pk_bf16_f32 v147, v214, v215\n"
"v_fma_mix_f32 v210, v149, v232, v240 op_sel:[0,0,0] op_sel_hi:[1,0,0]\n"
"v_mfma_f32_16x16x32_bf16 v[56:59], a[76:79], v[136:139], v[56:59]\n"
"ds_read_b64_tr_b16 a[124:125], v219 offset:9736\n"
"v_fma_mix_f32 v211, v149, v233, v241 op_sel:[1,0,0] op_sel_hi:[1,0,0]\n"
"v_mfma_f32_16x16x32_bf16 v[60:63], a[76:79], v[140:143], v[60:63]\n"
"ds_read_b64_tr_b16 a[126:127], v219 offset:13832\n"
"v_cvt_pk_bf16_f32 v148, v208, v209\n"
"v_fma_mix_f32 v212, v150, v234, v242 op_sel:[0,0,0] op_sel_hi:[1,0,0]\n"
"v_mfma_f32_16x16x32_bf16 v[64:67], a[80:83], v[128:131], v[64:67]\n"
"ds_read_b128 v[192:195], v229 offset:1280\n"
"v_fma_mix_f32 v213, v150, v235, v243 op_sel:[1,0,0] op_sel_hi:[1,0,0]\n"
"v_mfma_f32_16x16x32_bf16 v[68:71], a[80:83], v[132:135], v[68:71]\n"
"ds_read_b128 v[196:199], v229 offset:1344\n"
"v_cvt_pk_bf16_f32 v149, v210, v211\n"
"v_mfma_f32_16x16x32_bf16 v[72:75], a[80:83], v[136:139], v[72:75]\n"
"ds_read_b128 v[200:203], v229 offset:3328\n"
"v_fma_mix_f32 v214, v151, v236, v244 op_sel:[0,0,0] op_sel_hi:[1,0,0]\n"
"v_fma_mix_f32 v215, v151, v237, v245 op_sel:[1,0,0] op_sel_hi:[1,0,0]\n"
"v_mfma_f32_16x16x32_bf16 v[76:79], a[80:83], v[140:143], v[76:79]\n"
"ds_read_b128 v[204:207], v229 offset:3392\n"
"v_cvt_pk_bf16_f32 v150, v212, v213\n"
"v_mfma_f32_16x16x32_bf16 v[80:83], a[84:87], v[128:131], v[80:83]\n"
"s_add_u32 m0, s70, 122880\n"
"s_add_u32 s62, s71, 3932160\n"
"buffer_load_dwordx4 v216, s[44:47], s62 offen lds\n"
"v_mfma_f32_16x16x32_bf16 v[84:87], a[84:87], v[132:135], v[84:87]\n"
"v_fma_mix_f32 v208, v152, v230, v238 op_sel:[0,0,0] op_sel_hi:[1,0,0]\n"
"v_fma_mix_f32 v209, v152, v231, v239 op_sel:[1,0,0] op_sel_hi:[1,0,0]\n"
"v_cvt_pk_bf16_f32 v151, v214, v215\n"
"v_mfma_f32_16x16x32_bf16 v[88:91], a[84:87], v[136:139], v[88:91]\n"
"v_fma_mix_f32 v210, v153, v232, v240 op_sel:[0,0,0] op_sel_hi:[1,0,0]\n"
"v_fma_mix_f32 v211, v153, v233, v241 op_sel:[1,0,0] op_sel_hi:[1,0,0]\n"
"v_mfma_f32_16x16x32_bf16 v[92:95], a[84:87], v[140:143], v[92:95]\n"
"v_cvt_pk_bf16_f32 v152, v208, v209\n"
"v_fma_mix_f32 v212, v154, v234, v242 op_sel:[0,0,0] op_sel_hi:[1,0,0]\n"
"v_fma_mix_f32 v213, v154, v235, v243 op_sel:[1,0,0] op_sel_hi:[1,0,0]\n"
"v_mfma_f32_16x16x32_bf16 v[96:99], a[88:91], v[128:131], v[96:99]\n"
"v_cvt_pk_bf16_f32 v153, v210, v211\n"
"v_fma_mix_f32 v214, v155, v236, v244 op_sel:[0,0,0] op_sel_hi:[1,0,0]\n"
"v_mfma_f32_16x16x32_bf16 v[100:103], a[88:91], v[132:135], v[100:103]\n"
"v_fma_mix_f32 v215, v155, v237, v245 op_sel:[1,0,0] op_sel_hi:[1,0,0]\n"
"v_cvt_pk_bf16_f32 v154, v212, v213\n"
"v_mfma_f32_16x16x32_bf16 v[104:107], a[88:91], v[136:139], v[104:107]\n"
"buffer_load_dwordx4 v217, s[44:47], s62 offen offset:1024 lds\n"
"v_mfma_f32_16x16x32_bf16 v[108:111], a[88:91], v[140:143], v[108:111]\n"
"v_fma_mix_f32 v208, v156, v230, v238 op_sel:[0,0,0] op_sel_hi:[1,0,0]\n"
"v_fma_mix_f32 v209, v156, v231, v239 op_sel:[1,0,0] op_sel_hi:[1,0,0]\n"
"v_cvt_pk_bf16_f32 v155, v214, v215\n"
"v_mfma_f32_16x16x32_bf16 v[112:115], a[92:95], v[128:131], v[112:115]\n"
"v_fma_mix_f32 v210, v157, v232, v240 op_sel:[0,0,0] op_sel_hi:[1,0,0]\n"
"v_fma_mix_f32 v211, v157, v233, v241 op_sel:[1,0,0] op_sel_hi:[1,0,0]\n"
"v_mfma_f32_16x16x32_bf16 v[116:119], a[92:95], v[132:135], v[116:119]\n"
"v_cvt_pk_bf16_f32 v156, v208, v209\n"
"v_fma_mix_f32 v212, v158, v234, v242 op_sel:[0,0,0] op_sel_hi:[1,0,0]\n"
"v_fma_mix_f32 v213, v158, v235, v243 op_sel:[1,0,0] op_sel_hi:[1,0,0]\n"
"v_mfma_f32_16x16x32_bf16 v[120:123], a[92:95], v[136:139], v[120:123]\n"
"v_cvt_pk_bf16_f32 v157, v210, v211\n"
"v_fma_mix_f32 v214, v159, v236, v244 op_sel:[0,0,0] op_sel_hi:[1,0,0]\n"
"v_mfma_f32_16x16x32_bf16 v[124:127], a[92:95], v[140:143], v[124:127]\n"
"v_fma_mix_f32 v215, v159, v237, v245 op_sel:[1,0,0] op_sel_hi:[1,0,0]\n"
"v_cvt_pk_bf16_f32 v158, v212, v213\n"
"v_cvt_pk_bf16_f32 v159, v214, v215\n"
"s_waitcnt vmcnt(8) lgkmcnt(0)\n"
"v_mfma_f32_16x16x32_bf16 v[0:3], a[96:99], v[144:147], v[0:3]\n"
"ds_read_b64_tr_b16 a[64:65], v219 offset:16384\n"
"s_mov_b32 s63, 12288\n"
"buffer_load_dwordx4 v[128:131], v252, s[48:51], s63 offen offset:0\n"
"v_mfma_f32_16x16x32_bf16 v[4:7], a[96:99], v[148:151], v[4:7]\n"
"ds_read_b64_tr_b16 a[66:67], v219 offset:20480\n"
"v_fma_mix_f32 v208, v160, v192, v200 op_sel:[0,0,0] op_sel_hi:[1,0,0]\n"
"v_fma_mix_f32 v209, v160, v193, v201 op_sel:[1,0,0] op_sel_hi:[1,0,0]\n"
"v_fma_mix_f32 v210, v161, v194, v202 op_sel:[0,0,0] op_sel_hi:[1,0,0]\n"
"v_mfma_f32_16x16x32_bf16 v[8:11], a[96:99], v[152:155], v[8:11]\n"
"ds_read_b64_tr_b16 a[68:69], v219 offset:16392\n"
"s_mov_b32 s63, 28672\n"
"buffer_load_dwordx4 v[132:135], v252, s[48:51], s63 offen offset:0\n"
"v_mfma_f32_16x16x32_bf16 v[12:15], a[96:99], v[156:159], v[12:15]\n"
"ds_read_b64_tr_b16 a[70:71], v219 offset:20488\n"
"v_fma_mix_f32 v211, v161, v195, v203 op_sel:[1,0,0] op_sel_hi:[1,0,0]\n"
"v_mfma_f32_16x16x32_bf16 v[16:19], a[100:103], v[144:147], v[16:19]\n"
"ds_read_b64_tr_b16 a[72:73], v219 offset:16896\n"
"s_mov_b32 s63, 45056\n"
"buffer_load_dwordx4 v[136:139], v252, s[48:51], s63 offen offset:0\n"
"v_mfma_f32_16x16x32_bf16 v[20:23], a[100:103], v[148:151], v[20:23]\n"
"ds_read_b64_tr_b16 a[74:75], v219 offset:20992\n"
"v_cvt_pk_bf16_f32 v160, v208, v209\n"
"v_mfma_f32_16x16x32_bf16 v[24:27], a[100:103], v[152:155], v[24:27]\n"
"ds_read_b64_tr_b16 a[76:77], v219 offset:16904\n"
"s_mov_b32 s63, 61440\n"
"buffer_load_dwordx4 v[140:143], v252, s[48:51], s63 offen offset:0\n"
"v_mfma_f32_16x16x32_bf16 v[28:31], a[100:103], v[156:159], v[28:31]\n"
"ds_read_b64_tr_b16 a[78:79], v219 offset:21000\n"
"v_fma_mix_f32 v212, v162, v196, v204 op_sel:[0,0,0] op_sel_hi:[1,0,0]\n"
"v_mfma_f32_16x16x32_bf16 v[32:35], a[104:107], v[144:147], v[32:35]\n"
"ds_read_b64_tr_b16 a[80:81], v219 offset:17408\n"
"v_fma_mix_f32 v213, v162, v197, v205 op_sel:[1,0,0] op_sel_hi:[1,0,0]\n"
"v_mfma_f32_16x16x32_bf16 v[36:39], a[104:107], v[148:151], v[36:39]\n"
"ds_read_b64_tr_b16 a[82:83], v219 offset:21504\n"
"v_cvt_pk_bf16_f32 v161, v210, v211\n"
"v_mfma_f32_16x16x32_bf16 v[40:43], a[104:107], v[152:155], v[40:43]\n"
"ds_read_b64_tr_b16 a[84:85], v219 offset:17416\n"
"v_fma_mix_f32 v214, v163, v198, v206 op_sel:[0,0,0] op_sel_hi:[1,0,0]\n"
"v_mfma_f32_16x16x32_bf16 v[44:47], a[104:107], v[156:159], v[44:47]\n"
"ds_read_b64_tr_b16 a[86:87], v219 offset:21512\n"
"v_fma_mix_f32 v215, v163, v199, v207 op_sel:[1,0,0] op_sel_hi:[1,0,0]\n"
"v_cvt_pk_bf16_f32 v162, v212, v213\n"
"v_fma_mix_f32 v208, v164, v192, v200 op_sel:[0,0,0] op_sel_hi:[1,0,0]\n"
"v_mfma_f32_16x16x32_bf16 v[48:51], a[108:111], v[144:147], v[48:51]\n"
"ds_read_b64_tr_b16 a[88:89], v219 offset:17920\n"
"v_fma_mix_f32 v209, v164, v193, v201 op_sel:[1,0,0] op_sel_hi:[1,0,0]\n"
"v_mfma_f32_16x16x32_bf16 v[52:55], a[108:111], v[148:151], v[52:55]\n"
"ds_read_b64_tr_b16 a[90:91], v219 offset:22016\n"
"v_cvt_pk_bf16_f32 v163, v214, v215\n"
"v_mfma_f32_16x16x32_bf16 v[56:59], a[108:111], v[152:155], v[56:59]\n"
"ds_read_b64_tr_b16 a[92:93], v219 offset:17928\n"
"v_fma_mix_f32 v210, v165, v194, v202 op_sel:[0,0,0] op_sel_hi:[1,0,0]\n"
"v_mfma_f32_16x16x32_bf16 v[60:63], a[108:111], v[156:159], v[60:63]\n"
"ds_read_b64_tr_b16 a[94:95], v219 offset:22024\n"
"v_fma_mix_f32 v211, v165, v195, v203 op_sel:[1,0,0] op_sel_hi:[1,0,0]\n"
"v_mfma_f32_16x16x32_bf16 v[64:67], a[112:115], v[144:147], v[64:67]\n"
"ds_read_b128 v[230:233], v229 offset:1408\n"
"v_cvt_pk_bf16_f32 v164, v208, v209\n"
"v_mfma_f32_16x16x32_bf16 v[68:71], a[112:115], v[148:151], v[68:71]\n"
"ds_read_b128 v[234:237], v229 offset:1472\n"
"v_fma_mix_f32 v212, v166, v196, v204 op_sel:[0,0,0] op_sel_hi:[1,0,0]\n"
"v_mfma_f32_16x16x32_bf16 v[72:75], a[112:115], v[152:155], v[72:75]\n"
"ds_read_b128 v[238:241], v229 offset:3456\n"
"v_fma_mix_f32 v213, v166, v197, v205 op_sel:[1,0,0] op_sel_hi:[1,0,0]\n"
"v_cvt_pk_bf16_f32 v165, v210, v211\n"
"v_fma_mix_f32 v214, v167, v198, v206 op_sel:[0,0,0] op_sel_hi:[1,0,0]\n"
"v_mfma_f32_16x16x32_bf16 v[76:79], a[112:115], v[156:159], v[76:79]\n"
"ds_read_b128 v[242:245], v229 offset:3520\n"
"v_fma_mix_f32 v215, v167, v199, v207 op_sel:[1,0,0] op_sel_hi:[1,0,0]\n"
"v_mfma_f32_16x16x32_bf16 v[80:83], a[116:119], v[144:147], v[80:83]\n"
"v_cvt_pk_bf16_f32 v166, v212, v213\n"
"v_fma_mix_f32 v208, v168, v192, v200 op_sel:[0,0,0] op_sel_hi:[1,0,0]\n"
"v_mfma_f32_16x16x32_bf16 v[84:87], a[116:119], v[148:151], v[84:87]\n"
"v_fma_mix_f32 v209, v168, v193, v201 op_sel:[1,0,0] op_sel_hi:[1,0,0]\n"
"v_cvt_pk_bf16_f32 v167, v214, v215\n"
"v_mfma_f32_16x16x32_bf16 v[88:91], a[116:119], v[152:155], v[88:91]\n"
"v_fma_mix_f32 v210, v169, v194, v202 op_sel:[0,0,0] op_sel_hi:[1,0,0]\n"
"v_fma_mix_f32 v211, v169, v195, v203 op_sel:[1,0,0] op_sel_hi:[1,0,0]\n"
"v_mfma_f32_16x16x32_bf16 v[92:95], a[116:119], v[156:159], v[92:95]\n"
"v_cvt_pk_bf16_f32 v168, v208, v209\n"
"v_fma_mix_f32 v212, v170, v196, v204 op_sel:[0,0,0] op_sel_hi:[1,0,0]\n"
"v_mfma_f32_16x16x32_bf16 v[96:99], a[120:123], v[144:147], v[96:99]\n"
"v_fma_mix_f32 v213, v170, v197, v205 op_sel:[1,0,0] op_sel_hi:[1,0,0]\n"
"v_cvt_pk_bf16_f32 v169, v210, v211\n"
"v_mfma_f32_16x16x32_bf16 v[100:103], a[120:123], v[148:151], v[100:103]\n"
"v_fma_mix_f32 v214, v171, v198, v206 op_sel:[0,0,0] op_sel_hi:[1,0,0]\n"
"v_fma_mix_f32 v215, v171, v199, v207 op_sel:[1,0,0] op_sel_hi:[1,0,0]\n"
"v_cvt_pk_bf16_f32 v170, v212, v213\n"
"v_fma_mix_f32 v208, v172, v192, v200 op_sel:[0,0,0] op_sel_hi:[1,0,0]\n"
"v_mfma_f32_16x16x32_bf16 v[104:107], a[120:123], v[152:155], v[104:107]\n"
"v_fma_mix_f32 v209, v172, v193, v201 op_sel:[1,0,0] op_sel_hi:[1,0,0]\n"
"v_cvt_pk_bf16_f32 v171, v214, v215\n"
"v_mfma_f32_16x16x32_bf16 v[108:111], a[120:123], v[156:159], v[108:111]\n"
"v_fma_mix_f32 v210, v173, v194, v202 op_sel:[0,0,0] op_sel_hi:[1,0,0]\n"
"v_fma_mix_f32 v211, v173, v195, v203 op_sel:[1,0,0] op_sel_hi:[1,0,0]\n"
"v_mfma_f32_16x16x32_bf16 v[112:115], a[124:127], v[144:147], v[112:115]\n"
"v_cvt_pk_bf16_f32 v172, v208, v209\n"
"v_fma_mix_f32 v212, v174, v196, v204 op_sel:[0,0,0] op_sel_hi:[1,0,0]\n"
"v_mfma_f32_16x16x32_bf16 v[116:119], a[124:127], v[148:151], v[116:119]\n"
"v_fma_mix_f32 v213, v174, v197, v205 op_sel:[1,0,0] op_sel_hi:[1,0,0]\n"
"v_cvt_pk_bf16_f32 v173, v210, v211\n"
"v_mfma_f32_16x16x32_bf16 v[120:123], a[124:127], v[152:155], v[120:123]\n"
"v_fma_mix_f32 v214, v175, v198, v206 op_sel:[0,0,0] op_sel_hi:[1,0,0]\n"
"v_fma_mix_f32 v215, v175, v199, v207 op_sel:[1,0,0] op_sel_hi:[1,0,0]\n"
"v_mfma_f32_16x16x32_bf16 v[124:127], a[124:127], v[156:159], v[124:127]\n"
"v_cvt_pk_bf16_f32 v174, v212, v213\n"
"v_cvt_pk_bf16_f32 v175, v214, v215\n"
"s_waitcnt vmcnt(6) lgkmcnt(0)\n"
"v_mfma_f32_16x16x32_bf16 v[0:3], a[64:67], v[160:163], v[0:3]\n"
"ds_read_b64_tr_b16 a[96:97], v219 offset:24576\n"
"s_mov_b32 s63, 12288\n"
"buffer_load_dwordx4 v[144:147], v252, s[48:51], s63 offen offset:1024\n"
"v_mfma_f32_16x16x32_bf16 v[4:7], a[64:67], v[164:167], v[4:7]\n"
"ds_read_b64_tr_b16 a[98:99], v219 offset:28672\n"
"v_fma_mix_f32 v208, v176, v230, v238 op_sel:[0,0,0] op_sel_hi:[1,0,0]\n"
"v_fma_mix_f32 v209, v176, v231, v239 op_sel:[1,0,0] op_sel_hi:[1,0,0]\n"
"v_fma_mix_f32 v210, v177, v232, v240 op_sel:[0,0,0] op_sel_hi:[1,0,0]\n"
"v_mfma_f32_16x16x32_bf16 v[8:11], a[64:67], v[168:171], v[8:11]\n"
"ds_read_b64_tr_b16 a[100:101], v219 offset:24584\n"
"s_mov_b32 s63, 28672\n"
"buffer_load_dwordx4 v[148:151], v252, s[48:51], s63 offen offset:1024\n"
"v_mfma_f32_16x16x32_bf16 v[12:15], a[64:67], v[172:175], v[12:15]\n"
"ds_read_b64_tr_b16 a[102:103], v219 offset:28680\n"
"v_fma_mix_f32 v211, v177, v233, v241 op_sel:[1,0,0] op_sel_hi:[1,0,0]\n"
"v_mfma_f32_16x16x32_bf16 v[16:19], a[68:71], v[160:163], v[16:19]\n"
"ds_read_b64_tr_b16 a[104:105], v219 offset:25088\n"
"s_mov_b32 s63, 45056\n"
"buffer_load_dwordx4 v[152:155], v252, s[48:51], s63 offen offset:1024\n"
"v_mfma_f32_16x16x32_bf16 v[20:23], a[68:71], v[164:167], v[20:23]\n"
"ds_read_b64_tr_b16 a[106:107], v219 offset:29184\n"
"v_cvt_pk_bf16_f32 v176, v208, v209\n"
"v_mfma_f32_16x16x32_bf16 v[24:27], a[68:71], v[168:171], v[24:27]\n"
"ds_read_b64_tr_b16 a[108:109], v219 offset:25096\n"
"s_mov_b32 s63, 61440\n"
"buffer_load_dwordx4 v[156:159], v252, s[48:51], s63 offen offset:1024\n"
"v_mfma_f32_16x16x32_bf16 v[28:31], a[68:71], v[172:175], v[28:31]\n"
"ds_read_b64_tr_b16 a[110:111], v219 offset:29192\n"
"v_fma_mix_f32 v212, v178, v234, v242 op_sel:[0,0,0] op_sel_hi:[1,0,0]\n"
"v_mfma_f32_16x16x32_bf16 v[32:35], a[72:75], v[160:163], v[32:35]\n"
"ds_read_b64_tr_b16 a[112:113], v219 offset:25600\n"
"v_fma_mix_f32 v213, v178, v235, v243 op_sel:[1,0,0] op_sel_hi:[1,0,0]\n"
"v_mfma_f32_16x16x32_bf16 v[36:39], a[72:75], v[164:167], v[36:39]\n"
"ds_read_b64_tr_b16 a[114:115], v219 offset:29696\n"
"v_cvt_pk_bf16_f32 v177, v210, v211\n"
"v_mfma_f32_16x16x32_bf16 v[40:43], a[72:75], v[168:171], v[40:43]\n"
"ds_read_b64_tr_b16 a[116:117], v219 offset:25608\n"
"v_fma_mix_f32 v214, v179, v236, v244 op_sel:[0,0,0] op_sel_hi:[1,0,0]\n"
"v_mfma_f32_16x16x32_bf16 v[44:47], a[72:75], v[172:175], v[44:47]\n"
"ds_read_b64_tr_b16 a[118:119], v219 offset:29704\n"
"v_fma_mix_f32 v215, v179, v237, v245 op_sel:[1,0,0] op_sel_hi:[1,0,0]\n"
"v_cvt_pk_bf16_f32 v178, v212, v213\n"
"v_fma_mix_f32 v208, v180, v230, v238 op_sel:[0,0,0] op_sel_hi:[1,0,0]\n"
"v_mfma_f32_16x16x32_bf16 v[48:51], a[76:79], v[160:163], v[48:51]\n"
"ds_read_b64_tr_b16 a[120:121], v219 offset:26112\n"
"v_fma_mix_f32 v209, v180, v231, v239 op_sel:[1,0,0] op_sel_hi:[1,0,0]\n"
"v_mfma_f32_16x16x32_bf16 v[52:55], a[76:79], v[164:167], v[52:55]\n"
"ds_read_b64_tr_b16 a[122:123], v219 offset:30208\n"
"v_cvt_pk_bf16_f32 v179, v214, v215\n"
"v_mfma_f32_16x16x32_bf16 v[56:59], a[76:79], v[168:171], v[56:59]\n"
"ds_read_b64_tr_b16 a[124:125], v219 offset:26120\n"
"v_fma_mix_f32 v210, v181, v232, v240 op_sel:[0,0,0] op_sel_hi:[1,0,0]\n"
"v_mfma_f32_16x16x32_bf16 v[60:63], a[76:79], v[172:175], v[60:63]\n"
"ds_read_b64_tr_b16 a[126:127], v219 offset:30216\n"
"v_fma_mix_f32 v211, v181, v233, v241 op_sel:[1,0,0] op_sel_hi:[1,0,0]\n"
"v_mfma_f32_16x16x32_bf16 v[64:67], a[80:83], v[160:163], v[64:67]\n"
"ds_read_b128 v[192:195], v229 offset:1536\n"
"v_cvt_pk_bf16_f32 v180, v208, v209\n"
"v_mfma_f32_16x16x32_bf16 v[68:71], a[80:83], v[164:167], v[68:71]\n"
"ds_read_b128 v[196:199], v229 offset:1600\n"
"v_fma_mix_f32 v212, v182, v234, v242 op_sel:[0,0,0] op_sel_hi:[1,0,0]\n"
"v_mfma_f32_16x16x32_bf16 v[72:75], a[80:83], v[168:171], v[72:75]\n"
"ds_read_b128 v[200:203], v229 offset:3584\n"
"v_fma_mix_f32 v213, v182, v235, v243 op_sel:[1,0,0] op_sel_hi:[1,0,0]\n"
"v_cvt_pk_bf16_f32 v181, v210, v211\n"
"v_fma_mix_f32 v214, v183, v236, v244 op_sel:[0,0,0] op_sel_hi:[1,0,0]\n"
"v_mfma_f32_16x16x32_bf16 v[76:79], a[80:83], v[172:175], v[76:79]\n"
"ds_read_b128 v[204:207], v229 offset:3648\n"
"v_fma_mix_f32 v215, v183, v237, v245 op_sel:[1,0,0] op_sel_hi:[1,0,0]\n"
"v_mfma_f32_16x16x32_bf16 v[80:83], a[84:87], v[160:163], v[80:83]\n"
"v_cvt_pk_bf16_f32 v182, v212, v213\n"
"v_fma_mix_f32 v208, v184, v230, v238 op_sel:[0,0,0] op_sel_hi:[1,0,0]\n"
"v_mfma_f32_16x16x32_bf16 v[84:87], a[84:87], v[164:167], v[84:87]\n"
"v_fma_mix_f32 v209, v184, v231, v239 op_sel:[1,0,0] op_sel_hi:[1,0,0]\n"
"v_cvt_pk_bf16_f32 v183, v214, v215\n"
"v_mfma_f32_16x16x32_bf16 v[88:91], a[84:87], v[168:171], v[88:91]\n"
"v_fma_mix_f32 v210, v185, v232, v240 op_sel:[0,0,0] op_sel_hi:[1,0,0]\n"
"v_fma_mix_f32 v211, v185, v233, v241 op_sel:[1,0,0] op_sel_hi:[1,0,0]\n"
"v_mfma_f32_16x16x32_bf16 v[92:95], a[84:87], v[172:175], v[92:95]\n"
"v_cvt_pk_bf16_f32 v184, v208, v209\n"
"v_fma_mix_f32 v212, v186, v234, v242 op_sel:[0,0,0] op_sel_hi:[1,0,0]\n"
"v_mfma_f32_16x16x32_bf16 v[96:99], a[88:91], v[160:163], v[96:99]\n"
"v_fma_mix_f32 v213, v186, v235, v243 op_sel:[1,0,0] op_sel_hi:[1,0,0]\n"
"v_cvt_pk_bf16_f32 v185, v210, v211\n"
"v_mfma_f32_16x16x32_bf16 v[100:103], a[88:91], v[164:167], v[100:103]\n"
"v_fma_mix_f32 v214, v187, v236, v244 op_sel:[0,0,0] op_sel_hi:[1,0,0]\n"
"v_fma_mix_f32 v215, v187, v237, v245 op_sel:[1,0,0] op_sel_hi:[1,0,0]\n"
"v_cvt_pk_bf16_f32 v186, v212, v213\n"
"v_fma_mix_f32 v208, v188, v230, v238 op_sel:[0,0,0] op_sel_hi:[1,0,0]\n"
"v_mfma_f32_16x16x32_bf16 v[104:107], a[88:91], v[168:171], v[104:107]\n"
"v_fma_mix_f32 v209, v188, v231, v239 op_sel:[1,0,0] op_sel_hi:[1,0,0]\n"
"v_cvt_pk_bf16_f32 v187, v214, v215\n"
"v_mfma_f32_16x16x32_bf16 v[108:111], a[88:91], v[172:175], v[108:111]\n"
"v_fma_mix_f32 v210, v189, v232, v240 op_sel:[0,0,0] op_sel_hi:[1,0,0]\n"
"v_fma_mix_f32 v211, v189, v233, v241 op_sel:[1,0,0] op_sel_hi:[1,0,0]\n"
"v_mfma_f32_16x16x32_bf16 v[112:115], a[92:95], v[160:163], v[112:115]\n"
"v_cvt_pk_bf16_f32 v188, v208, v209\n"
"v_fma_mix_f32 v212, v190, v234, v242 op_sel:[0,0,0] op_sel_hi:[1,0,0]\n"
"v_mfma_f32_16x16x32_bf16 v[116:119], a[92:95], v[164:167], v[116:119]\n"
"v_fma_mix_f32 v213, v190, v235, v243 op_sel:[1,0,0] op_sel_hi:[1,0,0]\n"
"v_cvt_pk_bf16_f32 v189, v210, v211\n"
"v_mfma_f32_16x16x32_bf16 v[120:123], a[92:95], v[168:171], v[120:123]\n"
"v_fma_mix_f32 v214, v191, v236, v244 op_sel:[0,0,0] op_sel_hi:[1,0,0]\n"
"v_fma_mix_f32 v215, v191, v237, v245 op_sel:[1,0,0] op_sel_hi:[1,0,0]\n"
"v_mfma_f32_16x16x32_bf16 v[124:127], a[92:95], v[172:175], v[124:127]\n"
"v_cvt_pk_bf16_f32 v190, v212, v213\n"
"v_cvt_pk_bf16_f32 v191, v214, v215\n"
"s_waitcnt vmcnt(4) lgkmcnt(0)\n"
"s_barrier\n"
"v_mfma_f32_16x16x32_bf16 v[0:3], a[96:99], v[176:179], v[0:3]\n"
"ds_read_b64_tr_b16 a[64:65], v219 offset:32768\n"
"s_mov_b32 s63, 12288\n"
"buffer_load_dwordx4 v[160:163], v252, s[48:51], s63 offen offset:2048\n"
"v_mfma_f32_16x16x32_bf16 v[4:7], a[96:99], v[180:183], v[4:7]\n"
"ds_read_b64_tr_b16 a[66:67], v219 offset:36864\n"
"v_fma_mix_f32 v208, v128, v192, v200 op_sel:[0,0,0] op_sel_hi:[1,0,0]\n"
"v_fma_mix_f32 v209, v128, v193, v201 op_sel:[1,0,0] op_sel_hi:[1,0,0]\n"
"v_mfma_f32_16x16x32_bf16 v[8:11], a[96:99], v[184:187], v[8:11]\n"
"ds_read_b64_tr_b16 a[68:69], v219 offset:32776\n"
"s_mov_b32 s63, 28672\n"
"buffer_load_dwordx4 v[164:167], v252, s[48:51], s63 offen offset:2048\n"
"v_mfma_f32_16x16x32_bf16 v[12:15], a[96:99], v[188:191], v[12:15]\n"
"ds_read_b64_tr_b16 a[70:71], v219 offset:36872\n"
"v_fma_mix_f32 v210, v129, v194, v202 op_sel:[0,0,0] op_sel_hi:[1,0,0]\n"
"v_fma_mix_f32 v211, v129, v195, v203 op_sel:[1,0,0] op_sel_hi:[1,0,0]\n"
"v_mfma_f32_16x16x32_bf16 v[16:19], a[100:103], v[176:179], v[16:19]\n"
"ds_read_b64_tr_b16 a[72:73], v219 offset:33280\n"
"s_mov_b32 s63, 45056\n"
"buffer_load_dwordx4 v[168:171], v252, s[48:51], s63 offen offset:2048\n"
"v_mfma_f32_16x16x32_bf16 v[20:23], a[100:103], v[180:183], v[20:23]\n"
"ds_read_b64_tr_b16 a[74:75], v219 offset:37376\n"
"v_cvt_pk_bf16_f32 v128, v208, v209\n"
"v_fma_mix_f32 v212, v130, v196, v204 op_sel:[0,0,0] op_sel_hi:[1,0,0]\n"
"v_mfma_f32_16x16x32_bf16 v[24:27], a[100:103], v[184:187], v[24:27]\n"
"ds_read_b64_tr_b16 a[76:77], v219 offset:33288\n"
"s_mov_b32 s63, 61440\n"
"buffer_load_dwordx4 v[172:175], v252, s[48:51], s63 offen offset:2048\n"
"v_mfma_f32_16x16x32_bf16 v[28:31], a[100:103], v[188:191], v[28:31]\n"
"ds_read_b64_tr_b16 a[78:79], v219 offset:37384\n"
"v_fma_mix_f32 v213, v130, v197, v205 op_sel:[1,0,0] op_sel_hi:[1,0,0]\n"
"v_mfma_f32_16x16x32_bf16 v[32:35], a[104:107], v[176:179], v[32:35]\n"
"ds_read_b64_tr_b16 a[80:81], v219 offset:33792\n"
"v_cvt_pk_bf16_f32 v129, v210, v211\n"
"v_fma_mix_f32 v214, v131, v198, v206 op_sel:[0,0,0] op_sel_hi:[1,0,0]\n"
"v_mfma_f32_16x16x32_bf16 v[36:39], a[104:107], v[180:183], v[36:39]\n"
"ds_read_b64_tr_b16 a[82:83], v219 offset:37888\n"
"v_fma_mix_f32 v215, v131, v199, v207 op_sel:[1,0,0] op_sel_hi:[1,0,0]\n"
"v_cvt_pk_bf16_f32 v130, v212, v213\n"
"v_mfma_f32_16x16x32_bf16 v[40:43], a[104:107], v[184:187], v[40:43]\n"
"ds_read_b64_tr_b16 a[84:85], v219 offset:33800\n"
"v_fma_mix_f32 v208, v132, v192, v200 op_sel:[0,0,0] op_sel_hi:[1,0,0]\n"
"v_mfma_f32_16x16x32_bf16 v[44:47], a[104:107], v[188:191], v[44:47]\n"
"ds_read_b64_tr_b16 a[86:87], v219 offset:37896\n"
"v_fma_mix_f32 v209, v132, v193, v201 op_sel:[1,0,0] op_sel_hi:[1,0,0]\n"
"v_cvt_pk_bf16_f32 v131, v214, v215\n"
"v_mfma_f32_16x16x32_bf16 v[48:51], a[108:111], v[176:179], v[48:51]\n"
"ds_read_b64_tr_b16 a[88:89], v219 offset:34304\n"
"v_fma_mix_f32 v210, v133, v194, v202 op_sel:[0,0,0] op_sel_hi:[1,0,0]\n"
"v_fma_mix_f32 v211, v133, v195, v203 op_sel:[1,0,0] op_sel_hi:[1,0,0]\n"
"v_mfma_f32_16x16x32_bf16 v[52:55], a[108:111], v[180:183], v[52:55]\n"
"ds_read_b64_tr_b16 a[90:91], v219 offset:38400\n"
"v_cvt_pk_bf16_f32 v132, v208, v209\n"
"v_fma_mix_f32 v212, v134, v196, v204 op_sel:[0,0,0] op_sel_hi:[1,0,0]\n"
"v_mfma_f32_16x16x32_bf16 v[56:59], a[108:111], v[184:187], v[56:59]\n"
"ds_read_b64_tr_b16 a[92:93], v219 offset:34312\n"
"v_fma_mix_f32 v213, v134, v197, v205 op_sel:[1,0,0] op_sel_hi:[1,0,0]\n"
"v_mfma_f32_16x16x32_bf16 v[60:63], a[108:111], v[188:191], v[60:63]\n"
"ds_read_b64_tr_b16 a[94:95], v219 offset:38408\n"
"v_cvt_pk_bf16_f32 v133, v210, v211\n"
"v_fma_mix_f32 v214, v135, v198, v206 op_sel:[0,0,0] op_sel_hi:[1,0,0]\n"
"v_mfma_f32_16x16x32_bf16 v[64:67], a[112:115], v[176:179], v[64:67]\n"
"ds_read_b128 v[230:233], v229 offset:1664\n"
"v_fma_mix_f32 v215, v135, v199, v207 op_sel:[1,0,0] op_sel_hi:[1,0,0]\n"
"v_cvt_pk_bf16_f32 v134, v212, v213\n"
"v_mfma_f32_16x16x32_bf16 v[68:71], a[112:115], v[180:183], v[68:71]\n"
"ds_read_b128 v[234:237], v229 offset:1728\n"
"v_fma_mix_f32 v208, v136, v192, v200 op_sel:[0,0,0] op_sel_hi:[1,0,0]\n"
"v_mfma_f32_16x16x32_bf16 v[72:75], a[112:115], v[184:187], v[72:75]\n"
"ds_read_b128 v[238:241], v229 offset:3712\n"
"v_fma_mix_f32 v209, v136, v193, v201 op_sel:[1,0,0] op_sel_hi:[1,0,0]\n"
"v_cvt_pk_bf16_f32 v135, v214, v215\n"
"v_mfma_f32_16x16x32_bf16 v[76:79], a[112:115], v[188:191], v[76:79]\n"
"ds_read_b128 v[242:245], v229 offset:3776\n"
"v_fma_mix_f32 v210, v137, v194, v202 op_sel:[0,0,0] op_sel_hi:[1,0,0]\n"
"v_fma_mix_f32 v211, v137, v195, v203 op_sel:[1,0,0] op_sel_hi:[1,0,0]\n"
"v_mfma_f32_16x16x32_bf16 v[80:83], a[116:119], v[176:179], v[80:83]\n"
"s_lshl_b32 s64, s56, 12\n"
"s_add_u32 m0, s64, 131072\n"
"s_nop 0\n"
"buffer_load_dwordx4 v246, s[36:39], s64 offen offset:0 lds\n"
"v_mfma_f32_16x16x32_bf16 v[84:87], a[116:119], v[180:183], v[84:87]\n"
"v_cvt_pk_bf16_f32 v136, v208, v209\n"
"v_fma_mix_f32 v212, v138, v196, v204 op_sel:[0,0,0] op_sel_hi:[1,0,0]\n"
"v_mfma_f32_16x16x32_bf16 v[88:91], a[116:119], v[184:187], v[88:91]\n"
"v_fma_mix_f32 v213, v138, v197, v205 op_sel:[1,0,0] op_sel_hi:[1,0,0]\n"
"v_cvt_pk_bf16_f32 v137, v210, v211\n"
"v_mfma_f32_16x16x32_bf16 v[92:95], a[116:119], v[188:191], v[92:95]\n"
"s_lshl_b32 s64, s56, 12\n"
"s_add_u32 m0, s64, 131072\n"
"s_nop 0\n"
"buffer_load_dwordx4 v247, s[36:39], s64 offen offset:1024 lds\n"
"v_mfma_f32_16x16x32_bf16 v[96:99], a[120:123], v[176:179], v[96:99]\n"
"v_fma_mix_f32 v214, v139, v198, v206 op_sel:[0,0,0] op_sel_hi:[1,0,0]\n"
"v_fma_mix_f32 v215, v139, v199, v207 op_sel:[1,0,0] op_sel_hi:[1,0,0]\n"
"v_cvt_pk_bf16_f32 v138, v212, v213\n"
"v_mfma_f32_16x16x32_bf16 v[100:103], a[120:123], v[180:183], v[100:103]\n"
"v_fma_mix_f32 v208, v140, v192, v200 op_sel:[0,0,0] op_sel_hi:[1,0,0]\n"
"v_fma_mix_f32 v209, v140, v193, v201 op_sel:[1,0,0] op_sel_hi:[1,0,0]\n"
"v_cvt_pk_bf16_f32 v139, v214, v215\n"
"v_mfma_f32_16x16x32_bf16 v[104:107], a[120:123], v[184:187], v[104:107]\n"
"s_lshl_b32 s64, s56, 12\n"
"s_add_u32 m0, s64, 131072\n"
"s_nop 0\n"
"buffer_load_dwordx4 v248, s[36:39], s64 offen offset:2048 lds\n"
"v_mfma_f32_16x16x32_bf16 v[108:111], a[120:123], v[188:191], v[108:111]\n"
"v_fma_mix_f32 v210, v141, v194, v202 op_sel:[0,0,0] op_sel_hi:[1,0,0]\n"
"v_fma_mix_f32 v211, v141, v195, v203 op_sel:[1,0,0] op_sel_hi:[1,0,0]\n"
"v_mfma_f32_16x16x32_bf16 v[112:115], a[124:127], v[176:179], v[112:115]\n"
"v_cvt_pk_bf16_f32 v140, v208, v209\n"
"v_fma_mix_f32 v212, v142, v196, v204 op_sel:[0,0,0] op_sel_hi:[1,0,0]\n"
"v_fma_mix_f32 v213, v142, v197, v205 op_sel:[1,0,0] op_sel_hi:[1,0,0]\n"
"v_mfma_f32_16x16x32_bf16 v[116:119], a[124:127], v[180:183], v[116:119]\n"
"s_lshl_b32 s64, s56, 12\n"
"s_add_u32 m0, s64, 131072\n"
"s_nop 0\n"
"buffer_load_dwordx4 v249, s[36:39], s64 offen offset:3072 lds\n"
"v_mfma_f32_16x16x32_bf16 v[120:123], a[124:127], v[184:187], v[120:123]\n"
"v_cvt_pk_bf16_f32 v141, v210, v211\n"
"v_fma_mix_f32 v214, v143, v198, v206 op_sel:[0,0,0] op_sel_hi:[1,0,0]\n"
"v_fma_mix_f32 v215, v143, v199, v207 op_sel:[1,0,0] op_sel_hi:[1,0,0]\n"
"v_mfma_f32_16x16x32_bf16 v[124:127], a[124:127], v[188:191], v[124:127]\n"
"v_cvt_pk_bf16_f32 v142, v212, v213\n"
"v_cvt_pk_bf16_f32 v143, v214, v215\n"
"s_waitcnt vmcnt(8) lgkmcnt(0)\n"
"v_mfma_f32_16x16x32_bf16 v[0:3], a[64:67], v[128:131], v[0:3]\n"
"ds_read_b64_tr_b16 a[96:97], v219 offset:40960\n"
"s_mov_b32 s63, 12288\n"
"buffer_load_dwordx4 v[176:179], v252, s[48:51], s63 offen offset:3072\n"
"v_mfma_f32_16x16x32_bf16 v[4:7], a[64:67], v[132:135], v[4:7]\n"
"ds_read_b64_tr_b16 a[98:99], v219 offset:45056\n"
"v_fma_mix_f32 v208, v144, v230, v238 op_sel:[0,0,0] op_sel_hi:[1,0,0]\n"
"v_fma_mix_f32 v209, v144, v231, v239 op_sel:[1,0,0] op_sel_hi:[1,0,0]\n"
"v_mfma_f32_16x16x32_bf16 v[8:11], a[64:67], v[136:139], v[8:11]\n"
"ds_read_b64_tr_b16 a[100:101], v219 offset:40968\n"
"s_mov_b32 s63, 28672\n"
"buffer_load_dwordx4 v[180:183], v252, s[48:51], s63 offen offset:3072\n"
"v_mfma_f32_16x16x32_bf16 v[12:15], a[64:67], v[140:143], v[12:15]\n"
"ds_read_b64_tr_b16 a[102:103], v219 offset:45064\n"
"v_fma_mix_f32 v210, v145, v232, v240 op_sel:[0,0,0] op_sel_hi:[1,0,0]\n"
"v_fma_mix_f32 v211, v145, v233, v241 op_sel:[1,0,0] op_sel_hi:[1,0,0]\n"
"v_mfma_f32_16x16x32_bf16 v[16:19], a[68:71], v[128:131], v[16:19]\n"
"ds_read_b64_tr_b16 a[104:105], v219 offset:41472\n"
"s_mov_b32 s63, 45056\n"
"buffer_load_dwordx4 v[184:187], v252, s[48:51], s63 offen offset:3072\n"
"v_mfma_f32_16x16x32_bf16 v[20:23], a[68:71], v[132:135], v[20:23]\n"
"ds_read_b64_tr_b16 a[106:107], v219 offset:45568\n"
"v_cvt_pk_bf16_f32 v144, v208, v209\n"
"v_fma_mix_f32 v212, v146, v234, v242 op_sel:[0,0,0] op_sel_hi:[1,0,0]\n"
"v_mfma_f32_16x16x32_bf16 v[24:27], a[68:71], v[136:139], v[24:27]\n"
"ds_read_b64_tr_b16 a[108:109], v219 offset:41480\n"
"s_mov_b32 s63, 61440\n"
"buffer_load_dwordx4 v[188:191], v252, s[48:51], s63 offen offset:3072\n"
"v_mfma_f32_16x16x32_bf16 v[28:31], a[68:71], v[140:143], v[28:31]\n"
"ds_read_b64_tr_b16 a[110:111], v219 offset:45576\n"
"v_fma_mix_f32 v213, v146, v235, v243 op_sel:[1,0,0] op_sel_hi:[1,0,0]\n"
"v_mfma_f32_16x16x32_bf16 v[32:35], a[72:75], v[128:131], v[32:35]\n"
"ds_read_b64_tr_b16 a[112:113], v219 offset:41984\n"
"v_cvt_pk_bf16_f32 v145, v210, v211\n"
"v_fma_mix_f32 v214, v147, v236, v244 op_sel:[0,0,0] op_sel_hi:[1,0,0]\n"
"v_mfma_f32_16x16x32_bf16 v[36:39], a[72:75], v[132:135], v[36:39]\n"
"ds_read_b64_tr_b16 a[114:115], v219 offset:46080\n"
"v_fma_mix_f32 v215, v147, v237, v245 op_sel:[1,0,0] op_sel_hi:[1,0,0]\n"
"v_cvt_pk_bf16_f32 v146, v212, v213\n"
"v_mfma_f32_16x16x32_bf16 v[40:43], a[72:75], v[136:139], v[40:43]\n"
"ds_read_b64_tr_b16 a[116:117], v219 offset:41992\n"
"v_fma_mix_f32 v208, v148, v230, v238 op_sel:[0,0,0] op_sel_hi:[1,0,0]\n"
"v_mfma_f32_16x16x32_bf16 v[44:47], a[72:75], v[140:143], v[44:47]\n"
"ds_read_b64_tr_b16 a[118:119], v219 offset:46088\n"
"v_fma_mix_f32 v209, v148, v231, v239 op_sel:[1,0,0] op_sel_hi:[1,0,0]\n"
"v_cvt_pk_bf16_f32 v147, v214, v215\n"
"v_mfma_f32_16x16x32_bf16 v[48:51], a[76:79], v[128:131], v[48:51]\n"
"ds_read_b64_tr_b16 a[120:121], v219 offset:42496\n"
"v_fma_mix_f32 v210, v149, v232, v240 op_sel:[0,0,0] op_sel_hi:[1,0,0]\n"
"v_fma_mix_f32 v211, v149, v233, v241 op_sel:[1,0,0] op_sel_hi:[1,0,0]\n"
"v_mfma_f32_16x16x32_bf16 v[52:55], a[76:79], v[132:135], v[52:55]\n"
"ds_read_b64_tr_b16 a[122:123], v219 offset:46592\n"
"v_cvt_pk_bf16_f32 v148, v208, v209\n"
"v_fma_mix_f32 v212, v150, v234, v242 op_sel:[0,0,0] op_sel_hi:[1,0,0]\n"
"v_mfma_f32_16x16x32_bf16 v[56:59], a[76:79], v[136:139], v[56:59]\n"
"ds_read_b64_tr_b16 a[124:125], v219 offset:42504\n"
"v_fma_mix_f32 v213, v150, v235, v243 op_sel:[1,0,0] op_sel_hi:[1,0,0]\n"
"v_mfma_f32_16x16x32_bf16 v[60:63], a[76:79], v[140:143], v[60:63]\n"
"ds_read_b64_tr_b16 a[126:127], v219 offset:46600\n"
"v_cvt_pk_bf16_f32 v149, v210, v211\n"
"v_fma_mix_f32 v214, v151, v236, v244 op_sel:[0,0,0] op_sel_hi:[1,0,0]\n"
"v_mfma_f32_16x16x32_bf16 v[64:67], a[80:83], v[128:131], v[64:67]\n"
"ds_read_b128 v[192:195], v229 offset:1792\n"
"v_fma_mix_f32 v215, v151, v237, v245 op_sel:[1,0,0] op_sel_hi:[1,0,0]\n"
"v_cvt_pk_bf16_f32 v150, v212, v213\n"
"v_mfma_f32_16x16x32_bf16 v[68:71], a[80:83], v[132:135], v[68:71]\n"
"ds_read_b128 v[196:199], v229 offset:1856\n"
"v_fma_mix_f32 v208, v152, v230, v238 op_sel:[0,0,0] op_sel_hi:[1,0,0]\n"
"v_mfma_f32_16x16x32_bf16 v[72:75], a[80:83], v[136:139], v[72:75]\n"
"ds_read_b128 v[200:203], v229 offset:3840\n"
"v_fma_mix_f32 v209, v152, v231, v239 op_sel:[1,0,0] op_sel_hi:[1,0,0]\n"
"v_cvt_pk_bf16_f32 v151, v214, v215\n"
"v_mfma_f32_16x16x32_bf16 v[76:79], a[80:83], v[140:143], v[76:79]\n"
"ds_read_b128 v[204:207], v229 offset:3904\n"
"v_fma_mix_f32 v210, v153, v232, v240 op_sel:[0,0,0] op_sel_hi:[1,0,0]\n"
"v_fma_mix_f32 v211, v153, v233, v241 op_sel:[1,0,0] op_sel_hi:[1,0,0]\n"
"v_mfma_f32_16x16x32_bf16 v[80:83], a[84:87], v[128:131], v[80:83]\n"
"s_mov_b32 m0, s59\n"
"s_nop 0\n"
"buffer_load_dwordx4 v246, s[36:39], s57 offen offset:0 lds\n"
"v_mfma_f32_16x16x32_bf16 v[84:87], a[84:87], v[132:135], v[84:87]\n"
"v_cvt_pk_bf16_f32 v152, v208, v209\n"
"v_fma_mix_f32 v212, v154, v234, v242 op_sel:[0,0,0] op_sel_hi:[1,0,0]\n"
"v_mfma_f32_16x16x32_bf16 v[88:91], a[84:87], v[136:139], v[88:91]\n"
"v_fma_mix_f32 v213, v154, v235, v243 op_sel:[1,0,0] op_sel_hi:[1,0,0]\n"
"v_cvt_pk_bf16_f32 v153, v210, v211\n"
"v_mfma_f32_16x16x32_bf16 v[92:95], a[84:87], v[140:143], v[92:95]\n"
"s_mov_b32 m0, s59\n"
"s_nop 0\n"
"buffer_load_dwordx4 v247, s[36:39], s57 offen offset:1024 lds\n"
"v_mfma_f32_16x16x32_bf16 v[96:99], a[88:91], v[128:131], v[96:99]\n"
"v_fma_mix_f32 v214, v155, v236, v244 op_sel:[0,0,0] op_sel_hi:[1,0,0]\n"
"v_fma_mix_f32 v215, v155, v237, v245 op_sel:[1,0,0] op_sel_hi:[1,0,0]\n"
"v_cvt_pk_bf16_f32 v154, v212, v213\n"
"v_mfma_f32_16x16x32_bf16 v[100:103], a[88:91], v[132:135], v[100:103]\n"
"v_fma_mix_f32 v208, v156, v230, v238 op_sel:[0,0,0] op_sel_hi:[1,0,0]\n"
"v_fma_mix_f32 v209, v156, v231, v239 op_sel:[1,0,0] op_sel_hi:[1,0,0]\n"
"v_cvt_pk_bf16_f32 v155, v214, v215\n"
"v_mfma_f32_16x16x32_bf16 v[104:107], a[88:91], v[136:139], v[104:107]\n"
"s_mov_b32 m0, s59\n"
"s_nop 0\n"
"buffer_load_dwordx4 v248, s[36:39], s57 offen offset:2048 lds\n"
"v_mfma_f32_16x16x32_bf16 v[108:111], a[88:91], v[140:143], v[108:111]\n"
"v_fma_mix_f32 v210, v157, v232, v240 op_sel:[0,0,0] op_sel_hi:[1,0,0]\n"
"v_fma_mix_f32 v211, v157, v233, v241 op_sel:[1,0,0] op_sel_hi:[1,0,0]\n"
"v_mfma_f32_16x16x32_bf16 v[112:115], a[92:95], v[128:131], v[112:115]\n"
"v_cvt_pk_bf16_f32 v156, v208, v209\n"
"v_fma_mix_f32 v212, v158, v234, v242 op_sel:[0,0,0] op_sel_hi:[1,0,0]\n"
"v_fma_mix_f32 v213, v158, v235, v243 op_sel:[1,0,0] op_sel_hi:[1,0,0]\n"
"v_mfma_f32_16x16x32_bf16 v[116:119], a[92:95], v[132:135], v[116:119]\n"
"s_mov_b32 m0, s59\n"
"s_nop 0\n"
"buffer_load_dwordx4 v249, s[36:39], s57 offen offset:3072 lds\n"
"v_mfma_f32_16x16x32_bf16 v[120:123], a[92:95], v[136:139], v[120:123]\n"
"v_cvt_pk_bf16_f32 v157, v210, v211\n"
"v_fma_mix_f32 v214, v159, v236, v244 op_sel:[0,0,0] op_sel_hi:[1,0,0]\n"
"v_fma_mix_f32 v215, v159, v237, v245 op_sel:[1,0,0] op_sel_hi:[1,0,0]\n"
"v_mfma_f32_16x16x32_bf16 v[124:127], a[92:95], v[140:143], v[124:127]\n"
"v_cvt_pk_bf16_f32 v158, v212, v213\n"
"v_cvt_pk_bf16_f32 v159, v214, v215\n"
"s_waitcnt vmcnt(12) lgkmcnt(0)\n"
"v_mfma_f32_16x16x32_bf16 v[0:3], a[96:99], v[144:147], v[0:3]\n"
"ds_read_b64_tr_b16 a[64:65], v219 offset:49152\n"
"v_fma_mix_f32 v208, v160, v192, v200 op_sel:[0,0,0] op_sel_hi:[1,0,0]\n"
"v_fma_mix_f32 v209, v160, v193, v201 op_sel:[1,0,0] op_sel_hi:[1,0,0]\n"
"v_fma_mix_f32 v210, v161, v194, v202 op_sel:[0,0,0] op_sel_hi:[1,0,0]\n"
"v_fma_mix_f32 v211, v161, v195, v203 op_sel:[1,0,0] op_sel_hi:[1,0,0]\n"
"v_mfma_f32_16x16x32_bf16 v[4:7], a[96:99], v[148:151], v[4:7]\n"
"ds_read_b64_tr_b16 a[66:67], v219 offset:53248\n"
"v_cvt_pk_bf16_f32 v160, v208, v209\n"
"v_mfma_f32_16x16x32_bf16 v[8:11], a[96:99], v[152:155], v[8:11]\n"
"ds_read_b64_tr_b16 a[68:69], v219 offset:49160\n"
"v_fma_mix_f32 v212, v162, v196, v204 op_sel:[0,0,0] op_sel_hi:[1,0,0]\n"
"v_mfma_f32_16x16x32_bf16 v[12:15], a[96:99], v[156:159], v[12:15]\n"
"ds_read_b64_tr_b16 a[70:71], v219 offset:53256\n"
"v_fma_mix_f32 v213, v162, v197, v205 op_sel:[1,0,0] op_sel_hi:[1,0,0]\n"
"v_mfma_f32_16x16x32_bf16 v[16:19], a[100:103], v[144:147], v[16:19]\n"
"ds_read_b64_tr_b16 a[72:73], v219 offset:49664\n"
"v_cvt_pk_bf16_f32 v161, v210, v211\n"
"v_mfma_f32_16x16x32_bf16 v[20:23], a[100:103], v[148:151], v[20:23]\n"
"ds_read_b64_tr_b16 a[74:75], v219 offset:53760\n"
"v_fma_mix_f32 v214, v163, v198, v206 op_sel:[0,0,0] op_sel_hi:[1,0,0]\n"
"v_mfma_f32_16x16x32_bf16 v[24:27], a[100:103], v[152:155], v[24:27]\n"
"ds_read_b64_tr_b16 a[76:77], v219 offset:49672\n"
"v_fma_mix_f32 v215, v163, v199, v207 op_sel:[1,0,0] op_sel_hi:[1,0,0]\n"
"v_mfma_f32_16x16x32_bf16 v[28:31], a[100:103], v[156:159], v[28:31]\n"
"ds_read_b64_tr_b16 a[78:79], v219 offset:53768\n"
"v_cvt_pk_bf16_f32 v162, v212, v213\n"
"v_fma_mix_f32 v208, v164, v192, v200 op_sel:[0,0,0] op_sel_hi:[1,0,0]\n"
"v_fma_mix_f32 v209, v164, v193, v201 op_sel:[1,0,0] op_sel_hi:[1,0,0]\n"
"v_cvt_pk_bf16_f32 v163, v214, v215\n"
"v_mfma_f32_16x16x32_bf16 v[32:35], a[104:107], v[144:147], v[32:35]\n"
"ds_read_b64_tr_b16 a[80:81], v219 offset:50176\n"
"v_fma_mix_f32 v210, v165, v194, v202 op_sel:[0,0,0] op_sel_hi:[1,0,0]\n"
"v_mfma_f32_16x16x32_bf16 v[36:39], a[104:107], v[148:151], v[36:39]\n"
"ds_read_b64_tr_b16 a[82:83], v219 offset:54272\n"
"v_fma_mix_f32 v211, v165, v195, v203 op_sel:[1,0,0] op_sel_hi:[1,0,0]\n"
"v_mfma_f32_16x16x32_bf16 v[40:43], a[104:107], v[152:155], v[40:43]\n"
"ds_read_b64_tr_b16 a[84:85], v219 offset:50184\n"
"v_cvt_pk_bf16_f32 v164, v208, v209\n"
"v_mfma_f32_16x16x32_bf16 v[44:47], a[104:107], v[156:159], v[44:47]\n"
"ds_read_b64_tr_b16 a[86:87], v219 offset:54280\n"
"v_fma_mix_f32 v212, v166, v196, v204 op_sel:[0,0,0] op_sel_hi:[1,0,0]\n"
"v_mfma_f32_16x16x32_bf16 v[48:51], a[108:111], v[144:147], v[48:51]\n"
"ds_read_b64_tr_b16 a[88:89], v219 offset:50688\n"
"v_fma_mix_f32 v213, v166, v197, v205 op_sel:[1,0,0] op_sel_hi:[1,0,0]\n"
"v_mfma_f32_16x16x32_bf16 v[52:55], a[108:111], v[148:151], v[52:55]\n"
"ds_read_b64_tr_b16 a[90:91], v219 offset:54784\n"
"v_cvt_pk_bf16_f32 v165, v210, v211\n"
"v_mfma_f32_16x16x32_bf16 v[56:59], a[108:111], v[152:155], v[56:59]\n"
"ds_read_b64_tr_b16 a[92:93], v219 offset:50696\n"
"v_fma_mix_f32 v214, v167, v198, v206 op_sel:[0,0,0] op_sel_hi:[1,0,0]\n"
"v_fma_mix_f32 v215, v167, v199, v207 op_sel:[1,0,0] op_sel_hi:[1,0,0]\n"
"v_cvt_pk_bf16_f32 v166, v212, v213\n"
"v_fma_mix_f32 v208, v168, v192, v200 op_sel:[0,0,0] op_sel_hi:[1,0,0]\n"
"v_mfma_f32_16x16x32_bf16 v[60:63], a[108:111], v[156:159], v[60:63]\n"
"ds_read_b64_tr_b16 a[94:95], v219 offset:54792\n"
"v_fma_mix_f32 v209, v168, v193, v201 op_sel:[1,0,0] op_sel_hi:[1,0,0]\n"
"v_mfma_f32_16x16x32_bf16 v[64:67], a[112:115], v[144:147], v[64:67]\n"
"ds_read_b128 v[230:233], v229 offset:1920\n"
"v_cvt_pk_bf16_f32 v167, v214, v215\n"
"v_mfma_f32_16x16x32_bf16 v[68:71], a[112:115], v[148:151], v[68:71]\n"
"ds_read_b128 v[234:237], v229 offset:1984\n"
"v_fma_mix_f32 v210, v169, v194, v202 op_sel:[0,0,0] op_sel_hi:[1,0,0]\n"
"v_mfma_f32_16x16x32_bf16 v[72:75], a[112:115], v[152:155], v[72:75]\n"
"ds_read_b128 v[238:241], v229 offset:3968\n"
"v_fma_mix_f32 v211, v169, v195, v203 op_sel:[1,0,0] op_sel_hi:[1,0,0]\n"
"v_mfma_f32_16x16x32_bf16 v[76:79], a[112:115], v[156:159], v[76:79]\n"
"ds_read_b128 v[242:245], v229 offset:4032\n"
"v_cvt_pk_bf16_f32 v168, v208, v209\n"
"v_mfma_f32_16x16x32_bf16 v[80:83], a[116:119], v[144:147], v[80:83]\n"
"s_add_u32 s64, s57, 4096\n"
"s_add_u32 m0, s59, 4096\n"
"s_nop 0\n"
"buffer_load_dwordx4 v246, s[36:39], s64 offen offset:0 lds\n"
"v_mfma_f32_16x16x32_bf16 v[84:87], a[116:119], v[148:151], v[84:87]\n"
"v_fma_mix_f32 v212, v170, v196, v204 op_sel:[0,0,0] op_sel_hi:[1,0,0]\n"
"v_fma_mix_f32 v213, v170, v197, v205 op_sel:[1,0,0] op_sel_hi:[1,0,0]\n"
"v_mfma_f32_16x16x32_bf16 v[88:91], a[116:119], v[152:155], v[88:91]\n"
"v_cvt_pk_bf16_f32 v169, v210, v211\n"
"v_fma_mix_f32 v214, v171, v198, v206 op_sel:[0,0,0] op_sel_hi:[1,0,0]\n"
"v_fma_mix_f32 v215, v171, v199, v207 op_sel:[1,0,0] op_sel_hi:[1,0,0]\n"
"v_cvt_pk_bf16_f32 v170, v212, v213\n"
"v_fma_mix_f32 v208, v172, v192, v200 op_sel:[0,0,0] op_sel_hi:[1,0,0]\n"
"v_mfma_f32_16x16x32_bf16 v[92:95], a[116:119], v[156:159], v[92:95]\n"
"s_add_u32 s64, s57, 4096\n"
"s_add_u32 m0, s59, 4096\n"
"s_nop 0\n"
"buffer_load_dwordx4 v247, s[36:39], s64 offen offset:1024 lds\n"
"v_mfma_f32_16x16x32_bf16 v[96:99], a[120:123], v[144:147], v[96:99]\n"
"v_fma_mix_f32 v209, v172, v193, v201 op_sel:[1,0,0] op_sel_hi:[1,0,0]\n"
"v_cvt_pk_bf16_f32 v171, v214, v215\n"
"v_mfma_f32_16x16x32_bf16 v[100:103], a[120:123], v[148:151], v[100:103]\n"
"v_fma_mix_f32 v210, v173, v194, v202 op_sel:[0,0,0] op_sel_hi:[1,0,0]\n"
"v_fma_mix_f32 v211, v173, v195, v203 op_sel:[1,0,0] op_sel_hi:[1,0,0]\n"
"v_mfma_f32_16x16x32_bf16 v[104:107], a[120:123], v[152:155], v[104:107]\n"
"s_add_u32 s64, s57, 4096\n"
"s_add_u32 m0, s59, 4096\n"
"s_nop 0\n"
"buffer_load_dwordx4 v248, s[36:39], s64 offen offset:2048 lds\n"
"v_mfma_f32_16x16x32_bf16 v[108:111], a[120:123], v[156:159], v[108:111]\n"
"v_cvt_pk_bf16_f32 v172, v208, v209\n"
"v_fma_mix_f32 v212, v174, v196, v204 op_sel:[0,0,0] op_sel_hi:[1,0,0]\n"
"v_mfma_f32_16x16x32_bf16 v[112:115], a[124:127], v[144:147], v[112:115]\n"
"v_fma_mix_f32 v213, v174, v197, v205 op_sel:[1,0,0] op_sel_hi:[1,0,0]\n"
"v_cvt_pk_bf16_f32 v173, v210, v211\n"
"v_mfma_f32_16x16x32_bf16 v[116:119], a[124:127], v[148:151], v[116:119]\n"
"s_add_u32 s64, s57, 4096\n"
"s_add_u32 m0, s59, 4096\n"
"s_nop 0\n"
"buffer_load_dwordx4 v249, s[36:39], s64 offen offset:3072 lds\n"
"v_mfma_f32_16x16x32_bf16 v[120:123], a[124:127], v[152:155], v[120:123]\n"
"v_fma_mix_f32 v214, v175, v198, v206 op_sel:[0,0,0] op_sel_hi:[1,0,0]\n"
"v_fma_mix_f32 v215, v175, v199, v207 op_sel:[1,0,0] op_sel_hi:[1,0,0]\n"
"v_mfma_f32_16x16x32_bf16 v[124:127], a[124:127], v[156:159], v[124:127]\n"
"v_cvt_pk_bf16_f32 v174, v212, v213\n"
"v_cvt_pk_bf16_f32 v175, v214, v215\n"
"s_waitcnt vmcnt(8) lgkmcnt(0)\n"
"v_mfma_f32_16x16x32_bf16 v[0:3], a[64:67], v[160:163], v[0:3]\n"
"ds_read_b64_tr_b16 a[96:97], v219 offset:57344\n"
"v_fma_mix_f32 v208, v176, v230, v238 op_sel:[0,0,0] op_sel_hi:[1,0,0]\n"
"v_fma_mix_f32 v209, v176, v231, v239 op_sel:[1,0,0] op_sel_hi:[1,0,0]\n"
"v_fma_mix_f32 v210, v177, v232, v240 op_sel:[0,0,0] op_sel_hi:[1,0,0]\n"
"v_mfma_f32_16x16x32_bf16 v[4:7], a[64:67], v[164:167], v[4:7]\n"
"ds_read_b64_tr_b16 a[98:99], v219 offset:61440\n"
"v_fma_mix_f32 v211, v177, v233, v241 op_sel:[1,0,0] op_sel_hi:[1,0,0]\n"
"v_mfma_f32_16x16x32_bf16 v[8:11], a[64:67], v[168:171], v[8:11]\n"
"ds_read_b64_tr_b16 a[100:101], v219 offset:57352\n"
"v_cvt_pk_bf16_f32 v176, v208, v209\n"
"v_mfma_f32_16x16x32_bf16 v[12:15], a[64:67], v[172:175], v[12:15]\n"
"ds_read_b64_tr_b16 a[102:103], v219 offset:61448\n"
"v_fma_mix_f32 v212, v178, v234, v242 op_sel:[0,0,0] op_sel_hi:[1,0,0]\n"
"v_mfma_f32_16x16x32_bf16 v[16:19], a[68:71], v[160:163], v[16:19]\n"
"ds_read_b64_tr_b16 a[104:105], v219 offset:57856\n"
"v_fma_mix_f32 v213, v178, v235, v243 op_sel:[1,0,0] op_sel_hi:[1,0,0]\n"
"v_mfma_f32_16x16x32_bf16 v[20:23], a[68:71], v[164:167], v[20:23]\n"
"ds_read_b64_tr_b16 a[106:107], v219 offset:61952\n"
"v_cvt_pk_bf16_f32 v177, v210, v211\n"
"v_mfma_f32_16x16x32_bf16 v[24:27], a[68:71], v[168:171], v[24:27]\n"
"ds_read_b64_tr_b16 a[108:109], v219 offset:57864\n"
"v_fma_mix_f32 v214, v179, v236, v244 op_sel:[0,0,0] op_sel_hi:[1,0,0]\n"
"v_mfma_f32_16x16x32_bf16 v[28:31], a[68:71], v[172:175], v[28:31]\n"
"ds_read_b64_tr_b16 a[110:111], v219 offset:61960\n"
"v_fma_mix_f32 v215, v179, v237, v245 op_sel:[1,0,0] op_sel_hi:[1,0,0]\n"
"v_cvt_pk_bf16_f32 v178, v212, v213\n"
"v_fma_mix_f32 v208, v180, v230, v238 op_sel:[0,0,0] op_sel_hi:[1,0,0]\n"
"v_mfma_f32_16x16x32_bf16 v[32:35], a[72:75], v[160:163], v[32:35]\n"
"ds_read_b64_tr_b16 a[112:113], v219 offset:58368\n"
"v_fma_mix_f32 v209, v180, v231, v239 op_sel:[1,0,0] op_sel_hi:[1,0,0]\n"
"v_mfma_f32_16x16x32_bf16 v[36:39], a[72:75], v[164:167], v[36:39]\n"
"ds_read_b64_tr_b16 a[114:115], v219 offset:62464\n"
"v_cvt_pk_bf16_f32 v179, v214, v215\n"
"v_mfma_f32_16x16x32_bf16 v[40:43], a[72:75], v[168:171], v[40:43]\n"
"ds_read_b64_tr_b16 a[116:117], v219 offset:58376\n"
"v_fma_mix_f32 v210, v181, v232, v240 op_sel:[0,0,0] op_sel_hi:[1,0,0]\n"
"v_mfma_f32_16x16x32_bf16 v[44:47], a[72:75], v[172:175], v[44:47]\n"
"ds_read_b64_tr_b16 a[118:119], v219 offset:62472\n"
"v_fma_mix_f32 v211, v181, v233, v241 op_sel:[1,0,0] op_sel_hi:[1,0,0]\n"
"v_mfma_f32_16x16x32_bf16 v[48:51], a[76:79], v[160:163], v[48:51]\n"
"ds_read_b64_tr_b16 a[120:121], v219 offset:58880\n"
"v_cvt_pk_bf16_f32 v180, v208, v209\n"
"v_mfma_f32_16x16x32_bf16 v[52:55], a[76:79], v[164:167], v[52:55]\n"
"ds_read_b64_tr_b16 a[122:123], v219 offset:62976\n"
"v_fma_mix_f32 v212, v182, v234, v242 op_sel:[0,0,0] op_sel_hi:[1,0,0]\n"
"v_mfma_f32_16x16x32_bf16 v[56:59], a[76:79], v[168:171], v[56:59]\n"
"ds_read_b64_tr_b16 a[124:125], v219 offset:58888\n"
"v_fma_mix_f32 v213, v182, v235, v243 op_sel:[1,0,0] op_sel_hi:[1,0,0]\n"
"v_cvt_pk_bf16_f32 v181, v210, v211\n"
"v_fma_mix_f32 v214, v183, v236, v244 op_sel:[0,0,0] op_sel_hi:[1,0,0]\n"
"v_mfma_f32_16x16x32_bf16 v[60:63], a[76:79], v[172:175], v[60:63]\n"
"ds_read_b64_tr_b16 a[126:127], v219 offset:62984\n"
"v_fma_mix_f32 v215, v183, v237, v245 op_sel:[1,0,0] op_sel_hi:[1,0,0]\n"
"v_mfma_f32_16x16x32_bf16 v[64:67], a[80:83], v[160:163], v[64:67]\n"
"s_mov_b32 m0, s60\n"
"s_nop 0\n"
"buffer_load_dwordx4 v250, s[40:43], s58 offen offset:0 lds\n"
"v_mfma_f32_16x16x32_bf16 v[68:71], a[80:83], v[164:167], v[68:71]\n"
"v_cvt_pk_bf16_f32 v182, v212, v213\n"
"v_fma_mix_f32 v208, v184, v230, v238 op_sel:[0,0,0] op_sel_hi:[1,0,0]\n"
"v_mfma_f32_16x16x32_bf16 v[72:75], a[80:83], v[168:171], v[72:75]\n"
"v_fma_mix_f32 v209, v184, v231, v239 op_sel:[1,0,0] op_sel_hi:[1,0,0]\n"
"v_cvt_pk_bf16_f32 v183, v214, v215\n"
"v_mfma_f32_16x16x32_bf16 v[76:79], a[80:83], v[172:175], v[76:79]\n"
"v_fma_mix_f32 v210, v185, v232, v240 op_sel:[0,0,0] op_sel_hi:[1,0,0]\n"
"v_fma_mix_f32 v211, v185, v233, v241 op_sel:[1,0,0] op_sel_hi:[1,0,0]\n"
"v_mfma_f32_16x16x32_bf16 v[80:83], a[84:87], v[160:163], v[80:83]\n"
"s_mov_b32 m0, s60\n"
"s_nop 0\n"
"buffer_load_dwordx4 v251, s[40:43], s58 offen offset:1024 lds\n"
"v_mfma_f32_16x16x32_bf16 v[84:87], a[84:87], v[164:167], v[84:87]\n"
"v_cvt_pk_bf16_f32 v184, v208, v209\n"
"v_fma_mix_f32 v212, v186, v234, v242 op_sel:[0,0,0] op_sel_hi:[1,0,0]\n"
"v_mfma_f32_16x16x32_bf16 v[88:91], a[84:87], v[168:171], v[88:91]\n"
"v_fma_mix_f32 v213, v186, v235, v243 op_sel:[1,0,0] op_sel_hi:[1,0,0]\n"
"v_cvt_pk_bf16_f32 v185, v210, v211\n"
"v_mfma_f32_16x16x32_bf16 v[92:95], a[84:87], v[172:175], v[92:95]\n"
"v_fma_mix_f32 v214, v187, v236, v244 op_sel:[0,0,0] op_sel_hi:[1,0,0]\n"
"v_fma_mix_f32 v215, v187, v237, v245 op_sel:[1,0,0] op_sel_hi:[1,0,0]\n"
"v_cvt_pk_bf16_f32 v186, v212, v213\n"
"v_fma_mix_f32 v208, v188, v230, v238 op_sel:[0,0,0] op_sel_hi:[1,0,0]\n"
"v_mfma_f32_16x16x32_bf16 v[96:99], a[88:91], v[160:163], v[96:99]\n"
"s_mov_b32 m0, s60\n"
"s_nop 0\n"
"buffer_load_dwordx4 v250, s[40:43], s58 offen offset:2048 lds\n"
"v_mfma_f32_16x16x32_bf16 v[100:103], a[88:91], v[164:167], v[100:103]\n"
"v_fma_mix_f32 v209, v188, v231, v239 op_sel:[1,0,0] op_sel_hi:[1,0,0]\n"
"v_cvt_pk_bf16_f32 v187, v214, v215\n"
"v_mfma_f32_16x16x32_bf16 v[104:107], a[88:91], v[168:171], v[104:107]\n"
"v_fma_mix_f32 v210, v189, v232, v240 op_sel:[0,0,0] op_sel_hi:[1,0,0]\n"
"v_fma_mix_f32 v211, v189, v233, v241 op_sel:[1,0,0] op_sel_hi:[1,0,0]\n"
"v_mfma_f32_16x16x32_bf16 v[108:111], a[88:91], v[172:175], v[108:111]\n"
"v_cvt_pk_bf16_f32 v188, v208, v209\n"
"v_fma_mix_f32 v212, v190, v234, v242 op_sel:[0,0,0] op_sel_hi:[1,0,0]\n"
"v_mfma_f32_16x16x32_bf16 v[112:115], a[92:95], v[160:163], v[112:115]\n"
"s_mov_b32 m0, s60\n"
"s_nop 0\n"
"buffer_load_dwordx4 v251, s[40:43], s58 offen offset:3072 lds\n"
"v_mfma_f32_16x16x32_bf16 v[116:119], a[92:95], v[164:167], v[116:119]\n"
"v_fma_mix_f32 v213, v190, v235, v243 op_sel:[1,0,0] op_sel_hi:[1,0,0]\n"
"v_cvt_pk_bf16_f32 v189, v210, v211\n"
"v_mfma_f32_16x16x32_bf16 v[120:123], a[92:95], v[168:171], v[120:123]\n"
"v_fma_mix_f32 v214, v191, v236, v244 op_sel:[0,0,0] op_sel_hi:[1,0,0]\n"
"v_fma_mix_f32 v215, v191, v237, v245 op_sel:[1,0,0] op_sel_hi:[1,0,0]\n"
"v_mfma_f32_16x16x32_bf16 v[124:127], a[92:95], v[172:175], v[124:127]\n"
"v_cvt_pk_bf16_f32 v190, v212, v213\n"
"v_cvt_pk_bf16_f32 v191, v214, v215\n"
"s_waitcnt vmcnt(63) lgkmcnt(0)\n"
"v_mfma_f32_16x16x32_bf16 v[0:3], a[96:99], v[176:179], v[0:3]\n"
"s_add_u32 s65, s58, 4096\n"
"s_add_u32 m0, s60, 4096\n"
"s_nop 0\n"
"buffer_load_dwordx4 v250, s[40:43], s65 offen offset:0 lds\n"
"v_mfma_f32_16x16x32_bf16 v[4:7], a[96:99], v[180:183], v[4:7]\n"
"v_mfma_f32_16x16x32_bf16 v[8:11], a[96:99], v[184:187], v[8:11]\n"
"v_mfma_f32_16x16x32_bf16 v[12:15], a[96:99], v[188:191], v[12:15]\n"
"v_mfma_f32_16x16x32_bf16 v[16:19], a[100:103], v[176:179], v[16:19]\n"
"v_mfma_f32_16x16x32_bf16 v[20:23], a[100:103], v[180:183], v[20:23]\n"
"v_mfma_f32_16x16x32_bf16 v[24:27], a[100:103], v[184:187], v[24:27]\n"
"v_mfma_f32_16x16x32_bf16 v[28:31], a[100:103], v[188:191], v[28:31]\n"
"v_mfma_f32_16x16x32_bf16 v[32:35], a[104:107], v[176:179], v[32:35]\n"
"s_add_u32 s65, s58, 4096\n"
"s_add_u32 m0, s60, 4096\n"
"s_nop 0\n"
"buffer_load_dwordx4 v251, s[40:43], s65 offen offset:1024 lds\n"
"v_mfma_f32_16x16x32_bf16 v[36:39], a[104:107], v[180:183], v[36:39]\n"
"v_mfma_f32_16x16x32_bf16 v[40:43], a[104:107], v[184:187], v[40:43]\n"
"v_mfma_f32_16x16x32_bf16 v[44:47], a[104:107], v[188:191], v[44:47]\n"
"v_mfma_f32_16x16x32_bf16 v[48:51], a[108:111], v[176:179], v[48:51]\n"
"v_mfma_f32_16x16x32_bf16 v[52:55], a[108:111], v[180:183], v[52:55]\n"
"v_mfma_f32_16x16x32_bf16 v[56:59], a[108:111], v[184:187], v[56:59]\n"
"v_mfma_f32_16x16x32_bf16 v[60:63], a[108:111], v[188:191], v[60:63]\n"
"v_mfma_f32_16x16x32_bf16 v[64:67], a[112:115], v[176:179], v[64:67]\n"
"s_add_u32 s65, s58, 4096\n"
"s_add_u32 m0, s60, 4096\n"
"s_nop 0\n"
"buffer_load_dwordx4 v250, s[40:43], s65 offen offset:2048 lds\n"
"v_mfma_f32_16x16x32_bf16 v[68:71], a[112:115], v[180:183], v[68:71]\n"
"v_mfma_f32_16x16x32_bf16 v[72:75], a[112:115], v[184:187], v[72:75]\n"
"v_mfma_f32_16x16x32_bf16 v[76:79], a[112:115], v[188:191], v[76:79]\n"
"v_mfma_f32_16x16x32_bf16 v[80:83], a[116:119], v[176:179], v[80:83]\n"
"v_mfma_f32_16x16x32_bf16 v[84:87], a[116:119], v[180:183], v[84:87]\n"
"v_mfma_f32_16x16x32_bf16 v[88:91], a[116:119], v[184:187], v[88:91]\n"
"v_mfma_f32_16x16x32_bf16 v[92:95], a[116:119], v[188:191], v[92:95]\n"
"v_mfma_f32_16x16x32_bf16 v[96:99], a[120:123], v[176:179], v[96:99]\n"
"s_add_u32 s65, s58, 4096\n"
"s_add_u32 m0, s60, 4096\n"
"s_nop 0\n"
"buffer_load_dwordx4 v251, s[40:43], s65 offen offset:3072 lds\n"
"v_mfma_f32_16x16x32_bf16 v[100:103], a[120:123], v[180:183], v[100:103]\n"
"v_mfma_f32_16x16x32_bf16 v[104:107], a[120:123], v[184:187], v[104:107]\n"
"v_mfma_f32_16x16x32_bf16 v[108:111], a[120:123], v[188:191], v[108:111]\n"
"v_mfma_f32_16x16x32_bf16 v[112:115], a[124:127], v[176:179], v[112:115]\n"
"v_mfma_f32_16x16x32_bf16 v[116:119], a[124:127], v[180:183], v[116:119]\n"
"v_mfma_f32_16x16x32_bf16 v[120:123], a[124:127], v[184:187], v[120:123]\n"
"v_mfma_f32_16x16x32_bf16 v[124:127], a[124:127], v[188:191], v[124:127]\n"
"v_subrev_u32 v240, 131072, v220\n"
"v_subrev_u32 v241, 131072, v221\n"
"v_subrev_u32 v242, 131072, v222\n"
"v_subrev_u32 v243, 131072, v223\n"
"v_lshrrev_b32 v227, 1, v225\n"
"v_lshlrev_b32 v227, 11, v227\n"
"v_and_b32 v228, 1, v225\n"
"v_lshl_add_u32 v227, v228, 8, v227\n"
"v_bfe_u32 v253, v224, 2, 2\n"
"v_lshl_add_u32 v227, v253, 6, v227\n"
"v_and_b32 v253, 3, v224\n"
"v_lshl_add_u32 v254, v253, 3, v227\n"
"v_lshl_add_u32 v244, v228, 5, v254\n"
"v_xor_b32 v255, 1, v228\n"
"v_lshl_add_u32 v245, v255, 5, v254\n"
"s_nop 7\n"
"v_cvt_pk_bf16_f32 v253, v0, v1\n"
"v_accvgpr_write_b32 a0, v253\n"
"v_cvt_pk_bf16_f32 v254, v2, v3\n"
"v_accvgpr_write_b32 a1, v254\n"
"v_cvt_pk_bf16_f32 v253, v16, v17\n"
"v_accvgpr_write_b32 a2, v253\n"
"v_cvt_pk_bf16_f32 v254, v18, v19\n"
"v_accvgpr_write_b32 a3, v254\n"
"v_cvt_pk_bf16_f32 v253, v32, v33\n"
"v_accvgpr_write_b32 a4, v253\n"
"v_cvt_pk_bf16_f32 v254, v34, v35\n"
"v_accvgpr_write_b32 a5, v254\n"
"v_cvt_pk_bf16_f32 v253, v48, v49\n"
"v_accvgpr_write_b32 a6, v253\n"
"v_cvt_pk_bf16_f32 v254, v50, v51\n"
"v_accvgpr_write_b32 a7, v254\n"
"v_cvt_pk_bf16_f32 v253, v64, v65\n"
"v_accvgpr_write_b32 a8, v253\n"
"v_cvt_pk_bf16_f32 v254, v66, v67\n"
"v_accvgpr_write_b32 a9, v254\n"
"v_cvt_pk_bf16_f32 v253, v80, v81\n"
"v_accvgpr_write_b32 a10, v253\n"
"v_cvt_pk_bf16_f32 v254, v82, v83\n"
"v_accvgpr_write_b32 a11, v254\n"
"v_cvt_pk_bf16_f32 v253, v96, v97\n"
"v_accvgpr_write_b32 a12, v253\n"
"v_cvt_pk_bf16_f32 v254, v98, v99\n"
"v_accvgpr_write_b32 a13, v254\n"
"v_cvt_pk_bf16_f32 v253, v112, v113\n"
"v_accvgpr_write_b32 a14, v253\n"
"v_cvt_pk_bf16_f32 v254, v114, v115\n"
"v_accvgpr_write_b32 a15, v254\n"
"v_cvt_pk_bf16_f32 v253, v4, v5\n"
"v_accvgpr_write_b32 a16, v253\n"
"v_cvt_pk_bf16_f32 v254, v6, v7\n"
"v_accvgpr_write_b32 a17, v254\n"
"v_cvt_pk_bf16_f32 v253, v20, v21\n"
"v_accvgpr_write_b32 a18, v253\n"
"v_cvt_pk_bf16_f32 v254, v22, v23\n"
"v_accvgpr_write_b32 a19, v254\n"
"v_cvt_pk_bf16_f32 v253, v36, v37\n"
"v_accvgpr_write_b32 a20, v253\n"
"v_cvt_pk_bf16_f32 v254, v38, v39\n"
"v_accvgpr_write_b32 a21, v254\n"
"v_cvt_pk_bf16_f32 v253, v52, v53\n"
"v_accvgpr_write_b32 a22, v253\n"
"v_cvt_pk_bf16_f32 v254, v54, v55\n"
"v_accvgpr_write_b32 a23, v254\n"
"v_cvt_pk_bf16_f32 v253, v68, v69\n"
"v_accvgpr_write_b32 a24, v253\n"
"v_cvt_pk_bf16_f32 v254, v70, v71\n"
"v_accvgpr_write_b32 a25, v254\n"
"v_cvt_pk_bf16_f32 v253, v84, v85\n"
"v_accvgpr_write_b32 a26, v253\n"
"v_cvt_pk_bf16_f32 v254, v86, v87\n"
"v_accvgpr_write_b32 a27, v254\n"
"v_cvt_pk_bf16_f32 v253, v100, v101\n"
"v_accvgpr_write_b32 a28, v253\n"
"v_cvt_pk_bf16_f32 v254, v102, v103\n"
"v_accvgpr_write_b32 a29, v254\n"
"v_cvt_pk_bf16_f32 v253, v116, v117\n"
"v_accvgpr_write_b32 a30, v253\n"
"v_cvt_pk_bf16_f32 v254, v118, v119\n"
"v_accvgpr_write_b32 a31, v254\n"
"v_cvt_pk_bf16_f32 v253, v8, v9\n"
"v_accvgpr_write_b32 a32, v253\n"
"v_cvt_pk_bf16_f32 v254, v10, v11\n"
"v_accvgpr_write_b32 a33, v254\n"
"v_cvt_pk_bf16_f32 v253, v24, v25\n"
"v_accvgpr_write_b32 a34, v253\n"
"v_cvt_pk_bf16_f32 v254, v26, v27\n"
"v_accvgpr_write_b32 a35, v254\n"
"v_cvt_pk_bf16_f32 v253, v40, v41\n"
"v_accvgpr_write_b32 a36, v253\n"
"v_cvt_pk_bf16_f32 v254, v42, v43\n"
"v_accvgpr_write_b32 a37, v254\n"
"v_cvt_pk_bf16_f32 v253, v56, v57\n"
"v_accvgpr_write_b32 a38, v253\n"
"v_cvt_pk_bf16_f32 v254, v58, v59\n"
"v_accvgpr_write_b32 a39, v254\n"
"v_cvt_pk_bf16_f32 v253, v72, v73\n"
"v_accvgpr_write_b32 a40, v253\n"
"v_cvt_pk_bf16_f32 v254, v74, v75\n"
"v_accvgpr_write_b32 a41, v254\n"
"v_cvt_pk_bf16_f32 v253, v88, v89\n"
"v_accvgpr_write_b32 a42, v253\n"
"v_cvt_pk_bf16_f32 v254, v90, v91\n"
"v_accvgpr_write_b32 a43, v254\n"
"v_cvt_pk_bf16_f32 v253, v104, v105\n"
"v_accvgpr_write_b32 a44, v253\n"
"v_cvt_pk_bf16_f32 v254, v106, v107\n"
"v_accvgpr_write_b32 a45, v254\n"
"v_cvt_pk_bf16_f32 v253, v120, v121\n"
"v_accvgpr_write_b32 a46, v253\n"
"v_cvt_pk_bf16_f32 v254, v122, v123\n"
"v_accvgpr_write_b32 a47, v254\n"
"v_cvt_pk_bf16_f32 v253, v12, v13\n"
"v_accvgpr_write_b32 a48, v253\n"
"v_cvt_pk_bf16_f32 v254, v14, v15\n"
"v_accvgpr_write_b32 a49, v254\n"
"v_cvt_pk_bf16_f32 v253, v28, v29\n"
"v_accvgpr_write_b32 a50, v253\n"
"v_cvt_pk_bf16_f32 v254, v30, v31\n"
"v_accvgpr_write_b32 a51, v254\n"
"v_cvt_pk_bf16_f32 v253, v44, v45\n"
"v_accvgpr_write_b32 a52, v253\n"
"v_cvt_pk_bf16_f32 v254, v46, v47\n"
"v_accvgpr_write_b32 a53, v254\n"
"v_cvt_pk_bf16_f32 v253, v60, v61\n"
"v_accvgpr_write_b32 a54, v253\n"
"v_cvt_pk_bf16_f32 v254, v62, v63\n"
"v_accvgpr_write_b32 a55, v254\n"
"v_cvt_pk_bf16_f32 v253, v76, v77\n"
"v_accvgpr_write_b32 a56, v253\n"
"v_cvt_pk_bf16_f32 v254, v78, v79\n"
"v_accvgpr_write_b32 a57, v254\n"
"v_cvt_pk_bf16_f32 v253, v92, v93\n"
"v_accvgpr_write_b32 a58, v253\n"
"v_cvt_pk_bf16_f32 v254, v94, v95\n"
"v_accvgpr_write_b32 a59, v254\n"
"v_cvt_pk_bf16_f32 v253, v108, v109\n"
"v_accvgpr_write_b32 a60, v253\n"
"v_cvt_pk_bf16_f32 v254, v110, v111\n"
"v_accvgpr_write_b32 a61, v254\n"
"v_cvt_pk_bf16_f32 v253, v124, v125\n"
"v_accvgpr_write_b32 a62, v253\n"
"v_cvt_pk_bf16_f32 v254, v126, v127\n"
"v_accvgpr_write_b32 a63, v254\n"
"v_mov_b32 v253, 0x3f803f80\n"
"v_accvgpr_write_b32 a144, v253\n"
"v_accvgpr_write_b32 a145, v253\n"
"v_accvgpr_write_b32 a146, v253\n"
"v_accvgpr_write_b32 a147, v253\n"
"v_xor_b32 v205, 16, v224\n"
"v_lshlrev_b32 v205, 2, v205\n"
"v_xor_b32 v206, 32, v224\n"
"v_lshlrev_b32 v206, 2, v206\n"
"s_waitcnt vmcnt(16)\n"
"s_barrier\n"
"ds_read_b128 a[64:67], v220 offset:0\n"
"ds_read_b128 a[68:71], v221 offset:0\n"
"ds_read_b128 a[72:75], v222 offset:0\n"
"ds_read_b128 a[76:79], v223 offset:0\n"
"ds_read_b128 a[80:83], v220 offset:4096\n"
"ds_read_b128 a[84:87], v221 offset:4096\n"
"ds_read_b128 a[88:91], v222 offset:4096\n"
"ds_read_b128 a[92:95], v223 offset:4096\n"
"ds_read_b128 a[96:99], v220 offset:8192\n"
"ds_read_b128 a[100:103], v221 offset:8192\n"
"ds_read_b128 a[104:107], v222 offset:8192\n"
"ds_read_b128 a[108:111], v223 offset:8192\n"
"ds_read_b128 a[112:115], v220 offset:12288\n"
"ds_read_b128 a[116:119], v221 offset:12288\n"
"ds_read_b128 a[120:123], v222 offset:12288\n"
"ds_read_b128 a[124:127], v223 offset:12288\n"
"s_waitcnt lgkmcnt(8)\n"
"v_mfma_f32_16x16x32_bf16 v[128:131], a[64:67], a[0:3], 0\n"
"v_mfma_f32_16x16x32_bf16 v[132:135], a[64:67], a[16:19], 0\n"
"v_mfma_f32_16x16x32_bf16 v[136:139], a[64:67], a[32:35], 0\n"
"v_mfma_f32_16x16x32_bf16 v[140:143], a[64:67], a[48:51], 0\n"
"v_mfma_f32_16x16x32_bf16 v[128:131], a[68:71], a[4:7], v[128:131]\n"
"v_mfma_f32_16x16x32_bf16 v[132:135], a[68:71], a[20:23], v[132:135]\n"
"v_mfma_f32_16x16x32_bf16 v[136:139], a[68:71], a[36:39], v[136:139]\n"
"v_mfma_f32_16x16x32_bf16 v[140:143], a[68:71], a[52:55], v[140:143]\n"
"v_mfma_f32_16x16x32_bf16 v[128:131], a[72:75], a[8:11], v[128:131]\n"
"v_mfma_f32_16x16x32_bf16 v[132:135], a[72:75], a[24:27], v[132:135]\n"
"v_mfma_f32_16x16x32_bf16 v[136:139], a[72:75], a[40:43], v[136:139]\n"
"v_mfma_f32_16x16x32_bf16 v[140:143], a[72:75], a[56:59], v[140:143]\n"
"v_mfma_f32_16x16x32_bf16 v[128:131], a[76:79], a[12:15], v[128:131]\n"
"v_mfma_f32_16x16x32_bf16 v[132:135], a[76:79], a[28:31], v[132:135]\n"
"v_mfma_f32_16x16x32_bf16 v[136:139], a[76:79], a[44:47], v[136:139]\n"
"v_mfma_f32_16x16x32_bf16 v[140:143], a[76:79], a[60:63], v[140:143]\n"
"v_mfma_f32_16x16x32_bf16 v[144:147], a[80:83], a[0:3], 0\n"
"v_mfma_f32_16x16x32_bf16 v[148:151], a[80:83], a[16:19], 0\n"
"v_mfma_f32_16x16x32_bf16 v[152:155], a[80:83], a[32:35], 0\n"
"v_mfma_f32_16x16x32_bf16 v[156:159], a[80:83], a[48:51], 0\n"
"v_mfma_f32_16x16x32_bf16 v[144:147], a[84:87], a[4:7], v[144:147]\n"
"v_mfma_f32_16x16x32_bf16 v[148:151], a[84:87], a[20:23], v[148:151]\n"
"v_mfma_f32_16x16x32_bf16 v[152:155], a[84:87], a[36:39], v[152:155]\n"
"v_mfma_f32_16x16x32_bf16 v[156:159], a[84:87], a[52:55], v[156:159]\n"
"v_mfma_f32_16x16x32_bf16 v[144:147], a[88:91], a[8:11], v[144:147]\n"
"v_mfma_f32_16x16x32_bf16 v[148:151], a[88:91], a[24:27], v[148:151]\n"
"v_mfma_f32_16x16x32_bf16 v[152:155], a[88:91], a[40:43], v[152:155]\n"
"v_mfma_f32_16x16x32_bf16 v[156:159], a[88:91], a[56:59], v[156:159]\n"
"v_mfma_f32_16x16x32_bf16 v[144:147], a[92:95], a[12:15], v[144:147]\n"
"v_mfma_f32_16x16x32_bf16 v[148:151], a[92:95], a[28:31], v[148:151]\n"
"v_mfma_f32_16x16x32_bf16 v[152:155], a[92:95], a[44:47], v[152:155]\n"
"v_mfma_f32_16x16x32_bf16 v[156:159], a[92:95], a[60:63], v[156:159]\n"
"s_waitcnt lgkmcnt(0)\n"
"v_mfma_f32_16x16x32_bf16 v[160:163], a[96:99], a[0:3], 0\n"
"v_mfma_f32_16x16x32_bf16 v[164:167], a[96:99], a[16:19], 0\n"
"v_mfma_f32_16x16x32_bf16 v[168:171], a[96:99], a[32:35], 0\n"
"v_mfma_f32_16x16x32_bf16 v[172:175], a[96:99], a[48:51], 0\n"
"v_mfma_f32_16x16x32_bf16 v[160:163], a[100:103], a[4:7], v[160:163]\n"
"v_mfma_f32_16x16x32_bf16 v[164:167], a[100:103], a[20:23], v[164:167]\n"
"v_mfma_f32_16x16x32_bf16 v[168:171], a[100:103], a[36:39], v[168:171]\n"
"v_mfma_f32_16x16x32_bf16 v[172:175], a[100:103], a[52:55], v[172:175]\n"
"v_mfma_f32_16x16x32_bf16 v[160:163], a[104:107], a[8:11], v[160:163]\n"
"v_mfma_f32_16x16x32_bf16 v[164:167], a[104:107], a[24:27], v[164:167]\n"
"v_mfma_f32_16x16x32_bf16 v[168:171], a[104:107], a[40:43], v[168:171]\n"
"v_mfma_f32_16x16x32_bf16 v[172:175], a[104:107], a[56:59], v[172:175]\n"
"v_mfma_f32_16x16x32_bf16 v[160:163], a[108:111], a[12:15], v[160:163]\n"
"v_mfma_f32_16x16x32_bf16 v[164:167], a[108:111], a[28:31], v[164:167]\n"
"v_mfma_f32_16x16x32_bf16 v[168:171], a[108:111], a[44:47], v[168:171]\n"
"v_mfma_f32_16x16x32_bf16 v[172:175], a[108:111], a[60:63], v[172:175]\n"
"v_mfma_f32_16x16x32_bf16 v[176:179], a[112:115], a[0:3], 0\n"
"v_mfma_f32_16x16x32_bf16 v[180:183], a[112:115], a[16:19], 0\n"
"v_mfma_f32_16x16x32_bf16 v[184:187], a[112:115], a[32:35], 0\n"
"v_mfma_f32_16x16x32_bf16 v[188:191], a[112:115], a[48:51], 0\n"
"v_mfma_f32_16x16x32_bf16 v[176:179], a[116:119], a[4:7], v[176:179]\n"
"v_mfma_f32_16x16x32_bf16 v[180:183], a[116:119], a[20:23], v[180:183]\n"
"v_mfma_f32_16x16x32_bf16 v[184:187], a[116:119], a[36:39], v[184:187]\n"
"v_mfma_f32_16x16x32_bf16 v[188:191], a[116:119], a[52:55], v[188:191]\n"
"v_mfma_f32_16x16x32_bf16 v[176:179], a[120:123], a[8:11], v[176:179]\n"
"v_mfma_f32_16x16x32_bf16 v[180:183], a[120:123], a[24:27], v[180:183]\n"
"v_mfma_f32_16x16x32_bf16 v[184:187], a[120:123], a[40:43], v[184:187]\n"
"v_mfma_f32_16x16x32_bf16 v[188:191], a[120:123], a[56:59], v[188:191]\n"
"v_mfma_f32_16x16x32_bf16 v[176:179], a[124:127], a[12:15], v[176:179]\n"
"v_mfma_f32_16x16x32_bf16 v[180:183], a[124:127], a[28:31], v[180:183]\n"
"v_mfma_f32_16x16x32_bf16 v[184:187], a[124:127], a[44:47], v[184:187]\n"
"v_mfma_f32_16x16x32_bf16 v[188:191], a[124:127], a[60:63], v[188:191]\n"
"s_nop 7\n"
"v_max3_f32 v224, v128, v129, v130\n"
"v_max3_f32 v224, v224, v131, v144\n"
"v_max3_f32 v224, v224, v145, v146\n"
"v_max3_f32 v224, v224, v147, v160\n"
"v_max3_f32 v224, v224, v161, v162\n"
"v_max3_f32 v224, v224, v163, v176\n"
"v_max3_f32 v224, v224, v177, v178\n"
"v_max_f32 v224, v224, v179\n"
"v_max3_f32 v228, v132, v133, v134\n"
"v_max3_f32 v228, v228, v135, v148\n"
"v_max3_f32 v228, v228, v149, v150\n"
"v_max3_f32 v228, v228, v151, v164\n"
"v_max3_f32 v228, v228, v165, v166\n"
"v_max3_f32 v228, v228, v167, v180\n"
"v_max3_f32 v228, v228, v181, v182\n"
"v_max_f32 v228, v228, v183\n"
"v_max3_f32 v232, v136, v137, v138\n"
"v_max3_f32 v232, v232, v139, v152\n"
"v_max3_f32 v232, v232, v153, v154\n"
"v_max3_f32 v232, v232, v155, v168\n"
"v_max3_f32 v232, v232, v169, v170\n"
"v_max3_f32 v232, v232, v171, v184\n"
"v_max3_f32 v232, v232, v185, v186\n"
"v_max_f32 v232, v232, v187\n"
"v_max3_f32 v236, v140, v141, v142\n"
"v_max3_f32 v236, v236, v143, v156\n"
"v_max3_f32 v236, v236, v157, v158\n"
"v_max3_f32 v236, v236, v159, v172\n"
"v_max3_f32 v236, v236, v173, v174\n"
"v_max3_f32 v236, v236, v175, v188\n"
"v_max3_f32 v236, v236, v189, v190\n"
"v_max_f32 v236, v236, v191\n"
"ds_bpermute_b32 v225, v205, v224\n"
"ds_bpermute_b32 v229, v205, v228\n"
"ds_bpermute_b32 v233, v205, v232\n"
"ds_bpermute_b32 v237, v205, v236\n"
"s_waitcnt lgkmcnt(0)\n"
"v_max_f32 v224, v224, v225\n"
"v_max_f32 v228, v228, v229\n"
"v_max_f32 v232, v232, v233\n"
"v_max_f32 v236, v236, v237\n"
"ds_bpermute_b32 v225, v206, v224\n"
"ds_bpermute_b32 v229, v206, v228\n"
"ds_bpermute_b32 v233, v206, v232\n"
"ds_bpermute_b32 v237, v206, v236\n"
"s_waitcnt lgkmcnt(0)\n"
"v_max_f32 v224, v224, v225\n"
"v_max_f32 v228, v228, v229\n"
"v_max_f32 v232, v232, v233\n"
"v_max_f32 v236, v236, v237\n"
"v_mov_b32 v253, 0xc2c80000\n"
"v_sub_f32 v224, v253, v224\n"
"v_sub_f32 v228, v253, v228\n"
"v_sub_f32 v232, v253, v232\n"
"v_sub_f32 v236, v253, v236\n"
"v_mov_b32 v225, v224\n"
"v_mov_b32 v226, v224\n"
"v_mov_b32 v227, v224\n"
"v_mov_b32 v229, v228\n"
"v_mov_b32 v230, v228\n"
"v_mov_b32 v231, v228\n"
"v_mov_b32 v233, v232\n"
"v_mov_b32 v234, v232\n"
"v_mov_b32 v235, v232\n"
"v_mov_b32 v237, v236\n"
"v_mov_b32 v238, v236\n"
"v_mov_b32 v239, v236\n"
"v_add_f32 v128, v128, v224\n"
"v_add_f32 v129, v129, v224\n"
"v_add_f32 v130, v130, v224\n"
"v_add_f32 v131, v131, v224\n"
"v_add_f32 v132, v132, v228\n"
"v_add_f32 v133, v133, v228\n"
"v_add_f32 v134, v134, v228\n"
"v_add_f32 v135, v135, v228\n"
"v_add_f32 v136, v136, v232\n"
"v_add_f32 v137, v137, v232\n"
"v_add_f32 v138, v138, v232\n"
"v_add_f32 v139, v139, v232\n"
"v_add_f32 v140, v140, v236\n"
"v_add_f32 v141, v141, v236\n"
"v_add_f32 v142, v142, v236\n"
"v_add_f32 v143, v143, v236\n"
"v_add_f32 v144, v144, v224\n"
"v_add_f32 v145, v145, v224\n"
"v_add_f32 v146, v146, v224\n"
"v_add_f32 v147, v147, v224\n"
"v_add_f32 v148, v148, v228\n"
"v_add_f32 v149, v149, v228\n"
"v_add_f32 v150, v150, v228\n"
"v_add_f32 v151, v151, v228\n"
"v_add_f32 v152, v152, v232\n"
"v_add_f32 v153, v153, v232\n"
"v_add_f32 v154, v154, v232\n"
"v_add_f32 v155, v155, v232\n"
"v_add_f32 v156, v156, v236\n"
"v_add_f32 v157, v157, v236\n"
"v_add_f32 v158, v158, v236\n"
"v_add_f32 v159, v159, v236\n"
"v_add_f32 v160, v160, v224\n"
"v_add_f32 v161, v161, v224\n"
"v_add_f32 v162, v162, v224\n"
"v_add_f32 v163, v163, v224\n"
"v_add_f32 v164, v164, v228\n"
"v_add_f32 v165, v165, v228\n"
"v_add_f32 v166, v166, v228\n"
"v_add_f32 v167, v167, v228\n"
"v_add_f32 v168, v168, v232\n"
"v_add_f32 v169, v169, v232\n"
"v_add_f32 v170, v170, v232\n"
"v_add_f32 v171, v171, v232\n"
"v_add_f32 v172, v172, v236\n"
"v_add_f32 v173, v173, v236\n"
"v_add_f32 v174, v174, v236\n"
"v_add_f32 v175, v175, v236\n"
"v_add_f32 v176, v176, v224\n"
"v_add_f32 v177, v177, v224\n"
"v_add_f32 v178, v178, v224\n"
"v_add_f32 v179, v179, v224\n"
"v_add_f32 v180, v180, v228\n"
"v_add_f32 v181, v181, v228\n"
"v_add_f32 v182, v182, v228\n"
"v_add_f32 v183, v183, v228\n"
"v_add_f32 v184, v184, v232\n"
"v_add_f32 v185, v185, v232\n"
"v_add_f32 v186, v186, v232\n"
"v_add_f32 v187, v187, v232\n"
"v_add_f32 v188, v188, v236\n"
"v_add_f32 v189, v189, v236\n"
"v_add_f32 v190, v190, v236\n"
"v_add_f32 v191, v191, v236\n"
"s_waitcnt vmcnt(0) lgkmcnt(0)\n"
"s_barrier\n"
"s_xor_b32 s59, s59, 0x10000\n"
"s_xor_b32 s60, s60, 0x10000\n"
"s_add_u32 s57, s57, 32768\n"
"s_add_u32 s58, s58, 32768\n"
"s_mov_b32 m0, s59\n"
"s_nop 0\n"
"buffer_load_dwordx4 v246, s[36:39], s57 offen offset:0 lds\n"
"buffer_load_dwordx4 v247, s[36:39], s57 offen offset:1024 lds\n"
"buffer_load_dwordx4 v248, s[36:39], s57 offen offset:2048 lds\n"
"buffer_load_dwordx4 v249, s[36:39], s57 offen offset:3072 lds\n"
"s_add_u32 s64, s57, 4096\n"
"s_add_u32 m0, s59, 4096\n"
"s_nop 0\n"
"buffer_load_dwordx4 v246, s[36:39], s64 offen offset:0 lds\n"
"buffer_load_dwordx4 v247, s[36:39], s64 offen offset:1024 lds\n"
"buffer_load_dwordx4 v248, s[36:39], s64 offen offset:2048 lds\n"
"buffer_load_dwordx4 v249, s[36:39], s64 offen offset:3072 lds\n"
"ds_read_b128 a[64:67], v240 offset:0\n"
"ds_read_b128 a[68:71], v241 offset:0\n"
"ds_read_b128 a[72:75], v242 offset:0\n"
"ds_read_b128 a[76:79], v243 offset:0\n"
"ds_read_b128 a[80:83], v240 offset:4096\n"
"ds_read_b128 a[84:87], v241 offset:4096\n"
"ds_read_b128 a[88:91], v242 offset:4096\n"
"ds_read_b128 a[92:95], v243 offset:4096\n"
"ds_read_b64_tr_b16 a[96:97], v244 offset:32768\n"
"ds_read_b64_tr_b16 a[98:99], v244 offset:36864\n"
"ds_read_b64_tr_b16 a[100:101], v245 offset:32768\n"
"ds_read_b64_tr_b16 a[102:103], v245 offset:36864\n"
"ds_read_b64_tr_b16 a[104:105], v244 offset:33280\n"
"ds_read_b64_tr_b16 a[106:107], v244 offset:37376\n"
"ds_read_b64_tr_b16 a[108:109], v245 offset:33280\n"
"ds_read_b64_tr_b16 a[110:111], v245 offset:37376\n"
"ds_read_b64_tr_b16 a[112:113], v244 offset:33792\n"
"ds_read_b64_tr_b16 a[114:115], v244 offset:37888\n"
"ds_read_b64_tr_b16 a[116:117], v245 offset:33792\n"
"ds_read_b64_tr_b16 a[118:119], v245 offset:37888\n"
"ds_read_b64_tr_b16 a[120:121], v244 offset:34304\n"
"ds_read_b64_tr_b16 a[122:123], v244 offset:38400\n"
"ds_read_b64_tr_b16 a[124:125], v245 offset:34304\n"
"ds_read_b64_tr_b16 a[126:127], v245 offset:38400\n"
"v_exp_f32 v128, v128\n"
"v_exp_f32 v129, v129\n"
"v_exp_f32 v130, v130\n"
"v_exp_f32 v131, v131\n"
"v_exp_f32 v144, v144\n"
"v_exp_f32 v145, v145\n"
"v_exp_f32 v146, v146\n"
"v_exp_f32 v147, v147\n"
"v_cvt_pk_bf16_f32 v192, v128, v129\n"
"v_cvt_pk_bf16_f32 v193, v130, v131\n"
"v_cvt_pk_bf16_f32 v194, v144, v145\n"
"v_cvt_pk_bf16_f32 v195, v146, v147\n"
"v_exp_f32 v132, v132\n"
"v_exp_f32 v133, v133\n"
"v_exp_f32 v134, v134\n"
"v_exp_f32 v135, v135\n"
"v_exp_f32 v148, v148\n"
"v_exp_f32 v149, v149\n"
"v_exp_f32 v150, v150\n"
"v_exp_f32 v151, v151\n"
"v_cvt_pk_bf16_f32 v196, v132, v133\n"
"v_cvt_pk_bf16_f32 v197, v134, v135\n"
"v_cvt_pk_bf16_f32 v198, v148, v149\n"
"v_cvt_pk_bf16_f32 v199, v150, v151\n"
"v_exp_f32 v136, v136\n"
"v_exp_f32 v137, v137\n"
"v_exp_f32 v138, v138\n"
"v_exp_f32 v139, v139\n"
"v_exp_f32 v152, v152\n"
"v_exp_f32 v153, v153\n"
"v_exp_f32 v154, v154\n"
"v_exp_f32 v155, v155\n"
"v_cvt_pk_bf16_f32 v200, v136, v137\n"
"v_cvt_pk_bf16_f32 v201, v138, v139\n"
"v_cvt_pk_bf16_f32 v202, v152, v153\n"
"v_cvt_pk_bf16_f32 v203, v154, v155\n"
"v_exp_f32 v140, v140\n"
"v_exp_f32 v141, v141\n"
"v_exp_f32 v142, v142\n"
"v_exp_f32 v143, v143\n"
"v_exp_f32 v156, v156\n"
"v_exp_f32 v157, v157\n"
"v_exp_f32 v158, v158\n"
"v_exp_f32 v159, v159\n"
"v_cvt_pk_bf16_f32 v204, v140, v141\n"
"v_cvt_pk_bf16_f32 v205, v142, v143\n"
"v_cvt_pk_bf16_f32 v206, v156, v157\n"
"v_cvt_pk_bf16_f32 v207, v158, v159\n"
"s_nop 1\n"
"s_waitcnt lgkmcnt(0)\n"
"v_mfma_f32_16x16x32_bf16 v[128:131], a[64:67], a[0:3], v[224:227]\n"
"v_exp_f32 v160, v160\n"
"v_mfma_f32_16x16x32_bf16 v[132:135], a[64:67], a[16:19], v[228:231]\n"
"v_exp_f32 v161, v161\n"
"v_mfma_f32_16x16x32_bf16 v[136:139], a[64:67], a[32:35], v[232:235]\n"
"v_exp_f32 v162, v162\n"
"v_mfma_f32_16x16x32_bf16 v[140:143], a[64:67], a[48:51], v[236:239]\n"
"ds_read_b128 a[64:67], v240 offset:8192\n"
"v_cvt_pk_bf16_f32 v208, v160, v161\n"
"v_mfma_f32_16x16x32_bf16 v[128:131], a[68:71], a[4:7], v[128:131]\n"
"v_exp_f32 v163, v163\n"
"v_mfma_f32_16x16x32_bf16 v[132:135], a[68:71], a[20:23], v[132:135]\n"
"v_exp_f32 v176, v176\n"
"v_mfma_f32_16x16x32_bf16 v[136:139], a[68:71], a[36:39], v[136:139]\n"
"v_exp_f32 v177, v177\n"
"v_mfma_f32_16x16x32_bf16 v[140:143], a[68:71], a[52:55], v[140:143]\n"
"ds_read_b128 a[68:71], v241 offset:8192\n"
"v_cvt_pk_bf16_f32 v209, v162, v163\n"
"v_mfma_f32_16x16x32_bf16 v[128:131], a[72:75], a[8:11], v[128:131]\n"
"v_exp_f32 v178, v178\n"
"v_mfma_f32_16x16x32_bf16 v[132:135], a[72:75], a[24:27], v[132:135]\n"
"v_exp_f32 v179, v179\n"
"v_mfma_f32_16x16x32_bf16 v[136:139], a[72:75], a[40:43], v[136:139]\n"
"v_exp_f32 v164, v164\n"
"v_mfma_f32_16x16x32_bf16 v[140:143], a[72:75], a[56:59], v[140:143]\n"
"ds_read_b128 a[72:75], v242 offset:8192\n"
"v_cvt_pk_bf16_f32 v210, v176, v177\n"
"v_mfma_f32_16x16x32_bf16 v[128:131], a[76:79], a[12:15], v[128:131]\n"
"v_exp_f32 v165, v165\n"
"v_mfma_f32_16x16x32_bf16 v[132:135], a[76:79], a[28:31], v[132:135]\n"
"v_exp_f32 v166, v166\n"
"v_mfma_f32_16x16x32_bf16 v[136:139], a[76:79], a[44:47], v[136:139]\n"
"v_exp_f32 v167, v167\n"
"v_mfma_f32_16x16x32_bf16 v[140:143], a[76:79], a[60:63], v[140:143]\n"
"ds_read_b128 a[76:79], v243 offset:8192\n"
"v_cvt_pk_bf16_f32 v211, v178, v179\n"
"v_mfma_f32_16x16x32_bf16 v[144:147], a[80:83], a[0:3], v[224:227]\n"
"v_exp_f32 v180, v180\n"
"v_mfma_f32_16x16x32_bf16 v[148:151], a[80:83], a[16:19], v[228:231]\n"
"v_exp_f32 v181, v181\n"
"v_mfma_f32_16x16x32_bf16 v[152:155], a[80:83], a[32:35], v[232:235]\n"
"v_exp_f32 v182, v182\n"
"v_mfma_f32_16x16x32_bf16 v[156:159], a[80:83], a[48:51], v[236:239]\n"
"ds_read_b128 a[80:83], v240 offset:12288\n"
"v_cvt_pk_bf16_f32 v212, v164, v165\n"
"v_mfma_f32_16x16x32_bf16 v[144:147], a[84:87], a[4:7], v[144:147]\n"
"v_exp_f32 v183, v183\n"
"v_mfma_f32_16x16x32_bf16 v[148:151], a[84:87], a[20:23], v[148:151]\n"
"v_exp_f32 v168, v168\n"
"v_mfma_f32_16x16x32_bf16 v[152:155], a[84:87], a[36:39], v[152:155]\n"
"v_exp_f32 v169, v169\n"
"v_mfma_f32_16x16x32_bf16 v[156:159], a[84:87], a[52:55], v[156:159]\n"
"ds_read_b128 a[84:87], v241 offset:12288\n"
"v_cvt_pk_bf16_f32 v213, v166, v167\n"
"v_mfma_f32_16x16x32_bf16 v[144:147], a[88:91], a[8:11], v[144:147]\n"
"v_exp_f32 v170, v170\n"
"v_mfma_f32_16x16x32_bf16 v[148:151], a[88:91], a[24:27], v[148:151]\n"
"v_exp_f32 v171, v171\n"
"v_mfma_f32_16x16x32_bf16 v[152:155], a[88:91], a[40:43], v[152:155]\n"
"v_exp_f32 v184, v184\n"
"v_mfma_f32_16x16x32_bf16 v[156:159], a[88:91], a[56:59], v[156:159]\n"
"ds_read_b128 a[88:91], v242 offset:12288\n"
"v_cvt_pk_bf16_f32 v214, v180, v181\n"
"v_mfma_f32_16x16x32_bf16 v[144:147], a[92:95], a[12:15], v[144:147]\n"
"v_exp_f32 v185, v185\n"
"v_mfma_f32_16x16x32_bf16 v[148:151], a[92:95], a[28:31], v[148:151]\n"
"v_exp_f32 v186, v186\n"
"v_mfma_f32_16x16x32_bf16 v[152:155], a[92:95], a[44:47], v[152:155]\n"
"v_exp_f32 v187, v187\n"
"v_mfma_f32_16x16x32_bf16 v[156:159], a[92:95], a[60:63], v[156:159]\n"
"ds_read_b128 a[92:95], v243 offset:12288\n"
"v_cvt_pk_bf16_f32 v215, v182, v183\n"
"v_mfma_f32_16x16x32_bf16 v[0:3], a[96:99], v[192:195], 0\n"
"v_exp_f32 v172, v172\n"
"v_mfma_f32_16x16x32_bf16 v[4:7], a[96:99], v[196:199], 0\n"
"v_exp_f32 v173, v173\n"
"v_mfma_f32_16x16x32_bf16 v[8:11], a[96:99], v[200:203], 0\n"
"v_exp_f32 v174, v174\n"
"v_mfma_f32_16x16x32_bf16 v[12:15], a[96:99], v[204:207], 0\n"
"ds_read_b64_tr_b16 a[96:97], v244 offset:40960\n"
"v_cvt_pk_bf16_f32 v216, v168, v169\n"
"v_mfma_f32_16x16x32_bf16 v[16:19], a[100:103], v[192:195], 0\n"
"ds_read_b64_tr_b16 a[98:99], v244 offset:45056\n"
"v_cvt_pk_bf16_f32 v217, v170, v171\n"
"v_mfma_f32_16x16x32_bf16 v[20:23], a[100:103], v[196:199], 0\n"
"v_exp_f32 v175, v175\n"
"v_mfma_f32_16x16x32_bf16 v[24:27], a[100:103], v[200:203], 0\n"
"v_exp_f32 v188, v188\n"
"v_mfma_f32_16x16x32_bf16 v[28:31], a[100:103], v[204:207], 0\n"
"ds_read_b64_tr_b16 a[100:101], v245 offset:40960\n"
"v_cvt_pk_bf16_f32 v218, v184, v185\n"
"v_mfma_f32_16x16x32_bf16 v[32:35], a[104:107], v[192:195], 0\n"
"ds_read_b64_tr_b16 a[102:103], v245 offset:45056\n"
"v_cvt_pk_bf16_f32 v219, v186, v187\n"
"v_mfma_f32_16x16x32_bf16 v[36:39], a[104:107], v[196:199], 0\n"
"v_exp_f32 v189, v189\n"
"v_mfma_f32_16x16x32_bf16 v[40:43], a[104:107], v[200:203], 0\n"
"v_exp_f32 v190, v190\n"
"v_mfma_f32_16x16x32_bf16 v[44:47], a[104:107], v[204:207], 0\n"
"ds_read_b64_tr_b16 a[104:105], v244 offset:41472\n"
"v_cvt_pk_bf16_f32 v220, v172, v173\n"
"v_mfma_f32_16x16x32_bf16 v[48:51], a[108:111], v[192:195], 0\n"
"ds_read_b64_tr_b16 a[106:107], v244 offset:45568\n"
"v_cvt_pk_bf16_f32 v221, v174, v175\n"
"v_mfma_f32_16x16x32_bf16 v[52:55], a[108:111], v[196:199], 0\n"
"v_exp_f32 v191, v191\n"
"v_mfma_f32_16x16x32_bf16 v[56:59], a[108:111], v[200:203], 0\n"
"v_cvt_pk_bf16_f32 v222, v188, v189\n"
"v_cvt_pk_bf16_f32 v223, v190, v191\n"
"v_mfma_f32_16x16x32_bf16 v[60:63], a[108:111], v[204:207], 0\n"
"ds_read_b64_tr_b16 a[108:109], v245 offset:41472\n"
"v_mfma_f32_16x16x32_bf16 v[64:67], a[112:115], v[192:195], 0\n"
"ds_read_b64_tr_b16 a[110:111], v245 offset:45568\n"
"v_mfma_f32_16x16x32_bf16 v[68:71], a[112:115], v[196:199], 0\n"
"v_mfma_f32_16x16x32_bf16 v[72:75], a[112:115], v[200:203], 0\n"
"v_mfma_f32_16x16x32_bf16 v[76:79], a[112:115], v[204:207], 0\n"
"ds_read_b64_tr_b16 a[112:113], v244 offset:41984\n"
"v_mfma_f32_16x16x32_bf16 v[80:83], a[116:119], v[192:195], 0\n"
"ds_read_b64_tr_b16 a[114:115], v244 offset:46080\n"
"v_mfma_f32_16x16x32_bf16 v[84:87], a[116:119], v[196:199], 0\n"
"s_mov_b32 m0, s60\n"
"v_mfma_f32_16x16x32_bf16 v[88:91], a[116:119], v[200:203], 0\n"
"buffer_load_dwordx4 v250, s[40:43], s58 offen offset:0 lds\n"
"buffer_load_dwordx4 v251, s[40:43], s58 offen offset:1024 lds\n"
"buffer_load_dwordx4 v250, s[40:43], s58 offen offset:2048 lds\n"
"buffer_load_dwordx4 v251, s[40:43], s58 offen offset:3072 lds\n"
"v_mfma_f32_16x16x32_bf16 v[92:95], a[116:119], v[204:207], 0\n"
"ds_read_b64_tr_b16 a[116:117], v245 offset:41984\n"
"v_mfma_f32_16x16x32_bf16 v[96:99], a[120:123], v[192:195], 0\n"
"ds_read_b64_tr_b16 a[118:119], v245 offset:46080\n"
"v_mfma_f32_16x16x32_bf16 v[100:103], a[120:123], v[196:199], 0\n"
"v_mfma_f32_16x16x32_bf16 v[104:107], a[120:123], v[200:203], 0\n"
"v_mfma_f32_16x16x32_bf16 v[108:111], a[120:123], v[204:207], 0\n"
"ds_read_b64_tr_b16 a[120:121], v244 offset:42496\n"
"v_mfma_f32_16x16x32_bf16 v[112:115], a[124:127], v[192:195], 0\n"
"ds_read_b64_tr_b16 a[122:123], v244 offset:46592\n"
"v_mfma_f32_16x16x32_bf16 v[116:119], a[124:127], v[196:199], 0\n"
"s_add_u32 s65, s58, 4096\n"
"s_add_u32 m0, s60, 4096\n"
"v_mfma_f32_16x16x32_bf16 v[120:123], a[124:127], v[200:203], 0\n"
"buffer_load_dwordx4 v250, s[40:43], s65 offen offset:0 lds\n"
"buffer_load_dwordx4 v251, s[40:43], s65 offen offset:1024 lds\n"
"buffer_load_dwordx4 v250, s[40:43], s65 offen offset:2048 lds\n"
"buffer_load_dwordx4 v251, s[40:43], s65 offen offset:3072 lds\n"
"v_mfma_f32_16x16x32_bf16 v[124:127], a[124:127], v[204:207], 0\n"
"ds_read_b64_tr_b16 a[124:125], v245 offset:42496\n"
"v_mfma_f32_16x16x32_bf16 a[128:131], a[144:147], v[192:195], 0\n"
"ds_read_b64_tr_b16 a[126:127], v245 offset:46592\n"
"v_mfma_f32_16x16x32_bf16 a[132:135], a[144:147], v[196:199], 0\n"
"v_mfma_f32_16x16x32_bf16 a[136:139], a[144:147], v[200:203], 0\n"
"v_mfma_f32_16x16x32_bf16 a[140:143], a[144:147], v[204:207], 0\n"
"s_waitcnt lgkmcnt(15)\n"
"v_mfma_f32_16x16x32_bf16 v[160:163], a[64:67], a[0:3], v[224:227]\n"
"v_exp_f32 v128, v128\n"
"v_mfma_f32_16x16x32_bf16 v[164:167], a[64:67], a[16:19], v[228:231]\n"
"v_exp_f32 v129, v129\n"
"v_mfma_f32_16x16x32_bf16 v[168:171], a[64:67], a[32:35], v[232:235]\n"
"v_exp_f32 v130, v130\n"
"v_mfma_f32_16x16x32_bf16 v[172:175], a[64:67], a[48:51], v[236:239]\n"
"ds_read_b128 a[64:67], v240 offset:16384\n"
"v_cvt_pk_bf16_f32 v192, v128, v129\n"
"v_mfma_f32_16x16x32_bf16 v[160:163], a[68:71], a[4:7], v[160:163]\n"
"v_exp_f32 v131, v131\n"
"v_mfma_f32_16x16x32_bf16 v[164:167], a[68:71], a[20:23], v[164:167]\n"
"v_exp_f32 v144, v144\n"
"v_mfma_f32_16x16x32_bf16 v[168:171], a[68:71], a[36:39], v[168:171]\n"
"v_exp_f32 v145, v145\n"
"v_mfma_f32_16x16x32_bf16 v[172:175], a[68:71], a[52:55], v[172:175]\n"
"ds_read_b128 a[68:71], v241 offset:16384\n"
"v_cvt_pk_bf16_f32 v193, v130, v131\n"
"v_mfma_f32_16x16x32_bf16 v[160:163], a[72:75], a[8:11], v[160:163]\n"
"v_exp_f32 v146, v146\n"
"v_mfma_f32_16x16x32_bf16 v[164:167], a[72:75], a[24:27], v[164:167]\n"
"v_exp_f32 v147, v147\n"
"v_mfma_f32_16x16x32_bf16 v[168:171], a[72:75], a[40:43], v[168:171]\n"
"v_exp_f32 v132, v132\n"
"v_mfma_f32_16x16x32_bf16 v[172:175], a[72:75], a[56:59], v[172:175]\n"
"ds_read_b128 a[72:75], v242 offset:16384\n"
"v_cvt_pk_bf16_f32 v194, v144, v145\n"
"v_mfma_f32_16x16x32_bf16 v[160:163], a[76:79], a[12:15], v[160:163]\n"
"v_exp_f32 v133, v133\n"
"v_mfma_f32_16x16x32_bf16 v[164:167], a[76:79], a[28:31], v[164:167]\n"
"v_exp_f32 v134, v134\n"
"v_mfma_f32_16x16x32_bf16 v[168:171], a[76:79], a[44:47], v[168:171]\n"
"v_exp_f32 v135, v135\n"
"v_mfma_f32_16x16x32_bf16 v[172:175], a[76:79], a[60:63], v[172:175]\n"
"ds_read_b128 a[76:79], v243 offset:16384\n"
"v_cvt_pk_bf16_f32 v195, v146, v147\n"
"v_mfma_f32_16x16x32_bf16 v[176:179], a[80:83], a[0:3], v[224:227]\n"
"v_exp_f32 v148, v148\n"
"v_mfma_f32_16x16x32_bf16 v[180:183], a[80:83], a[16:19], v[228:231]\n"
"v_exp_f32 v149, v149\n"
"v_mfma_f32_16x16x32_bf16 v[184:187], a[80:83], a[32:35], v[232:235]\n"
"v_exp_f32 v150, v150\n"
"v_mfma_f32_16x16x32_bf16 v[188:191], a[80:83], a[48:51], v[236:239]\n"
"ds_read_b128 a[80:83], v240 offset:20480\n"
"v_cvt_pk_bf16_f32 v196, v132, v133\n"
"v_mfma_f32_16x16x32_bf16 v[176:179], a[84:87], a[4:7], v[176:179]\n"
"v_exp_f32 v151, v151\n"
"v_mfma_f32_16x16x32_bf16 v[180:183], a[84:87], a[20:23], v[180:183]\n"
"v_exp_f32 v136, v136\n"
"v_mfma_f32_16x16x32_bf16 v[184:187], a[84:87], a[36:39], v[184:187]\n"
"v_exp_f32 v137, v137\n"
"v_mfma_f32_16x16x32_bf16 v[188:191], a[84:87], a[52:55], v[188:191]\n"
"ds_read_b128 a[84:87], v241 offset:20480\n"
"v_cvt_pk_bf16_f32 v197, v134, v135\n"
"v_mfma_f32_16x16x32_bf16 v[176:179], a[88:91], a[8:11], v[176:179]\n"
"v_exp_f32 v138, v138\n"
"v_mfma_f32_16x16x32_bf16 v[180:183], a[88:91], a[24:27], v[180:183]\n"
"v_exp_f32 v139, v139\n"
"v_mfma_f32_16x16x32_bf16 v[184:187], a[88:91], a[40:43], v[184:187]\n"
"v_exp_f32 v152, v152\n"
"v_mfma_f32_16x16x32_bf16 v[188:191], a[88:91], a[56:59], v[188:191]\n"
"ds_read_b128 a[88:91], v242 offset:20480\n"
"v_cvt_pk_bf16_f32 v198, v148, v149\n"
"v_mfma_f32_16x16x32_bf16 v[176:179], a[92:95], a[12:15], v[176:179]\n"
"v_exp_f32 v153, v153\n"
"v_mfma_f32_16x16x32_bf16 v[180:183], a[92:95], a[28:31], v[180:183]\n"
"v_exp_f32 v154, v154\n"
"v_mfma_f32_16x16x32_bf16 v[184:187], a[92:95], a[44:47], v[184:187]\n"
"v_exp_f32 v155, v155\n"
"v_mfma_f32_16x16x32_bf16 v[188:191], a[92:95], a[60:63], v[188:191]\n"
"ds_read_b128 a[92:95], v243 offset:20480\n"
"v_cvt_pk_bf16_f32 v199, v150, v151\n"
"s_waitcnt lgkmcnt(8)\n"
"v_mfma_f32_16x16x32_bf16 v[0:3], a[96:99], v[208:211], v[0:3]\n"
"v_exp_f32 v140, v140\n"
"v_mfma_f32_16x16x32_bf16 v[4:7], a[96:99], v[212:215], v[4:7]\n"
"v_exp_f32 v141, v141\n"
"v_mfma_f32_16x16x32_bf16 v[8:11], a[96:99], v[216:219], v[8:11]\n"
"v_exp_f32 v142, v142\n"
"v_mfma_f32_16x16x32_bf16 v[12:15], a[96:99], v[220:223], v[12:15]\n"
"ds_read_b64_tr_b16 a[96:97], v244 offset:49152\n"
"v_cvt_pk_bf16_f32 v200, v136, v137\n"
"v_mfma_f32_16x16x32_bf16 v[16:19], a[100:103], v[208:211], v[16:19]\n"
"ds_read_b64_tr_b16 a[98:99], v244 offset:53248\n"
"v_cvt_pk_bf16_f32 v201, v138, v139\n"
"v_mfma_f32_16x16x32_bf16 v[20:23], a[100:103], v[212:215], v[20:23]\n"
"v_exp_f32 v143, v143\n"
"v_mfma_f32_16x16x32_bf16 v[24:27], a[100:103], v[216:219], v[24:27]\n"
"v_exp_f32 v156, v156\n"
"v_mfma_f32_16x16x32_bf16 v[28:31], a[100:103], v[220:223], v[28:31]\n"
"ds_read_b64_tr_b16 a[100:101], v245 offset:49152\n"
"v_cvt_pk_bf16_f32 v202, v152, v153\n"
"v_mfma_f32_16x16x32_bf16 v[32:35], a[104:107], v[208:211], v[32:35]\n"
"ds_read_b64_tr_b16 a[102:103], v245 offset:53248\n"
"v_cvt_pk_bf16_f32 v203, v154, v155\n"
"v_mfma_f32_16x16x32_bf16 v[36:39], a[104:107], v[212:215], v[36:39]\n"
"v_exp_f32 v157, v157\n"
"v_mfma_f32_16x16x32_bf16 v[40:43], a[104:107], v[216:219], v[40:43]\n"
"v_exp_f32 v158, v158\n"
"v_mfma_f32_16x16x32_bf16 v[44:47], a[104:107], v[220:223], v[44:47]\n"
"ds_read_b64_tr_b16 a[104:105], v244 offset:49664\n"
"v_cvt_pk_bf16_f32 v204, v140, v141\n"
"v_mfma_f32_16x16x32_bf16 v[48:51], a[108:111], v[208:211], v[48:51]\n"
"ds_read_b64_tr_b16 a[106:107], v244 offset:53760\n"
"v_cvt_pk_bf16_f32 v205, v142, v143\n"
"v_mfma_f32_16x16x32_bf16 v[52:55], a[108:111], v[212:215], v[52:55]\n"
"v_exp_f32 v159, v159\n"
"v_mfma_f32_16x16x32_bf16 v[56:59], a[108:111], v[216:219], v[56:59]\n"
"v_cvt_pk_bf16_f32 v206, v156, v157\n"
"v_cvt_pk_bf16_f32 v207, v158, v159\n"
"v_mfma_f32_16x16x32_bf16 v[60:63], a[108:111], v[220:223], v[60:63]\n"
"ds_read_b64_tr_b16 a[108:109], v245 offset:49664\n"
"v_mfma_f32_16x16x32_bf16 v[64:67], a[112:115], v[208:211], v[64:67]\n"
"ds_read_b64_tr_b16 a[110:111], v245 offset:53760\n"
"v_mfma_f32_16x16x32_bf16 v[68:71], a[112:115], v[212:215], v[68:71]\n"
"v_mfma_f32_16x16x32_bf16 v[72:75], a[112:115], v[216:219], v[72:75]\n"
"v_mfma_f32_16x16x32_bf16 v[76:79], a[112:115], v[220:223], v[76:79]\n"
"ds_read_b64_tr_b16 a[112:113], v244 offset:50176\n"
"v_mfma_f32_16x16x32_bf16 v[80:83], a[116:119], v[208:211], v[80:83]\n"
"ds_read_b64_tr_b16 a[114:115], v244 offset:54272\n"
"v_mfma_f32_16x16x32_bf16 v[84:87], a[116:119], v[212:215], v[84:87]\n"
"v_mfma_f32_16x16x32_bf16 v[88:91], a[116:119], v[216:219], v[88:91]\n"
"v_mfma_f32_16x16x32_bf16 v[92:95], a[116:119], v[220:223], v[92:95]\n"
"ds_read_b64_tr_b16 a[116:117], v245 offset:50176\n"
"v_mfma_f32_16x16x32_bf16 v[96:99], a[120:123], v[208:211], v[96:99]\n"
"ds_read_b64_tr_b16 a[118:119], v245 offset:54272\n"
"v_mfma_f32_16x16x32_bf16 v[100:103], a[120:123], v[212:215], v[100:103]\n"
"v_mfma_f32_16x16x32_bf16 v[104:107], a[120:123], v[216:219], v[104:107]\n"
"v_mfma_f32_16x16x32_bf16 v[108:111], a[120:123], v[220:223], v[108:111]\n"
"ds_read_b64_tr_b16 a[120:121], v244 offset:50688\n"
"v_mfma_f32_16x16x32_bf16 v[112:115], a[124:127], v[208:211], v[112:115]\n"
"ds_read_b64_tr_b16 a[122:123], v244 offset:54784\n"
"v_mfma_f32_16x16x32_bf16 v[116:119], a[124:127], v[212:215], v[116:119]\n"
"v_mfma_f32_16x16x32_bf16 v[120:123], a[124:127], v[216:219], v[120:123]\n"
"v_mfma_f32_16x16x32_bf16 v[124:127], a[124:127], v[220:223], v[124:127]\n"
"ds_read_b64_tr_b16 a[124:125], v245 offset:50688\n"
"v_mfma_f32_16x16x32_bf16 a[128:131], a[144:147], v[208:211], a[128:131]\n"
"ds_read_b64_tr_b16 a[126:127], v245 offset:54784\n"
"v_mfma_f32_16x16x32_bf16 a[132:135], a[144:147], v[212:215], a[132:135]\n"
"v_mfma_f32_16x16x32_bf16 a[136:139], a[144:147], v[216:219], a[136:139]\n"
"v_mfma_f32_16x16x32_bf16 a[140:143], a[144:147], v[220:223], a[140:143]\n"
"s_waitcnt lgkmcnt(15)\n"
"v_mfma_f32_16x16x32_bf16 v[128:131], a[64:67], a[0:3], v[224:227]\n"
"v_exp_f32 v160, v160\n"
"v_mfma_f32_16x16x32_bf16 v[132:135], a[64:67], a[16:19], v[228:231]\n"
"v_exp_f32 v161, v161\n"
"v_mfma_f32_16x16x32_bf16 v[136:139], a[64:67], a[32:35], v[232:235]\n"
"v_exp_f32 v162, v162\n"
"v_mfma_f32_16x16x32_bf16 v[140:143], a[64:67], a[48:51], v[236:239]\n"
"ds_read_b128 a[64:67], v240 offset:24576\n"
"v_cvt_pk_bf16_f32 v208, v160, v161\n"
"v_mfma_f32_16x16x32_bf16 v[128:131], a[68:71], a[4:7], v[128:131]\n"
"v_exp_f32 v163, v163\n"
"v_mfma_f32_16x16x32_bf16 v[132:135], a[68:71], a[20:23], v[132:135]\n"
"v_exp_f32 v176, v176\n"
"v_mfma_f32_16x16x32_bf16 v[136:139], a[68:71], a[36:39], v[136:139]\n"
"v_exp_f32 v177, v177\n"
"v_mfma_f32_16x16x32_bf16 v[140:143], a[68:71], a[52:55], v[140:143]\n"
"ds_read_b128 a[68:71], v241 offset:24576\n"
"v_cvt_pk_bf16_f32 v209, v162, v163\n"
"v_mfma_f32_16x16x32_bf16 v[128:131], a[72:75], a[8:11], v[128:131]\n"
"v_exp_f32 v178, v178\n"
"v_mfma_f32_16x16x32_bf16 v[132:135], a[72:75], a[24:27], v[132:135]\n"
"v_exp_f32 v179, v179\n"
"v_mfma_f32_16x16x32_bf16 v[136:139], a[72:75], a[40:43], v[136:139]\n"
"v_exp_f32 v164, v164\n"
"v_mfma_f32_16x16x32_bf16 v[140:143], a[72:75], a[56:59], v[140:143]\n"
"ds_read_b128 a[72:75], v242 offset:24576\n"
"v_cvt_pk_bf16_f32 v210, v176, v177\n"
"v_mfma_f32_16x16x32_bf16 v[128:131], a[76:79], a[12:15], v[128:131]\n"
"v_exp_f32 v165, v165\n"
"v_mfma_f32_16x16x32_bf16 v[132:135], a[76:79], a[28:31], v[132:135]\n"
"v_exp_f32 v166, v166\n"
"v_mfma_f32_16x16x32_bf16 v[136:139], a[76:79], a[44:47], v[136:139]\n"
"v_exp_f32 v167, v167\n"
"v_mfma_f32_16x16x32_bf16 v[140:143], a[76:79], a[60:63], v[140:143]\n"
"ds_read_b128 a[76:79], v243 offset:24576\n"
"v_cvt_pk_bf16_f32 v211, v178, v179\n"
"v_mfma_f32_16x16x32_bf16 v[144:147], a[80:83], a[0:3], v[224:227]\n"
"v_exp_f32 v180, v180\n"
"v_mfma_f32_16x16x32_bf16 v[148:151], a[80:83], a[16:19], v[228:231]\n"
"v_exp_f32 v181, v181\n"
"v_mfma_f32_16x16x32_bf16 v[152:155], a[80:83], a[32:35], v[232:235]\n"
"v_exp_f32 v182, v182\n"
"v_mfma_f32_16x16x32_bf16 v[156:159], a[80:83], a[48:51], v[236:239]\n"
"ds_read_b128 a[80:83], v240 offset:28672\n"
"v_cvt_pk_bf16_f32 v212, v164, v165\n"
"v_mfma_f32_16x16x32_bf16 v[144:147], a[84:87], a[4:7], v[144:147]\n"
"v_exp_f32 v183, v183\n"
"v_mfma_f32_16x16x32_bf16 v[148:151], a[84:87], a[20:23], v[148:151]\n"
"v_exp_f32 v168, v168\n"
"v_mfma_f32_16x16x32_bf16 v[152:155], a[84:87], a[36:39], v[152:155]\n"
"v_exp_f32 v169, v169\n"
"v_mfma_f32_16x16x32_bf16 v[156:159], a[84:87], a[52:55], v[156:159]\n"
"ds_read_b128 a[84:87], v241 offset:28672\n"
"v_cvt_pk_bf16_f32 v213, v166, v167\n"
"v_mfma_f32_16x16x32_bf16 v[144:147], a[88:91], a[8:11], v[144:147]\n"
"v_exp_f32 v170, v170\n"
"v_mfma_f32_16x16x32_bf16 v[148:151], a[88:91], a[24:27], v[148:151]\n"
"v_exp_f32 v171, v171\n"
"v_mfma_f32_16x16x32_bf16 v[152:155], a[88:91], a[40:43], v[152:155]\n"
"v_exp_f32 v184, v184\n"
"v_mfma_f32_16x16x32_bf16 v[156:159], a[88:91], a[56:59], v[156:159]\n"
"ds_read_b128 a[88:91], v242 offset:28672\n"
"v_cvt_pk_bf16_f32 v214, v180, v181\n"
"v_mfma_f32_16x16x32_bf16 v[144:147], a[92:95], a[12:15], v[144:147]\n"
"v_exp_f32 v185, v185\n"
"v_mfma_f32_16x16x32_bf16 v[148:151], a[92:95], a[28:31], v[148:151]\n"
"v_exp_f32 v186, v186\n"
"v_mfma_f32_16x16x32_bf16 v[152:155], a[92:95], a[44:47], v[152:155]\n"
"v_exp_f32 v187, v187\n"
"v_mfma_f32_16x16x32_bf16 v[156:159], a[92:95], a[60:63], v[156:159]\n"
"ds_read_b128 a[92:95], v243 offset:28672\n"
"v_cvt_pk_bf16_f32 v215, v182, v183\n"
"s_waitcnt lgkmcnt(8)\n"
"v_mfma_f32_16x16x32_bf16 v[0:3], a[96:99], v[192:195], v[0:3]\n"
"v_exp_f32 v172, v172\n"
"v_mfma_f32_16x16x32_bf16 v[4:7], a[96:99], v[196:199], v[4:7]\n"
"v_exp_f32 v173, v173\n"
"v_mfma_f32_16x16x32_bf16 v[8:11], a[96:99], v[200:203], v[8:11]\n"
"v_exp_f32 v174, v174\n"
"v_mfma_f32_16x16x32_bf16 v[12:15], a[96:99], v[204:207], v[12:15]\n"
"ds_read_b64_tr_b16 a[96:97], v244 offset:57344\n"
"v_cvt_pk_bf16_f32 v216, v168, v169\n"
"v_mfma_f32_16x16x32_bf16 v[16:19], a[100:103], v[192:195], v[16:19]\n"
"ds_read_b64_tr_b16 a[98:99], v244 offset:61440\n"
"v_cvt_pk_bf16_f32 v217, v170, v171\n"
"v_mfma_f32_16x16x32_bf16 v[20:23], a[100:103], v[196:199], v[20:23]\n"
"v_exp_f32 v175, v175\n"
"v_mfma_f32_16x16x32_bf16 v[24:27], a[100:103], v[200:203], v[24:27]\n"
"v_exp_f32 v188, v188\n"
"v_mfma_f32_16x16x32_bf16 v[28:31], a[100:103], v[204:207], v[28:31]\n"
"ds_read_b64_tr_b16 a[100:101], v245 offset:57344\n"
"v_cvt_pk_bf16_f32 v218, v184, v185\n"
"v_mfma_f32_16x16x32_bf16 v[32:35], a[104:107], v[192:195], v[32:35]\n"
"ds_read_b64_tr_b16 a[102:103], v245 offset:61440\n"
"v_cvt_pk_bf16_f32 v219, v186, v187\n"
"v_mfma_f32_16x16x32_bf16 v[36:39], a[104:107], v[196:199], v[36:39]\n"
"v_exp_f32 v189, v189\n"
"v_mfma_f32_16x16x32_bf16 v[40:43], a[104:107], v[200:203], v[40:43]\n"
"v_exp_f32 v190, v190\n"
"v_mfma_f32_16x16x32_bf16 v[44:47], a[104:107], v[204:207], v[44:47]\n"
"ds_read_b64_tr_b16 a[104:105], v244 offset:57856\n"
"v_cvt_pk_bf16_f32 v220, v172, v173\n"
"v_mfma_f32_16x16x32_bf16 v[48:51], a[108:111], v[192:195], v[48:51]\n"
"ds_read_b64_tr_b16 a[106:107], v244 offset:61952\n"
"v_cvt_pk_bf16_f32 v221, v174, v175\n"
"v_mfma_f32_16x16x32_bf16 v[52:55], a[108:111], v[196:199], v[52:55]\n"
"v_exp_f32 v191, v191\n"
"v_mfma_f32_16x16x32_bf16 v[56:59], a[108:111], v[200:203], v[56:59]\n"
"v_cvt_pk_bf16_f32 v222, v188, v189\n"
"v_cvt_pk_bf16_f32 v223, v190, v191\n"
"v_mfma_f32_16x16x32_bf16 v[60:63], a[108:111], v[204:207], v[60:63]\n"
"ds_read_b64_tr_b16 a[108:109], v245 offset:57856\n"
"v_mfma_f32_16x16x32_bf16 v[64:67], a[112:115], v[192:195], v[64:67]\n"
"ds_read_b64_tr_b16 a[110:111], v245 offset:61952\n"
"v_mfma_f32_16x16x32_bf16 v[68:71], a[112:115], v[196:199], v[68:71]\n"
"v_mfma_f32_16x16x32_bf16 v[72:75], a[112:115], v[200:203], v[72:75]\n"
"v_mfma_f32_16x16x32_bf16 v[76:79], a[112:115], v[204:207], v[76:79]\n"
"ds_read_b64_tr_b16 a[112:113], v244 offset:58368\n"
"v_mfma_f32_16x16x32_bf16 v[80:83], a[116:119], v[192:195], v[80:83]\n"
"ds_read_b64_tr_b16 a[114:115], v244 offset:62464\n"
"v_mfma_f32_16x16x32_bf16 v[84:87], a[116:119], v[196:199], v[84:87]\n"
"v_mfma_f32_16x16x32_bf16 v[88:91], a[116:119], v[200:203], v[88:91]\n"
"v_mfma_f32_16x16x32_bf16 v[92:95], a[116:119], v[204:207], v[92:95]\n"
"ds_read_b64_tr_b16 a[116:117], v245 offset:58368\n"
"v_mfma_f32_16x16x32_bf16 v[96:99], a[120:123], v[192:195], v[96:99]\n"
"ds_read_b64_tr_b16 a[118:119], v245 offset:62464\n"
"v_mfma_f32_16x16x32_bf16 v[100:103], a[120:123], v[196:199], v[100:103]\n"
"v_mfma_f32_16x16x32_bf16 v[104:107], a[120:123], v[200:203], v[104:107]\n"
"v_mfma_f32_16x16x32_bf16 v[108:111], a[120:123], v[204:207], v[108:111]\n"
"ds_read_b64_tr_b16 a[120:121], v244 offset:58880\n"
"v_mfma_f32_16x16x32_bf16 v[112:115], a[124:127], v[192:195], v[112:115]\n"
"ds_read_b64_tr_b16 a[122:123], v244 offset:62976\n"
"s_xor_b32 s59, s59, 0x10000\n"
"v_mfma_f32_16x16x32_bf16 v[116:119], a[124:127], v[196:199], v[116:119]\n"
"s_xor_b32 s60, s60, 0x10000\n"
"v_mfma_f32_16x16x32_bf16 v[120:123], a[124:127], v[200:203], v[120:123]\n"
"s_add_u32 s57, s57, 32768\n"
"v_mfma_f32_16x16x32_bf16 v[124:127], a[124:127], v[204:207], v[124:127]\n"
"ds_read_b64_tr_b16 a[124:125], v245 offset:58880\n"
"s_add_u32 s58, s58, 32768\n"
"v_mfma_f32_16x16x32_bf16 a[128:131], a[144:147], v[192:195], a[128:131]\n"
"ds_read_b64_tr_b16 a[126:127], v245 offset:62976\n"
"v_mfma_f32_16x16x32_bf16 a[132:135], a[144:147], v[196:199], a[132:135]\n"
"v_mfma_f32_16x16x32_bf16 a[136:139], a[144:147], v[200:203], a[136:139]\n"
"v_mfma_f32_16x16x32_bf16 a[140:143], a[144:147], v[204:207], a[140:143]\n"
"v_mbcnt_lo_u32_b32 v252, -1, 0\n"
"v_mbcnt_hi_u32_b32 v252, -1, v252\n"
"v_lshlrev_b32 v252, 4, v252\n"
"s_mov_b32 s76, %15\n"
"s_mov_b32 s77, %16\n"
"s_mov_b32 s78, %17\n"
"s_mov_b32 s61, 14\n"
"Lat_loop:\n"
"s_waitcnt lgkmcnt(15)\n"
"v_mfma_f32_16x16x32_bf16 v[160:163], a[64:67], a[0:3], v[224:227]\n"
"v_exp_f32 v128, v128\n"
"v_mfma_f32_16x16x32_bf16 v[164:167], a[64:67], a[16:19], v[228:231]\n"
"s_waitcnt vmcnt(0) lgkmcnt(0)\n"
"s_barrier\n"
"v_xor_b32 v240, 0x10000, v240\n"
"v_xor_b32 v241, 0x10000, v241\n"
"v_mfma_f32_16x16x32_bf16 v[168:171], a[64:67], a[32:35], v[232:235]\n"
"v_xor_b32 v242, 0x10000, v242\n"
"v_xor_b32 v243, 0x10000, v243\n"
"v_mfma_f32_16x16x32_bf16 v[172:175], a[64:67], a[48:51], v[236:239]\n"
"ds_read_b128 a[64:67], v240 offset:0\n"
"v_mfma_f32_16x16x32_bf16 v[160:163], a[68:71], a[4:7], v[160:163]\n"
"v_exp_f32 v129, v129\n"
"v_mfma_f32_16x16x32_bf16 v[164:167], a[68:71], a[20:23], v[164:167]\n"
"v_xor_b32 v244, 0x10000, v244\n"
"v_xor_b32 v245, 0x10000, v245\n"
"v_mfma_f32_16x16x32_bf16 v[168:171], a[68:71], a[36:39], v[168:171]\n"
"v_exp_f32 v130, v130\n"
"v_mfma_f32_16x16x32_bf16 v[172:175], a[68:71], a[52:55], v[172:175]\n"
"ds_read_b128 a[68:71], v241 offset:0\n"
"v_cvt_pk_bf16_f32 v192, v128, v129\n"
"v_mfma_f32_16x16x32_bf16 v[160:163], a[72:75], a[8:11], v[160:163]\n"
"v_exp_f32 v131, v131\n"
"v_mfma_f32_16x16x32_bf16 v[164:167], a[72:75], a[24:27], v[164:167]\n"
"global_load_dwordx4 a[148:151], v252, s[76:77]\n"
"v_exp_f32 v144, v144\n"
"v_mfma_f32_16x16x32_bf16 v[168:171], a[72:75], a[40:43], v[168:171]\n"
"v_exp_f32 v145, v145\n"
"v_mfma_f32_16x16x32_bf16 v[172:175], a[72:75], a[56:59], v[172:175]\n"
"ds_read_b128 a[72:75], v242 offset:0\n"
"v_cvt_pk_bf16_f32 v193, v130, v131\n"
"v_mfma_f32_16x16x32_bf16 v[160:163], a[76:79], a[12:15], v[160:163]\n"
"v_exp_f32 v146, v146\n"
"v_mfma_f32_16x16x32_bf16 v[164:167], a[76:79], a[28:31], v[164:167]\n"
"s_add_u32 s76, s76, s78\n"
"v_exp_f32 v147, v147\n"
"v_mfma_f32_16x16x32_bf16 v[168:171], a[76:79], a[44:47], v[168:171]\n"
"s_addc_u32 s77, s77, 0\n"
"v_exp_f32 v132, v132\n"
"v_mfma_f32_16x16x32_bf16 v[172:175], a[76:79], a[60:63], v[172:175]\n"
"ds_read_b128 a[76:79], v243 offset:0\n"
"v_cvt_pk_bf16_f32 v194, v144, v145\n"
"v_mfma_f32_16x16x32_bf16 v[176:179], a[80:83], a[0:3], v[224:227]\n"
"v_exp_f32 v133, v133\n"
"v_mfma_f32_16x16x32_bf16 v[180:183], a[80:83], a[16:19], v[228:231]\n"
"v_exp_f32 v134, v134\n"
"v_mfma_f32_16x16x32_bf16 v[184:187], a[80:83], a[32:35], v[232:235]\n"
"v_exp_f32 v135, v135\n"
"v_mfma_f32_16x16x32_bf16 v[188:191], a[80:83], a[48:51], v[236:239]\n"
"ds_read_b128 a[80:83], v240 offset:4096\n"
"v_cvt_pk_bf16_f32 v195, v146, v147\n"
"v_mfma_f32_16x16x32_bf16 v[176:179], a[84:87], a[4:7], v[176:179]\n"
"v_exp_f32 v148, v148\n"
"v_mfma_f32_16x16x32_bf16 v[180:183], a[84:87], a[20:23], v[180:183]\n"
"v_exp_f32 v149, v149\n"
"v_mfma_f32_16x16x32_bf16 v[184:187], a[84:87], a[36:39], v[184:187]\n"
"v_exp_f32 v150, v150\n"
"v_mfma_f32_16x16x32_bf16 v[188:191], a[84:87], a[52:55], v[188:191]\n"
"ds_read_b128 a[84:87], v241 offset:4096\n"
"v_cvt_pk_bf16_f32 v196, v132, v133\n"
"v_mfma_f32_16x16x32_bf16 v[176:179], a[88:91], a[8:11], v[176:179]\n"
"v_exp_f32 v151, v151\n"
"v_mfma_f32_16x16x32_bf16 v[180:183], a[88:91], a[24:27], v[180:183]\n"
"v_exp_f32 v136, v136\n"
"v_mfma_f32_16x16x32_bf16 v[184:187], a[88:91], a[40:43], v[184:187]\n"
"v_exp_f32 v137, v137\n"
"v_mfma_f32_16x16x32_bf16 v[188:191], a[88:91], a[56:59], v[188:191]\n"
"ds_read_b128 a[88:91], v242 offset:4096\n"
"v_cvt_pk_bf16_f32 v197, v134, v135\n"
"v_mfma_f32_16x16x32_bf16 v[176:179], a[92:95], a[12:15], v[176:179]\n"
"v_exp_f32 v138, v138\n"
"v_mfma_f32_16x16x32_bf16 v[180:183], a[92:95], a[28:31], v[180:183]\n"
"v_exp_f32 v139, v139\n"
"v_mfma_f32_16x16x32_bf16 v[184:187], a[92:95], a[44:47], v[184:187]\n"
"v_exp_f32 v152, v152\n"
"v_mfma_f32_16x16x32_bf16 v[188:191], a[92:95], a[60:63], v[188:191]\n"
"ds_read_b128 a[92:95], v243 offset:4096\n"
"v_cvt_pk_bf16_f32 v198, v148, v149\n"
"s_waitcnt lgkmcnt(8)\n"
"v_mfma_f32_16x16x32_bf16 v[0:3], a[96:99], v[208:211], v[0:3]\n"
"v_exp_f32 v153, v153\n"
"v_mfma_f32_16x16x32_bf16 v[4:7], a[96:99], v[212:215], v[4:7]\n"
"v_exp_f32 v154, v154\n"
"v_mfma_f32_16x16x32_bf16 v[8:11], a[96:99], v[216:219], v[8:11]\n"
"v_exp_f32 v155, v155\n"
"v_mfma_f32_16x16x32_bf16 v[12:15], a[96:99], v[220:223], v[12:15]\n"
"ds_read_b64_tr_b16 a[96:97], v244 offset:32768\n"
"v_cvt_pk_bf16_f32 v199, v150, v151\n"
"v_mfma_f32_16x16x32_bf16 v[16:19], a[100:103], v[208:211], v[16:19]\n"
"ds_read_b64_tr_b16 a[98:99], v244 offset:36864\n"
"v_cvt_pk_bf16_f32 v200, v136, v137\n"
"v_mfma_f32_16x16x32_bf16 v[20:23], a[100:103], v[212:215], v[20:23]\n"
"v_exp_f32 v140, v140\n"
"v_mfma_f32_16x16x32_bf16 v[24:27], a[100:103], v[216:219], v[24:27]\n"
"v_exp_f32 v141, v141\n"
"v_mfma_f32_16x16x32_bf16 v[28:31], a[100:103], v[220:223], v[28:31]\n"
"ds_read_b64_tr_b16 a[100:101], v245 offset:32768\n"
"v_cvt_pk_bf16_f32 v201, v138, v139\n"
"v_mfma_f32_16x16x32_bf16 v[32:35], a[104:107], v[208:211], v[32:35]\n"
"ds_read_b64_tr_b16 a[102:103], v245 offset:36864\n"
"v_cvt_pk_bf16_f32 v202, v152, v153\n"
"v_mfma_f32_16x16x32_bf16 v[36:39], a[104:107], v[212:215], v[36:39]\n"
"v_exp_f32 v142, v142\n"
"v_mfma_f32_16x16x32_bf16 v[40:43], a[104:107], v[216:219], v[40:43]\n"
"v_exp_f32 v143, v143\n"
"v_mfma_f32_16x16x32_bf16 v[44:47], a[104:107], v[220:223], v[44:47]\n"
"ds_read_b64_tr_b16 a[104:105], v244 offset:33280\n"
"v_cvt_pk_bf16_f32 v203, v154, v155\n"
"v_mfma_f32_16x16x32_bf16 v[48:51], a[108:111], v[208:211], v[48:51]\n"
"ds_read_b64_tr_b16 a[106:107], v244 offset:37376\n"
"v_cvt_pk_bf16_f32 v204, v140, v141\n"
"v_mfma_f32_16x16x32_bf16 v[52:55], a[108:111], v[212:215], v[52:55]\n"
"v_exp_f32 v156, v156\n"
"v_mfma_f32_16x16x32_bf16 v[56:59], a[108:111], v[216:219], v[56:59]\n"
"v_exp_f32 v157, v157\n"
"v_mfma_f32_16x16x32_bf16 v[60:63], a[108:111], v[220:223], v[60:63]\n"
"ds_read_b64_tr_b16 a[108:109], v245 offset:33280\n"
"v_cvt_pk_bf16_f32 v205, v142, v143\n"
"v_mfma_f32_16x16x32_bf16 v[64:67], a[112:115], v[208:211], v[64:67]\n"
"ds_read_b64_tr_b16 a[110:111], v245 offset:37376\n"
"v_cvt_pk_bf16_f32 v206, v156, v157\n"
"v_mfma_f32_16x16x32_bf16 v[68:71], a[112:115], v[212:215], v[68:71]\n"
"v_exp_f32 v158, v158\n"
"v_mfma_f32_16x16x32_bf16 v[72:75], a[112:115], v[216:219], v[72:75]\n"
"v_exp_f32 v159, v159\n"
"v_mfma_f32_16x16x32_bf16 v[76:79], a[112:115], v[220:223], v[76:79]\n"
"ds_read_b64_tr_b16 a[112:113], v244 offset:33792\n"
"v_cvt_pk_bf16_f32 v207, v158, v159\n"
"v_mfma_f32_16x16x32_bf16 v[80:83], a[116:119], v[208:211], v[80:83]\n"
"ds_read_b64_tr_b16 a[114:115], v244 offset:37888\n"
"v_mfma_f32_16x16x32_bf16 v[84:87], a[116:119], v[212:215], v[84:87]\n"
"s_mov_b32 m0, s59\n"
"v_mfma_f32_16x16x32_bf16 v[88:91], a[116:119], v[216:219], v[88:91]\n"
"buffer_load_dwordx4 v246, s[36:39], s57 offen offset:0 lds\n"
"buffer_load_dwordx4 v247, s[36:39], s57 offen offset:1024 lds\n"
"buffer_load_dwordx4 v248, s[36:39], s57 offen offset:2048 lds\n"
"buffer_load_dwordx4 v249, s[36:39], s57 offen offset:3072 lds\n"
"v_mfma_f32_16x16x32_bf16 v[92:95], a[116:119], v[220:223], v[92:95]\n"
"ds_read_b64_tr_b16 a[116:117], v245 offset:33792\n"
"v_mfma_f32_16x16x32_bf16 v[96:99], a[120:123], v[208:211], v[96:99]\n"
"ds_read_b64_tr_b16 a[118:119], v245 offset:37888\n"
"v_mfma_f32_16x16x32_bf16 v[100:103], a[120:123], v[212:215], v[100:103]\n"
"v_mfma_f32_16x16x32_bf16 v[104:107], a[120:123], v[216:219], v[104:107]\n"
"v_mfma_f32_16x16x32_bf16 v[108:111], a[120:123], v[220:223], v[108:111]\n"
"ds_read_b64_tr_b16 a[120:121], v244 offset:34304\n"
"v_mfma_f32_16x16x32_bf16 v[112:115], a[124:127], v[208:211], v[112:115]\n"
"ds_read_b64_tr_b16 a[122:123], v244 offset:38400\n"
"v_mfma_f32_16x16x32_bf16 v[116:119], a[124:127], v[212:215], v[116:119]\n"
"s_add_u32 s64, s57, 4096\n"
"s_add_u32 m0, s59, 4096\n"
"v_mfma_f32_16x16x32_bf16 v[120:123], a[124:127], v[216:219], v[120:123]\n"
"buffer_load_dwordx4 v246, s[36:39], s64 offen offset:0 lds\n"
"buffer_load_dwordx4 v247, s[36:39], s64 offen offset:1024 lds\n"
"buffer_load_dwordx4 v248, s[36:39], s64 offen offset:2048 lds\n"
"buffer_load_dwordx4 v249, s[36:39], s64 offen offset:3072 lds\n"
"v_mfma_f32_16x16x32_bf16 v[124:127], a[124:127], v[220:223], v[124:127]\n"
"ds_read_b64_tr_b16 a[124:125], v245 offset:34304\n"
"v_mfma_f32_16x16x32_bf16 a[128:131], a[144:147], v[208:211], a[128:131]\n"
"ds_read_b64_tr_b16 a[126:127], v245 offset:38400\n"
"v_mfma_f32_16x16x32_bf16 a[132:135], a[144:147], v[212:215], a[132:135]\n"
"v_mfma_f32_16x16x32_bf16 a[136:139], a[144:147], v[216:219], a[136:139]\n"
"v_mfma_f32_16x16x32_bf16 a[140:143], a[144:147], v[220:223], a[140:143]\n"
"s_waitcnt lgkmcnt(15)\n"
"v_mfma_f32_16x16x32_bf16 v[128:131], a[64:67], a[0:3], v[224:227]\n"
"v_exp_f32 v160, v160\n"
"v_mfma_f32_16x16x32_bf16 v[132:135], a[64:67], a[16:19], v[228:231]\n"
"v_exp_f32 v161, v161\n"
"v_mfma_f32_16x16x32_bf16 v[136:139], a[64:67], a[32:35], v[232:235]\n"
"v_exp_f32 v162, v162\n"
"v_mfma_f32_16x16x32_bf16 v[140:143], a[64:67], a[48:51], v[236:239]\n"
"ds_read_b128 a[64:67], v240 offset:8192\n"
"v_cvt_pk_bf16_f32 v208, v160, v161\n"
"v_mfma_f32_16x16x32_bf16 v[128:131], a[68:71], a[4:7], v[128:131]\n"
"v_exp_f32 v163, v163\n"
"v_mfma_f32_16x16x32_bf16 v[132:135], a[68:71], a[20:23], v[132:135]\n"
"v_exp_f32 v176, v176\n"
"v_mfma_f32_16x16x32_bf16 v[136:139], a[68:71], a[36:39], v[136:139]\n"
"v_exp_f32 v177, v177\n"
"v_mfma_f32_16x16x32_bf16 v[140:143], a[68:71], a[52:55], v[140:143]\n"
"ds_read_b128 a[68:71], v241 offset:8192\n"
"v_cvt_pk_bf16_f32 v209, v162, v163\n"
"v_mfma_f32_16x16x32_bf16 v[128:131], a[72:75], a[8:11], v[128:131]\n"
"v_exp_f32 v178, v178\n"
"v_mfma_f32_16x16x32_bf16 v[132:135], a[72:75], a[24:27], v[132:135]\n"
"v_exp_f32 v179, v179\n"
"v_mfma_f32_16x16x32_bf16 v[136:139], a[72:75], a[40:43], v[136:139]\n"
"v_exp_f32 v164, v164\n"
"v_mfma_f32_16x16x32_bf16 v[140:143], a[72:75], a[56:59], v[140:143]\n"
"ds_read_b128 a[72:75], v242 offset:8192\n"
"v_cvt_pk_bf16_f32 v210, v176, v177\n"
"v_mfma_f32_16x16x32_bf16 v[128:131], a[76:79], a[12:15], v[128:131]\n"
"v_exp_f32 v165, v165\n"
"v_mfma_f32_16x16x32_bf16 v[132:135], a[76:79], a[28:31], v[132:135]\n"
"v_exp_f32 v166, v166\n"
"v_mfma_f32_16x16x32_bf16 v[136:139], a[76:79], a[44:47], v[136:139]\n"
"v_exp_f32 v167, v167\n"
"v_mfma_f32_16x16x32_bf16 v[140:143], a[76:79], a[60:63], v[140:143]\n"
"ds_read_b128 a[76:79], v243 offset:8192\n"
"v_cvt_pk_bf16_f32 v211, v178, v179\n"
"v_mfma_f32_16x16x32_bf16 v[144:147], a[80:83], a[0:3], v[224:227]\n"
"v_exp_f32 v180, v180\n"
"v_mfma_f32_16x16x32_bf16 v[148:151], a[80:83], a[16:19], v[228:231]\n"
"v_exp_f32 v181, v181\n"
"v_mfma_f32_16x16x32_bf16 v[152:155], a[80:83], a[32:35], v[232:235]\n"
"v_exp_f32 v182, v182\n"
"v_mfma_f32_16x16x32_bf16 v[156:159], a[80:83], a[48:51], v[236:239]\n"
"ds_read_b128 a[80:83], v240 offset:12288\n"
"v_cvt_pk_bf16_f32 v212, v164, v165\n"
"v_mfma_f32_16x16x32_bf16 v[144:147], a[84:87], a[4:7], v[144:147]\n"
"v_exp_f32 v183, v183\n"
"v_mfma_f32_16x16x32_bf16 v[148:151], a[84:87], a[20:23], v[148:151]\n"
"v_exp_f32 v168, v168\n"
"v_mfma_f32_16x16x32_bf16 v[152:155], a[84:87], a[36:39], v[152:155]\n"
"v_exp_f32 v169, v169\n"
"v_mfma_f32_16x16x32_bf16 v[156:159], a[84:87], a[52:55], v[156:159]\n"
"ds_read_b128 a[84:87], v241 offset:12288\n"
"v_cvt_pk_bf16_f32 v213, v166, v167\n"
"v_mfma_f32_16x16x32_bf16 v[144:147], a[88:91], a[8:11], v[144:147]\n"
"v_exp_f32 v170, v170\n"
"v_mfma_f32_16x16x32_bf16 v[148:151], a[88:91], a[24:27], v[148:151]\n"
"v_exp_f32 v171, v171\n"
"v_mfma_f32_16x16x32_bf16 v[152:155], a[88:91], a[40:43], v[152:155]\n"
"v_exp_f32 v184, v184\n"
"v_mfma_f32_16x16x32_bf16 v[156:159], a[88:91], a[56:59], v[156:159]\n"
"ds_read_b128 a[88:91], v242 offset:12288\n"
"v_cvt_pk_bf16_f32 v214, v180, v181\n"
"v_mfma_f32_16x16x32_bf16 v[144:147], a[92:95], a[12:15], v[144:147]\n"
"v_exp_f32 v185, v185\n"
"v_mfma_f32_16x16x32_bf16 v[148:151], a[92:95], a[28:31], v[148:151]\n"
"v_exp_f32 v186, v186\n"
"v_mfma_f32_16x16x32_bf16 v[152:155], a[92:95], a[44:47], v[152:155]\n"
"v_exp_f32 v187, v187\n"
"v_mfma_f32_16x16x32_bf16 v[156:159], a[92:95], a[60:63], v[156:159]\n"
"ds_read_b128 a[92:95], v243 offset:12288\n"
"v_cvt_pk_bf16_f32 v215, v182, v183\n"
"s_waitcnt lgkmcnt(8)\n"
"v_mfma_f32_16x16x32_bf16 v[0:3], a[96:99], v[192:195], v[0:3]\n"
"v_exp_f32 v172, v172\n"
"v_mfma_f32_16x16x32_bf16 v[4:7], a[96:99], v[196:199], v[4:7]\n"
"v_exp_f32 v173, v173\n"
"v_mfma_f32_16x16x32_bf16 v[8:11], a[96:99], v[200:203], v[8:11]\n"
"v_exp_f32 v174, v174\n"
"v_mfma_f32_16x16x32_bf16 v[12:15], a[96:99], v[204:207], v[12:15]\n"
"ds_read_b64_tr_b16 a[96:97], v244 offset:40960\n"
"v_cvt_pk_bf16_f32 v216, v168, v169\n"
"v_mfma_f32_16x16x32_bf16 v[16:19], a[100:103], v[192:195], v[16:19]\n"
"ds_read_b64_tr_b16 a[98:99], v244 offset:45056\n"
"v_cvt_pk_bf16_f32 v217, v170, v171\n"
"v_mfma_f32_16x16x32_bf16 v[20:23], a[100:103], v[196:199], v[20:23]\n"
"v_exp_f32 v175, v175\n"
"v_mfma_f32_16x16x32_bf16 v[24:27], a[100:103], v[200:203], v[24:27]\n"
"v_exp_f32 v188, v188\n"
"v_mfma_f32_16x16x32_bf16 v[28:31], a[100:103], v[204:207], v[28:31]\n"
"ds_read_b64_tr_b16 a[100:101], v245 offset:40960\n"
"v_cvt_pk_bf16_f32 v218, v184, v185\n"
"v_mfma_f32_16x16x32_bf16 v[32:35], a[104:107], v[192:195], v[32:35]\n"
"ds_read_b64_tr_b16 a[102:103], v245 offset:45056\n"
"v_cvt_pk_bf16_f32 v219, v186, v187\n"
"v_mfma_f32_16x16x32_bf16 v[36:39], a[104:107], v[196:199], v[36:39]\n"
"v_exp_f32 v189, v189\n"
"v_mfma_f32_16x16x32_bf16 v[40:43], a[104:107], v[200:203], v[40:43]\n"
"v_exp_f32 v190, v190\n"
"v_mfma_f32_16x16x32_bf16 v[44:47], a[104:107], v[204:207], v[44:47]\n"
"ds_read_b64_tr_b16 a[104:105], v244 offset:41472\n"
"v_cvt_pk_bf16_f32 v220, v172, v173\n"
"v_mfma_f32_16x16x32_bf16 v[48:51], a[108:111], v[192:195], v[48:51]\n"
"ds_read_b64_tr_b16 a[106:107], v244 offset:45568\n"
"v_cvt_pk_bf16_f32 v221, v174, v175\n"
"v_mfma_f32_16x16x32_bf16 v[52:55], a[108:111], v[196:199], v[52:55]\n"
"v_exp_f32 v191, v191\n"
"v_mfma_f32_16x16x32_bf16 v[56:59], a[108:111], v[200:203], v[56:59]\n"
"v_cvt_pk_bf16_f32 v222, v188, v189\n"
"v_cvt_pk_bf16_f32 v223, v190, v191\n"
"v_mfma_f32_16x16x32_bf16 v[60:63], a[108:111], v[204:207], v[60:63]\n"
"ds_read_b64_tr_b16 a[108:109], v245 offset:41472\n"
"v_mfma_f32_16x16x32_bf16 v[64:67], a[112:115], v[192:195], v[64:67]\n"
"ds_read_b64_tr_b16 a[110:111], v245 offset:45568\n"
"v_mfma_f32_16x16x32_bf16 v[68:71], a[112:115], v[196:199], v[68:71]\n"
"v_mfma_f32_16x16x32_bf16 v[72:75], a[112:115], v[200:203], v[72:75]\n"
"v_mfma_f32_16x16x32_bf16 v[76:79], a[112:115], v[204:207], v[76:79]\n"
"ds_read_b64_tr_b16 a[112:113], v244 offset:41984\n"
"v_mfma_f32_16x16x32_bf16 v[80:83], a[116:119], v[192:195], v[80:83]\n"
"ds_read_b64_tr_b16 a[114:115], v244 offset:46080\n"
"v_mfma_f32_16x16x32_bf16 v[84:87], a[116:119], v[196:199], v[84:87]\n"
"s_mov_b32 m0, s60\n"
"v_mfma_f32_16x16x32_bf16 v[88:91], a[116:119], v[200:203], v[88:91]\n"
"buffer_load_dwordx4 v250, s[40:43], s58 offen offset:0 lds\n"
"buffer_load_dwordx4 v251, s[40:43], s58 offen offset:1024 lds\n"
"buffer_load_dwordx4 v250, s[40:43], s58 offen offset:2048 lds\n"
"buffer_load_dwordx4 v251, s[40:43], s58 offen offset:3072 lds\n"
"v_mfma_f32_16x16x32_bf16 v[92:95], a[116:119], v[204:207], v[92:95]\n"
"ds_read_b64_tr_b16 a[116:117], v245 offset:41984\n"
"v_mfma_f32_16x16x32_bf16 v[96:99], a[120:123], v[192:195], v[96:99]\n"
"ds_read_b64_tr_b16 a[118:119], v245 offset:46080\n"
"v_mfma_f32_16x16x32_bf16 v[100:103], a[120:123], v[196:199], v[100:103]\n"
"v_mfma_f32_16x16x32_bf16 v[104:107], a[120:123], v[200:203], v[104:107]\n"
"v_mfma_f32_16x16x32_bf16 v[108:111], a[120:123], v[204:207], v[108:111]\n"
"ds_read_b64_tr_b16 a[120:121], v244 offset:42496\n"
"v_mfma_f32_16x16x32_bf16 v[112:115], a[124:127], v[192:195], v[112:115]\n"
"ds_read_b64_tr_b16 a[122:123], v244 offset:46592\n"
"v_mfma_f32_16x16x32_bf16 v[116:119], a[124:127], v[196:199], v[116:119]\n"
"s_add_u32 s65, s58, 4096\n"
"s_add_u32 m0, s60, 4096\n"
"v_mfma_f32_16x16x32_bf16 v[120:123], a[124:127], v[200:203], v[120:123]\n"
"buffer_load_dwordx4 v250, s[40:43], s65 offen offset:0 lds\n"
"buffer_load_dwordx4 v251, s[40:43], s65 offen offset:1024 lds\n"
"buffer_load_dwordx4 v250, s[40:43], s65 offen offset:2048 lds\n"
"buffer_load_dwordx4 v251, s[40:43], s65 offen offset:3072 lds\n"
"v_mfma_f32_16x16x32_bf16 v[124:127], a[124:127], v[204:207], v[124:127]\n"
"ds_read_b64_tr_b16 a[124:125], v245 offset:42496\n"
"v_mfma_f32_16x16x32_bf16 a[128:131], a[144:147], v[192:195], a[128:131]\n"
"ds_read_b64_tr_b16 a[126:127], v245 offset:46592\n"
"v_mfma_f32_16x16x32_bf16 a[132:135], a[144:147], v[196:199], a[132:135]\n"
"v_mfma_f32_16x16x32_bf16 a[136:139], a[144:147], v[200:203], a[136:139]\n"
"v_mfma_f32_16x16x32_bf16 a[140:143], a[144:147], v[204:207], a[140:143]\n"
"s_waitcnt lgkmcnt(15)\n"
"v_mfma_f32_16x16x32_bf16 v[160:163], a[64:67], a[0:3], v[224:227]\n"
"v_exp_f32 v128, v128\n"
"v_mfma_f32_16x16x32_bf16 v[164:167], a[64:67], a[16:19], v[228:231]\n"
"v_exp_f32 v129, v129\n"
"v_mfma_f32_16x16x32_bf16 v[168:171], a[64:67], a[32:35], v[232:235]\n"
"v_exp_f32 v130, v130\n"
"v_mfma_f32_16x16x32_bf16 v[172:175], a[64:67], a[48:51], v[236:239]\n"
"ds_read_b128 a[64:67], v240 offset:16384\n"
"v_cvt_pk_bf16_f32 v192, v128, v129\n"
"v_mfma_f32_16x16x32_bf16 v[160:163], a[68:71], a[4:7], v[160:163]\n"
"v_exp_f32 v131, v131\n"
"v_mfma_f32_16x16x32_bf16 v[164:167], a[68:71], a[20:23], v[164:167]\n"
"v_exp_f32 v144, v144\n"
"v_mfma_f32_16x16x32_bf16 v[168:171], a[68:71], a[36:39], v[168:171]\n"
"v_exp_f32 v145, v145\n"
"v_mfma_f32_16x16x32_bf16 v[172:175], a[68:71], a[52:55], v[172:175]\n"
"ds_read_b128 a[68:71], v241 offset:16384\n"
"v_cvt_pk_bf16_f32 v193, v130, v131\n"
"v_mfma_f32_16x16x32_bf16 v[160:163], a[72:75], a[8:11], v[160:163]\n"
"v_exp_f32 v146, v146\n"
"v_mfma_f32_16x16x32_bf16 v[164:167], a[72:75], a[24:27], v[164:167]\n"
"v_exp_f32 v147, v147\n"
"v_mfma_f32_16x16x32_bf16 v[168:171], a[72:75], a[40:43], v[168:171]\n"
"v_exp_f32 v132, v132\n"
"v_mfma_f32_16x16x32_bf16 v[172:175], a[72:75], a[56:59], v[172:175]\n"
"ds_read_b128 a[72:75], v242 offset:16384\n"
"v_cvt_pk_bf16_f32 v194, v144, v145\n"
"v_mfma_f32_16x16x32_bf16 v[160:163], a[76:79], a[12:15], v[160:163]\n"
"v_exp_f32 v133, v133\n"
"v_mfma_f32_16x16x32_bf16 v[164:167], a[76:79], a[28:31], v[164:167]\n"
"v_exp_f32 v134, v134\n"
"v_mfma_f32_16x16x32_bf16 v[168:171], a[76:79], a[44:47], v[168:171]\n"
"v_exp_f32 v135, v135\n"
"v_mfma_f32_16x16x32_bf16 v[172:175], a[76:79], a[60:63], v[172:175]\n"
"ds_read_b128 a[76:79], v243 offset:16384\n"
"v_cvt_pk_bf16_f32 v195, v146, v147\n"
"v_mfma_f32_16x16x32_bf16 v[176:179], a[80:83], a[0:3], v[224:227]\n"
"v_exp_f32 v148, v148\n"
"v_mfma_f32_16x16x32_bf16 v[180:183], a[80:83], a[16:19], v[228:231]\n"
"v_exp_f32 v149, v149\n"
"v_mfma_f32_16x16x32_bf16 v[184:187], a[80:83], a[32:35], v[232:235]\n"
"v_exp_f32 v150, v150\n"
"v_mfma_f32_16x16x32_bf16 v[188:191], a[80:83], a[48:51], v[236:239]\n"
"ds_read_b128 a[80:83], v240 offset:20480\n"
"v_cvt_pk_bf16_f32 v196, v132, v133\n"
"v_mfma_f32_16x16x32_bf16 v[176:179], a[84:87], a[4:7], v[176:179]\n"
"v_exp_f32 v151, v151\n"
"v_mfma_f32_16x16x32_bf16 v[180:183], a[84:87], a[20:23], v[180:183]\n"
"v_exp_f32 v136, v136\n"
"v_mfma_f32_16x16x32_bf16 v[184:187], a[84:87], a[36:39], v[184:187]\n"
"v_exp_f32 v137, v137\n"
"v_mfma_f32_16x16x32_bf16 v[188:191], a[84:87], a[52:55], v[188:191]\n"
"ds_read_b128 a[84:87], v241 offset:20480\n"
"v_cvt_pk_bf16_f32 v197, v134, v135\n"
"v_mfma_f32_16x16x32_bf16 v[176:179], a[88:91], a[8:11], v[176:179]\n"
"v_exp_f32 v138, v138\n"
"v_mfma_f32_16x16x32_bf16 v[180:183], a[88:91], a[24:27], v[180:183]\n"
"v_exp_f32 v139, v139\n"
"v_mfma_f32_16x16x32_bf16 v[184:187], a[88:91], a[40:43], v[184:187]\n"
"v_exp_f32 v152, v152\n"
"v_mfma_f32_16x16x32_bf16 v[188:191], a[88:91], a[56:59], v[188:191]\n"
"ds_read_b128 a[88:91], v242 offset:20480\n"
"v_cvt_pk_bf16_f32 v198, v148, v149\n"
"v_mfma_f32_16x16x32_bf16 v[176:179], a[92:95], a[12:15], v[176:179]\n"
"v_exp_f32 v153, v153\n"
"v_mfma_f32_16x16x32_bf16 v[180:183], a[92:95], a[28:31], v[180:183]\n"
"v_exp_f32 v154, v154\n"
"v_mfma_f32_16x16x32_bf16 v[184:187], a[92:95], a[44:47], v[184:187]\n"
"v_exp_f32 v155, v155\n"
"v_mfma_f32_16x16x32_bf16 v[188:191], a[92:95], a[60:63], v[188:191]\n"
"ds_read_b128 a[92:95], v243 offset:20480\n"
"v_cvt_pk_bf16_f32 v199, v150, v151\n"
"s_waitcnt lgkmcnt(8)\n"
"v_mfma_f32_16x16x32_bf16 v[0:3], a[96:99], v[208:211], v[0:3]\n"
"v_exp_f32 v140, v140\n"
"v_mfma_f32_16x16x32_bf16 v[4:7], a[96:99], v[212:215], v[4:7]\n"
"v_exp_f32 v141, v141\n"
"v_mfma_f32_16x16x32_bf16 v[8:11], a[96:99], v[216:219], v[8:11]\n"
"v_exp_f32 v142, v142\n"
"v_mfma_f32_16x16x32_bf16 v[12:15], a[96:99], v[220:223], v[12:15]\n"
"ds_read_b64_tr_b16 a[96:97], v244 offset:49152\n"
"v_cvt_pk_bf16_f32 v200, v136, v137\n"
"v_mfma_f32_16x16x32_bf16 v[16:19], a[100:103], v[208:211], v[16:19]\n"
"ds_read_b64_tr_b16 a[98:99], v244 offset:53248\n"
"v_cvt_pk_bf16_f32 v201, v138, v139\n"
"v_mfma_f32_16x16x32_bf16 v[20:23], a[100:103], v[212:215], v[20:23]\n"
"v_exp_f32 v143, v143\n"
"v_mfma_f32_16x16x32_bf16 v[24:27], a[100:103], v[216:219], v[24:27]\n"
"v_exp_f32 v156, v156\n"
"v_mfma_f32_16x16x32_bf16 v[28:31], a[100:103], v[220:223], v[28:31]\n"
"ds_read_b64_tr_b16 a[100:101], v245 offset:49152\n"
"v_cvt_pk_bf16_f32 v202, v152, v153\n"
"v_mfma_f32_16x16x32_bf16 v[32:35], a[104:107], v[208:211], v[32:35]\n"
"ds_read_b64_tr_b16 a[102:103], v245 offset:53248\n"
"v_cvt_pk_bf16_f32 v203, v154, v155\n"
"v_mfma_f32_16x16x32_bf16 v[36:39], a[104:107], v[212:215], v[36:39]\n"
"v_exp_f32 v157, v157\n"
"v_mfma_f32_16x16x32_bf16 v[40:43], a[104:107], v[216:219], v[40:43]\n"
"v_exp_f32 v158, v158\n"
"v_mfma_f32_16x16x32_bf16 v[44:47], a[104:107], v[220:223], v[44:47]\n"
"ds_read_b64_tr_b16 a[104:105], v244 offset:49664\n"
"v_cvt_pk_bf16_f32 v204, v140, v141\n"
"v_mfma_f32_16x16x32_bf16 v[48:51], a[108:111], v[208:211], v[48:51]\n"
"ds_read_b64_tr_b16 a[106:107], v244 offset:53760\n"
"v_cvt_pk_bf16_f32 v205, v142, v143\n"
"v_mfma_f32_16x16x32_bf16 v[52:55], a[108:111], v[212:215], v[52:55]\n"
"v_exp_f32 v159, v159\n"
"v_mfma_f32_16x16x32_bf16 v[56:59], a[108:111], v[216:219], v[56:59]\n"
"v_cvt_pk_bf16_f32 v206, v156, v157\n"
"v_cvt_pk_bf16_f32 v207, v158, v159\n"
"v_mfma_f32_16x16x32_bf16 v[60:63], a[108:111], v[220:223], v[60:63]\n"
"ds_read_b64_tr_b16 a[108:109], v245 offset:49664\n"
"v_mfma_f32_16x16x32_bf16 v[64:67], a[112:115], v[208:211], v[64:67]\n"
"ds_read_b64_tr_b16 a[110:111], v245 offset:53760\n"
"v_mfma_f32_16x16x32_bf16 v[68:71], a[112:115], v[212:215], v[68:71]\n"
"v_mfma_f32_16x16x32_bf16 v[72:75], a[112:115], v[216:219], v[72:75]\n"
"v_mfma_f32_16x16x32_bf16 v[76:79], a[112:115], v[220:223], v[76:79]\n"
"ds_read_b64_tr_b16 a[112:113], v244 offset:50176\n"
"v_mfma_f32_16x16x32_bf16 v[80:83], a[116:119], v[208:211], v[80:83]\n"
"ds_read_b64_tr_b16 a[114:115], v244 offset:54272\n"
"v_mfma_f32_16x16x32_bf16 v[84:87], a[116:119], v[212:215], v[84:87]\n"
"v_mfma_f32_16x16x32_bf16 v[88:91], a[116:119], v[216:219], v[88:91]\n"
"v_mfma_f32_16x16x32_bf16 v[92:95], a[116:119], v[220:223], v[92:95]\n"
"ds_read_b64_tr_b16 a[116:117], v245 offset:50176\n"
"v_mfma_f32_16x16x32_bf16 v[96:99], a[120:123], v[208:211], v[96:99]\n"
"ds_read_b64_tr_b16 a[118:119], v245 offset:54272\n"
"v_mfma_f32_16x16x32_bf16 v[100:103], a[120:123], v[212:215], v[100:103]\n"
"v_mfma_f32_16x16x32_bf16 v[104:107], a[120:123], v[216:219], v[104:107]\n"
"v_mfma_f32_16x16x32_bf16 v[108:111], a[120:123], v[220:223], v[108:111]\n"
"ds_read_b64_tr_b16 a[120:121], v244 offset:50688\n"
"v_mfma_f32_16x16x32_bf16 v[112:115], a[124:127], v[208:211], v[112:115]\n"
"ds_read_b64_tr_b16 a[122:123], v244 offset:54784\n"
"v_mfma_f32_16x16x32_bf16 v[116:119], a[124:127], v[212:215], v[116:119]\n"
"v_mfma_f32_16x16x32_bf16 v[120:123], a[124:127], v[216:219], v[120:123]\n"
"v_mfma_f32_16x16x32_bf16 v[124:127], a[124:127], v[220:223], v[124:127]\n"
"ds_read_b64_tr_b16 a[124:125], v245 offset:50688\n"
"v_mfma_f32_16x16x32_bf16 a[128:131], a[144:147], v[208:211], a[128:131]\n"
"ds_read_b64_tr_b16 a[126:127], v245 offset:54784\n"
"v_mfma_f32_16x16x32_bf16 a[132:135], a[144:147], v[212:215], a[132:135]\n"
"v_mfma_f32_16x16x32_bf16 a[136:139], a[144:147], v[216:219], a[136:139]\n"
"v_mfma_f32_16x16x32_bf16 a[140:143], a[144:147], v[220:223], a[140:143]\n"
"s_waitcnt lgkmcnt(15)\n"
"v_mfma_f32_16x16x32_bf16 v[128:131], a[64:67], a[0:3], v[224:227]\n"
"v_exp_f32 v160, v160\n"
"v_mfma_f32_16x16x32_bf16 v[132:135], a[64:67], a[16:19], v[228:231]\n"
"v_exp_f32 v161, v161\n"
"v_mfma_f32_16x16x32_bf16 v[136:139], a[64:67], a[32:35], v[232:235]\n"
"v_exp_f32 v162, v162\n"
"v_mfma_f32_16x16x32_bf16 v[140:143], a[64:67], a[48:51], v[236:239]\n"
"ds_read_b128 a[64:67], v240 offset:24576\n"
"v_cvt_pk_bf16_f32 v208, v160, v161\n"
"v_mfma_f32_16x16x32_bf16 v[128:131], a[68:71], a[4:7], v[128:131]\n"
"v_exp_f32 v163, v163\n"
"v_mfma_f32_16x16x32_bf16 v[132:135], a[68:71], a[20:23], v[132:135]\n"
"v_exp_f32 v176, v176\n"
"v_mfma_f32_16x16x32_bf16 v[136:139], a[68:71], a[36:39], v[136:139]\n"
"v_exp_f32 v177, v177\n"
"v_mfma_f32_16x16x32_bf16 v[140:143], a[68:71], a[52:55], v[140:143]\n"
"ds_read_b128 a[68:71], v241 offset:24576\n"
"v_cvt_pk_bf16_f32 v209, v162, v163\n"
"v_mfma_f32_16x16x32_bf16 v[128:131], a[72:75], a[8:11], v[128:131]\n"
"v_exp_f32 v178, v178\n"
"v_mfma_f32_16x16x32_bf16 v[132:135], a[72:75], a[24:27], v[132:135]\n"
"v_exp_f32 v179, v179\n"
"v_mfma_f32_16x16x32_bf16 v[136:139], a[72:75], a[40:43], v[136:139]\n"
"v_exp_f32 v164, v164\n"
"v_mfma_f32_16x16x32_bf16 v[140:143], a[72:75], a[56:59], v[140:143]\n"
"ds_read_b128 a[72:75], v242 offset:24576\n"
"v_cvt_pk_bf16_f32 v210, v176, v177\n"
"v_mfma_f32_16x16x32_bf16 v[128:131], a[76:79], a[12:15], v[128:131]\n"
"v_exp_f32 v165, v165\n"
"v_mfma_f32_16x16x32_bf16 v[132:135], a[76:79], a[28:31], v[132:135]\n"
"v_exp_f32 v166, v166\n"
"v_mfma_f32_16x16x32_bf16 v[136:139], a[76:79], a[44:47], v[136:139]\n"
"v_exp_f32 v167, v167\n"
"v_mfma_f32_16x16x32_bf16 v[140:143], a[76:79], a[60:63], v[140:143]\n"
"ds_read_b128 a[76:79], v243 offset:24576\n"
"v_cvt_pk_bf16_f32 v211, v178, v179\n"
"v_mfma_f32_16x16x32_bf16 v[144:147], a[80:83], a[0:3], v[224:227]\n"
"v_exp_f32 v180, v180\n"
"v_mfma_f32_16x16x32_bf16 v[148:151], a[80:83], a[16:19], v[228:231]\n"
"v_exp_f32 v181, v181\n"
"v_mfma_f32_16x16x32_bf16 v[152:155], a[80:83], a[32:35], v[232:235]\n"
"v_exp_f32 v182, v182\n"
"v_mfma_f32_16x16x32_bf16 v[156:159], a[80:83], a[48:51], v[236:239]\n"
"ds_read_b128 a[80:83], v240 offset:28672\n"
"v_cvt_pk_bf16_f32 v212, v164, v165\n"
"v_mfma_f32_16x16x32_bf16 v[144:147], a[84:87], a[4:7], v[144:147]\n"
"v_exp_f32 v183, v183\n"
"v_mfma_f32_16x16x32_bf16 v[148:151], a[84:87], a[20:23], v[148:151]\n"
"v_exp_f32 v168, v168\n"
"v_mfma_f32_16x16x32_bf16 v[152:155], a[84:87], a[36:39], v[152:155]\n"
"v_exp_f32 v169, v169\n"
"v_mfma_f32_16x16x32_bf16 v[156:159], a[84:87], a[52:55], v[156:159]\n"
"ds_read_b128 a[84:87], v241 offset:28672\n"
"v_cvt_pk_bf16_f32 v213, v166, v167\n"
"v_mfma_f32_16x16x32_bf16 v[144:147], a[88:91], a[8:11], v[144:147]\n"
"v_exp_f32 v170, v170\n"
"v_mfma_f32_16x16x32_bf16 v[148:151], a[88:91], a[24:27], v[148:151]\n"
"v_exp_f32 v171, v171\n"
"v_mfma_f32_16x16x32_bf16 v[152:155], a[88:91], a[40:43], v[152:155]\n"
"v_exp_f32 v184, v184\n"
"v_mfma_f32_16x16x32_bf16 v[156:159], a[88:91], a[56:59], v[156:159]\n"
"ds_read_b128 a[88:91], v242 offset:28672\n"
"v_cvt_pk_bf16_f32 v214, v180, v181\n"
"v_mfma_f32_16x16x32_bf16 v[144:147], a[92:95], a[12:15], v[144:147]\n"
"v_exp_f32 v185, v185\n"
"v_mfma_f32_16x16x32_bf16 v[148:151], a[92:95], a[28:31], v[148:151]\n"
"v_exp_f32 v186, v186\n"
"v_mfma_f32_16x16x32_bf16 v[152:155], a[92:95], a[44:47], v[152:155]\n"
"v_exp_f32 v187, v187\n"
"v_mfma_f32_16x16x32_bf16 v[156:159], a[92:95], a[60:63], v[156:159]\n"
"ds_read_b128 a[92:95], v243 offset:28672\n"
"v_cvt_pk_bf16_f32 v215, v182, v183\n"
"s_waitcnt lgkmcnt(8)\n"
"v_mfma_f32_16x16x32_bf16 v[0:3], a[96:99], v[192:195], v[0:3]\n"
"v_exp_f32 v172, v172\n"
"v_mfma_f32_16x16x32_bf16 v[4:7], a[96:99], v[196:199], v[4:7]\n"
"v_exp_f32 v173, v173\n"
"v_mfma_f32_16x16x32_bf16 v[8:11], a[96:99], v[200:203], v[8:11]\n"
"v_exp_f32 v174, v174\n"
"v_mfma_f32_16x16x32_bf16 v[12:15], a[96:99], v[204:207], v[12:15]\n"
"ds_read_b64_tr_b16 a[96:97], v244 offset:57344\n"
"v_cvt_pk_bf16_f32 v216, v168, v169\n"
"v_mfma_f32_16x16x32_bf16 v[16:19], a[100:103], v[192:195], v[16:19]\n"
"ds_read_b64_tr_b16 a[98:99], v244 offset:61440\n"
"v_cvt_pk_bf16_f32 v217, v170, v171\n"
"v_mfma_f32_16x16x32_bf16 v[20:23], a[100:103], v[196:199], v[20:23]\n"
"v_exp_f32 v175, v175\n"
"v_mfma_f32_16x16x32_bf16 v[24:27], a[100:103], v[200:203], v[24:27]\n"
"v_exp_f32 v188, v188\n"
"v_mfma_f32_16x16x32_bf16 v[28:31], a[100:103], v[204:207], v[28:31]\n"
"ds_read_b64_tr_b16 a[100:101], v245 offset:57344\n"
"v_cvt_pk_bf16_f32 v218, v184, v185\n"
"v_mfma_f32_16x16x32_bf16 v[32:35], a[104:107], v[192:195], v[32:35]\n"
"ds_read_b64_tr_b16 a[102:103], v245 offset:61440\n"
"v_cvt_pk_bf16_f32 v219, v186, v187\n"
"v_mfma_f32_16x16x32_bf16 v[36:39], a[104:107], v[196:199], v[36:39]\n"
"v_exp_f32 v189, v189\n"
"v_mfma_f32_16x16x32_bf16 v[40:43], a[104:107], v[200:203], v[40:43]\n"
"v_exp_f32 v190, v190\n"
"v_mfma_f32_16x16x32_bf16 v[44:47], a[104:107], v[204:207], v[44:47]\n"
"ds_read_b64_tr_b16 a[104:105], v244 offset:57856\n"
"v_cvt_pk_bf16_f32 v220, v172, v173\n"
"v_mfma_f32_16x16x32_bf16 v[48:51], a[108:111], v[192:195], v[48:51]\n"
"ds_read_b64_tr_b16 a[106:107], v244 offset:61952\n"
"v_cvt_pk_bf16_f32 v221, v174, v175\n"
"v_mfma_f32_16x16x32_bf16 v[52:55], a[108:111], v[196:199], v[52:55]\n"
"v_exp_f32 v191, v191\n"
"v_mfma_f32_16x16x32_bf16 v[56:59], a[108:111], v[200:203], v[56:59]\n"
"v_cvt_pk_bf16_f32 v222, v188, v189\n"
"v_cvt_pk_bf16_f32 v223, v190, v191\n"
"v_mfma_f32_16x16x32_bf16 v[60:63], a[108:111], v[204:207], v[60:63]\n"
"ds_read_b64_tr_b16 a[108:109], v245 offset:57856\n"
"v_mfma_f32_16x16x32_bf16 v[64:67], a[112:115], v[192:195], v[64:67]\n"
"ds_read_b64_tr_b16 a[110:111], v245 offset:61952\n"
"v_mfma_f32_16x16x32_bf16 v[68:71], a[112:115], v[196:199], v[68:71]\n"
"v_mfma_f32_16x16x32_bf16 v[72:75], a[112:115], v[200:203], v[72:75]\n"
"v_mfma_f32_16x16x32_bf16 v[76:79], a[112:115], v[204:207], v[76:79]\n"
"ds_read_b64_tr_b16 a[112:113], v244 offset:58368\n"
"v_mfma_f32_16x16x32_bf16 v[80:83], a[116:119], v[192:195], v[80:83]\n"
"ds_read_b64_tr_b16 a[114:115], v244 offset:62464\n"
"v_mfma_f32_16x16x32_bf16 v[84:87], a[116:119], v[196:199], v[84:87]\n"
"v_mfma_f32_16x16x32_bf16 v[88:91], a[116:119], v[200:203], v[88:91]\n"
"v_mfma_f32_16x16x32_bf16 v[92:95], a[116:119], v[204:207], v[92:95]\n"
"ds_read_b64_tr_b16 a[116:117], v245 offset:58368\n"
"v_mfma_f32_16x16x32_bf16 v[96:99], a[120:123], v[192:195], v[96:99]\n"
"ds_read_b64_tr_b16 a[118:119], v245 offset:62464\n"
"v_mfma_f32_16x16x32_bf16 v[100:103], a[120:123], v[196:199], v[100:103]\n"
"v_mfma_f32_16x16x32_bf16 v[104:107], a[120:123], v[200:203], v[104:107]\n"
"v_mfma_f32_16x16x32_bf16 v[108:111], a[120:123], v[204:207], v[108:111]\n"
"ds_read_b64_tr_b16 a[120:121], v244 offset:58880\n"
"v_mfma_f32_16x16x32_bf16 v[112:115], a[124:127], v[192:195], v[112:115]\n"
"ds_read_b64_tr_b16 a[122:123], v244 offset:62976\n"
"s_xor_b32 s59, s59, 0x10000\n"
"v_mfma_f32_16x16x32_bf16 v[116:119], a[124:127], v[196:199], v[116:119]\n"
"s_xor_b32 s60, s60, 0x10000\n"
"v_mfma_f32_16x16x32_bf16 v[120:123], a[124:127], v[200:203], v[120:123]\n"
"s_add_u32 s57, s57, 32768\n"
"v_mfma_f32_16x16x32_bf16 v[124:127], a[124:127], v[204:207], v[124:127]\n"
"ds_read_b64_tr_b16 a[124:125], v245 offset:58880\n"
"s_add_u32 s58, s58, 32768\n"
"v_mfma_f32_16x16x32_bf16 a[128:131], a[144:147], v[192:195], a[128:131]\n"
"ds_read_b64_tr_b16 a[126:127], v245 offset:62976\n"
"v_mfma_f32_16x16x32_bf16 a[132:135], a[144:147], v[196:199], a[132:135]\n"
"v_mfma_f32_16x16x32_bf16 a[136:139], a[144:147], v[200:203], a[136:139]\n"
"v_mfma_f32_16x16x32_bf16 a[140:143], a[144:147], v[204:207], a[140:143]\n"
"s_sub_u32 s61, s61, 1\n"
"s_cmp_lg_u32 s61, 0\n"
"s_cbranch_scc1 Lat_loop\n"
"s_mov_b32 s44, %18\n"
"s_and_b32 s45, %19, 0xffff\n"
"s_mov_b32 s46, 525184\n"
"v_mbcnt_lo_u32_b32 v252, -1, 0\n"
"v_mbcnt_hi_u32_b32 v252, -1, v252\n"
"v_lshrrev_b32 v253, 4, v252\n"
"v_and_b32 v254, 1, v253\n"
"v_lshlrev_b32 v254, 1, v254\n"
"v_and_b32 v255, 3, v252\n"
"v_xor_b32 v254, v254, v255\n"
"v_lshlrev_b32 v254, 4, v254\n"
"v_lshrrev_b32 v255, 5, v252\n"
"v_lshl_add_u32 v254, v255, 6, v254\n"
"v_bfe_u32 v255, v252, 2, 3\n"
"s_mov_b32 s64, 1024\n"
"v_mul_lo_u32 v255, v255, s64\n"
"v_add_u32 v247, v255, v254\n"
"v_add_u32 v246, 896, v247\n"
"s_mul_i32 s70, s56, 2048\n"
"s_mul_i32 s71, s56, 8192\n"
"s_waitcnt lgkmcnt(15)\n"
"v_mfma_f32_16x16x32_bf16 v[160:163], a[64:67], a[0:3], v[224:227]\n"
"v_exp_f32 v128, v128\n"
"v_mfma_f32_16x16x32_bf16 v[164:167], a[64:67], a[16:19], v[228:231]\n"
"s_waitcnt vmcnt(0) lgkmcnt(0)\n"
"s_barrier\n"
"v_xor_b32 v240, 0x10000, v240\n"
"v_xor_b32 v241, 0x10000, v241\n"
"v_mfma_f32_16x16x32_bf16 v[168:171], a[64:67], a[32:35], v[232:235]\n"
"v_xor_b32 v242, 0x10000, v242\n"
"v_xor_b32 v243, 0x10000, v243\n"
"v_mfma_f32_16x16x32_bf16 v[172:175], a[64:67], a[48:51], v[236:239]\n"
"ds_read_b128 a[64:67], v240 offset:0\n"
"v_mfma_f32_16x16x32_bf16 v[160:163], a[68:71], a[4:7], v[160:163]\n"
"v_exp_f32 v129, v129\n"
"v_mfma_f32_16x16x32_bf16 v[164:167], a[68:71], a[20:23], v[164:167]\n"
"v_xor_b32 v244, 0x10000, v244\n"
"v_xor_b32 v245, 0x10000, v245\n"
"v_mfma_f32_16x16x32_bf16 v[168:171], a[68:71], a[36:39], v[168:171]\n"
"v_exp_f32 v130, v130\n"
"v_mfma_f32_16x16x32_bf16 v[172:175], a[68:71], a[52:55], v[172:175]\n"
"ds_read_b128 a[68:71], v241 offset:0\n"
"v_cvt_pk_bf16_f32 v192, v128, v129\n"
"v_mfma_f32_16x16x32_bf16 v[160:163], a[72:75], a[8:11], v[160:163]\n"
"v_exp_f32 v131, v131\n"
"v_mfma_f32_16x16x32_bf16 v[164:167], a[72:75], a[24:27], v[164:167]\n"
"v_exp_f32 v144, v144\n"
"v_mfma_f32_16x16x32_bf16 v[168:171], a[72:75], a[40:43], v[168:171]\n"
"v_exp_f32 v145, v145\n"
"v_mfma_f32_16x16x32_bf16 v[172:175], a[72:75], a[56:59], v[172:175]\n"
"ds_read_b128 a[72:75], v242 offset:0\n"
"v_cvt_pk_bf16_f32 v193, v130, v131\n"
"v_mfma_f32_16x16x32_bf16 v[160:163], a[76:79], a[12:15], v[160:163]\n"
"v_exp_f32 v146, v146\n"
"v_mfma_f32_16x16x32_bf16 v[164:167], a[76:79], a[28:31], v[164:167]\n"
"v_exp_f32 v147, v147\n"
"v_mfma_f32_16x16x32_bf16 v[168:171], a[76:79], a[44:47], v[168:171]\n"
"v_exp_f32 v132, v132\n"
"v_mfma_f32_16x16x32_bf16 v[172:175], a[76:79], a[60:63], v[172:175]\n"
"ds_read_b128 a[76:79], v243 offset:0\n"
"v_cvt_pk_bf16_f32 v194, v144, v145\n"
"v_mfma_f32_16x16x32_bf16 v[176:179], a[80:83], a[0:3], v[224:227]\n"
"v_exp_f32 v133, v133\n"
"v_mfma_f32_16x16x32_bf16 v[180:183], a[80:83], a[16:19], v[228:231]\n"
"v_exp_f32 v134, v134\n"
"v_mfma_f32_16x16x32_bf16 v[184:187], a[80:83], a[32:35], v[232:235]\n"
"v_exp_f32 v135, v135\n"
"v_mfma_f32_16x16x32_bf16 v[188:191], a[80:83], a[48:51], v[236:239]\n"
"ds_read_b128 a[80:83], v240 offset:4096\n"
"v_cvt_pk_bf16_f32 v195, v146, v147\n"
"v_mfma_f32_16x16x32_bf16 v[176:179], a[84:87], a[4:7], v[176:179]\n"
"v_exp_f32 v148, v148\n"
"v_mfma_f32_16x16x32_bf16 v[180:183], a[84:87], a[20:23], v[180:183]\n"
"v_exp_f32 v149, v149\n"
"v_mfma_f32_16x16x32_bf16 v[184:187], a[84:87], a[36:39], v[184:187]\n"
"v_exp_f32 v150, v150\n"
"v_mfma_f32_16x16x32_bf16 v[188:191], a[84:87], a[52:55], v[188:191]\n"
"ds_read_b128 a[84:87], v241 offset:4096\n"
"v_cvt_pk_bf16_f32 v196, v132, v133\n"
"v_mfma_f32_16x16x32_bf16 v[176:179], a[88:91], a[8:11], v[176:179]\n"
"v_exp_f32 v151, v151\n"
"v_mfma_f32_16x16x32_bf16 v[180:183], a[88:91], a[24:27], v[180:183]\n"
"v_exp_f32 v136, v136\n"
"v_mfma_f32_16x16x32_bf16 v[184:187], a[88:91], a[40:43], v[184:187]\n"
"v_exp_f32 v137, v137\n"
"v_mfma_f32_16x16x32_bf16 v[188:191], a[88:91], a[56:59], v[188:191]\n"
"ds_read_b128 a[88:91], v242 offset:4096\n"
"v_cvt_pk_bf16_f32 v197, v134, v135\n"
"v_mfma_f32_16x16x32_bf16 v[176:179], a[92:95], a[12:15], v[176:179]\n"
"v_exp_f32 v138, v138\n"
"v_mfma_f32_16x16x32_bf16 v[180:183], a[92:95], a[28:31], v[180:183]\n"
"v_exp_f32 v139, v139\n"
"v_mfma_f32_16x16x32_bf16 v[184:187], a[92:95], a[44:47], v[184:187]\n"
"v_exp_f32 v152, v152\n"
"v_mfma_f32_16x16x32_bf16 v[188:191], a[92:95], a[60:63], v[188:191]\n"
"ds_read_b128 a[92:95], v243 offset:4096\n"
"v_cvt_pk_bf16_f32 v198, v148, v149\n"
"s_waitcnt lgkmcnt(8)\n"
"v_mfma_f32_16x16x32_bf16 v[0:3], a[96:99], v[208:211], v[0:3]\n"
"v_exp_f32 v153, v153\n"
"v_mfma_f32_16x16x32_bf16 v[4:7], a[96:99], v[212:215], v[4:7]\n"
"v_exp_f32 v154, v154\n"
"v_mfma_f32_16x16x32_bf16 v[8:11], a[96:99], v[216:219], v[8:11]\n"
"v_exp_f32 v155, v155\n"
"v_mfma_f32_16x16x32_bf16 v[12:15], a[96:99], v[220:223], v[12:15]\n"
"ds_read_b64_tr_b16 a[96:97], v244 offset:32768\n"
"v_cvt_pk_bf16_f32 v199, v150, v151\n"
"v_mfma_f32_16x16x32_bf16 v[16:19], a[100:103], v[208:211], v[16:19]\n"
"ds_read_b64_tr_b16 a[98:99], v244 offset:36864\n"
"v_cvt_pk_bf16_f32 v200, v136, v137\n"
"v_mfma_f32_16x16x32_bf16 v[20:23], a[100:103], v[212:215], v[20:23]\n"
"v_exp_f32 v140, v140\n"
"v_mfma_f32_16x16x32_bf16 v[24:27], a[100:103], v[216:219], v[24:27]\n"
"v_exp_f32 v141, v141\n"
"v_mfma_f32_16x16x32_bf16 v[28:31], a[100:103], v[220:223], v[28:31]\n"
"ds_read_b64_tr_b16 a[100:101], v245 offset:32768\n"
"v_cvt_pk_bf16_f32 v201, v138, v139\n"
"v_mfma_f32_16x16x32_bf16 v[32:35], a[104:107], v[208:211], v[32:35]\n"
"ds_read_b64_tr_b16 a[102:103], v245 offset:36864\n"
"v_cvt_pk_bf16_f32 v202, v152, v153\n"
"v_mfma_f32_16x16x32_bf16 v[36:39], a[104:107], v[212:215], v[36:39]\n"
"v_exp_f32 v142, v142\n"
"v_mfma_f32_16x16x32_bf16 v[40:43], a[104:107], v[216:219], v[40:43]\n"
"v_exp_f32 v143, v143\n"
"v_mfma_f32_16x16x32_bf16 v[44:47], a[104:107], v[220:223], v[44:47]\n"
"ds_read_b64_tr_b16 a[104:105], v244 offset:33280\n"
"v_cvt_pk_bf16_f32 v203, v154, v155\n"
"v_mfma_f32_16x16x32_bf16 v[48:51], a[108:111], v[208:211], v[48:51]\n"
"ds_read_b64_tr_b16 a[106:107], v244 offset:37376\n"
"v_cvt_pk_bf16_f32 v204, v140, v141\n"
"v_mfma_f32_16x16x32_bf16 v[52:55], a[108:111], v[212:215], v[52:55]\n"
"v_exp_f32 v156, v156\n"
"v_mfma_f32_16x16x32_bf16 v[56:59], a[108:111], v[216:219], v[56:59]\n"
"v_exp_f32 v157, v157\n"
"v_mfma_f32_16x16x32_bf16 v[60:63], a[108:111], v[220:223], v[60:63]\n"
"ds_read_b64_tr_b16 a[108:109], v245 offset:33280\n"
"v_cvt_pk_bf16_f32 v205, v142, v143\n"
"v_mfma_f32_16x16x32_bf16 v[64:67], a[112:115], v[208:211], v[64:67]\n"
"ds_read_b64_tr_b16 a[110:111], v245 offset:37376\n"
"v_cvt_pk_bf16_f32 v206, v156, v157\n"
"v_mfma_f32_16x16x32_bf16 v[68:71], a[112:115], v[212:215], v[68:71]\n"
"v_exp_f32 v158, v158\n"
"v_mfma_f32_16x16x32_bf16 v[72:75], a[112:115], v[216:219], v[72:75]\n"
"v_exp_f32 v159, v159\n"
"v_mfma_f32_16x16x32_bf16 v[76:79], a[112:115], v[220:223], v[76:79]\n"
"ds_read_b64_tr_b16 a[112:113], v244 offset:33792\n"
"v_cvt_pk_bf16_f32 v207, v158, v159\n"
"v_mfma_f32_16x16x32_bf16 v[80:83], a[116:119], v[208:211], v[80:83]\n"
"ds_read_b64_tr_b16 a[114:115], v244 offset:37888\n"
"v_mfma_f32_16x16x32_bf16 v[84:87], a[116:119], v[212:215], v[84:87]\n"
"v_mfma_f32_16x16x32_bf16 v[88:91], a[116:119], v[216:219], v[88:91]\n"
"s_add_u32 m0, s70, 0\n"
"s_add_u32 s64, s71, 0\n"
"buffer_load_dwordx4 v246, s[44:47], s64 offen lds\n"
"buffer_load_dwordx4 v247, s[44:47], s64 offen offset:1024 lds\n"
"s_add_u32 m0, s70, 8192\n"
"s_add_u32 s64, s71, 32768\n"
"buffer_load_dwordx4 v246, s[44:47], s64 offen lds\n"
"buffer_load_dwordx4 v247, s[44:47], s64 offen offset:1024 lds\n"
"v_mfma_f32_16x16x32_bf16 v[92:95], a[116:119], v[220:223], v[92:95]\n"
"ds_read_b64_tr_b16 a[116:117], v245 offset:33792\n"
"v_mfma_f32_16x16x32_bf16 v[96:99], a[120:123], v[208:211], v[96:99]\n"
"ds_read_b64_tr_b16 a[118:119], v245 offset:37888\n"
"v_mfma_f32_16x16x32_bf16 v[100:103], a[120:123], v[212:215], v[100:103]\n"
"v_mfma_f32_16x16x32_bf16 v[104:107], a[120:123], v[216:219], v[104:107]\n"
"v_mfma_f32_16x16x32_bf16 v[108:111], a[120:123], v[220:223], v[108:111]\n"
"ds_read_b64_tr_b16 a[120:121], v244 offset:34304\n"
"v_mfma_f32_16x16x32_bf16 v[112:115], a[124:127], v[208:211], v[112:115]\n"
"ds_read_b64_tr_b16 a[122:123], v244 offset:38400\n"
"v_mfma_f32_16x16x32_bf16 v[116:119], a[124:127], v[212:215], v[116:119]\n"
"v_mfma_f32_16x16x32_bf16 v[120:123], a[124:127], v[216:219], v[120:123]\n"
"s_add_u32 m0, s70, 16384\n"
"s_add_u32 s64, s71, 65536\n"
"buffer_load_dwordx4 v246, s[44:47], s64 offen lds\n"
"buffer_load_dwordx4 v247, s[44:47], s64 offen offset:1024 lds\n"
"s_add_u32 m0, s70, 24576\n"
"s_add_u32 s64, s71, 98304\n"
"buffer_load_dwordx4 v246, s[44:47], s64 offen lds\n"
"buffer_load_dwordx4 v247, s[44:47], s64 offen offset:1024 lds\n"
"v_mfma_f32_16x16x32_bf16 v[124:127], a[124:127], v[220:223], v[124:127]\n"
"ds_read_b64_tr_b16 a[124:125], v245 offset:34304\n"
"v_mfma_f32_16x16x32_bf16 a[128:131], a[144:147], v[208:211], a[128:131]\n"
"ds_read_b64_tr_b16 a[126:127], v245 offset:38400\n"
"v_mfma_f32_16x16x32_bf16 a[132:135], a[144:147], v[212:215], a[132:135]\n"
"v_mfma_f32_16x16x32_bf16 a[136:139], a[144:147], v[216:219], a[136:139]\n"
"v_mfma_f32_16x16x32_bf16 a[140:143], a[144:147], v[220:223], a[140:143]\n"
"s_waitcnt lgkmcnt(15)\n"
"v_mfma_f32_16x16x32_bf16 v[128:131], a[64:67], a[0:3], v[224:227]\n"
"v_exp_f32 v160, v160\n"
"v_mfma_f32_16x16x32_bf16 v[132:135], a[64:67], a[16:19], v[228:231]\n"
"v_exp_f32 v161, v161\n"
"v_mfma_f32_16x16x32_bf16 v[136:139], a[64:67], a[32:35], v[232:235]\n"
"v_exp_f32 v162, v162\n"
"v_mfma_f32_16x16x32_bf16 v[140:143], a[64:67], a[48:51], v[236:239]\n"
"ds_read_b128 a[64:67], v240 offset:8192\n"
"v_cvt_pk_bf16_f32 v208, v160, v161\n"
"v_mfma_f32_16x16x32_bf16 v[128:131], a[68:71], a[4:7], v[128:131]\n"
"v_exp_f32 v163, v163\n"
"v_mfma_f32_16x16x32_bf16 v[132:135], a[68:71], a[20:23], v[132:135]\n"
"v_exp_f32 v176, v176\n"
"v_mfma_f32_16x16x32_bf16 v[136:139], a[68:71], a[36:39], v[136:139]\n"
"v_exp_f32 v177, v177\n"
"v_mfma_f32_16x16x32_bf16 v[140:143], a[68:71], a[52:55], v[140:143]\n"
"ds_read_b128 a[68:71], v241 offset:8192\n"
"v_cvt_pk_bf16_f32 v209, v162, v163\n"
"v_mfma_f32_16x16x32_bf16 v[128:131], a[72:75], a[8:11], v[128:131]\n"
"v_exp_f32 v178, v178\n"
"v_mfma_f32_16x16x32_bf16 v[132:135], a[72:75], a[24:27], v[132:135]\n"
"v_exp_f32 v179, v179\n"
"v_mfma_f32_16x16x32_bf16 v[136:139], a[72:75], a[40:43], v[136:139]\n"
"v_exp_f32 v164, v164\n"
"v_mfma_f32_16x16x32_bf16 v[140:143], a[72:75], a[56:59], v[140:143]\n"
"ds_read_b128 a[72:75], v242 offset:8192\n"
"v_cvt_pk_bf16_f32 v210, v176, v177\n"
"v_mfma_f32_16x16x32_bf16 v[128:131], a[76:79], a[12:15], v[128:131]\n"
"v_exp_f32 v165, v165\n"
"v_mfma_f32_16x16x32_bf16 v[132:135], a[76:79], a[28:31], v[132:135]\n"
"v_exp_f32 v166, v166\n"
"v_mfma_f32_16x16x32_bf16 v[136:139], a[76:79], a[44:47], v[136:139]\n"
"v_exp_f32 v167, v167\n"
"v_mfma_f32_16x16x32_bf16 v[140:143], a[76:79], a[60:63], v[140:143]\n"
"ds_read_b128 a[76:79], v243 offset:8192\n"
"v_cvt_pk_bf16_f32 v211, v178, v179\n"
"v_mfma_f32_16x16x32_bf16 v[144:147], a[80:83], a[0:3], v[224:227]\n"
"v_exp_f32 v180, v180\n"
"v_mfma_f32_16x16x32_bf16 v[148:151], a[80:83], a[16:19], v[228:231]\n"
"v_exp_f32 v181, v181\n"
"v_mfma_f32_16x16x32_bf16 v[152:155], a[80:83], a[32:35], v[232:235]\n"
"v_exp_f32 v182, v182\n"
"v_mfma_f32_16x16x32_bf16 v[156:159], a[80:83], a[48:51], v[236:239]\n"
"ds_read_b128 a[80:83], v240 offset:12288\n"
"v_cvt_pk_bf16_f32 v212, v164, v165\n"
"v_mfma_f32_16x16x32_bf16 v[144:147], a[84:87], a[4:7], v[144:147]\n"
"v_exp_f32 v183, v183\n"
"v_mfma_f32_16x16x32_bf16 v[148:151], a[84:87], a[20:23], v[148:151]\n"
"v_exp_f32 v168, v168\n"
"v_mfma_f32_16x16x32_bf16 v[152:155], a[84:87], a[36:39], v[152:155]\n"
"v_exp_f32 v169, v169\n"
"v_mfma_f32_16x16x32_bf16 v[156:159], a[84:87], a[52:55], v[156:159]\n"
"ds_read_b128 a[84:87], v241 offset:12288\n"
"v_cvt_pk_bf16_f32 v213, v166, v167\n"
"v_mfma_f32_16x16x32_bf16 v[144:147], a[88:91], a[8:11], v[144:147]\n"
"v_exp_f32 v170, v170\n"
"v_mfma_f32_16x16x32_bf16 v[148:151], a[88:91], a[24:27], v[148:151]\n"
"v_exp_f32 v171, v171\n"
"v_mfma_f32_16x16x32_bf16 v[152:155], a[88:91], a[40:43], v[152:155]\n"
"v_exp_f32 v184, v184\n"
"v_mfma_f32_16x16x32_bf16 v[156:159], a[88:91], a[56:59], v[156:159]\n"
"ds_read_b128 a[88:91], v242 offset:12288\n"
"v_cvt_pk_bf16_f32 v214, v180, v181\n"
"v_mfma_f32_16x16x32_bf16 v[144:147], a[92:95], a[12:15], v[144:147]\n"
"v_exp_f32 v185, v185\n"
"v_mfma_f32_16x16x32_bf16 v[148:151], a[92:95], a[28:31], v[148:151]\n"
"v_exp_f32 v186, v186\n"
"v_mfma_f32_16x16x32_bf16 v[152:155], a[92:95], a[44:47], v[152:155]\n"
"v_exp_f32 v187, v187\n"
"v_mfma_f32_16x16x32_bf16 v[156:159], a[92:95], a[60:63], v[156:159]\n"
"ds_read_b128 a[92:95], v243 offset:12288\n"
"v_cvt_pk_bf16_f32 v215, v182, v183\n"
"s_waitcnt lgkmcnt(8)\n"
"v_mfma_f32_16x16x32_bf16 v[0:3], a[96:99], v[192:195], v[0:3]\n"
"v_exp_f32 v172, v172\n"
"v_mfma_f32_16x16x32_bf16 v[4:7], a[96:99], v[196:199], v[4:7]\n"
"v_exp_f32 v173, v173\n"
"v_mfma_f32_16x16x32_bf16 v[8:11], a[96:99], v[200:203], v[8:11]\n"
"v_exp_f32 v174, v174\n"
"v_mfma_f32_16x16x32_bf16 v[12:15], a[96:99], v[204:207], v[12:15]\n"
"ds_read_b64_tr_b16 a[96:97], v244 offset:40960\n"
"v_cvt_pk_bf16_f32 v216, v168, v169\n"
"v_mfma_f32_16x16x32_bf16 v[16:19], a[100:103], v[192:195], v[16:19]\n"
"ds_read_b64_tr_b16 a[98:99], v244 offset:45056\n"
"v_cvt_pk_bf16_f32 v217, v170, v171\n"
"v_mfma_f32_16x16x32_bf16 v[20:23], a[100:103], v[196:199], v[20:23]\n"
"v_exp_f32 v175, v175\n"
"v_mfma_f32_16x16x32_bf16 v[24:27], a[100:103], v[200:203], v[24:27]\n"
"v_exp_f32 v188, v188\n"
"v_mfma_f32_16x16x32_bf16 v[28:31], a[100:103], v[204:207], v[28:31]\n"
"ds_read_b64_tr_b16 a[100:101], v245 offset:40960\n"
"v_cvt_pk_bf16_f32 v218, v184, v185\n"
"v_mfma_f32_16x16x32_bf16 v[32:35], a[104:107], v[192:195], v[32:35]\n"
"ds_read_b64_tr_b16 a[102:103], v245 offset:45056\n"
"v_cvt_pk_bf16_f32 v219, v186, v187\n"
"v_mfma_f32_16x16x32_bf16 v[36:39], a[104:107], v[196:199], v[36:39]\n"
"v_exp_f32 v189, v189\n"
"v_mfma_f32_16x16x32_bf16 v[40:43], a[104:107], v[200:203], v[40:43]\n"
"v_exp_f32 v190, v190\n"
"v_mfma_f32_16x16x32_bf16 v[44:47], a[104:107], v[204:207], v[44:47]\n"
"ds_read_b64_tr_b16 a[104:105], v244 offset:41472\n"
"v_cvt_pk_bf16_f32 v220, v172, v173\n"
"v_mfma_f32_16x16x32_bf16 v[48:51], a[108:111], v[192:195], v[48:51]\n"
"ds_read_b64_tr_b16 a[106:107], v244 offset:45568\n"
"v_cvt_pk_bf16_f32 v221, v174, v175\n"
"v_mfma_f32_16x16x32_bf16 v[52:55], a[108:111], v[196:199], v[52:55]\n"
"v_exp_f32 v191, v191\n"
"v_mfma_f32_16x16x32_bf16 v[56:59], a[108:111], v[200:203], v[56:59]\n"
"v_cvt_pk_bf16_f32 v222, v188, v189\n"
"v_cvt_pk_bf16_f32 v223, v190, v191\n"
"v_mfma_f32_16x16x32_bf16 v[60:63], a[108:111], v[204:207], v[60:63]\n"
"ds_read_b64_tr_b16 a[108:109], v245 offset:41472\n"
"v_mfma_f32_16x16x32_bf16 v[64:67], a[112:115], v[192:195], v[64:67]\n"
"ds_read_b64_tr_b16 a[110:111], v245 offset:45568\n"
"v_mfma_f32_16x16x32_bf16 v[68:71], a[112:115], v[196:199], v[68:71]\n"
"v_mfma_f32_16x16x32_bf16 v[72:75], a[112:115], v[200:203], v[72:75]\n"
"v_mfma_f32_16x16x32_bf16 v[76:79], a[112:115], v[204:207], v[76:79]\n"
"ds_read_b64_tr_b16 a[112:113], v244 offset:41984\n"
"v_mfma_f32_16x16x32_bf16 v[80:83], a[116:119], v[192:195], v[80:83]\n"
"ds_read_b64_tr_b16 a[114:115], v244 offset:46080\n"
"v_mfma_f32_16x16x32_bf16 v[84:87], a[116:119], v[196:199], v[84:87]\n"
"v_mfma_f32_16x16x32_bf16 v[88:91], a[116:119], v[200:203], v[88:91]\n"
"s_add_u32 m0, s70, 32768\n"
"s_add_u32 s64, s71, 131072\n"
"buffer_load_dwordx4 v246, s[44:47], s64 offen lds\n"
"buffer_load_dwordx4 v247, s[44:47], s64 offen offset:1024 lds\n"
"s_add_u32 m0, s70, 40960\n"
"s_add_u32 s64, s71, 163840\n"
"buffer_load_dwordx4 v246, s[44:47], s64 offen lds\n"
"buffer_load_dwordx4 v247, s[44:47], s64 offen offset:1024 lds\n"
"v_mfma_f32_16x16x32_bf16 v[92:95], a[116:119], v[204:207], v[92:95]\n"
"ds_read_b64_tr_b16 a[116:117], v245 offset:41984\n"
"v_mfma_f32_16x16x32_bf16 v[96:99], a[120:123], v[192:195], v[96:99]\n"
"ds_read_b64_tr_b16 a[118:119], v245 offset:46080\n"
"v_mfma_f32_16x16x32_bf16 v[100:103], a[120:123], v[196:199], v[100:103]\n"
"v_mfma_f32_16x16x32_bf16 v[104:107], a[120:123], v[200:203], v[104:107]\n"
"v_mfma_f32_16x16x32_bf16 v[108:111], a[120:123], v[204:207], v[108:111]\n"
"ds_read_b64_tr_b16 a[120:121], v244 offset:42496\n"
"v_mfma_f32_16x16x32_bf16 v[112:115], a[124:127], v[192:195], v[112:115]\n"
"ds_read_b64_tr_b16 a[122:123], v244 offset:46592\n"
"v_mfma_f32_16x16x32_bf16 v[116:119], a[124:127], v[196:199], v[116:119]\n"
"v_mfma_f32_16x16x32_bf16 v[120:123], a[124:127], v[200:203], v[120:123]\n"
"s_add_u32 m0, s70, 49152\n"
"s_add_u32 s64, s71, 196608\n"
"buffer_load_dwordx4 v246, s[44:47], s64 offen lds\n"
"buffer_load_dwordx4 v247, s[44:47], s64 offen offset:1024 lds\n"
"s_add_u32 m0, s70, 57344\n"
"s_add_u32 s64, s71, 229376\n"
"buffer_load_dwordx4 v246, s[44:47], s64 offen lds\n"
"buffer_load_dwordx4 v247, s[44:47], s64 offen offset:1024 lds\n"
"v_mfma_f32_16x16x32_bf16 v[124:127], a[124:127], v[204:207], v[124:127]\n"
"ds_read_b64_tr_b16 a[124:125], v245 offset:42496\n"
"v_mfma_f32_16x16x32_bf16 a[128:131], a[144:147], v[192:195], a[128:131]\n"
"ds_read_b64_tr_b16 a[126:127], v245 offset:46592\n"
"v_mfma_f32_16x16x32_bf16 a[132:135], a[144:147], v[196:199], a[132:135]\n"
"v_mfma_f32_16x16x32_bf16 a[136:139], a[144:147], v[200:203], a[136:139]\n"
"v_mfma_f32_16x16x32_bf16 a[140:143], a[144:147], v[204:207], a[140:143]\n"
"s_waitcnt lgkmcnt(15)\n"
"v_mfma_f32_16x16x32_bf16 v[160:163], a[64:67], a[0:3], v[224:227]\n"
"v_exp_f32 v128, v128\n"
"v_mfma_f32_16x16x32_bf16 v[164:167], a[64:67], a[16:19], v[228:231]\n"
"v_exp_f32 v129, v129\n"
"v_mfma_f32_16x16x32_bf16 v[168:171], a[64:67], a[32:35], v[232:235]\n"
"v_exp_f32 v130, v130\n"
"v_mfma_f32_16x16x32_bf16 v[172:175], a[64:67], a[48:51], v[236:239]\n"
"v_exp_f32 v131, v131\n"
"v_mfma_f32_16x16x32_bf16 v[160:163], a[68:71], a[4:7], v[160:163]\n"
"v_exp_f32 v144, v144\n"
"v_mfma_f32_16x16x32_bf16 v[164:167], a[68:71], a[20:23], v[164:167]\n"
"v_exp_f32 v145, v145\n"
"v_mfma_f32_16x16x32_bf16 v[168:171], a[68:71], a[36:39], v[168:171]\n"
"v_exp_f32 v146, v146\n"
"v_mfma_f32_16x16x32_bf16 v[172:175], a[68:71], a[52:55], v[172:175]\n"
"v_exp_f32 v147, v147\n"
"v_mfma_f32_16x16x32_bf16 v[160:163], a[72:75], a[8:11], v[160:163]\n"
"v_exp_f32 v132, v132\n"
"v_mfma_f32_16x16x32_bf16 v[164:167], a[72:75], a[24:27], v[164:167]\n"
"v_exp_f32 v133, v133\n"
"v_mfma_f32_16x16x32_bf16 v[168:171], a[72:75], a[40:43], v[168:171]\n"
"v_exp_f32 v134, v134\n"
"v_mfma_f32_16x16x32_bf16 v[172:175], a[72:75], a[56:59], v[172:175]\n"
"v_exp_f32 v135, v135\n"
"v_mfma_f32_16x16x32_bf16 v[160:163], a[76:79], a[12:15], v[160:163]\n"
"v_exp_f32 v148, v148\n"
"v_mfma_f32_16x16x32_bf16 v[164:167], a[76:79], a[28:31], v[164:167]\n"
"v_exp_f32 v149, v149\n"
"v_mfma_f32_16x16x32_bf16 v[168:171], a[76:79], a[44:47], v[168:171]\n"
"v_exp_f32 v150, v150\n"
"v_mfma_f32_16x16x32_bf16 v[172:175], a[76:79], a[60:63], v[172:175]\n"
"v_exp_f32 v151, v151\n"
"v_mfma_f32_16x16x32_bf16 v[176:179], a[80:83], a[0:3], v[224:227]\n"
"v_exp_f32 v136, v136\n"
"v_mfma_f32_16x16x32_bf16 v[180:183], a[80:83], a[16:19], v[228:231]\n"
"v_exp_f32 v137, v137\n"
"v_mfma_f32_16x16x32_bf16 v[184:187], a[80:83], a[32:35], v[232:235]\n"
"v_exp_f32 v138, v138\n"
"v_mfma_f32_16x16x32_bf16 v[188:191], a[80:83], a[48:51], v[236:239]\n"
"v_exp_f32 v139, v139\n"
"v_mfma_f32_16x16x32_bf16 v[176:179], a[84:87], a[4:7], v[176:179]\n"
"v_exp_f32 v152, v152\n"
"v_mfma_f32_16x16x32_bf16 v[180:183], a[84:87], a[20:23], v[180:183]\n"
"v_exp_f32 v153, v153\n"
"v_mfma_f32_16x16x32_bf16 v[184:187], a[84:87], a[36:39], v[184:187]\n"
"v_exp_f32 v154, v154\n"
"v_mfma_f32_16x16x32_bf16 v[188:191], a[84:87], a[52:55], v[188:191]\n"
"v_exp_f32 v155, v155\n"
"v_mfma_f32_16x16x32_bf16 v[176:179], a[88:91], a[8:11], v[176:179]\n"
"v_exp_f32 v140, v140\n"
"v_mfma_f32_16x16x32_bf16 v[180:183], a[88:91], a[24:27], v[180:183]\n"
"v_exp_f32 v141, v141\n"
"v_mfma_f32_16x16x32_bf16 v[184:187], a[88:91], a[40:43], v[184:187]\n"
"v_exp_f32 v142, v142\n"
"v_mfma_f32_16x16x32_bf16 v[188:191], a[88:91], a[56:59], v[188:191]\n"
"v_exp_f32 v143, v143\n"
"v_mfma_f32_16x16x32_bf16 v[176:179], a[92:95], a[12:15], v[176:179]\n"
"v_exp_f32 v156, v156\n"
"v_mfma_f32_16x16x32_bf16 v[180:183], a[92:95], a[28:31], v[180:183]\n"
"v_exp_f32 v157, v157\n"
"v_mfma_f32_16x16x32_bf16 v[184:187], a[92:95], a[44:47], v[184:187]\n"
"v_exp_f32 v158, v158\n"
"v_mfma_f32_16x16x32_bf16 v[188:191], a[92:95], a[60:63], v[188:191]\n"
"v_exp_f32 v159, v159\n"
"s_waitcnt lgkmcnt(0)\n"
"v_mfma_f32_16x16x32_bf16 v[0:3], a[96:99], v[208:211], v[0:3]\n"
"v_cvt_pk_bf16_f32 v192, v128, v129\n"
"v_cvt_pk_bf16_f32 v193, v130, v131\n"
"v_mfma_f32_16x16x32_bf16 v[4:7], a[96:99], v[212:215], v[4:7]\n"
"v_cvt_pk_bf16_f32 v194, v144, v145\n"
"v_cvt_pk_bf16_f32 v195, v146, v147\n"
"v_mfma_f32_16x16x32_bf16 v[8:11], a[96:99], v[216:219], v[8:11]\n"
"v_cvt_pk_bf16_f32 v196, v132, v133\n"
"v_cvt_pk_bf16_f32 v197, v134, v135\n"
"v_mfma_f32_16x16x32_bf16 v[12:15], a[96:99], v[220:223], v[12:15]\n"
"ds_read_b64_tr_b16 a[96:97], v244 offset:49152\n"
"v_cvt_pk_bf16_f32 v198, v148, v149\n"
"v_mfma_f32_16x16x32_bf16 v[16:19], a[100:103], v[208:211], v[16:19]\n"
"ds_read_b64_tr_b16 a[98:99], v244 offset:53248\n"
"v_cvt_pk_bf16_f32 v199, v150, v151\n"
"v_mfma_f32_16x16x32_bf16 v[20:23], a[100:103], v[212:215], v[20:23]\n"
"v_cvt_pk_bf16_f32 v200, v136, v137\n"
"v_cvt_pk_bf16_f32 v201, v138, v139\n"
"v_mfma_f32_16x16x32_bf16 v[24:27], a[100:103], v[216:219], v[24:27]\n"
"v_cvt_pk_bf16_f32 v202, v152, v153\n"
"v_cvt_pk_bf16_f32 v203, v154, v155\n"
"v_mfma_f32_16x16x32_bf16 v[28:31], a[100:103], v[220:223], v[28:31]\n"
"ds_read_b64_tr_b16 a[100:101], v245 offset:49152\n"
"v_cvt_pk_bf16_f32 v204, v140, v141\n"
"v_mfma_f32_16x16x32_bf16 v[32:35], a[104:107], v[208:211], v[32:35]\n"
"ds_read_b64_tr_b16 a[102:103], v245 offset:53248\n"
"v_cvt_pk_bf16_f32 v205, v142, v143\n"
"v_mfma_f32_16x16x32_bf16 v[36:39], a[104:107], v[212:215], v[36:39]\n"
"v_cvt_pk_bf16_f32 v206, v156, v157\n"
"v_cvt_pk_bf16_f32 v207, v158, v159\n"
"v_mfma_f32_16x16x32_bf16 v[40:43], a[104:107], v[216:219], v[40:43]\n"
"v_mfma_f32_16x16x32_bf16 v[44:47], a[104:107], v[220:223], v[44:47]\n"
"ds_read_b64_tr_b16 a[104:105], v244 offset:49664\n"
"v_mfma_f32_16x16x32_bf16 v[48:51], a[108:111], v[208:211], v[48:51]\n"
"ds_read_b64_tr_b16 a[106:107], v244 offset:53760\n"
"v_mfma_f32_16x16x32_bf16 v[52:55], a[108:111], v[212:215], v[52:55]\n"
"v_mfma_f32_16x16x32_bf16 v[56:59], a[108:111], v[216:219], v[56:59]\n"
"v_mfma_f32_16x16x32_bf16 v[60:63], a[108:111], v[220:223], v[60:63]\n"
"ds_read_b64_tr_b16 a[108:109], v245 offset:49664\n"
"v_mfma_f32_16x16x32_bf16 v[64:67], a[112:115], v[208:211], v[64:67]\n"
"ds_read_b64_tr_b16 a[110:111], v245 offset:53760\n"
"v_mfma_f32_16x16x32_bf16 v[68:71], a[112:115], v[212:215], v[68:71]\n"
"v_mfma_f32_16x16x32_bf16 v[72:75], a[112:115], v[216:219], v[72:75]\n"
"v_mfma_f32_16x16x32_bf16 v[76:79], a[112:115], v[220:223], v[76:79]\n"
"ds_read_b64_tr_b16 a[112:113], v244 offset:50176\n"
"v_mfma_f32_16x16x32_bf16 v[80:83], a[116:119], v[208:211], v[80:83]\n"
"ds_read_b64_tr_b16 a[114:115], v244 offset:54272\n"
"v_mfma_f32_16x16x32_bf16 v[84:87], a[116:119], v[212:215], v[84:87]\n"
"v_mfma_f32_16x16x32_bf16 v[88:91], a[116:119], v[216:219], v[88:91]\n"
"v_mfma_f32_16x16x32_bf16 v[92:95], a[116:119], v[220:223], v[92:95]\n"
"ds_read_b64_tr_b16 a[116:117], v245 offset:50176\n"
"v_mfma_f32_16x16x32_bf16 v[96:99], a[120:123], v[208:211], v[96:99]\n"
"ds_read_b64_tr_b16 a[118:119], v245 offset:54272\n"
"v_mfma_f32_16x16x32_bf16 v[100:103], a[120:123], v[212:215], v[100:103]\n"
"v_mfma_f32_16x16x32_bf16 v[104:107], a[120:123], v[216:219], v[104:107]\n"
"v_mfma_f32_16x16x32_bf16 v[108:111], a[120:123], v[220:223], v[108:111]\n"
"ds_read_b64_tr_b16 a[120:121], v244 offset:50688\n"
"v_mfma_f32_16x16x32_bf16 v[112:115], a[124:127], v[208:211], v[112:115]\n"
"ds_read_b64_tr_b16 a[122:123], v244 offset:54784\n"
"v_mfma_f32_16x16x32_bf16 v[116:119], a[124:127], v[212:215], v[116:119]\n"
"v_mfma_f32_16x16x32_bf16 v[120:123], a[124:127], v[216:219], v[120:123]\n"
"v_mfma_f32_16x16x32_bf16 v[124:127], a[124:127], v[220:223], v[124:127]\n"
"ds_read_b64_tr_b16 a[124:125], v245 offset:50688\n"
"v_mfma_f32_16x16x32_bf16 a[128:131], a[144:147], v[208:211], a[128:131]\n"
"ds_read_b64_tr_b16 a[126:127], v245 offset:54784\n"
"v_mfma_f32_16x16x32_bf16 a[132:135], a[144:147], v[212:215], a[132:135]\n"
"v_mfma_f32_16x16x32_bf16 a[136:139], a[144:147], v[216:219], a[136:139]\n"
"v_mfma_f32_16x16x32_bf16 a[140:143], a[144:147], v[220:223], a[140:143]\n"
"s_waitcnt lgkmcnt(0)\n"
"v_mfma_f32_16x16x32_bf16 v[0:3], a[96:99], v[192:195], v[0:3]\n"
"v_exp_f32 v160, v160\n"
"v_mfma_f32_16x16x32_bf16 v[4:7], a[96:99], v[196:199], v[4:7]\n"
"v_exp_f32 v161, v161\n"
"v_mfma_f32_16x16x32_bf16 v[8:11], a[96:99], v[200:203], v[8:11]\n"
"v_exp_f32 v162, v162\n"
"v_mfma_f32_16x16x32_bf16 v[12:15], a[96:99], v[204:207], v[12:15]\n"
"ds_read_b64_tr_b16 a[96:97], v244 offset:57344\n"
"v_cvt_pk_bf16_f32 v208, v160, v161\n"
"v_mfma_f32_16x16x32_bf16 v[16:19], a[100:103], v[192:195], v[16:19]\n"
"ds_read_b64_tr_b16 a[98:99], v244 offset:61440\n"
"v_mfma_f32_16x16x32_bf16 v[20:23], a[100:103], v[196:199], v[20:23]\n"
"v_exp_f32 v163, v163\n"
"v_mfma_f32_16x16x32_bf16 v[24:27], a[100:103], v[200:203], v[24:27]\n"
"v_exp_f32 v176, v176\n"
"v_mfma_f32_16x16x32_bf16 v[28:31], a[100:103], v[204:207], v[28:31]\n"
"ds_read_b64_tr_b16 a[100:101], v245 offset:57344\n"
"v_cvt_pk_bf16_f32 v209, v162, v163\n"
"v_mfma_f32_16x16x32_bf16 v[32:35], a[104:107], v[192:195], v[32:35]\n"
"ds_read_b64_tr_b16 a[102:103], v245 offset:61440\n"
"v_mfma_f32_16x16x32_bf16 v[36:39], a[104:107], v[196:199], v[36:39]\n"
"v_exp_f32 v177, v177\n"
"v_mfma_f32_16x16x32_bf16 v[40:43], a[104:107], v[200:203], v[40:43]\n"
"v_exp_f32 v178, v178\n"
"v_mfma_f32_16x16x32_bf16 v[44:47], a[104:107], v[204:207], v[44:47]\n"
"ds_read_b64_tr_b16 a[104:105], v244 offset:57856\n"
"v_cvt_pk_bf16_f32 v210, v176, v177\n"
"v_mfma_f32_16x16x32_bf16 v[48:51], a[108:111], v[192:195], v[48:51]\n"
"ds_read_b64_tr_b16 a[106:107], v244 offset:61952\n"
"v_mfma_f32_16x16x32_bf16 v[52:55], a[108:111], v[196:199], v[52:55]\n"
"v_exp_f32 v179, v179\n"
"v_mfma_f32_16x16x32_bf16 v[56:59], a[108:111], v[200:203], v[56:59]\n"
"v_exp_f32 v164, v164\n"
"v_mfma_f32_16x16x32_bf16 v[60:63], a[108:111], v[204:207], v[60:63]\n"
"ds_read_b64_tr_b16 a[108:109], v245 offset:57856\n"
"v_cvt_pk_bf16_f32 v211, v178, v179\n"
"v_mfma_f32_16x16x32_bf16 v[64:67], a[112:115], v[192:195], v[64:67]\n"
"ds_read_b64_tr_b16 a[110:111], v245 offset:61952\n"
"v_mfma_f32_16x16x32_bf16 v[68:71], a[112:115], v[196:199], v[68:71]\n"
"v_exp_f32 v165, v165\n"
"v_mfma_f32_16x16x32_bf16 v[72:75], a[112:115], v[200:203], v[72:75]\n"
"v_exp_f32 v166, v166\n"
"v_mfma_f32_16x16x32_bf16 v[76:79], a[112:115], v[204:207], v[76:79]\n"
"ds_read_b64_tr_b16 a[112:113], v244 offset:58368\n"
"v_cvt_pk_bf16_f32 v212, v164, v165\n"
"v_mfma_f32_16x16x32_bf16 v[80:83], a[116:119], v[192:195], v[80:83]\n"
"ds_read_b64_tr_b16 a[114:115], v244 offset:62464\n"
"v_mfma_f32_16x16x32_bf16 v[84:87], a[116:119], v[196:199], v[84:87]\n"
"v_exp_f32 v167, v167\n"
"v_mfma_f32_16x16x32_bf16 v[88:91], a[116:119], v[200:203], v[88:91]\n"
"v_exp_f32 v180, v180\n"
"v_mfma_f32_16x16x32_bf16 v[92:95], a[116:119], v[204:207], v[92:95]\n"
"ds_read_b64_tr_b16 a[116:117], v245 offset:58368\n"
"v_cvt_pk_bf16_f32 v213, v166, v167\n"
"v_mfma_f32_16x16x32_bf16 v[96:99], a[120:123], v[192:195], v[96:99]\n"
"ds_read_b64_tr_b16 a[118:119], v245 offset:62464\n"
"v_mfma_f32_16x16x32_bf16 v[100:103], a[120:123], v[196:199], v[100:103]\n"
"v_exp_f32 v181, v181\n"
"v_mfma_f32_16x16x32_bf16 v[104:107], a[120:123], v[200:203], v[104:107]\n"
"v_exp_f32 v182, v182\n"
"v_mfma_f32_16x16x32_bf16 v[108:111], a[120:123], v[204:207], v[108:111]\n"
"ds_read_b64_tr_b16 a[120:121], v244 offset:58880\n"
"v_cvt_pk_bf16_f32 v214, v180, v181\n"
"v_mfma_f32_16x16x32_bf16 v[112:115], a[124:127], v[192:195], v[112:115]\n"
"ds_read_b64_tr_b16 a[122:123], v244 offset:62976\n"
"v_mfma_f32_16x16x32_bf16 v[116:119], a[124:127], v[196:199], v[116:119]\n"
"v_exp_f32 v183, v183\n"
"v_mfma_f32_16x16x32_bf16 v[120:123], a[124:127], v[200:203], v[120:123]\n"
"v_exp_f32 v168, v168\n"
"v_mfma_f32_16x16x32_bf16 v[124:127], a[124:127], v[204:207], v[124:127]\n"
"ds_read_b64_tr_b16 a[124:125], v245 offset:58880\n"
"v_cvt_pk_bf16_f32 v215, v182, v183\n"
"v_mfma_f32_16x16x32_bf16 a[128:131], a[144:147], v[192:195], a[128:131]\n"
"ds_read_b64_tr_b16 a[126:127], v245 offset:62976\n"
"v_mfma_f32_16x16x32_bf16 a[132:135], a[144:147], v[196:199], a[132:135]\n"
"v_exp_f32 v169, v169\n"
"v_mfma_f32_16x16x32_bf16 a[136:139], a[144:147], v[200:203], a[136:139]\n"
"v_exp_f32 v170, v170\n"
"v_mfma_f32_16x16x32_bf16 a[140:143], a[144:147], v[204:207], a[140:143]\n"
"v_exp_f32 v171, v171\n"
"v_exp_f32 v184, v184\n"
"v_exp_f32 v185, v185\n"
"v_exp_f32 v186, v186\n"
"v_exp_f32 v187, v187\n"
"v_exp_f32 v172, v172\n"
"v_exp_f32 v173, v173\n"
"v_exp_f32 v174, v174\n"
"v_exp_f32 v175, v175\n"
"v_exp_f32 v188, v188\n"
"v_exp_f32 v189, v189\n"
"v_exp_f32 v190, v190\n"
"v_exp_f32 v191, v191\n"
"s_nop 0\n"
"v_cvt_pk_bf16_f32 v216, v168, v169\n"
"v_cvt_pk_bf16_f32 v217, v170, v171\n"
"v_cvt_pk_bf16_f32 v218, v184, v185\n"
"v_cvt_pk_bf16_f32 v219, v186, v187\n"
"v_cvt_pk_bf16_f32 v220, v172, v173\n"
"v_cvt_pk_bf16_f32 v221, v174, v175\n"
"v_cvt_pk_bf16_f32 v222, v188, v189\n"
"v_cvt_pk_bf16_f32 v223, v190, v191\n"
"s_waitcnt lgkmcnt(0)\n"
"v_mfma_f32_16x16x32_bf16 v[0:3], a[96:99], v[208:211], v[0:3]\n"
"v_mfma_f32_16x16x32_bf16 v[4:7], a[96:99], v[212:215], v[4:7]\n"
"v_mfma_f32_16x16x32_bf16 v[8:11], a[96:99], v[216:219], v[8:11]\n"
"v_mfma_f32_16x16x32_bf16 v[12:15], a[96:99], v[220:223], v[12:15]\n"
"v_mfma_f32_16x16x32_bf16 v[16:19], a[100:103], v[208:211], v[16:19]\n"
"v_mfma_f32_16x16x32_bf16 v[20:23], a[100:103], v[212:215], v[20:23]\n"
"v_mfma_f32_16x16x32_bf16 v[24:27], a[100:103], v[216:219], v[24:27]\n"
"v_mfma_f32_16x16x32_bf16 v[28:31], a[100:103], v[220:223], v[28:31]\n"
"v_mfma_f32_16x16x32_bf16 v[32:35], a[104:107], v[208:211], v[32:35]\n"
"v_mfma_f32_16x16x32_bf16 v[36:39], a[104:107], v[212:215], v[36:39]\n"
"v_mfma_f32_16x16x32_bf16 v[40:43], a[104:107], v[216:219], v[40:43]\n"
"v_mfma_f32_16x16x32_bf16 v[44:47], a[104:107], v[220:223], v[44:47]\n"
"v_mfma_f32_16x16x32_bf16 v[48:51], a[108:111], v[208:211], v[48:51]\n"
"v_mfma_f32_16x16x32_bf16 v[52:55], a[108:111], v[212:215], v[52:55]\n"
"v_mfma_f32_16x16x32_bf16 v[56:59], a[108:111], v[216:219], v[56:59]\n"
"v_mfma_f32_16x16x32_bf16 v[60:63], a[108:111], v[220:223], v[60:63]\n"
"v_mfma_f32_16x16x32_bf16 v[64:67], a[112:115], v[208:211], v[64:67]\n"
"v_mfma_f32_16x16x32_bf16 v[68:71], a[112:115], v[212:215], v[68:71]\n"
"v_mfma_f32_16x16x32_bf16 v[72:75], a[112:115], v[216:219], v[72:75]\n"
"v_mfma_f32_16x16x32_bf16 v[76:79], a[112:115], v[220:223], v[76:79]\n"
"v_mfma_f32_16x16x32_bf16 v[80:83], a[116:119], v[208:211], v[80:83]\n"
"v_mfma_f32_16x16x32_bf16 v[84:87], a[116:119], v[212:215], v[84:87]\n"
"v_mfma_f32_16x16x32_bf16 v[88:91], a[116:119], v[216:219], v[88:91]\n"
"v_mfma_f32_16x16x32_bf16 v[92:95], a[116:119], v[220:223], v[92:95]\n"
"v_mfma_f32_16x16x32_bf16 v[96:99], a[120:123], v[208:211], v[96:99]\n"
"v_mfma_f32_16x16x32_bf16 v[100:103], a[120:123], v[212:215], v[100:103]\n"
"v_mfma_f32_16x16x32_bf16 v[104:107], a[120:123], v[216:219], v[104:107]\n"
"v_mfma_f32_16x16x32_bf16 v[108:111], a[120:123], v[220:223], v[108:111]\n"
"v_mfma_f32_16x16x32_bf16 v[112:115], a[124:127], v[208:211], v[112:115]\n"
"v_mfma_f32_16x16x32_bf16 v[116:119], a[124:127], v[212:215], v[116:119]\n"
"v_mfma_f32_16x16x32_bf16 v[120:123], a[124:127], v[216:219], v[120:123]\n"
"v_mfma_f32_16x16x32_bf16 v[124:127], a[124:127], v[220:223], v[124:127]\n"
"v_mfma_f32_16x16x32_bf16 a[128:131], a[144:147], v[208:211], a[128:131]\n"
"v_mfma_f32_16x16x32_bf16 a[132:135], a[144:147], v[212:215], a[132:135]\n"
"v_mfma_f32_16x16x32_bf16 a[136:139], a[144:147], v[216:219], a[136:139]\n"
"v_mfma_f32_16x16x32_bf16 a[140:143], a[144:147], v[220:223], a[140:143]\n"
"s_nop 7\n"
"s_mov_b32 s79, 0\n"
"v_mbcnt_lo_u32_b32 v252, -1, 0\n"
"v_mbcnt_hi_u32_b32 v252, -1, v252\n"
"v_lshlrev_b32 v252, 4, v252\n"
"Lat_store:\n"
"v_accvgpr_read_b32 v132, a128\n"
"v_accvgpr_read_b32 v133, a132\n"
"v_accvgpr_read_b32 v134, a136\n"
"v_accvgpr_read_b32 v135, a140\n"
"s_nop 0\n"
"v_rcp_f32 v128, v132\n"
"v_rcp_f32 v129, v133\n"
"v_rcp_f32 v130, v134\n"
"v_rcp_f32 v131, v135\n"
"v_maximum3_f32 v136, |v0|, |v1|, |v2|\n"
"v_maximum3_f32 v136, v136, |v3|, |v16|\n"
"v_maximum3_f32 v136, v136, |v17|, |v18|\n"
"v_maximum3_f32 v136, v136, |v19|, |v32|\n"
"v_maximum3_f32 v136, v136, |v33|, |v34|\n"
"v_maximum3_f32 v136, v136, |v35|, |v48|\n"
"v_maximum3_f32 v136, v136, |v49|, |v50|\n"
"v_maximum3_f32 v136, v136, |v51|, |v64|\n"
"v_maximum3_f32 v136, v136, |v65|, |v66|\n"
"v_maximum3_f32 v136, v136, |v67|, |v80|\n"
"v_maximum3_f32 v136, v136, |v81|, |v82|\n"
"v_maximum3_f32 v136, v136, |v83|, |v96|\n"
"v_maximum3_f32 v136, v136, |v97|, |v98|\n"
"v_maximum3_f32 v136, v136, |v99|, |v112|\n"
"v_maximum3_f32 v136, v136, |v113|, |v114|\n"
"v_maximum3_f32 v136, v136, |v115|, |v115|\n"
"s_mov_b32 s64, 0\n"
"v_mul_f32 v0, v0, v128\n"
"v_mul_f32 v1, v1, v128\n"
"v_cvt_pk_bf16_f32 v144, v0, v1\n"
"v_mul_f32 v2, v2, v128\n"
"v_mul_f32 v3, v3, v128\n"
"v_cvt_pk_bf16_f32 v145, v2, v3\n"
"v_mul_f32 v16, v16, v128\n"
"v_mul_f32 v17, v17, v128\n"
"v_cvt_pk_bf16_f32 v146, v16, v17\n"
"v_mul_f32 v18, v18, v128\n"
"v_mul_f32 v19, v19, v128\n"
"v_cvt_pk_bf16_f32 v147, v18, v19\n"
"buffer_store_dwordx4 v[144:147], v252, s[52:55], s64 offen offset:0 sc1\n"
"v_mul_f32 v32, v32, v128\n"
"v_mul_f32 v33, v33, v128\n"
"v_cvt_pk_bf16_f32 v148, v32, v33\n"
"v_mul_f32 v34, v34, v128\n"
"v_mul_f32 v35, v35, v128\n"
"v_cvt_pk_bf16_f32 v149, v34, v35\n"
"v_mul_f32 v48, v48, v128\n"
"v_mul_f32 v49, v49, v128\n"
"v_cvt_pk_bf16_f32 v150, v48, v49\n"
"v_mul_f32 v50, v50, v128\n"
"v_mul_f32 v51, v51, v128\n"
"v_cvt_pk_bf16_f32 v151, v50, v51\n"
"buffer_store_dwordx4 v[148:151], v252, s[52:55], s64 offen offset:1024 sc1\n"
"v_mul_f32 v64, v64, v128\n"
"v_mul_f32 v65, v65, v128\n"
"v_cvt_pk_bf16_f32 v152, v64, v65\n"
"v_mul_f32 v66, v66, v128\n"
"v_mul_f32 v67, v67, v128\n"
"v_cvt_pk_bf16_f32 v153, v66, v67\n"
"v_mul_f32 v80, v80, v128\n"
"v_mul_f32 v81, v81, v128\n"
"v_cvt_pk_bf16_f32 v154, v80, v81\n"
"v_mul_f32 v82, v82, v128\n"
"v_mul_f32 v83, v83, v128\n"
"v_cvt_pk_bf16_f32 v155, v82, v83\n"
"buffer_store_dwordx4 v[152:155], v252, s[52:55], s64 offen offset:2048 sc1\n"
"v_mul_f32 v96, v96, v128\n"
"v_mul_f32 v97, v97, v128\n"
"v_cvt_pk_bf16_f32 v156, v96, v97\n"
"v_mul_f32 v98, v98, v128\n"
"v_mul_f32 v99, v99, v128\n"
"v_cvt_pk_bf16_f32 v157, v98, v99\n"
"v_mul_f32 v112, v112, v128\n"
"v_mul_f32 v113, v113, v128\n"
"v_cvt_pk_bf16_f32 v158, v112, v113\n"
"v_mul_f32 v114, v114, v128\n"
"v_mul_f32 v115, v115, v128\n"
"v_cvt_pk_bf16_f32 v159, v114, v115\n"
"buffer_store_dwordx4 v[156:159], v252, s[52:55], s64 offen offset:3072 sc1\n"
"v_maximum3_f32 v137, |v4|, |v5|, |v6|\n"
"v_maximum3_f32 v137, v137, |v7|, |v20|\n"
"v_maximum3_f32 v137, v137, |v21|, |v22|\n"
"v_maximum3_f32 v137, v137, |v23|, |v36|\n"
"v_maximum3_f32 v137, v137, |v37|, |v38|\n"
"v_maximum3_f32 v137, v137, |v39|, |v52|\n"
"v_maximum3_f32 v137, v137, |v53|, |v54|\n"
"v_maximum3_f32 v137, v137, |v55|, |v68|\n"
"v_maximum3_f32 v137, v137, |v69|, |v70|\n"
"v_maximum3_f32 v137, v137, |v71|, |v84|\n"
"v_maximum3_f32 v137, v137, |v85|, |v86|\n"
"v_maximum3_f32 v137, v137, |v87|, |v100|\n"
"v_maximum3_f32 v137, v137, |v101|, |v102|\n"
"v_maximum3_f32 v137, v137, |v103|, |v116|\n"
"v_maximum3_f32 v137, v137, |v117|, |v118|\n"
"v_maximum3_f32 v137, v137, |v119|, |v119|\n"
"s_mov_b32 s64, 131072\n"
"v_mul_f32 v4, v4, v129\n"
"v_mul_f32 v5, v5, v129\n"
"v_cvt_pk_bf16_f32 v144, v4, v5\n"
"v_mul_f32 v6, v6, v129\n"
"v_mul_f32 v7, v7, v129\n"
"v_cvt_pk_bf16_f32 v145, v6, v7\n"
"v_mul_f32 v20, v20, v129\n"
"v_mul_f32 v21, v21, v129\n"
"v_cvt_pk_bf16_f32 v146, v20, v21\n"
"v_mul_f32 v22, v22, v129\n"
"v_mul_f32 v23, v23, v129\n"
"v_cvt_pk_bf16_f32 v147, v22, v23\n"
"buffer_store_dwordx4 v[144:147], v252, s[52:55], s64 offen offset:0 sc1\n"
"v_mul_f32 v36, v36, v129\n"
"v_mul_f32 v37, v37, v129\n"
"v_cvt_pk_bf16_f32 v148, v36, v37\n"
"v_mul_f32 v38, v38, v129\n"
"v_mul_f32 v39, v39, v129\n"
"v_cvt_pk_bf16_f32 v149, v38, v39\n"
"v_mul_f32 v52, v52, v129\n"
"v_mul_f32 v53, v53, v129\n"
"v_cvt_pk_bf16_f32 v150, v52, v53\n"
"v_mul_f32 v54, v54, v129\n"
"v_mul_f32 v55, v55, v129\n"
"v_cvt_pk_bf16_f32 v151, v54, v55\n"
"buffer_store_dwordx4 v[148:151], v252, s[52:55], s64 offen offset:1024 sc1\n"
"v_mul_f32 v68, v68, v129\n"
"v_mul_f32 v69, v69, v129\n"
"v_cvt_pk_bf16_f32 v152, v68, v69\n"
"v_mul_f32 v70, v70, v129\n"
"v_mul_f32 v71, v71, v129\n"
"v_cvt_pk_bf16_f32 v153, v70, v71\n"
"v_mul_f32 v84, v84, v129\n"
"v_mul_f32 v85, v85, v129\n"
"v_cvt_pk_bf16_f32 v154, v84, v85\n"
"v_mul_f32 v86, v86, v129\n"
"v_mul_f32 v87, v87, v129\n"
"v_cvt_pk_bf16_f32 v155, v86, v87\n"
"buffer_store_dwordx4 v[152:155], v252, s[52:55], s64 offen offset:2048 sc1\n"
"v_mul_f32 v100, v100, v129\n"
"v_mul_f32 v101, v101, v129\n"
"v_cvt_pk_bf16_f32 v156, v100, v101\n"
"v_mul_f32 v102, v102, v129\n"
"v_mul_f32 v103, v103, v129\n"
"v_cvt_pk_bf16_f32 v157, v102, v103\n"
"v_mul_f32 v116, v116, v129\n"
"v_mul_f32 v117, v117, v129\n"
"v_cvt_pk_bf16_f32 v158, v116, v117\n"
"v_mul_f32 v118, v118, v129\n"
"v_mul_f32 v119, v119, v129\n"
"v_cvt_pk_bf16_f32 v159, v118, v119\n"
"buffer_store_dwordx4 v[156:159], v252, s[52:55], s64 offen offset:3072 sc1\n"
"v_maximum3_f32 v138, |v8|, |v9|, |v10|\n"
"v_maximum3_f32 v138, v138, |v11|, |v24|\n"
"v_maximum3_f32 v138, v138, |v25|, |v26|\n"
"v_maximum3_f32 v138, v138, |v27|, |v40|\n"
"v_maximum3_f32 v138, v138, |v41|, |v42|\n"
"v_maximum3_f32 v138, v138, |v43|, |v56|\n"
"v_maximum3_f32 v138, v138, |v57|, |v58|\n"
"v_maximum3_f32 v138, v138, |v59|, |v72|\n"
"v_maximum3_f32 v138, v138, |v73|, |v74|\n"
"v_maximum3_f32 v138, v138, |v75|, |v88|\n"
"v_maximum3_f32 v138, v138, |v89|, |v90|\n"
"v_maximum3_f32 v138, v138, |v91|, |v104|\n"
"v_maximum3_f32 v138, v138, |v105|, |v106|\n"
"v_maximum3_f32 v138, v138, |v107|, |v120|\n"
"v_maximum3_f32 v138, v138, |v121|, |v122|\n"
"v_maximum3_f32 v138, v138, |v123|, |v123|\n"
"v_maximum3_f32 v139, |v12|, |v13|, |v14|\n"
"v_maximum3_f32 v139, v139, |v15|, |v28|\n"
"v_maximum3_f32 v139, v139, |v29|, |v30|\n"
"v_maximum3_f32 v139, v139, |v31|, |v44|\n"
"v_maximum3_f32 v139, v139, |v45|, |v46|\n"
"v_maximum3_f32 v139, v139, |v47|, |v60|\n"
"v_maximum3_f32 v139, v139, |v61|, |v62|\n"
"v_maximum3_f32 v139, v139, |v63|, |v76|\n"
"v_maximum3_f32 v139, v139, |v77|, |v78|\n"
"v_maximum3_f32 v139, v139, |v79|, |v92|\n"
"v_maximum3_f32 v139, v139, |v93|, |v94|\n"
"v_maximum3_f32 v139, v139, |v95|, |v108|\n"
"v_maximum3_f32 v139, v139, |v109|, |v110|\n"
"v_maximum3_f32 v139, v139, |v111|, |v124|\n"
"v_maximum3_f32 v139, v139, |v125|, |v126|\n"
"v_maximum3_f32 v139, v139, |v127|, |v127|\n"
"s_mov_b64 s[62:63], exec\n"
"s_mov_b32 s64, 0x6000000\n"
"v_cmp_le_f32 vcc, s64, v132\n"
"s_and_b64 s[62:63], s[62:63], vcc\n"
"s_mov_b32 s64, 0x7d800000\n"
"v_cmp_ge_f32 vcc, s64, v132\n"
"s_and_b64 s[62:63], s[62:63], vcc\n"
"s_mov_b32 s64, 0x3800000\n"
"v_cmp_le_f32 vcc, s64, v136\n"
"s_and_b64 s[62:63], s[62:63], vcc\n"
"s_mov_b32 s64, 0x7e800000\n"
"v_cmp_ge_f32 vcc, s64, v136\n"
"s_and_b64 s[62:63], s[62:63], vcc\n"
"s_mov_b32 s64, 0x6000000\n"
"v_cmp_le_f32 vcc, s64, v133\n"
"s_and_b64 s[62:63], s[62:63], vcc\n"
"s_mov_b32 s64, 0x7d800000\n"
"v_cmp_ge_f32 vcc, s64, v133\n"
"s_and_b64 s[62:63], s[62:63], vcc\n"
"s_mov_b32 s64, 0x3800000\n"
"v_cmp_le_f32 vcc, s64, v137\n"
"s_and_b64 s[62:63], s[62:63], vcc\n"
"s_mov_b32 s64, 0x7e800000\n"
"v_cmp_ge_f32 vcc, s64, v137\n"
"s_and_b64 s[62:63], s[62:63], vcc\n"
"s_mov_b32 s64, 0x6000000\n"
"v_cmp_le_f32 vcc, s64, v134\n"
"s_and_b64 s[62:63], s[62:63], vcc\n"
"s_mov_b32 s64, 0x7d800000\n"
"v_cmp_ge_f32 vcc, s64, v134\n"
"s_and_b64 s[62:63], s[62:63], vcc\n"
"s_mov_b32 s64, 0x3800000\n"
"v_cmp_le_f32 vcc, s64, v138\n"
"s_and_b64 s[62:63], s[62:63], vcc\n"
"s_mov_b32 s64, 0x7e800000\n"
"v_cmp_ge_f32 vcc, s64, v138\n"
"s_and_b64 s[62:63], s[62:63], vcc\n"
"s_mov_b32 s64, 0x6000000\n"
"v_cmp_le_f32 vcc, s64, v135\n"
"s_and_b64 s[62:63], s[62:63], vcc\n"
"s_mov_b32 s64, 0x7d800000\n"
"v_cmp_ge_f32 vcc, s64, v135\n"
"s_and_b64 s[62:63], s[62:63], vcc\n"
"s_mov_b32 s64, 0x3800000\n"
"v_cmp_le_f32 vcc, s64, v139\n"
"s_and_b64 s[62:63], s[62:63], vcc\n"
"s_mov_b32 s64, 0x7e800000\n"
"v_cmp_ge_f32 vcc, s64, v139\n"
"s_and_b64 s[62:63], s[62:63], vcc\n"
"s_cmp_eq_u64 s[62:63], exec\n"
"s_cselect_b32 s64, 0, 1\n"
"s_cmp_eq_u32 s79, 1\n"
"s_cselect_b32 s64, 0, s64\n"
"v_mov_b32 v253, s64\n"
"s_lshl_b32 s65, s56, 2\n"
"s_add_u32 s65, s65, 131072\n"
"v_mov_b32 v254, s65\n"
"ds_write_b32 v254, v253\n"
"s_mov_b32 s64, 262144\n"
"v_mul_f32 v8, v8, v130\n"
"v_mul_f32 v9, v9, v130\n"
"v_cvt_pk_bf16_f32 v144, v8, v9\n"
"v_mul_f32 v10, v10, v130\n"
"v_mul_f32 v11, v11, v130\n"
"v_cvt_pk_bf16_f32 v145, v10, v11\n"
"v_mul_f32 v24, v24, v130\n"
"v_mul_f32 v25, v25, v130\n"
"v_cvt_pk_bf16_f32 v146, v24, v25\n"
"v_mul_f32 v26, v26, v130\n"
"v_mul_f32 v27, v27, v130\n"
"v_cvt_pk_bf16_f32 v147, v26, v27\n"
"buffer_store_dwordx4 v[144:147], v252, s[52:55], s64 offen offset:0 sc1\n"
"v_mul_f32 v40, v40, v130\n"
"v_mul_f32 v41, v41, v130\n"
"v_cvt_pk_bf16_f32 v148, v40, v41\n"
"v_mul_f32 v42, v42, v130\n"
"v_mul_f32 v43, v43, v130\n"
"v_cvt_pk_bf16_f32 v149, v42, v43\n"
"v_mul_f32 v56, v56, v130\n"
"v_mul_f32 v57, v57, v130\n"
"v_cvt_pk_bf16_f32 v150, v56, v57\n"
"v_mul_f32 v58, v58, v130\n"
"v_mul_f32 v59, v59, v130\n"
"v_cvt_pk_bf16_f32 v151, v58, v59\n"
"buffer_store_dwordx4 v[148:151], v252, s[52:55], s64 offen offset:1024 sc1\n"
"v_mul_f32 v72, v72, v130\n"
"v_mul_f32 v73, v73, v130\n"
"v_cvt_pk_bf16_f32 v152, v72, v73\n"
"v_mul_f32 v74, v74, v130\n"
"v_mul_f32 v75, v75, v130\n"
"v_cvt_pk_bf16_f32 v153, v74, v75\n"
"v_mul_f32 v88, v88, v130\n"
"v_mul_f32 v89, v89, v130\n"
"v_cvt_pk_bf16_f32 v154, v88, v89\n"
"v_mul_f32 v90, v90, v130\n"
"v_mul_f32 v91, v91, v130\n"
"v_cvt_pk_bf16_f32 v155, v90, v91\n"
"buffer_store_dwordx4 v[152:155], v252, s[52:55], s64 offen offset:2048 sc1\n"
"v_mul_f32 v104, v104, v130\n"
"v_mul_f32 v105, v105, v130\n"
"v_cvt_pk_bf16_f32 v156, v104, v105\n"
"v_mul_f32 v106, v106, v130\n"
"v_mul_f32 v107, v107, v130\n"
"v_cvt_pk_bf16_f32 v157, v106, v107\n"
"v_mul_f32 v120, v120, v130\n"
"v_mul_f32 v121, v121, v130\n"
"v_cvt_pk_bf16_f32 v158, v120, v121\n"
"v_mul_f32 v122, v122, v130\n"
"v_mul_f32 v123, v123, v130\n"
"v_cvt_pk_bf16_f32 v159, v122, v123\n"
"buffer_store_dwordx4 v[156:159], v252, s[52:55], s64 offen offset:3072 sc1\n"
"s_mov_b32 s64, 393216\n"
"v_mul_f32 v12, v12, v131\n"
"v_mul_f32 v13, v13, v131\n"
"v_cvt_pk_bf16_f32 v144, v12, v13\n"
"v_mul_f32 v14, v14, v131\n"
"v_mul_f32 v15, v15, v131\n"
"v_cvt_pk_bf16_f32 v145, v14, v15\n"
"v_mul_f32 v28, v28, v131\n"
"v_mul_f32 v29, v29, v131\n"
"v_cvt_pk_bf16_f32 v146, v28, v29\n"
"v_mul_f32 v30, v30, v131\n"
"v_mul_f32 v31, v31, v131\n"
"v_cvt_pk_bf16_f32 v147, v30, v31\n"
"buffer_store_dwordx4 v[144:147], v252, s[52:55], s64 offen offset:0 sc1\n"
"v_mul_f32 v44, v44, v131\n"
"v_mul_f32 v45, v45, v131\n"
"v_cvt_pk_bf16_f32 v148, v44, v45\n"
"v_mul_f32 v46, v46, v131\n"
"v_mul_f32 v47, v47, v131\n"
"v_cvt_pk_bf16_f32 v149, v46, v47\n"
"v_mul_f32 v60, v60, v131\n"
"v_mul_f32 v61, v61, v131\n"
"v_cvt_pk_bf16_f32 v150, v60, v61\n"
"v_mul_f32 v62, v62, v131\n"
"v_mul_f32 v63, v63, v131\n"
"v_cvt_pk_bf16_f32 v151, v62, v63\n"
"buffer_store_dwordx4 v[148:151], v252, s[52:55], s64 offen offset:1024 sc1\n"
"v_mul_f32 v76, v76, v131\n"
"v_mul_f32 v77, v77, v131\n"
"v_cvt_pk_bf16_f32 v152, v76, v77\n"
"v_mul_f32 v78, v78, v131\n"
"v_mul_f32 v79, v79, v131\n"
"v_cvt_pk_bf16_f32 v153, v78, v79\n"
"v_mul_f32 v92, v92, v131\n"
"v_mul_f32 v93, v93, v131\n"
"v_cvt_pk_bf16_f32 v154, v92, v93\n"
"v_mul_f32 v94, v94, v131\n"
"v_mul_f32 v95, v95, v131\n"
"v_cvt_pk_bf16_f32 v155, v94, v95\n"
"buffer_store_dwordx4 v[152:155], v252, s[52:55], s64 offen offset:2048 sc1\n"
"v_mul_f32 v108, v108, v131\n"
"v_mul_f32 v109, v109, v131\n"
"v_cvt_pk_bf16_f32 v156, v108, v109\n"
"v_mul_f32 v110, v110, v131\n"
"v_mul_f32 v111, v111, v131\n"
"v_cvt_pk_bf16_f32 v157, v110, v111\n"
"v_mul_f32 v124, v124, v131\n"
"v_mul_f32 v125, v125, v131\n"
"v_cvt_pk_bf16_f32 v158, v124, v125\n"
"v_mul_f32 v126, v126, v131\n"
"v_mul_f32 v127, v127, v131\n"
"v_cvt_pk_bf16_f32 v159, v126, v127\n"
"buffer_store_dwordx4 v[156:159], v252, s[52:55], s64 offen offset:3072 sc1\n"
"s_waitcnt lgkmcnt(0)\n"
"s_barrier\n"
"v_mov_b32 v254, 131072\n"
"ds_read_b128 v[140:143], v254\n"
"s_waitcnt lgkmcnt(0)\n"
"v_or_b32 v140, v140, v141\n"
"v_or_b32 v142, v142, v143\n"
"v_or_b32 v140, v140, v142\n"
"s_nop 0\n"
"v_readfirstlane_b32 s64, v140\n"
"s_nop 3\n"
"s_cmp_lg_u32 s64, 0\n"
"s_cbranch_scc1 Lat_safe\n"
"s_add_u32 m0, s70, 65536\n"
"s_add_u32 s64, s71, 262144\n"
"buffer_load_dwordx4 v246, s[44:47], s64 offen lds\n"
"buffer_load_dwordx4 v247, s[44:47], s64 offen offset:1024 lds\n"
"s_add_u32 m0, s70, 73728\n"
"s_add_u32 s64, s71, 294912\n"
"buffer_load_dwordx4 v246, s[44:47], s64 offen lds\n"
"buffer_load_dwordx4 v247, s[44:47], s64 offen offset:1024 lds\n"
"s_add_u32 m0, s70, 81920\n"
"s_add_u32 s64, s71, 327680\n"
"buffer_load_dwordx4 v246, s[44:47], s64 offen lds\n"
"buffer_load_dwordx4 v247, s[44:47], s64 offen offset:1024 lds\n"
"s_add_u32 m0, s70, 90112\n"
"s_add_u32 s64, s71, 360448\n"
"buffer_load_dwordx4 v246, s[44:47], s64 offen lds\n"
"buffer_load_dwordx4 v247, s[44:47], s64 offen offset:1024 lds\n"
"s_add_u32 m0, s70, 98304\n"
"s_add_u32 s64, s71, 393216\n"
"buffer_load_dwordx4 v246, s[44:47], s64 offen lds\n"
"buffer_load_dwordx4 v247, s[44:47], s64 offen offset:1024 lds\n"
"s_add_u32 m0, s70, 106496\n"
"s_add_u32 s64, s71, 425984\n"
"buffer_load_dwordx4 v246, s[44:47], s64 offen lds\n"
"buffer_load_dwordx4 v247, s[44:47], s64 offen offset:1024 lds\n"
"s_add_u32 m0, s70, 114688\n"
"s_add_u32 s64, s71, 458752\n"
"buffer_load_dwordx4 v246, s[44:47], s64 offen lds\n"
"buffer_load_dwordx4 v247, s[44:47], s64 offen offset:1024 lds\n"
"s_add_u32 m0, s70, 122880\n"
"s_add_u32 s64, s71, 491520\n"
"buffer_load_dwordx4 v246, s[44:47], s64 offen lds\n"
"buffer_load_dwordx4 v247, s[44:47], s64 offen offset:1024 lds\n"
"s_waitcnt vmcnt(24)\n"
"s_barrier\n"
"s_mov_b32 s72, %21\n"
"s_mov_b32 s73, %22\n"
"s_cmp_eq_u32 s56, 0\n"
"s_cbranch_scc0 Lsig_aa\n"
"s_mov_b64 s[64:65], exec\n"
"s_mov_b64 exec, 1\n"
"v_mov_b32 v140, 0\n"
"v_mov_b32 v141, 1\n"
"global_atomic_add v140, v141, s[72:73]\n"
"s_mov_b64 exec, s[64:65]\n"
"Lsig_aa:\n"
"s_waitcnt vmcnt(16)\n"
"s_barrier\n"
"s_branch Lat_end\n"
"Lat_safe:\n"
"s_waitcnt vmcnt(0) lgkmcnt(0)\n"
"s_barrier\n"
"v_and_b32 v240, 0xffff, v240\n"
"v_and_b32 v241, 0xffff, v241\n"
"v_and_b32 v242, 0xffff, v242\n"
"v_and_b32 v243, 0xffff, v243\n"
"v_and_b32 v244, 0xffff, v244\n"
"v_and_b32 v245, 0xffff, v245\n"
"s_lshl_b32 s62, s56, 13\n"
"s_mov_b32 s59, s62\n"
"s_add_u32 s60, s62, 32768\n"
"s_mov_b32 s57, s62\n"
"s_mov_b32 s58, s62\n"
"v_mov_b32 v253, 0xff7fffff\n"
"v_mov_b32 v224, v253\n"
"v_accvgpr_write_b32 a128, 0\n"
"v_accvgpr_write_b32 a129, 0\n"
"v_accvgpr_write_b32 a130, 0\n"
"v_accvgpr_write_b32 a131, 0\n"
"v_mov_b32 v228, v253\n"
"v_accvgpr_write_b32 a132, 0\n"
"v_accvgpr_write_b32 a133, 0\n"
"v_accvgpr_write_b32 a134, 0\n"
"v_accvgpr_write_b32 a135, 0\n"
"v_mov_b32 v232, v253\n"
"v_accvgpr_write_b32 a136, 0\n"
"v_accvgpr_write_b32 a137, 0\n"
"v_accvgpr_write_b32 a138, 0\n"
"v_accvgpr_write_b32 a139, 0\n"
"v_mov_b32 v236, v253\n"
"v_accvgpr_write_b32 a140, 0\n"
"v_accvgpr_write_b32 a141, 0\n"
"v_accvgpr_write_b32 a142, 0\n"
"v_accvgpr_write_b32 a143, 0\n"
"v_mov_b32 v0, 0\n"
"v_mov_b32 v1, 0\n"
"v_mov_b32 v2, 0\n"
"v_mov_b32 v3, 0\n"
"v_mov_b32 v4, 0\n"
"v_mov_b32 v5, 0\n"
"v_mov_b32 v6, 0\n"
"v_mov_b32 v7, 0\n"
"v_mov_b32 v8, 0\n"
"v_mov_b32 v9, 0\n"
"v_mov_b32 v10, 0\n"
"v_mov_b32 v11, 0\n"
"v_mov_b32 v12, 0\n"
"v_mov_b32 v13, 0\n"
"v_mov_b32 v14, 0\n"
"v_mov_b32 v15, 0\n"
"v_mov_b32 v16, 0\n"
"v_mov_b32 v17, 0\n"
"v_mov_b32 v18, 0\n"
"v_mov_b32 v19, 0\n"
"v_mov_b32 v20, 0\n"
"v_mov_b32 v21, 0\n"
"v_mov_b32 v22, 0\n"
"v_mov_b32 v23, 0\n"
"v_mov_b32 v24, 0\n"
"v_mov_b32 v25, 0\n"
"v_mov_b32 v26, 0\n"
"v_mov_b32 v27, 0\n"
"v_mov_b32 v28, 0\n"
"v_mov_b32 v29, 0\n"
"v_mov_b32 v30, 0\n"
"v_mov_b32 v31, 0\n"
"v_mov_b32 v32, 0\n"
"v_mov_b32 v33, 0\n"
"v_mov_b32 v34, 0\n"
"v_mov_b32 v35, 0\n"
"v_mov_b32 v36, 0\n"
"v_mov_b32 v37, 0\n"
"v_mov_b32 v38, 0\n"
"v_mov_b32 v39, 0\n"
"v_mov_b32 v40, 0\n"
"v_mov_b32 v41, 0\n"
"v_mov_b32 v42, 0\n"
"v_mov_b32 v43, 0\n"
"v_mov_b32 v44, 0\n"
"v_mov_b32 v45, 0\n"
"v_mov_b32 v46, 0\n"
"v_mov_b32 v47, 0\n"
"v_mov_b32 v48, 0\n"
"v_mov_b32 v49, 0\n"
"v_mov_b32 v50, 0\n"
"v_mov_b32 v51, 0\n"
"v_mov_b32 v52, 0\n"
"v_mov_b32 v53, 0\n"
"v_mov_b32 v54, 0\n"
"v_mov_b32 v55, 0\n"
"v_mov_b32 v56, 0\n"
"v_mov_b32 v57, 0\n"
"v_mov_b32 v58, 0\n"
"v_mov_b32 v59, 0\n"
"v_mov_b32 v60, 0\n"
"v_mov_b32 v61, 0\n"
"v_mov_b32 v62, 0\n"
"v_mov_b32 v63, 0\n"
"v_mov_b32 v64, 0\n"
"v_mov_b32 v65, 0\n"
"v_mov_b32 v66, 0\n"
"v_mov_b32 v67, 0\n"
"v_mov_b32 v68, 0\n"
"v_mov_b32 v69, 0\n"
"v_mov_b32 v70, 0\n"
"v_mov_b32 v71, 0\n"
"v_mov_b32 v72, 0\n"
"v_mov_b32 v73, 0\n"
"v_mov_b32 v74, 0\n"
"v_mov_b32 v75, 0\n"
"v_mov_b32 v76, 0\n"
"v_mov_b32 v77, 0\n"
"v_mov_b32 v78, 0\n"
"v_mov_b32 v79, 0\n"
"v_mov_b32 v80, 0\n"
"v_mov_b32 v81, 0\n"
"v_mov_b32 v82, 0\n"
"v_mov_b32 v83, 0\n"
"v_mov_b32 v84, 0\n"
"v_mov_b32 v85, 0\n"
"v_mov_b32 v86, 0\n"
"v_mov_b32 v87, 0\n"
"v_mov_b32 v88, 0\n"
"v_mov_b32 v89, 0\n"
"v_mov_b32 v90, 0\n"
"v_mov_b32 v91, 0\n"
"v_mov_b32 v92, 0\n"
"v_mov_b32 v93, 0\n"
"v_mov_b32 v94, 0\n"
"v_mov_b32 v95, 0\n"
"v_mov_b32 v96, 0\n"
"v_mov_b32 v97, 0\n"
"v_mov_b32 v98, 0\n"
"v_mov_b32 v99, 0\n"
"v_mov_b32 v100, 0\n"
"v_mov_b32 v101, 0\n"
"v_mov_b32 v102, 0\n"
"v_mov_b32 v103, 0\n"
"v_mov_b32 v104, 0\n"
"v_mov_b32 v105, 0\n"
"v_mov_b32 v106, 0\n"
"v_mov_b32 v107, 0\n"
"v_mov_b32 v108, 0\n"
"v_mov_b32 v109, 0\n"
"v_mov_b32 v110, 0\n"
"v_mov_b32 v111, 0\n"
"v_mov_b32 v112, 0\n"
"v_mov_b32 v113, 0\n"
"v_mov_b32 v114, 0\n"
"v_mov_b32 v115, 0\n"
"v_mov_b32 v116, 0\n"
"v_mov_b32 v117, 0\n"
"v_mov_b32 v118, 0\n"
"v_mov_b32 v119, 0\n"
"v_mov_b32 v120, 0\n"
"v_mov_b32 v121, 0\n"
"v_mov_b32 v122, 0\n"
"v_mov_b32 v123, 0\n"
"v_mov_b32 v124, 0\n"
"v_mov_b32 v125, 0\n"
"v_mov_b32 v126, 0\n"
"v_mov_b32 v127, 0\n"
"v_mbcnt_lo_u32_b32 v253, -1, 0\n"
"v_mbcnt_hi_u32_b32 v253, -1, v253\n"
"v_lshrrev_b32 v254, 4, v253\n"
"v_and_b32 v255, 15, v253\n"
"v_xor_b32 v255, v255, v254\n"
"v_xor_b32 v246, 0, v255\n"
"v_lshlrev_b32 v246, 4, v246\n"
"v_lshl_add_u32 v246, v254, 8, v246\n"
"v_xor_b32 v247, 4, v255\n"
"v_lshlrev_b32 v247, 4, v247\n"
"v_lshl_add_u32 v247, v254, 8, v247\n"
"v_xor_b32 v222, 16, v253\n"
"v_lshlrev_b32 v222, 2, v222\n"
"v_xor_b32 v223, 32, v253\n"
"v_lshlrev_b32 v223, 2, v223\n"
"s_mov_b32 s61, 16\n"
"Lat_sloop:\n"
"s_mov_b32 m0, s59\n"
"s_nop 0\n"
"buffer_load_dwordx4 v246, s[36:39], s57 offen offset:0 lds\n"
"buffer_load_dwordx4 v247, s[36:39], s57 offen offset:1024 lds\n"
"buffer_load_dwordx4 v248, s[36:39], s57 offen offset:2048 lds\n"
"buffer_load_dwordx4 v249, s[36:39], s57 offen offset:3072 lds\n"
"s_add_u32 s64, s57, 4096\n"
"s_add_u32 m0, s59, 4096\n"
"s_nop 0\n"
"buffer_load_dwordx4 v246, s[36:39], s64 offen offset:0 lds\n"
"buffer_load_dwordx4 v247, s[36:39], s64 offen offset:1024 lds\n"
"buffer_load_dwordx4 v248, s[36:39], s64 offen offset:2048 lds\n"
"buffer_load_dwordx4 v249, s[36:39], s64 offen offset:3072 lds\n"
"s_mov_b32 m0, s60\n"
"s_nop 0\n"
"buffer_load_dwordx4 v250, s[40:43], s58 offen offset:0 lds\n"
"buffer_load_dwordx4 v251, s[40:43], s58 offen offset:1024 lds\n"
"buffer_load_dwordx4 v250, s[40:43], s58 offen offset:2048 lds\n"
"buffer_load_dwordx4 v251, s[40:43], s58 offen offset:3072 lds\n"
"s_add_u32 s65, s58, 4096\n"
"s_add_u32 m0, s60, 4096\n"
"s_nop 0\n"
"buffer_load_dwordx4 v250, s[40:43], s65 offen offset:0 lds\n"
"buffer_load_dwordx4 v251, s[40:43], s65 offen offset:1024 lds\n"
"buffer_load_dwordx4 v250, s[40:43], s65 offen offset:2048 lds\n"
"buffer_load_dwordx4 v251, s[40:43], s65 offen offset:3072 lds\n"
"s_add_u32 s57, s57, 32768\n"
"s_add_u32 s58, s58, 32768\n"
"s_waitcnt vmcnt(0)\n"
"s_barrier\n"
"ds_read_b128 a[64:67], v240 offset:0\n"
"ds_read_b128 a[68:71], v241 offset:0\n"
"ds_read_b128 a[72:75], v242 offset:0\n"
"ds_read_b128 a[76:79], v243 offset:0\n"
"ds_read_b128 a[80:83], v240 offset:4096\n"
"ds_read_b128 a[84:87], v241 offset:4096\n"
"ds_read_b128 a[88:91], v242 offset:4096\n"
"ds_read_b128 a[92:95], v243 offset:4096\n"
"ds_read_b64_tr_b16 a[96:97], v244 offset:32768\n"
"ds_read_b64_tr_b16 a[98:99], v244 offset:36864\n"
"ds_read_b64_tr_b16 a[100:101], v245 offset:32768\n"
"ds_read_b64_tr_b16 a[102:103], v245 offset:36864\n"
"ds_read_b64_tr_b16 a[104:105], v244 offset:33280\n"
"ds_read_b64_tr_b16 a[106:107], v244 offset:37376\n"
"ds_read_b64_tr_b16 a[108:109], v245 offset:33280\n"
"ds_read_b64_tr_b16 a[110:111], v245 offset:37376\n"
"ds_read_b64_tr_b16 a[112:113], v244 offset:33792\n"
"ds_read_b64_tr_b16 a[114:115], v244 offset:37888\n"
"ds_read_b64_tr_b16 a[116:117], v245 offset:33792\n"
"ds_read_b64_tr_b16 a[118:119], v245 offset:37888\n"
"ds_read_b64_tr_b16 a[120:121], v244 offset:34304\n"
"ds_read_b64_tr_b16 a[122:123], v244 offset:38400\n"
"ds_read_b64_tr_b16 a[124:125], v245 offset:34304\n"
"ds_read_b64_tr_b16 a[126:127], v245 offset:38400\n"
"s_waitcnt lgkmcnt(0)\n"
"v_mfma_f32_16x16x32_bf16 v[128:131], a[64:67], a[0:3], 0\n"
"v_mfma_f32_16x16x32_bf16 v[132:135], a[64:67], a[16:19], 0\n"
"v_mfma_f32_16x16x32_bf16 v[136:139], a[64:67], a[32:35], 0\n"
"v_mfma_f32_16x16x32_bf16 v[140:143], a[64:67], a[48:51], 0\n"
"v_mfma_f32_16x16x32_bf16 v[128:131], a[68:71], a[4:7], v[128:131]\n"
"v_mfma_f32_16x16x32_bf16 v[132:135], a[68:71], a[20:23], v[132:135]\n"
"v_mfma_f32_16x16x32_bf16 v[136:139], a[68:71], a[36:39], v[136:139]\n"
"v_mfma_f32_16x16x32_bf16 v[140:143], a[68:71], a[52:55], v[140:143]\n"
"v_mfma_f32_16x16x32_bf16 v[128:131], a[72:75], a[8:11], v[128:131]\n"
"v_mfma_f32_16x16x32_bf16 v[132:135], a[72:75], a[24:27], v[132:135]\n"
"v_mfma_f32_16x16x32_bf16 v[136:139], a[72:75], a[40:43], v[136:139]\n"
"v_mfma_f32_16x16x32_bf16 v[140:143], a[72:75], a[56:59], v[140:143]\n"
"v_mfma_f32_16x16x32_bf16 v[128:131], a[76:79], a[12:15], v[128:131]\n"
"v_mfma_f32_16x16x32_bf16 v[132:135], a[76:79], a[28:31], v[132:135]\n"
"v_mfma_f32_16x16x32_bf16 v[136:139], a[76:79], a[44:47], v[136:139]\n"
"v_mfma_f32_16x16x32_bf16 v[140:143], a[76:79], a[60:63], v[140:143]\n"
"v_mfma_f32_16x16x32_bf16 v[144:147], a[80:83], a[0:3], 0\n"
"v_mfma_f32_16x16x32_bf16 v[148:151], a[80:83], a[16:19], 0\n"
"v_mfma_f32_16x16x32_bf16 v[152:155], a[80:83], a[32:35], 0\n"
"v_mfma_f32_16x16x32_bf16 v[156:159], a[80:83], a[48:51], 0\n"
"v_mfma_f32_16x16x32_bf16 v[144:147], a[84:87], a[4:7], v[144:147]\n"
"v_mfma_f32_16x16x32_bf16 v[148:151], a[84:87], a[20:23], v[148:151]\n"
"v_mfma_f32_16x16x32_bf16 v[152:155], a[84:87], a[36:39], v[152:155]\n"
"v_mfma_f32_16x16x32_bf16 v[156:159], a[84:87], a[52:55], v[156:159]\n"
"v_mfma_f32_16x16x32_bf16 v[144:147], a[88:91], a[8:11], v[144:147]\n"
"v_mfma_f32_16x16x32_bf16 v[148:151], a[88:91], a[24:27], v[148:151]\n"
"v_mfma_f32_16x16x32_bf16 v[152:155], a[88:91], a[40:43], v[152:155]\n"
"v_mfma_f32_16x16x32_bf16 v[156:159], a[88:91], a[56:59], v[156:159]\n"
"v_mfma_f32_16x16x32_bf16 v[144:147], a[92:95], a[12:15], v[144:147]\n"
"v_mfma_f32_16x16x32_bf16 v[148:151], a[92:95], a[28:31], v[148:151]\n"
"v_mfma_f32_16x16x32_bf16 v[152:155], a[92:95], a[44:47], v[152:155]\n"
"v_mfma_f32_16x16x32_bf16 v[156:159], a[92:95], a[60:63], v[156:159]\n"
"s_nop 7\n"
"v_max3_f32 v225, v128, v129, v130\n"
"v_max3_f32 v225, v225, v131, v144\n"
"v_max3_f32 v225, v225, v145, v146\n"
"v_max_f32 v225, v225, v147\n"
"v_max3_f32 v229, v132, v133, v134\n"
"v_max3_f32 v229, v229, v135, v148\n"
"v_max3_f32 v229, v229, v149, v150\n"
"v_max_f32 v229, v229, v151\n"
"v_max3_f32 v233, v136, v137, v138\n"
"v_max3_f32 v233, v233, v139, v152\n"
"v_max3_f32 v233, v233, v153, v154\n"
"v_max_f32 v233, v233, v155\n"
"v_max3_f32 v237, v140, v141, v142\n"
"v_max3_f32 v237, v237, v143, v156\n"
"v_max3_f32 v237, v237, v157, v158\n"
"v_max_f32 v237, v237, v159\n"
"ds_bpermute_b32 v226, v222, v225\n"
"ds_bpermute_b32 v230, v222, v229\n"
"ds_bpermute_b32 v234, v222, v233\n"
"ds_bpermute_b32 v238, v222, v237\n"
"s_waitcnt lgkmcnt(0)\n"
"v_max_f32 v225, v225, v226\n"
"v_max_f32 v229, v229, v230\n"
"v_max_f32 v233, v233, v234\n"
"v_max_f32 v237, v237, v238\n"
"ds_bpermute_b32 v226, v223, v225\n"
"ds_bpermute_b32 v230, v223, v229\n"
"ds_bpermute_b32 v234, v223, v233\n"
"ds_bpermute_b32 v238, v223, v237\n"
"s_waitcnt lgkmcnt(0)\n"
"v_max_f32 v225, v225, v226\n"
"v_max_f32 v229, v229, v230\n"
"v_max_f32 v233, v233, v234\n"
"v_max_f32 v237, v237, v238\n"
"v_max_f32 v225, v225, v224\n"
"v_sub_f32 v226, v224, v225\n"
"v_exp_f32 v226, v226\n"
"v_mov_b32 v224, v225\n"
"v_max_f32 v229, v229, v228\n"
"v_sub_f32 v230, v228, v229\n"
"v_exp_f32 v230, v230\n"
"v_mov_b32 v228, v229\n"
"v_max_f32 v233, v233, v232\n"
"v_sub_f32 v234, v232, v233\n"
"v_exp_f32 v234, v234\n"
"v_mov_b32 v232, v233\n"
"v_max_f32 v237, v237, v236\n"
"v_sub_f32 v238, v236, v237\n"
"v_exp_f32 v238, v238\n"
"v_mov_b32 v236, v237\n"
"v_mul_f32 v0, v0, v226\n"
"v_mul_f32 v1, v1, v226\n"
"v_mul_f32 v2, v2, v226\n"
"v_mul_f32 v3, v3, v226\n"
"v_mul_f32 v16, v16, v226\n"
"v_mul_f32 v17, v17, v226\n"
"v_mul_f32 v18, v18, v226\n"
"v_mul_f32 v19, v19, v226\n"
"v_mul_f32 v32, v32, v226\n"
"v_mul_f32 v33, v33, v226\n"
"v_mul_f32 v34, v34, v226\n"
"v_mul_f32 v35, v35, v226\n"
"v_mul_f32 v48, v48, v226\n"
"v_mul_f32 v49, v49, v226\n"
"v_mul_f32 v50, v50, v226\n"
"v_mul_f32 v51, v51, v226\n"
"v_mul_f32 v64, v64, v226\n"
"v_mul_f32 v65, v65, v226\n"
"v_mul_f32 v66, v66, v226\n"
"v_mul_f32 v67, v67, v226\n"
"v_mul_f32 v80, v80, v226\n"
"v_mul_f32 v81, v81, v226\n"
"v_mul_f32 v82, v82, v226\n"
"v_mul_f32 v83, v83, v226\n"
"v_mul_f32 v96, v96, v226\n"
"v_mul_f32 v97, v97, v226\n"
"v_mul_f32 v98, v98, v226\n"
"v_mul_f32 v99, v99, v226\n"
"v_mul_f32 v112, v112, v226\n"
"v_mul_f32 v113, v113, v226\n"
"v_mul_f32 v114, v114, v226\n"
"v_mul_f32 v115, v115, v226\n"
"v_accvgpr_read_b32 v227, a128\n"
"v_mul_f32 v227, v227, v226\n"
"v_accvgpr_write_b32 a128, v227\n"
"v_accvgpr_read_b32 v227, a129\n"
"v_mul_f32 v227, v227, v226\n"
"v_accvgpr_write_b32 a129, v227\n"
"v_accvgpr_read_b32 v227, a130\n"
"v_mul_f32 v227, v227, v226\n"
"v_accvgpr_write_b32 a130, v227\n"
"v_accvgpr_read_b32 v227, a131\n"
"v_mul_f32 v227, v227, v226\n"
"v_accvgpr_write_b32 a131, v227\n"
"v_sub_f32 v128, v128, v224\n"
"v_sub_f32 v129, v129, v224\n"
"v_sub_f32 v130, v130, v224\n"
"v_sub_f32 v131, v131, v224\n"
"v_sub_f32 v144, v144, v224\n"
"v_sub_f32 v145, v145, v224\n"
"v_sub_f32 v146, v146, v224\n"
"v_sub_f32 v147, v147, v224\n"
"v_exp_f32 v128, v128\n"
"v_exp_f32 v129, v129\n"
"v_exp_f32 v130, v130\n"
"v_exp_f32 v131, v131\n"
"v_exp_f32 v144, v144\n"
"v_exp_f32 v145, v145\n"
"v_exp_f32 v146, v146\n"
"v_exp_f32 v147, v147\n"
"s_nop 0\n"
"v_cvt_pk_bf16_f32 v192, v128, v129\n"
"v_cvt_pk_bf16_f32 v193, v130, v131\n"
"v_cvt_pk_bf16_f32 v194, v144, v145\n"
"v_cvt_pk_bf16_f32 v195, v146, v147\n"
"v_mul_f32 v4, v4, v230\n"
"v_mul_f32 v5, v5, v230\n"
"v_mul_f32 v6, v6, v230\n"
"v_mul_f32 v7, v7, v230\n"
"v_mul_f32 v20, v20, v230\n"
"v_mul_f32 v21, v21, v230\n"
"v_mul_f32 v22, v22, v230\n"
"v_mul_f32 v23, v23, v230\n"
"v_mul_f32 v36, v36, v230\n"
"v_mul_f32 v37, v37, v230\n"
"v_mul_f32 v38, v38, v230\n"
"v_mul_f32 v39, v39, v230\n"
"v_mul_f32 v52, v52, v230\n"
"v_mul_f32 v53, v53, v230\n"
"v_mul_f32 v54, v54, v230\n"
"v_mul_f32 v55, v55, v230\n"
"v_mul_f32 v68, v68, v230\n"
"v_mul_f32 v69, v69, v230\n"
"v_mul_f32 v70, v70, v230\n"
"v_mul_f32 v71, v71, v230\n"
"v_mul_f32 v84, v84, v230\n"
"v_mul_f32 v85, v85, v230\n"
"v_mul_f32 v86, v86, v230\n"
"v_mul_f32 v87, v87, v230\n"
"v_mul_f32 v100, v100, v230\n"
"v_mul_f32 v101, v101, v230\n"
"v_mul_f32 v102, v102, v230\n"
"v_mul_f32 v103, v103, v230\n"
"v_mul_f32 v116, v116, v230\n"
"v_mul_f32 v117, v117, v230\n"
"v_mul_f32 v118, v118, v230\n"
"v_mul_f32 v119, v119, v230\n"
"v_accvgpr_read_b32 v231, a132\n"
"v_mul_f32 v231, v231, v230\n"
"v_accvgpr_write_b32 a132, v231\n"
"v_accvgpr_read_b32 v231, a133\n"
"v_mul_f32 v231, v231, v230\n"
"v_accvgpr_write_b32 a133, v231\n"
"v_accvgpr_read_b32 v231, a134\n"
"v_mul_f32 v231, v231, v230\n"
"v_accvgpr_write_b32 a134, v231\n"
"v_accvgpr_read_b32 v231, a135\n"
"v_mul_f32 v231, v231, v230\n"
"v_accvgpr_write_b32 a135, v231\n"
"v_sub_f32 v132, v132, v228\n"
"v_sub_f32 v133, v133, v228\n"
"v_sub_f32 v134, v134, v228\n"
"v_sub_f32 v135, v135, v228\n"
"v_sub_f32 v148, v148, v228\n"
"v_sub_f32 v149, v149, v228\n"
"v_sub_f32 v150, v150, v228\n"
"v_sub_f32 v151, v151, v228\n"
"v_exp_f32 v132, v132\n"
"v_exp_f32 v133, v133\n"
"v_exp_f32 v134, v134\n"
"v_exp_f32 v135, v135\n"
"v_exp_f32 v148, v148\n"
"v_exp_f32 v149, v149\n"
"v_exp_f32 v150, v150\n"
"v_exp_f32 v151, v151\n"
"s_nop 0\n"
"v_cvt_pk_bf16_f32 v196, v132, v133\n"
"v_cvt_pk_bf16_f32 v197, v134, v135\n"
"v_cvt_pk_bf16_f32 v198, v148, v149\n"
"v_cvt_pk_bf16_f32 v199, v150, v151\n"
"v_mul_f32 v8, v8, v234\n"
"v_mul_f32 v9, v9, v234\n"
"v_mul_f32 v10, v10, v234\n"
"v_mul_f32 v11, v11, v234\n"
"v_mul_f32 v24, v24, v234\n"
"v_mul_f32 v25, v25, v234\n"
"v_mul_f32 v26, v26, v234\n"
"v_mul_f32 v27, v27, v234\n"
"v_mul_f32 v40, v40, v234\n"
"v_mul_f32 v41, v41, v234\n"
"v_mul_f32 v42, v42, v234\n"
"v_mul_f32 v43, v43, v234\n"
"v_mul_f32 v56, v56, v234\n"
"v_mul_f32 v57, v57, v234\n"
"v_mul_f32 v58, v58, v234\n"
"v_mul_f32 v59, v59, v234\n"
"v_mul_f32 v72, v72, v234\n"
"v_mul_f32 v73, v73, v234\n"
"v_mul_f32 v74, v74, v234\n"
"v_mul_f32 v75, v75, v234\n"
"v_mul_f32 v88, v88, v234\n"
"v_mul_f32 v89, v89, v234\n"
"v_mul_f32 v90, v90, v234\n"
"v_mul_f32 v91, v91, v234\n"
"v_mul_f32 v104, v104, v234\n"
"v_mul_f32 v105, v105, v234\n"
"v_mul_f32 v106, v106, v234\n"
"v_mul_f32 v107, v107, v234\n"
"v_mul_f32 v120, v120, v234\n"
"v_mul_f32 v121, v121, v234\n"
"v_mul_f32 v122, v122, v234\n"
"v_mul_f32 v123, v123, v234\n"
"v_accvgpr_read_b32 v235, a136\n"
"v_mul_f32 v235, v235, v234\n"
"v_accvgpr_write_b32 a136, v235\n"
"v_accvgpr_read_b32 v235, a137\n"
"v_mul_f32 v235, v235, v234\n"
"v_accvgpr_write_b32 a137, v235\n"
"v_accvgpr_read_b32 v235, a138\n"
"v_mul_f32 v235, v235, v234\n"
"v_accvgpr_write_b32 a138, v235\n"
"v_accvgpr_read_b32 v235, a139\n"
"v_mul_f32 v235, v235, v234\n"
"v_accvgpr_write_b32 a139, v235\n"
"v_sub_f32 v136, v136, v232\n"
"v_sub_f32 v137, v137, v232\n"
"v_sub_f32 v138, v138, v232\n"
"v_sub_f32 v139, v139, v232\n"
"v_sub_f32 v152, v152, v232\n"
"v_sub_f32 v153, v153, v232\n"
"v_sub_f32 v154, v154, v232\n"
"v_sub_f32 v155, v155, v232\n"
"v_exp_f32 v136, v136\n"
"v_exp_f32 v137, v137\n"
"v_exp_f32 v138, v138\n"
"v_exp_f32 v139, v139\n"
"v_exp_f32 v152, v152\n"
"v_exp_f32 v153, v153\n"
"v_exp_f32 v154, v154\n"
"v_exp_f32 v155, v155\n"
"s_nop 0\n"
"v_cvt_pk_bf16_f32 v200, v136, v137\n"
"v_cvt_pk_bf16_f32 v201, v138, v139\n"
"v_cvt_pk_bf16_f32 v202, v152, v153\n"
"v_cvt_pk_bf16_f32 v203, v154, v155\n"
"v_mul_f32 v12, v12, v238\n"
"v_mul_f32 v13, v13, v238\n"
"v_mul_f32 v14, v14, v238\n"
"v_mul_f32 v15, v15, v238\n"
"v_mul_f32 v28, v28, v238\n"
"v_mul_f32 v29, v29, v238\n"
"v_mul_f32 v30, v30, v238\n"
"v_mul_f32 v31, v31, v238\n"
"v_mul_f32 v44, v44, v238\n"
"v_mul_f32 v45, v45, v238\n"
"v_mul_f32 v46, v46, v238\n"
"v_mul_f32 v47, v47, v238\n"
"v_mul_f32 v60, v60, v238\n"
"v_mul_f32 v61, v61, v238\n"
"v_mul_f32 v62, v62, v238\n"
"v_mul_f32 v63, v63, v238\n"
"v_mul_f32 v76, v76, v238\n"
"v_mul_f32 v77, v77, v238\n"
"v_mul_f32 v78, v78, v238\n"
"v_mul_f32 v79, v79, v238\n"
"v_mul_f32 v92, v92, v238\n"
"v_mul_f32 v93, v93, v238\n"
"v_mul_f32 v94, v94, v238\n"
"v_mul_f32 v95, v95, v238\n"
"v_mul_f32 v108, v108, v238\n"
"v_mul_f32 v109, v109, v238\n"
"v_mul_f32 v110, v110, v238\n"
"v_mul_f32 v111, v111, v238\n"
"v_mul_f32 v124, v124, v238\n"
"v_mul_f32 v125, v125, v238\n"
"v_mul_f32 v126, v126, v238\n"
"v_mul_f32 v127, v127, v238\n"
"v_accvgpr_read_b32 v239, a140\n"
"v_mul_f32 v239, v239, v238\n"
"v_accvgpr_write_b32 a140, v239\n"
"v_accvgpr_read_b32 v239, a141\n"
"v_mul_f32 v239, v239, v238\n"
"v_accvgpr_write_b32 a141, v239\n"
"v_accvgpr_read_b32 v239, a142\n"
"v_mul_f32 v239, v239, v238\n"
"v_accvgpr_write_b32 a142, v239\n"
"v_accvgpr_read_b32 v239, a143\n"
"v_mul_f32 v239, v239, v238\n"
"v_accvgpr_write_b32 a143, v239\n"
"v_sub_f32 v140, v140, v236\n"
"v_sub_f32 v141, v141, v236\n"
"v_sub_f32 v142, v142, v236\n"
"v_sub_f32 v143, v143, v236\n"
"v_sub_f32 v156, v156, v236\n"
"v_sub_f32 v157, v157, v236\n"
"v_sub_f32 v158, v158, v236\n"
"v_sub_f32 v159, v159, v236\n"
"v_exp_f32 v140, v140\n"
"v_exp_f32 v141, v141\n"
"v_exp_f32 v142, v142\n"
"v_exp_f32 v143, v143\n"
"v_exp_f32 v156, v156\n"
"v_exp_f32 v157, v157\n"
"v_exp_f32 v158, v158\n"
"v_exp_f32 v159, v159\n"
"s_nop 0\n"
"v_cvt_pk_bf16_f32 v204, v140, v141\n"
"v_cvt_pk_bf16_f32 v205, v142, v143\n"
"v_cvt_pk_bf16_f32 v206, v156, v157\n"
"v_cvt_pk_bf16_f32 v207, v158, v159\n"
"s_nop 1\n"
"v_mfma_f32_16x16x32_bf16 v[0:3], a[96:99], v[192:195], v[0:3]\n"
"v_mfma_f32_16x16x32_bf16 v[4:7], a[96:99], v[196:199], v[4:7]\n"
"v_mfma_f32_16x16x32_bf16 v[8:11], a[96:99], v[200:203], v[8:11]\n"
"v_mfma_f32_16x16x32_bf16 v[12:15], a[96:99], v[204:207], v[12:15]\n"
"v_mfma_f32_16x16x32_bf16 v[16:19], a[100:103], v[192:195], v[16:19]\n"
"v_mfma_f32_16x16x32_bf16 v[20:23], a[100:103], v[196:199], v[20:23]\n"
"v_mfma_f32_16x16x32_bf16 v[24:27], a[100:103], v[200:203], v[24:27]\n"
"v_mfma_f32_16x16x32_bf16 v[28:31], a[100:103], v[204:207], v[28:31]\n"
"v_mfma_f32_16x16x32_bf16 v[32:35], a[104:107], v[192:195], v[32:35]\n"
"v_mfma_f32_16x16x32_bf16 v[36:39], a[104:107], v[196:199], v[36:39]\n"
"v_mfma_f32_16x16x32_bf16 v[40:43], a[104:107], v[200:203], v[40:43]\n"
"v_mfma_f32_16x16x32_bf16 v[44:47], a[104:107], v[204:207], v[44:47]\n"
"v_mfma_f32_16x16x32_bf16 v[48:51], a[108:111], v[192:195], v[48:51]\n"
"v_mfma_f32_16x16x32_bf16 v[52:55], a[108:111], v[196:199], v[52:55]\n"
"v_mfma_f32_16x16x32_bf16 v[56:59], a[108:111], v[200:203], v[56:59]\n"
"v_mfma_f32_16x16x32_bf16 v[60:63], a[108:111], v[204:207], v[60:63]\n"
"v_mfma_f32_16x16x32_bf16 v[64:67], a[112:115], v[192:195], v[64:67]\n"
"v_mfma_f32_16x16x32_bf16 v[68:71], a[112:115], v[196:199], v[68:71]\n"
"v_mfma_f32_16x16x32_bf16 v[72:75], a[112:115], v[200:203], v[72:75]\n"
"v_mfma_f32_16x16x32_bf16 v[76:79], a[112:115], v[204:207], v[76:79]\n"
"v_mfma_f32_16x16x32_bf16 v[80:83], a[116:119], v[192:195], v[80:83]\n"
"v_mfma_f32_16x16x32_bf16 v[84:87], a[116:119], v[196:199], v[84:87]\n"
"v_mfma_f32_16x16x32_bf16 v[88:91], a[116:119], v[200:203], v[88:91]\n"
"v_mfma_f32_16x16x32_bf16 v[92:95], a[116:119], v[204:207], v[92:95]\n"
"v_mfma_f32_16x16x32_bf16 v[96:99], a[120:123], v[192:195], v[96:99]\n"
"v_mfma_f32_16x16x32_bf16 v[100:103], a[120:123], v[196:199], v[100:103]\n"
"v_mfma_f32_16x16x32_bf16 v[104:107], a[120:123], v[200:203], v[104:107]\n"
"v_mfma_f32_16x16x32_bf16 v[108:111], a[120:123], v[204:207], v[108:111]\n"
"v_mfma_f32_16x16x32_bf16 v[112:115], a[124:127], v[192:195], v[112:115]\n"
"v_mfma_f32_16x16x32_bf16 v[116:119], a[124:127], v[196:199], v[116:119]\n"
"v_mfma_f32_16x16x32_bf16 v[120:123], a[124:127], v[200:203], v[120:123]\n"
"v_mfma_f32_16x16x32_bf16 v[124:127], a[124:127], v[204:207], v[124:127]\n"
"v_mfma_f32_16x16x32_bf16 a[128:131], a[144:147], v[192:195], a[128:131]\n"
"v_mfma_f32_16x16x32_bf16 a[132:135], a[144:147], v[196:199], a[132:135]\n"
"v_mfma_f32_16x16x32_bf16 a[136:139], a[144:147], v[200:203], a[136:139]\n"
"v_mfma_f32_16x16x32_bf16 a[140:143], a[144:147], v[204:207], a[140:143]\n"
"s_nop 7\n"
"ds_read_b128 a[64:67], v240 offset:8192\n"
"ds_read_b128 a[68:71], v241 offset:8192\n"
"ds_read_b128 a[72:75], v242 offset:8192\n"
"ds_read_b128 a[76:79], v243 offset:8192\n"
"ds_read_b128 a[80:83], v240 offset:12288\n"
"ds_read_b128 a[84:87], v241 offset:12288\n"
"ds_read_b128 a[88:91], v242 offset:12288\n"
"ds_read_b128 a[92:95], v243 offset:12288\n"
"ds_read_b64_tr_b16 a[96:97], v244 offset:40960\n"
"ds_read_b64_tr_b16 a[98:99], v244 offset:45056\n"
"ds_read_b64_tr_b16 a[100:101], v245 offset:40960\n"
"ds_read_b64_tr_b16 a[102:103], v245 offset:45056\n"
"ds_read_b64_tr_b16 a[104:105], v244 offset:41472\n"
"ds_read_b64_tr_b16 a[106:107], v244 offset:45568\n"
"ds_read_b64_tr_b16 a[108:109], v245 offset:41472\n"
"ds_read_b64_tr_b16 a[110:111], v245 offset:45568\n"
"ds_read_b64_tr_b16 a[112:113], v244 offset:41984\n"
"ds_read_b64_tr_b16 a[114:115], v244 offset:46080\n"
"ds_read_b64_tr_b16 a[116:117], v245 offset:41984\n"
"ds_read_b64_tr_b16 a[118:119], v245 offset:46080\n"
"ds_read_b64_tr_b16 a[120:121], v244 offset:42496\n"
"ds_read_b64_tr_b16 a[122:123], v244 offset:46592\n"
"ds_read_b64_tr_b16 a[124:125], v245 offset:42496\n"
"ds_read_b64_tr_b16 a[126:127], v245 offset:46592\n"
"s_waitcnt lgkmcnt(0)\n"
"v_mfma_f32_16x16x32_bf16 v[128:131], a[64:67], a[0:3], 0\n"
"v_mfma_f32_16x16x32_bf16 v[132:135], a[64:67], a[16:19], 0\n"
"v_mfma_f32_16x16x32_bf16 v[136:139], a[64:67], a[32:35], 0\n"
"v_mfma_f32_16x16x32_bf16 v[140:143], a[64:67], a[48:51], 0\n"
"v_mfma_f32_16x16x32_bf16 v[128:131], a[68:71], a[4:7], v[128:131]\n"
"v_mfma_f32_16x16x32_bf16 v[132:135], a[68:71], a[20:23], v[132:135]\n"
"v_mfma_f32_16x16x32_bf16 v[136:139], a[68:71], a[36:39], v[136:139]\n"
"v_mfma_f32_16x16x32_bf16 v[140:143], a[68:71], a[52:55], v[140:143]\n"
"v_mfma_f32_16x16x32_bf16 v[128:131], a[72:75], a[8:11], v[128:131]\n"
"v_mfma_f32_16x16x32_bf16 v[132:135], a[72:75], a[24:27], v[132:135]\n"
"v_mfma_f32_16x16x32_bf16 v[136:139], a[72:75], a[40:43], v[136:139]\n"
"v_mfma_f32_16x16x32_bf16 v[140:143], a[72:75], a[56:59], v[140:143]\n"
"v_mfma_f32_16x16x32_bf16 v[128:131], a[76:79], a[12:15], v[128:131]\n"
"v_mfma_f32_16x16x32_bf16 v[132:135], a[76:79], a[28:31], v[132:135]\n"
"v_mfma_f32_16x16x32_bf16 v[136:139], a[76:79], a[44:47], v[136:139]\n"
"v_mfma_f32_16x16x32_bf16 v[140:143], a[76:79], a[60:63], v[140:143]\n"
"v_mfma_f32_16x16x32_bf16 v[144:147], a[80:83], a[0:3], 0\n"
"v_mfma_f32_16x16x32_bf16 v[148:151], a[80:83], a[16:19], 0\n"
"v_mfma_f32_16x16x32_bf16 v[152:155], a[80:83], a[32:35], 0\n"
"v_mfma_f32_16x16x32_bf16 v[156:159], a[80:83], a[48:51], 0\n"
"v_mfma_f32_16x16x32_bf16 v[144:147], a[84:87], a[4:7], v[144:147]\n"
"v_mfma_f32_16x16x32_bf16 v[148:151], a[84:87], a[20:23], v[148:151]\n"
"v_mfma_f32_16x16x32_bf16 v[152:155], a[84:87], a[36:39], v[152:155]\n"
"v_mfma_f32_16x16x32_bf16 v[156:159], a[84:87], a[52:55], v[156:159]\n"
"v_mfma_f32_16x16x32_bf16 v[144:147], a[88:91], a[8:11], v[144:147]\n"
"v_mfma_f32_16x16x32_bf16 v[148:151], a[88:91], a[24:27], v[148:151]\n"
"v_mfma_f32_16x16x32_bf16 v[152:155], a[88:91], a[40:43], v[152:155]\n"
"v_mfma_f32_16x16x32_bf16 v[156:159], a[88:91], a[56:59], v[156:159]\n"
"v_mfma_f32_16x16x32_bf16 v[144:147], a[92:95], a[12:15], v[144:147]\n"
"v_mfma_f32_16x16x32_bf16 v[148:151], a[92:95], a[28:31], v[148:151]\n"
"v_mfma_f32_16x16x32_bf16 v[152:155], a[92:95], a[44:47], v[152:155]\n"
"v_mfma_f32_16x16x32_bf16 v[156:159], a[92:95], a[60:63], v[156:159]\n"
"s_nop 7\n"
"v_max3_f32 v225, v128, v129, v130\n"
"v_max3_f32 v225, v225, v131, v144\n"
"v_max3_f32 v225, v225, v145, v146\n"
"v_max_f32 v225, v225, v147\n"
"v_max3_f32 v229, v132, v133, v134\n"
"v_max3_f32 v229, v229, v135, v148\n"
"v_max3_f32 v229, v229, v149, v150\n"
"v_max_f32 v229, v229, v151\n"
"v_max3_f32 v233, v136, v137, v138\n"
"v_max3_f32 v233, v233, v139, v152\n"
"v_max3_f32 v233, v233, v153, v154\n"
"v_max_f32 v233, v233, v155\n"
"v_max3_f32 v237, v140, v141, v142\n"
"v_max3_f32 v237, v237, v143, v156\n"
"v_max3_f32 v237, v237, v157, v158\n"
"v_max_f32 v237, v237, v159\n"
"ds_bpermute_b32 v226, v222, v225\n"
"ds_bpermute_b32 v230, v222, v229\n"
"ds_bpermute_b32 v234, v222, v233\n"
"ds_bpermute_b32 v238, v222, v237\n"
"s_waitcnt lgkmcnt(0)\n"
"v_max_f32 v225, v225, v226\n"
"v_max_f32 v229, v229, v230\n"
"v_max_f32 v233, v233, v234\n"
"v_max_f32 v237, v237, v238\n"
"ds_bpermute_b32 v226, v223, v225\n"
"ds_bpermute_b32 v230, v223, v229\n"
"ds_bpermute_b32 v234, v223, v233\n"
"ds_bpermute_b32 v238, v223, v237\n"
"s_waitcnt lgkmcnt(0)\n"
"v_max_f32 v225, v225, v226\n"
"v_max_f32 v229, v229, v230\n"
"v_max_f32 v233, v233, v234\n"
"v_max_f32 v237, v237, v238\n"
"v_max_f32 v225, v225, v224\n"
"v_sub_f32 v226, v224, v225\n"
"v_exp_f32 v226, v226\n"
"v_mov_b32 v224, v225\n"
"v_max_f32 v229, v229, v228\n"
"v_sub_f32 v230, v228, v229\n"
"v_exp_f32 v230, v230\n"
"v_mov_b32 v228, v229\n"
"v_max_f32 v233, v233, v232\n"
"v_sub_f32 v234, v232, v233\n"
"v_exp_f32 v234, v234\n"
"v_mov_b32 v232, v233\n"
"v_max_f32 v237, v237, v236\n"
"v_sub_f32 v238, v236, v237\n"
"v_exp_f32 v238, v238\n"
"v_mov_b32 v236, v237\n"
"v_mul_f32 v0, v0, v226\n"
"v_mul_f32 v1, v1, v226\n"
"v_mul_f32 v2, v2, v226\n"
"v_mul_f32 v3, v3, v226\n"
"v_mul_f32 v16, v16, v226\n"
"v_mul_f32 v17, v17, v226\n"
"v_mul_f32 v18, v18, v226\n"
"v_mul_f32 v19, v19, v226\n"
"v_mul_f32 v32, v32, v226\n"
"v_mul_f32 v33, v33, v226\n"
"v_mul_f32 v34, v34, v226\n"
"v_mul_f32 v35, v35, v226\n"
"v_mul_f32 v48, v48, v226\n"
"v_mul_f32 v49, v49, v226\n"
"v_mul_f32 v50, v50, v226\n"
"v_mul_f32 v51, v51, v226\n"
"v_mul_f32 v64, v64, v226\n"
"v_mul_f32 v65, v65, v226\n"
"v_mul_f32 v66, v66, v226\n"
"v_mul_f32 v67, v67, v226\n"
"v_mul_f32 v80, v80, v226\n"
"v_mul_f32 v81, v81, v226\n"
"v_mul_f32 v82, v82, v226\n"
"v_mul_f32 v83, v83, v226\n"
"v_mul_f32 v96, v96, v226\n"
"v_mul_f32 v97, v97, v226\n"
"v_mul_f32 v98, v98, v226\n"
"v_mul_f32 v99, v99, v226\n"
"v_mul_f32 v112, v112, v226\n"
"v_mul_f32 v113, v113, v226\n"
"v_mul_f32 v114, v114, v226\n"
"v_mul_f32 v115, v115, v226\n"
"v_accvgpr_read_b32 v227, a128\n"
"v_mul_f32 v227, v227, v226\n"
"v_accvgpr_write_b32 a128, v227\n"
"v_accvgpr_read_b32 v227, a129\n"
"v_mul_f32 v227, v227, v226\n"
"v_accvgpr_write_b32 a129, v227\n"
"v_accvgpr_read_b32 v227, a130\n"
"v_mul_f32 v227, v227, v226\n"
"v_accvgpr_write_b32 a130, v227\n"
"v_accvgpr_read_b32 v227, a131\n"
"v_mul_f32 v227, v227, v226\n"
"v_accvgpr_write_b32 a131, v227\n"
"v_sub_f32 v128, v128, v224\n"
"v_sub_f32 v129, v129, v224\n"
"v_sub_f32 v130, v130, v224\n"
"v_sub_f32 v131, v131, v224\n"
"v_sub_f32 v144, v144, v224\n"
"v_sub_f32 v145, v145, v224\n"
"v_sub_f32 v146, v146, v224\n"
"v_sub_f32 v147, v147, v224\n"
"v_exp_f32 v128, v128\n"
"v_exp_f32 v129, v129\n"
"v_exp_f32 v130, v130\n"
"v_exp_f32 v131, v131\n"
"v_exp_f32 v144, v144\n"
"v_exp_f32 v145, v145\n"
"v_exp_f32 v146, v146\n"
"v_exp_f32 v147, v147\n"
"s_nop 0\n"
"v_cvt_pk_bf16_f32 v192, v128, v129\n"
"v_cvt_pk_bf16_f32 v193, v130, v131\n"
"v_cvt_pk_bf16_f32 v194, v144, v145\n"
"v_cvt_pk_bf16_f32 v195, v146, v147\n"
"v_mul_f32 v4, v4, v230\n"
"v_mul_f32 v5, v5, v230\n"
"v_mul_f32 v6, v6, v230\n"
"v_mul_f32 v7, v7, v230\n"
"v_mul_f32 v20, v20, v230\n"
"v_mul_f32 v21, v21, v230\n"
"v_mul_f32 v22, v22, v230\n"
"v_mul_f32 v23, v23, v230\n"
"v_mul_f32 v36, v36, v230\n"
"v_mul_f32 v37, v37, v230\n"
"v_mul_f32 v38, v38, v230\n"
"v_mul_f32 v39, v39, v230\n"
"v_mul_f32 v52, v52, v230\n"
"v_mul_f32 v53, v53, v230\n"
"v_mul_f32 v54, v54, v230\n"
"v_mul_f32 v55, v55, v230\n"
"v_mul_f32 v68, v68, v230\n"
"v_mul_f32 v69, v69, v230\n"
"v_mul_f32 v70, v70, v230\n"
"v_mul_f32 v71, v71, v230\n"
"v_mul_f32 v84, v84, v230\n"
"v_mul_f32 v85, v85, v230\n"
"v_mul_f32 v86, v86, v230\n"
"v_mul_f32 v87, v87, v230\n"
"v_mul_f32 v100, v100, v230\n"
"v_mul_f32 v101, v101, v230\n"
"v_mul_f32 v102, v102, v230\n"
"v_mul_f32 v103, v103, v230\n"
"v_mul_f32 v116, v116, v230\n"
"v_mul_f32 v117, v117, v230\n"
"v_mul_f32 v118, v118, v230\n"
"v_mul_f32 v119, v119, v230\n"
"v_accvgpr_read_b32 v231, a132\n"
"v_mul_f32 v231, v231, v230\n"
"v_accvgpr_write_b32 a132, v231\n"
"v_accvgpr_read_b32 v231, a133\n"
"v_mul_f32 v231, v231, v230\n"
"v_accvgpr_write_b32 a133, v231\n"
"v_accvgpr_read_b32 v231, a134\n"
"v_mul_f32 v231, v231, v230\n"
"v_accvgpr_write_b32 a134, v231\n"
"v_accvgpr_read_b32 v231, a135\n"
"v_mul_f32 v231, v231, v230\n"
"v_accvgpr_write_b32 a135, v231\n"
"v_sub_f32 v132, v132, v228\n"
"v_sub_f32 v133, v133, v228\n"
"v_sub_f32 v134, v134, v228\n"
"v_sub_f32 v135, v135, v228\n"
"v_sub_f32 v148, v148, v228\n"
"v_sub_f32 v149, v149, v228\n"
"v_sub_f32 v150, v150, v228\n"
"v_sub_f32 v151, v151, v228\n"
"v_exp_f32 v132, v132\n"
"v_exp_f32 v133, v133\n"
"v_exp_f32 v134, v134\n"
"v_exp_f32 v135, v135\n"
"v_exp_f32 v148, v148\n"
"v_exp_f32 v149, v149\n"
"v_exp_f32 v150, v150\n"
"v_exp_f32 v151, v151\n"
"s_nop 0\n"
"v_cvt_pk_bf16_f32 v196, v132, v133\n"
"v_cvt_pk_bf16_f32 v197, v134, v135\n"
"v_cvt_pk_bf16_f32 v198, v148, v149\n"
"v_cvt_pk_bf16_f32 v199, v150, v151\n"
"v_mul_f32 v8, v8, v234\n"
"v_mul_f32 v9, v9, v234\n"
"v_mul_f32 v10, v10, v234\n"
"v_mul_f32 v11, v11, v234\n"
"v_mul_f32 v24, v24, v234\n"
"v_mul_f32 v25, v25, v234\n"
"v_mul_f32 v26, v26, v234\n"
"v_mul_f32 v27, v27, v234\n"
"v_mul_f32 v40, v40, v234\n"
"v_mul_f32 v41, v41, v234\n"
"v_mul_f32 v42, v42, v234\n"
"v_mul_f32 v43, v43, v234\n"
"v_mul_f32 v56, v56, v234\n"
"v_mul_f32 v57, v57, v234\n"
"v_mul_f32 v58, v58, v234\n"
"v_mul_f32 v59, v59, v234\n"
"v_mul_f32 v72, v72, v234\n"
"v_mul_f32 v73, v73, v234\n"
"v_mul_f32 v74, v74, v234\n"
"v_mul_f32 v75, v75, v234\n"
"v_mul_f32 v88, v88, v234\n"
"v_mul_f32 v89, v89, v234\n"
"v_mul_f32 v90, v90, v234\n"
"v_mul_f32 v91, v91, v234\n"
"v_mul_f32 v104, v104, v234\n"
"v_mul_f32 v105, v105, v234\n"
"v_mul_f32 v106, v106, v234\n"
"v_mul_f32 v107, v107, v234\n"
"v_mul_f32 v120, v120, v234\n"
"v_mul_f32 v121, v121, v234\n"
"v_mul_f32 v122, v122, v234\n"
"v_mul_f32 v123, v123, v234\n"
"v_accvgpr_read_b32 v235, a136\n"
"v_mul_f32 v235, v235, v234\n"
"v_accvgpr_write_b32 a136, v235\n"
"v_accvgpr_read_b32 v235, a137\n"
"v_mul_f32 v235, v235, v234\n"
"v_accvgpr_write_b32 a137, v235\n"
"v_accvgpr_read_b32 v235, a138\n"
"v_mul_f32 v235, v235, v234\n"
"v_accvgpr_write_b32 a138, v235\n"
"v_accvgpr_read_b32 v235, a139\n"
"v_mul_f32 v235, v235, v234\n"
"v_accvgpr_write_b32 a139, v235\n"
"v_sub_f32 v136, v136, v232\n"
"v_sub_f32 v137, v137, v232\n"
"v_sub_f32 v138, v138, v232\n"
"v_sub_f32 v139, v139, v232\n"
"v_sub_f32 v152, v152, v232\n"
"v_sub_f32 v153, v153, v232\n"
"v_sub_f32 v154, v154, v232\n"
"v_sub_f32 v155, v155, v232\n"
"v_exp_f32 v136, v136\n"
"v_exp_f32 v137, v137\n"
"v_exp_f32 v138, v138\n"
"v_exp_f32 v139, v139\n"
"v_exp_f32 v152, v152\n"
"v_exp_f32 v153, v153\n"
"v_exp_f32 v154, v154\n"
"v_exp_f32 v155, v155\n"
"s_nop 0\n"
"v_cvt_pk_bf16_f32 v200, v136, v137\n"
"v_cvt_pk_bf16_f32 v201, v138, v139\n"
"v_cvt_pk_bf16_f32 v202, v152, v153\n"
"v_cvt_pk_bf16_f32 v203, v154, v155\n"
"v_mul_f32 v12, v12, v238\n"
"v_mul_f32 v13, v13, v238\n"
"v_mul_f32 v14, v14, v238\n"
"v_mul_f32 v15, v15, v238\n"
"v_mul_f32 v28, v28, v238\n"
"v_mul_f32 v29, v29, v238\n"
"v_mul_f32 v30, v30, v238\n"
"v_mul_f32 v31, v31, v238\n"
"v_mul_f32 v44, v44, v238\n"
"v_mul_f32 v45, v45, v238\n"
"v_mul_f32 v46, v46, v238\n"
"v_mul_f32 v47, v47, v238\n"
"v_mul_f32 v60, v60, v238\n"
"v_mul_f32 v61, v61, v238\n"
"v_mul_f32 v62, v62, v238\n"
"v_mul_f32 v63, v63, v238\n"
"v_mul_f32 v76, v76, v238\n"
"v_mul_f32 v77, v77, v238\n"
"v_mul_f32 v78, v78, v238\n"
"v_mul_f32 v79, v79, v238\n"
"v_mul_f32 v92, v92, v238\n"
"v_mul_f32 v93, v93, v238\n"
"v_mul_f32 v94, v94, v238\n"
"v_mul_f32 v95, v95, v238\n"
"v_mul_f32 v108, v108, v238\n"
"v_mul_f32 v109, v109, v238\n"
"v_mul_f32 v110, v110, v238\n"
"v_mul_f32 v111, v111, v238\n"
"v_mul_f32 v124, v124, v238\n"
"v_mul_f32 v125, v125, v238\n"
"v_mul_f32 v126, v126, v238\n"
"v_mul_f32 v127, v127, v238\n"
"v_accvgpr_read_b32 v239, a140\n"
"v_mul_f32 v239, v239, v238\n"
"v_accvgpr_write_b32 a140, v239\n"
"v_accvgpr_read_b32 v239, a141\n"
"v_mul_f32 v239, v239, v238\n"
"v_accvgpr_write_b32 a141, v239\n"
"v_accvgpr_read_b32 v239, a142\n"
"v_mul_f32 v239, v239, v238\n"
"v_accvgpr_write_b32 a142, v239\n"
"v_accvgpr_read_b32 v239, a143\n"
"v_mul_f32 v239, v239, v238\n"
"v_accvgpr_write_b32 a143, v239\n"
"v_sub_f32 v140, v140, v236\n"
"v_sub_f32 v141, v141, v236\n"
"v_sub_f32 v142, v142, v236\n"
"v_sub_f32 v143, v143, v236\n"
"v_sub_f32 v156, v156, v236\n"
"v_sub_f32 v157, v157, v236\n"
"v_sub_f32 v158, v158, v236\n"
"v_sub_f32 v159, v159, v236\n"
"v_exp_f32 v140, v140\n"
"v_exp_f32 v141, v141\n"
"v_exp_f32 v142, v142\n"
"v_exp_f32 v143, v143\n"
"v_exp_f32 v156, v156\n"
"v_exp_f32 v157, v157\n"
"v_exp_f32 v158, v158\n"
"v_exp_f32 v159, v159\n"
"s_nop 0\n"
"v_cvt_pk_bf16_f32 v204, v140, v141\n"
"v_cvt_pk_bf16_f32 v205, v142, v143\n"
"v_cvt_pk_bf16_f32 v206, v156, v157\n"
"v_cvt_pk_bf16_f32 v207, v158, v159\n"
"s_nop 1\n"
"v_mfma_f32_16x16x32_bf16 v[0:3], a[96:99], v[192:195], v[0:3]\n"
"v_mfma_f32_16x16x32_bf16 v[4:7], a[96:99], v[196:199], v[4:7]\n"
"v_mfma_f32_16x16x32_bf16 v[8:11], a[96:99], v[200:203], v[8:11]\n"
"v_mfma_f32_16x16x32_bf16 v[12:15], a[96:99], v[204:207], v[12:15]\n"
"v_mfma_f32_16x16x32_bf16 v[16:19], a[100:103], v[192:195], v[16:19]\n"
"v_mfma_f32_16x16x32_bf16 v[20:23], a[100:103], v[196:199], v[20:23]\n"
"v_mfma_f32_16x16x32_bf16 v[24:27], a[100:103], v[200:203], v[24:27]\n"
"v_mfma_f32_16x16x32_bf16 v[28:31], a[100:103], v[204:207], v[28:31]\n"
"v_mfma_f32_16x16x32_bf16 v[32:35], a[104:107], v[192:195], v[32:35]\n"
"v_mfma_f32_16x16x32_bf16 v[36:39], a[104:107], v[196:199], v[36:39]\n"
"v_mfma_f32_16x16x32_bf16 v[40:43], a[104:107], v[200:203], v[40:43]\n"
"v_mfma_f32_16x16x32_bf16 v[44:47], a[104:107], v[204:207], v[44:47]\n"
"v_mfma_f32_16x16x32_bf16 v[48:51], a[108:111], v[192:195], v[48:51]\n"
"v_mfma_f32_16x16x32_bf16 v[52:55], a[108:111], v[196:199], v[52:55]\n"
"v_mfma_f32_16x16x32_bf16 v[56:59], a[108:111], v[200:203], v[56:59]\n"
"v_mfma_f32_16x16x32_bf16 v[60:63], a[108:111], v[204:207], v[60:63]\n"
"v_mfma_f32_16x16x32_bf16 v[64:67], a[112:115], v[192:195], v[64:67]\n"
"v_mfma_f32_16x16x32_bf16 v[68:71], a[112:115], v[196:199], v[68:71]\n"
"v_mfma_f32_16x16x32_bf16 v[72:75], a[112:115], v[200:203], v[72:75]\n"
"v_mfma_f32_16x16x32_bf16 v[76:79], a[112:115], v[204:207], v[76:79]\n"
"v_mfma_f32_16x16x32_bf16 v[80:83], a[116:119], v[192:195], v[80:83]\n"
"v_mfma_f32_16x16x32_bf16 v[84:87], a[116:119], v[196:199], v[84:87]\n"
"v_mfma_f32_16x16x32_bf16 v[88:91], a[116:119], v[200:203], v[88:91]\n"
"v_mfma_f32_16x16x32_bf16 v[92:95], a[116:119], v[204:207], v[92:95]\n"
"v_mfma_f32_16x16x32_bf16 v[96:99], a[120:123], v[192:195], v[96:99]\n"
"v_mfma_f32_16x16x32_bf16 v[100:103], a[120:123], v[196:199], v[100:103]\n"
"v_mfma_f32_16x16x32_bf16 v[104:107], a[120:123], v[200:203], v[104:107]\n"
"v_mfma_f32_16x16x32_bf16 v[108:111], a[120:123], v[204:207], v[108:111]\n"
"v_mfma_f32_16x16x32_bf16 v[112:115], a[124:127], v[192:195], v[112:115]\n"
"v_mfma_f32_16x16x32_bf16 v[116:119], a[124:127], v[196:199], v[116:119]\n"
"v_mfma_f32_16x16x32_bf16 v[120:123], a[124:127], v[200:203], v[120:123]\n"
"v_mfma_f32_16x16x32_bf16 v[124:127], a[124:127], v[204:207], v[124:127]\n"
"v_mfma_f32_16x16x32_bf16 a[128:131], a[144:147], v[192:195], a[128:131]\n"
"v_mfma_f32_16x16x32_bf16 a[132:135], a[144:147], v[196:199], a[132:135]\n"
"v_mfma_f32_16x16x32_bf16 a[136:139], a[144:147], v[200:203], a[136:139]\n"
"v_mfma_f32_16x16x32_bf16 a[140:143], a[144:147], v[204:207], a[140:143]\n"
"s_nop 7\n"
"ds_read_b128 a[64:67], v240 offset:16384\n"
"ds_read_b128 a[68:71], v241 offset:16384\n"
"ds_read_b128 a[72:75], v242 offset:16384\n"
"ds_read_b128 a[76:79], v243 offset:16384\n"
"ds_read_b128 a[80:83], v240 offset:20480\n"
"ds_read_b128 a[84:87], v241 offset:20480\n"
"ds_read_b128 a[88:91], v242 offset:20480\n"
"ds_read_b128 a[92:95], v243 offset:20480\n"
"ds_read_b64_tr_b16 a[96:97], v244 offset:49152\n"
"ds_read_b64_tr_b16 a[98:99], v244 offset:53248\n"
"ds_read_b64_tr_b16 a[100:101], v245 offset:49152\n"
"ds_read_b64_tr_b16 a[102:103], v245 offset:53248\n"
"ds_read_b64_tr_b16 a[104:105], v244 offset:49664\n"
"ds_read_b64_tr_b16 a[106:107], v244 offset:53760\n"
"ds_read_b64_tr_b16 a[108:109], v245 offset:49664\n"
"ds_read_b64_tr_b16 a[110:111], v245 offset:53760\n"
"ds_read_b64_tr_b16 a[112:113], v244 offset:50176\n"
"ds_read_b64_tr_b16 a[114:115], v244 offset:54272\n"
"ds_read_b64_tr_b16 a[116:117], v245 offset:50176\n"
"ds_read_b64_tr_b16 a[118:119], v245 offset:54272\n"
"ds_read_b64_tr_b16 a[120:121], v244 offset:50688\n"
"ds_read_b64_tr_b16 a[122:123], v244 offset:54784\n"
"ds_read_b64_tr_b16 a[124:125], v245 offset:50688\n"
"ds_read_b64_tr_b16 a[126:127], v245 offset:54784\n"
"s_waitcnt lgkmcnt(0)\n"
"v_mfma_f32_16x16x32_bf16 v[128:131], a[64:67], a[0:3], 0\n"
"v_mfma_f32_16x16x32_bf16 v[132:135], a[64:67], a[16:19], 0\n"
"v_mfma_f32_16x16x32_bf16 v[136:139], a[64:67], a[32:35], 0\n"
"v_mfma_f32_16x16x32_bf16 v[140:143], a[64:67], a[48:51], 0\n"
"v_mfma_f32_16x16x32_bf16 v[128:131], a[68:71], a[4:7], v[128:131]\n"
"v_mfma_f32_16x16x32_bf16 v[132:135], a[68:71], a[20:23], v[132:135]\n"
"v_mfma_f32_16x16x32_bf16 v[136:139], a[68:71], a[36:39], v[136:139]\n"
"v_mfma_f32_16x16x32_bf16 v[140:143], a[68:71], a[52:55], v[140:143]\n"
"v_mfma_f32_16x16x32_bf16 v[128:131], a[72:75], a[8:11], v[128:131]\n"
"v_mfma_f32_16x16x32_bf16 v[132:135], a[72:75], a[24:27], v[132:135]\n"
"v_mfma_f32_16x16x32_bf16 v[136:139], a[72:75], a[40:43], v[136:139]\n"
"v_mfma_f32_16x16x32_bf16 v[140:143], a[72:75], a[56:59], v[140:143]\n"
"v_mfma_f32_16x16x32_bf16 v[128:131], a[76:79], a[12:15], v[128:131]\n"
"v_mfma_f32_16x16x32_bf16 v[132:135], a[76:79], a[28:31], v[132:135]\n"
"v_mfma_f32_16x16x32_bf16 v[136:139], a[76:79], a[44:47], v[136:139]\n"
"v_mfma_f32_16x16x32_bf16 v[140:143], a[76:79], a[60:63], v[140:143]\n"
"v_mfma_f32_16x16x32_bf16 v[144:147], a[80:83], a[0:3], 0\n"
"v_mfma_f32_16x16x32_bf16 v[148:151], a[80:83], a[16:19], 0\n"
"v_mfma_f32_16x16x32_bf16 v[152:155], a[80:83], a[32:35], 0\n"
"v_mfma_f32_16x16x32_bf16 v[156:159], a[80:83], a[48:51], 0\n"
"v_mfma_f32_16x16x32_bf16 v[144:147], a[84:87], a[4:7], v[144:147]\n"
"v_mfma_f32_16x16x32_bf16 v[148:151], a[84:87], a[20:23], v[148:151]\n"
"v_mfma_f32_16x16x32_bf16 v[152:155], a[84:87], a[36:39], v[152:155]\n"
"v_mfma_f32_16x16x32_bf16 v[156:159], a[84:87], a[52:55], v[156:159]\n"
"v_mfma_f32_16x16x32_bf16 v[144:147], a[88:91], a[8:11], v[144:147]\n"
"v_mfma_f32_16x16x32_bf16 v[148:151], a[88:91], a[24:27], v[148:151]\n"
"v_mfma_f32_16x16x32_bf16 v[152:155], a[88:91], a[40:43], v[152:155]\n"
"v_mfma_f32_16x16x32_bf16 v[156:159], a[88:91], a[56:59], v[156:159]\n"
"v_mfma_f32_16x16x32_bf16 v[144:147], a[92:95], a[12:15], v[144:147]\n"
"v_mfma_f32_16x16x32_bf16 v[148:151], a[92:95], a[28:31], v[148:151]\n"
"v_mfma_f32_16x16x32_bf16 v[152:155], a[92:95], a[44:47], v[152:155]\n"
"v_mfma_f32_16x16x32_bf16 v[156:159], a[92:95], a[60:63], v[156:159]\n"
"s_nop 7\n"
"v_max3_f32 v225, v128, v129, v130\n"
"v_max3_f32 v225, v225, v131, v144\n"
"v_max3_f32 v225, v225, v145, v146\n"
"v_max_f32 v225, v225, v147\n"
"v_max3_f32 v229, v132, v133, v134\n"
"v_max3_f32 v229, v229, v135, v148\n"
"v_max3_f32 v229, v229, v149, v150\n"
"v_max_f32 v229, v229, v151\n"
"v_max3_f32 v233, v136, v137, v138\n"
"v_max3_f32 v233, v233, v139, v152\n"
"v_max3_f32 v233, v233, v153, v154\n"
"v_max_f32 v233, v233, v155\n"
"v_max3_f32 v237, v140, v141, v142\n"
"v_max3_f32 v237, v237, v143, v156\n"
"v_max3_f32 v237, v237, v157, v158\n"
"v_max_f32 v237, v237, v159\n"
"ds_bpermute_b32 v226, v222, v225\n"
"ds_bpermute_b32 v230, v222, v229\n"
"ds_bpermute_b32 v234, v222, v233\n"
"ds_bpermute_b32 v238, v222, v237\n"
"s_waitcnt lgkmcnt(0)\n"
"v_max_f32 v225, v225, v226\n"
"v_max_f32 v229, v229, v230\n"
"v_max_f32 v233, v233, v234\n"
"v_max_f32 v237, v237, v238\n"
"ds_bpermute_b32 v226, v223, v225\n"
"ds_bpermute_b32 v230, v223, v229\n"
"ds_bpermute_b32 v234, v223, v233\n"
"ds_bpermute_b32 v238, v223, v237\n"
"s_waitcnt lgkmcnt(0)\n"
"v_max_f32 v225, v225, v226\n"
"v_max_f32 v229, v229, v230\n"
"v_max_f32 v233, v233, v234\n"
"v_max_f32 v237, v237, v238\n"
"v_max_f32 v225, v225, v224\n"
"v_sub_f32 v226, v224, v225\n"
"v_exp_f32 v226, v226\n"
"v_mov_b32 v224, v225\n"
"v_max_f32 v229, v229, v228\n"
"v_sub_f32 v230, v228, v229\n"
"v_exp_f32 v230, v230\n"
"v_mov_b32 v228, v229\n"
"v_max_f32 v233, v233, v232\n"
"v_sub_f32 v234, v232, v233\n"
"v_exp_f32 v234, v234\n"
"v_mov_b32 v232, v233\n"
"v_max_f32 v237, v237, v236\n"
"v_sub_f32 v238, v236, v237\n"
"v_exp_f32 v238, v238\n"
"v_mov_b32 v236, v237\n"
"v_mul_f32 v0, v0, v226\n"
"v_mul_f32 v1, v1, v226\n"
"v_mul_f32 v2, v2, v226\n"
"v_mul_f32 v3, v3, v226\n"
"v_mul_f32 v16, v16, v226\n"
"v_mul_f32 v17, v17, v226\n"
"v_mul_f32 v18, v18, v226\n"
"v_mul_f32 v19, v19, v226\n"
"v_mul_f32 v32, v32, v226\n"
"v_mul_f32 v33, v33, v226\n"
"v_mul_f32 v34, v34, v226\n"
"v_mul_f32 v35, v35, v226\n"
"v_mul_f32 v48, v48, v226\n"
"v_mul_f32 v49, v49, v226\n"
"v_mul_f32 v50, v50, v226\n"
"v_mul_f32 v51, v51, v226\n"
"v_mul_f32 v64, v64, v226\n"
"v_mul_f32 v65, v65, v226\n"
"v_mul_f32 v66, v66, v226\n"
"v_mul_f32 v67, v67, v226\n"
"v_mul_f32 v80, v80, v226\n"
"v_mul_f32 v81, v81, v226\n"
"v_mul_f32 v82, v82, v226\n"
"v_mul_f32 v83, v83, v226\n"
"v_mul_f32 v96, v96, v226\n"
"v_mul_f32 v97, v97, v226\n"
"v_mul_f32 v98, v98, v226\n"
"v_mul_f32 v99, v99, v226\n"
"v_mul_f32 v112, v112, v226\n"
"v_mul_f32 v113, v113, v226\n"
"v_mul_f32 v114, v114, v226\n"
"v_mul_f32 v115, v115, v226\n"
"v_accvgpr_read_b32 v227, a128\n"
"v_mul_f32 v227, v227, v226\n"
"v_accvgpr_write_b32 a128, v227\n"
"v_accvgpr_read_b32 v227, a129\n"
"v_mul_f32 v227, v227, v226\n"
"v_accvgpr_write_b32 a129, v227\n"
"v_accvgpr_read_b32 v227, a130\n"
"v_mul_f32 v227, v227, v226\n"
"v_accvgpr_write_b32 a130, v227\n"
"v_accvgpr_read_b32 v227, a131\n"
"v_mul_f32 v227, v227, v226\n"
"v_accvgpr_write_b32 a131, v227\n"
"v_sub_f32 v128, v128, v224\n"
"v_sub_f32 v129, v129, v224\n"
"v_sub_f32 v130, v130, v224\n"
"v_sub_f32 v131, v131, v224\n"
"v_sub_f32 v144, v144, v224\n"
"v_sub_f32 v145, v145, v224\n"
"v_sub_f32 v146, v146, v224\n"
"v_sub_f32 v147, v147, v224\n"
"v_exp_f32 v128, v128\n"
"v_exp_f32 v129, v129\n"
"v_exp_f32 v130, v130\n"
"v_exp_f32 v131, v131\n"
"v_exp_f32 v144, v144\n"
"v_exp_f32 v145, v145\n"
"v_exp_f32 v146, v146\n"
"v_exp_f32 v147, v147\n"
"s_nop 0\n"
"v_cvt_pk_bf16_f32 v192, v128, v129\n"
"v_cvt_pk_bf16_f32 v193, v130, v131\n"
"v_cvt_pk_bf16_f32 v194, v144, v145\n"
"v_cvt_pk_bf16_f32 v195, v146, v147\n"
"v_mul_f32 v4, v4, v230\n"
"v_mul_f32 v5, v5, v230\n"
"v_mul_f32 v6, v6, v230\n"
"v_mul_f32 v7, v7, v230\n"
"v_mul_f32 v20, v20, v230\n"
"v_mul_f32 v21, v21, v230\n"
"v_mul_f32 v22, v22, v230\n"
"v_mul_f32 v23, v23, v230\n"
"v_mul_f32 v36, v36, v230\n"
"v_mul_f32 v37, v37, v230\n"
"v_mul_f32 v38, v38, v230\n"
"v_mul_f32 v39, v39, v230\n"
"v_mul_f32 v52, v52, v230\n"
"v_mul_f32 v53, v53, v230\n"
"v_mul_f32 v54, v54, v230\n"
"v_mul_f32 v55, v55, v230\n"
"v_mul_f32 v68, v68, v230\n"
"v_mul_f32 v69, v69, v230\n"
"v_mul_f32 v70, v70, v230\n"
"v_mul_f32 v71, v71, v230\n"
"v_mul_f32 v84, v84, v230\n"
"v_mul_f32 v85, v85, v230\n"
"v_mul_f32 v86, v86, v230\n"
"v_mul_f32 v87, v87, v230\n"
"v_mul_f32 v100, v100, v230\n"
"v_mul_f32 v101, v101, v230\n"
"v_mul_f32 v102, v102, v230\n"
"v_mul_f32 v103, v103, v230\n"
"v_mul_f32 v116, v116, v230\n"
"v_mul_f32 v117, v117, v230\n"
"v_mul_f32 v118, v118, v230\n"
"v_mul_f32 v119, v119, v230\n"
"v_accvgpr_read_b32 v231, a132\n"
"v_mul_f32 v231, v231, v230\n"
"v_accvgpr_write_b32 a132, v231\n"
"v_accvgpr_read_b32 v231, a133\n"
"v_mul_f32 v231, v231, v230\n"
"v_accvgpr_write_b32 a133, v231\n"
"v_accvgpr_read_b32 v231, a134\n"
"v_mul_f32 v231, v231, v230\n"
"v_accvgpr_write_b32 a134, v231\n"
"v_accvgpr_read_b32 v231, a135\n"
"v_mul_f32 v231, v231, v230\n"
"v_accvgpr_write_b32 a135, v231\n"
"v_sub_f32 v132, v132, v228\n"
"v_sub_f32 v133, v133, v228\n"
"v_sub_f32 v134, v134, v228\n"
"v_sub_f32 v135, v135, v228\n"
"v_sub_f32 v148, v148, v228\n"
"v_sub_f32 v149, v149, v228\n"
"v_sub_f32 v150, v150, v228\n"
"v_sub_f32 v151, v151, v228\n"
"v_exp_f32 v132, v132\n"
"v_exp_f32 v133, v133\n"
"v_exp_f32 v134, v134\n"
"v_exp_f32 v135, v135\n"
"v_exp_f32 v148, v148\n"
"v_exp_f32 v149, v149\n"
"v_exp_f32 v150, v150\n"
"v_exp_f32 v151, v151\n"
"s_nop 0\n"
"v_cvt_pk_bf16_f32 v196, v132, v133\n"
"v_cvt_pk_bf16_f32 v197, v134, v135\n"
"v_cvt_pk_bf16_f32 v198, v148, v149\n"
"v_cvt_pk_bf16_f32 v199, v150, v151\n"
"v_mul_f32 v8, v8, v234\n"
"v_mul_f32 v9, v9, v234\n"
"v_mul_f32 v10, v10, v234\n"
"v_mul_f32 v11, v11, v234\n"
"v_mul_f32 v24, v24, v234\n"
"v_mul_f32 v25, v25, v234\n"
"v_mul_f32 v26, v26, v234\n"
"v_mul_f32 v27, v27, v234\n"
"v_mul_f32 v40, v40, v234\n"
"v_mul_f32 v41, v41, v234\n"
"v_mul_f32 v42, v42, v234\n"
"v_mul_f32 v43, v43, v234\n"
"v_mul_f32 v56, v56, v234\n"
"v_mul_f32 v57, v57, v234\n"
"v_mul_f32 v58, v58, v234\n"
"v_mul_f32 v59, v59, v234\n"
"v_mul_f32 v72, v72, v234\n"
"v_mul_f32 v73, v73, v234\n"
"v_mul_f32 v74, v74, v234\n"
"v_mul_f32 v75, v75, v234\n"
"v_mul_f32 v88, v88, v234\n"
"v_mul_f32 v89, v89, v234\n"
"v_mul_f32 v90, v90, v234\n"
"v_mul_f32 v91, v91, v234\n"
"v_mul_f32 v104, v104, v234\n"
"v_mul_f32 v105, v105, v234\n"
"v_mul_f32 v106, v106, v234\n"
"v_mul_f32 v107, v107, v234\n"
"v_mul_f32 v120, v120, v234\n"
"v_mul_f32 v121, v121, v234\n"
"v_mul_f32 v122, v122, v234\n"
"v_mul_f32 v123, v123, v234\n"
"v_accvgpr_read_b32 v235, a136\n"
"v_mul_f32 v235, v235, v234\n"
"v_accvgpr_write_b32 a136, v235\n"
"v_accvgpr_read_b32 v235, a137\n"
"v_mul_f32 v235, v235, v234\n"
"v_accvgpr_write_b32 a137, v235\n"
"v_accvgpr_read_b32 v235, a138\n"
"v_mul_f32 v235, v235, v234\n"
"v_accvgpr_write_b32 a138, v235\n"
"v_accvgpr_read_b32 v235, a139\n"
"v_mul_f32 v235, v235, v234\n"
"v_accvgpr_write_b32 a139, v235\n"
"v_sub_f32 v136, v136, v232\n"
"v_sub_f32 v137, v137, v232\n"
"v_sub_f32 v138, v138, v232\n"
"v_sub_f32 v139, v139, v232\n"
"v_sub_f32 v152, v152, v232\n"
"v_sub_f32 v153, v153, v232\n"
"v_sub_f32 v154, v154, v232\n"
"v_sub_f32 v155, v155, v232\n"
"v_exp_f32 v136, v136\n"
"v_exp_f32 v137, v137\n"
"v_exp_f32 v138, v138\n"
"v_exp_f32 v139, v139\n"
"v_exp_f32 v152, v152\n"
"v_exp_f32 v153, v153\n"
"v_exp_f32 v154, v154\n"
"v_exp_f32 v155, v155\n"
"s_nop 0\n"
"v_cvt_pk_bf16_f32 v200, v136, v137\n"
"v_cvt_pk_bf16_f32 v201, v138, v139\n"
"v_cvt_pk_bf16_f32 v202, v152, v153\n"
"v_cvt_pk_bf16_f32 v203, v154, v155\n"
"v_mul_f32 v12, v12, v238\n"
"v_mul_f32 v13, v13, v238\n"
"v_mul_f32 v14, v14, v238\n"
"v_mul_f32 v15, v15, v238\n"
"v_mul_f32 v28, v28, v238\n"
"v_mul_f32 v29, v29, v238\n"
"v_mul_f32 v30, v30, v238\n"
"v_mul_f32 v31, v31, v238\n"
"v_mul_f32 v44, v44, v238\n"
"v_mul_f32 v45, v45, v238\n"
"v_mul_f32 v46, v46, v238\n"
"v_mul_f32 v47, v47, v238\n"
"v_mul_f32 v60, v60, v238\n"
"v_mul_f32 v61, v61, v238\n"
"v_mul_f32 v62, v62, v238\n"
"v_mul_f32 v63, v63, v238\n"
"v_mul_f32 v76, v76, v238\n"
"v_mul_f32 v77, v77, v238\n"
"v_mul_f32 v78, v78, v238\n"
"v_mul_f32 v79, v79, v238\n"
"v_mul_f32 v92, v92, v238\n"
"v_mul_f32 v93, v93, v238\n"
"v_mul_f32 v94, v94, v238\n"
"v_mul_f32 v95, v95, v238\n"
"v_mul_f32 v108, v108, v238\n"
"v_mul_f32 v109, v109, v238\n"
"v_mul_f32 v110, v110, v238\n"
"v_mul_f32 v111, v111, v238\n"
"v_mul_f32 v124, v124, v238\n"
"v_mul_f32 v125, v125, v238\n"
"v_mul_f32 v126, v126, v238\n"
"v_mul_f32 v127, v127, v238\n"
"v_accvgpr_read_b32 v239, a140\n"
"v_mul_f32 v239, v239, v238\n"
"v_accvgpr_write_b32 a140, v239\n"
"v_accvgpr_read_b32 v239, a141\n"
"v_mul_f32 v239, v239, v238\n"
"v_accvgpr_write_b32 a141, v239\n"
"v_accvgpr_read_b32 v239, a142\n"
"v_mul_f32 v239, v239, v238\n"
"v_accvgpr_write_b32 a142, v239\n"
"v_accvgpr_read_b32 v239, a143\n"
"v_mul_f32 v239, v239, v238\n"
"v_accvgpr_write_b32 a143, v239\n"
"v_sub_f32 v140, v140, v236\n"
"v_sub_f32 v141, v141, v236\n"
"v_sub_f32 v142, v142, v236\n"
"v_sub_f32 v143, v143, v236\n"
"v_sub_f32 v156, v156, v236\n"
"v_sub_f32 v157, v157, v236\n"
"v_sub_f32 v158, v158, v236\n"
"v_sub_f32 v159, v159, v236\n"
"v_exp_f32 v140, v140\n"
"v_exp_f32 v141, v141\n"
"v_exp_f32 v142, v142\n"
"v_exp_f32 v143, v143\n"
"v_exp_f32 v156, v156\n"
"v_exp_f32 v157, v157\n"
"v_exp_f32 v158, v158\n"
"v_exp_f32 v159, v159\n"
"s_nop 0\n"
"v_cvt_pk_bf16_f32 v204, v140, v141\n"
"v_cvt_pk_bf16_f32 v205, v142, v143\n"
"v_cvt_pk_bf16_f32 v206, v156, v157\n"
"v_cvt_pk_bf16_f32 v207, v158, v159\n"
"s_nop 1\n"
"v_mfma_f32_16x16x32_bf16 v[0:3], a[96:99], v[192:195], v[0:3]\n"
"v_mfma_f32_16x16x32_bf16 v[4:7], a[96:99], v[196:199], v[4:7]\n"
"v_mfma_f32_16x16x32_bf16 v[8:11], a[96:99], v[200:203], v[8:11]\n"
"v_mfma_f32_16x16x32_bf16 v[12:15], a[96:99], v[204:207], v[12:15]\n"
"v_mfma_f32_16x16x32_bf16 v[16:19], a[100:103], v[192:195], v[16:19]\n"
"v_mfma_f32_16x16x32_bf16 v[20:23], a[100:103], v[196:199], v[20:23]\n"
"v_mfma_f32_16x16x32_bf16 v[24:27], a[100:103], v[200:203], v[24:27]\n"
"v_mfma_f32_16x16x32_bf16 v[28:31], a[100:103], v[204:207], v[28:31]\n"
"v_mfma_f32_16x16x32_bf16 v[32:35], a[104:107], v[192:195], v[32:35]\n"
"v_mfma_f32_16x16x32_bf16 v[36:39], a[104:107], v[196:199], v[36:39]\n"
"v_mfma_f32_16x16x32_bf16 v[40:43], a[104:107], v[200:203], v[40:43]\n"
"v_mfma_f32_16x16x32_bf16 v[44:47], a[104:107], v[204:207], v[44:47]\n"
"v_mfma_f32_16x16x32_bf16 v[48:51], a[108:111], v[192:195], v[48:51]\n"
"v_mfma_f32_16x16x32_bf16 v[52:55], a[108:111], v[196:199], v[52:55]\n"
"v_mfma_f32_16x16x32_bf16 v[56:59], a[108:111], v[200:203], v[56:59]\n"
"v_mfma_f32_16x16x32_bf16 v[60:63], a[108:111], v[204:207], v[60:63]\n"
"v_mfma_f32_16x16x32_bf16 v[64:67], a[112:115], v[192:195], v[64:67]\n"
"v_mfma_f32_16x16x32_bf16 v[68:71], a[112:115], v[196:199], v[68:71]\n"
"v_mfma_f32_16x16x32_bf16 v[72:75], a[112:115], v[200:203], v[72:75]\n"
"v_mfma_f32_16x16x32_bf16 v[76:79], a[112:115], v[204:207], v[76:79]\n"
"v_mfma_f32_16x16x32_bf16 v[80:83], a[116:119], v[192:195], v[80:83]\n"
"v_mfma_f32_16x16x32_bf16 v[84:87], a[116:119], v[196:199], v[84:87]\n"
"v_mfma_f32_16x16x32_bf16 v[88:91], a[116:119], v[200:203], v[88:91]\n"
"v_mfma_f32_16x16x32_bf16 v[92:95], a[116:119], v[204:207], v[92:95]\n"
"v_mfma_f32_16x16x32_bf16 v[96:99], a[120:123], v[192:195], v[96:99]\n"
"v_mfma_f32_16x16x32_bf16 v[100:103], a[120:123], v[196:199], v[100:103]\n"
"v_mfma_f32_16x16x32_bf16 v[104:107], a[120:123], v[200:203], v[104:107]\n"
"v_mfma_f32_16x16x32_bf16 v[108:111], a[120:123], v[204:207], v[108:111]\n"
"v_mfma_f32_16x16x32_bf16 v[112:115], a[124:127], v[192:195], v[112:115]\n"
"v_mfma_f32_16x16x32_bf16 v[116:119], a[124:127], v[196:199], v[116:119]\n"
"v_mfma_f32_16x16x32_bf16 v[120:123], a[124:127], v[200:203], v[120:123]\n"
"v_mfma_f32_16x16x32_bf16 v[124:127], a[124:127], v[204:207], v[124:127]\n"
"v_mfma_f32_16x16x32_bf16 a[128:131], a[144:147], v[192:195], a[128:131]\n"
"v_mfma_f32_16x16x32_bf16 a[132:135], a[144:147], v[196:199], a[132:135]\n"
"v_mfma_f32_16x16x32_bf16 a[136:139], a[144:147], v[200:203], a[136:139]\n"
"v_mfma_f32_16x16x32_bf16 a[140:143], a[144:147], v[204:207], a[140:143]\n"
"s_nop 7\n"
"ds_read_b128 a[64:67], v240 offset:24576\n"
"ds_read_b128 a[68:71], v241 offset:24576\n"
"ds_read_b128 a[72:75], v242 offset:24576\n"
"ds_read_b128 a[76:79], v243 offset:24576\n"
"ds_read_b128 a[80:83], v240 offset:28672\n"
"ds_read_b128 a[84:87], v241 offset:28672\n"
"ds_read_b128 a[88:91], v242 offset:28672\n"
"ds_read_b128 a[92:95], v243 offset:28672\n"
"ds_read_b64_tr_b16 a[96:97], v244 offset:57344\n"
"ds_read_b64_tr_b16 a[98:99], v244 offset:61440\n"
"ds_read_b64_tr_b16 a[100:101], v245 offset:57344\n"
"ds_read_b64_tr_b16 a[102:103], v245 offset:61440\n"
"ds_read_b64_tr_b16 a[104:105], v244 offset:57856\n"
"ds_read_b64_tr_b16 a[106:107], v244 offset:61952\n"
"ds_read_b64_tr_b16 a[108:109], v245 offset:57856\n"
"ds_read_b64_tr_b16 a[110:111], v245 offset:61952\n"
"ds_read_b64_tr_b16 a[112:113], v244 offset:58368\n"
"ds_read_b64_tr_b16 a[114:115], v244 offset:62464\n"
"ds_read_b64_tr_b16 a[116:117], v245 offset:58368\n"
"ds_read_b64_tr_b16 a[118:119], v245 offset:62464\n"
"ds_read_b64_tr_b16 a[120:121], v244 offset:58880\n"
"ds_read_b64_tr_b16 a[122:123], v244 offset:62976\n"
"ds_read_b64_tr_b16 a[124:125], v245 offset:58880\n"
"ds_read_b64_tr_b16 a[126:127], v245 offset:62976\n"
"s_waitcnt lgkmcnt(0)\n"
"v_mfma_f32_16x16x32_bf16 v[128:131], a[64:67], a[0:3], 0\n"
"v_mfma_f32_16x16x32_bf16 v[132:135], a[64:67], a[16:19], 0\n"
"v_mfma_f32_16x16x32_bf16 v[136:139], a[64:67], a[32:35], 0\n"
"v_mfma_f32_16x16x32_bf16 v[140:143], a[64:67], a[48:51], 0\n"
"v_mfma_f32_16x16x32_bf16 v[128:131], a[68:71], a[4:7], v[128:131]\n"
"v_mfma_f32_16x16x32_bf16 v[132:135], a[68:71], a[20:23], v[132:135]\n"
"v_mfma_f32_16x16x32_bf16 v[136:139], a[68:71], a[36:39], v[136:139]\n"
"v_mfma_f32_16x16x32_bf16 v[140:143], a[68:71], a[52:55], v[140:143]\n"
"v_mfma_f32_16x16x32_bf16 v[128:131], a[72:75], a[8:11], v[128:131]\n"
"v_mfma_f32_16x16x32_bf16 v[132:135], a[72:75], a[24:27], v[132:135]\n"
"v_mfma_f32_16x16x32_bf16 v[136:139], a[72:75], a[40:43], v[136:139]\n"
"v_mfma_f32_16x16x32_bf16 v[140:143], a[72:75], a[56:59], v[140:143]\n"
"v_mfma_f32_16x16x32_bf16 v[128:131], a[76:79], a[12:15], v[128:131]\n"
"v_mfma_f32_16x16x32_bf16 v[132:135], a[76:79], a[28:31], v[132:135]\n"
"v_mfma_f32_16x16x32_bf16 v[136:139], a[76:79], a[44:47], v[136:139]\n"
"v_mfma_f32_16x16x32_bf16 v[140:143], a[76:79], a[60:63], v[140:143]\n"
"v_mfma_f32_16x16x32_bf16 v[144:147], a[80:83], a[0:3], 0\n"
"v_mfma_f32_16x16x32_bf16 v[148:151], a[80:83], a[16:19], 0\n"
"v_mfma_f32_16x16x32_bf16 v[152:155], a[80:83], a[32:35], 0\n"
"v_mfma_f32_16x16x32_bf16 v[156:159], a[80:83], a[48:51], 0\n"
"v_mfma_f32_16x16x32_bf16 v[144:147], a[84:87], a[4:7], v[144:147]\n"
"v_mfma_f32_16x16x32_bf16 v[148:151], a[84:87], a[20:23], v[148:151]\n"
"v_mfma_f32_16x16x32_bf16 v[152:155], a[84:87], a[36:39], v[152:155]\n"
"v_mfma_f32_16x16x32_bf16 v[156:159], a[84:87], a[52:55], v[156:159]\n"
"v_mfma_f32_16x16x32_bf16 v[144:147], a[88:91], a[8:11], v[144:147]\n"
"v_mfma_f32_16x16x32_bf16 v[148:151], a[88:91], a[24:27], v[148:151]\n"
"v_mfma_f32_16x16x32_bf16 v[152:155], a[88:91], a[40:43], v[152:155]\n"
"v_mfma_f32_16x16x32_bf16 v[156:159], a[88:91], a[56:59], v[156:159]\n"
"v_mfma_f32_16x16x32_bf16 v[144:147], a[92:95], a[12:15], v[144:147]\n"
"v_mfma_f32_16x16x32_bf16 v[148:151], a[92:95], a[28:31], v[148:151]\n"
"v_mfma_f32_16x16x32_bf16 v[152:155], a[92:95], a[44:47], v[152:155]\n"
"v_mfma_f32_16x16x32_bf16 v[156:159], a[92:95], a[60:63], v[156:159]\n"
"s_nop 7\n"
"v_max3_f32 v225, v128, v129, v130\n"
"v_max3_f32 v225, v225, v131, v144\n"
"v_max3_f32 v225, v225, v145, v146\n"
"v_max_f32 v225, v225, v147\n"
"v_max3_f32 v229, v132, v133, v134\n"
"v_max3_f32 v229, v229, v135, v148\n"
"v_max3_f32 v229, v229, v149, v150\n"
"v_max_f32 v229, v229, v151\n"
"v_max3_f32 v233, v136, v137, v138\n"
"v_max3_f32 v233, v233, v139, v152\n"
"v_max3_f32 v233, v233, v153, v154\n"
"v_max_f32 v233, v233, v155\n"
"v_max3_f32 v237, v140, v141, v142\n"
"v_max3_f32 v237, v237, v143, v156\n"
"v_max3_f32 v237, v237, v157, v158\n"
"v_max_f32 v237, v237, v159\n"
"ds_bpermute_b32 v226, v222, v225\n"
"ds_bpermute_b32 v230, v222, v229\n"
"ds_bpermute_b32 v234, v222, v233\n"
"ds_bpermute_b32 v238, v222, v237\n"
"s_waitcnt lgkmcnt(0)\n"
"v_max_f32 v225, v225, v226\n"
"v_max_f32 v229, v229, v230\n"
"v_max_f32 v233, v233, v234\n"
"v_max_f32 v237, v237, v238\n"
"ds_bpermute_b32 v226, v223, v225\n"
"ds_bpermute_b32 v230, v223, v229\n"
"ds_bpermute_b32 v234, v223, v233\n"
"ds_bpermute_b32 v238, v223, v237\n"
"s_waitcnt lgkmcnt(0)\n"
"v_max_f32 v225, v225, v226\n"
"v_max_f32 v229, v229, v230\n"
"v_max_f32 v233, v233, v234\n"
"v_max_f32 v237, v237, v238\n"
"v_max_f32 v225, v225, v224\n"
"v_sub_f32 v226, v224, v225\n"
"v_exp_f32 v226, v226\n"
"v_mov_b32 v224, v225\n"
"v_max_f32 v229, v229, v228\n"
"v_sub_f32 v230, v228, v229\n"
"v_exp_f32 v230, v230\n"
"v_mov_b32 v228, v229\n"
"v_max_f32 v233, v233, v232\n"
"v_sub_f32 v234, v232, v233\n"
"v_exp_f32 v234, v234\n"
"v_mov_b32 v232, v233\n"
"v_max_f32 v237, v237, v236\n"
"v_sub_f32 v238, v236, v237\n"
"v_exp_f32 v238, v238\n"
"v_mov_b32 v236, v237\n"
"v_mul_f32 v0, v0, v226\n"
"v_mul_f32 v1, v1, v226\n"
"v_mul_f32 v2, v2, v226\n"
"v_mul_f32 v3, v3, v226\n"
"v_mul_f32 v16, v16, v226\n"
"v_mul_f32 v17, v17, v226\n"
"v_mul_f32 v18, v18, v226\n"
"v_mul_f32 v19, v19, v226\n"
"v_mul_f32 v32, v32, v226\n"
"v_mul_f32 v33, v33, v226\n"
"v_mul_f32 v34, v34, v226\n"
"v_mul_f32 v35, v35, v226\n"
"v_mul_f32 v48, v48, v226\n"
"v_mul_f32 v49, v49, v226\n"
"v_mul_f32 v50, v50, v226\n"
"v_mul_f32 v51, v51, v226\n"
"v_mul_f32 v64, v64, v226\n"
"v_mul_f32 v65, v65, v226\n"
"v_mul_f32 v66, v66, v226\n"
"v_mul_f32 v67, v67, v226\n"
"v_mul_f32 v80, v80, v226\n"
"v_mul_f32 v81, v81, v226\n"
"v_mul_f32 v82, v82, v226\n"
"v_mul_f32 v83, v83, v226\n"
"v_mul_f32 v96, v96, v226\n"
"v_mul_f32 v97, v97, v226\n"
"v_mul_f32 v98, v98, v226\n"
"v_mul_f32 v99, v99, v226\n"
"v_mul_f32 v112, v112, v226\n"
"v_mul_f32 v113, v113, v226\n"
"v_mul_f32 v114, v114, v226\n"
"v_mul_f32 v115, v115, v226\n"
"v_accvgpr_read_b32 v227, a128\n"
"v_mul_f32 v227, v227, v226\n"
"v_accvgpr_write_b32 a128, v227\n"
"v_accvgpr_read_b32 v227, a129\n"
"v_mul_f32 v227, v227, v226\n"
"v_accvgpr_write_b32 a129, v227\n"
"v_accvgpr_read_b32 v227, a130\n"
"v_mul_f32 v227, v227, v226\n"
"v_accvgpr_write_b32 a130, v227\n"
"v_accvgpr_read_b32 v227, a131\n"
"v_mul_f32 v227, v227, v226\n"
"v_accvgpr_write_b32 a131, v227\n"
"v_sub_f32 v128, v128, v224\n"
"v_sub_f32 v129, v129, v224\n"
"v_sub_f32 v130, v130, v224\n"
"v_sub_f32 v131, v131, v224\n"
"v_sub_f32 v144, v144, v224\n"
"v_sub_f32 v145, v145, v224\n"
"v_sub_f32 v146, v146, v224\n"
"v_sub_f32 v147, v147, v224\n"
"v_exp_f32 v128, v128\n"
"v_exp_f32 v129, v129\n"
"v_exp_f32 v130, v130\n"
"v_exp_f32 v131, v131\n"
"v_exp_f32 v144, v144\n"
"v_exp_f32 v145, v145\n"
"v_exp_f32 v146, v146\n"
"v_exp_f32 v147, v147\n"
"s_nop 0\n"
"v_cvt_pk_bf16_f32 v192, v128, v129\n"
"v_cvt_pk_bf16_f32 v193, v130, v131\n"
"v_cvt_pk_bf16_f32 v194, v144, v145\n"
"v_cvt_pk_bf16_f32 v195, v146, v147\n"
"v_mul_f32 v4, v4, v230\n"
"v_mul_f32 v5, v5, v230\n"
"v_mul_f32 v6, v6, v230\n"
"v_mul_f32 v7, v7, v230\n"
"v_mul_f32 v20, v20, v230\n"
"v_mul_f32 v21, v21, v230\n"
"v_mul_f32 v22, v22, v230\n"
"v_mul_f32 v23, v23, v230\n"
"v_mul_f32 v36, v36, v230\n"
"v_mul_f32 v37, v37, v230\n"
"v_mul_f32 v38, v38, v230\n"
"v_mul_f32 v39, v39, v230\n"
"v_mul_f32 v52, v52, v230\n"
"v_mul_f32 v53, v53, v230\n"
"v_mul_f32 v54, v54, v230\n"
"v_mul_f32 v55, v55, v230\n"
"v_mul_f32 v68, v68, v230\n"
"v_mul_f32 v69, v69, v230\n"
"v_mul_f32 v70, v70, v230\n"
"v_mul_f32 v71, v71, v230\n"
"v_mul_f32 v84, v84, v230\n"
"v_mul_f32 v85, v85, v230\n"
"v_mul_f32 v86, v86, v230\n"
"v_mul_f32 v87, v87, v230\n"
"v_mul_f32 v100, v100, v230\n"
"v_mul_f32 v101, v101, v230\n"
"v_mul_f32 v102, v102, v230\n"
"v_mul_f32 v103, v103, v230\n"
"v_mul_f32 v116, v116, v230\n"
"v_mul_f32 v117, v117, v230\n"
"v_mul_f32 v118, v118, v230\n"
"v_mul_f32 v119, v119, v230\n"
"v_accvgpr_read_b32 v231, a132\n"
"v_mul_f32 v231, v231, v230\n"
"v_accvgpr_write_b32 a132, v231\n"
"v_accvgpr_read_b32 v231, a133\n"
"v_mul_f32 v231, v231, v230\n"
"v_accvgpr_write_b32 a133, v231\n"
"v_accvgpr_read_b32 v231, a134\n"
"v_mul_f32 v231, v231, v230\n"
"v_accvgpr_write_b32 a134, v231\n"
"v_accvgpr_read_b32 v231, a135\n"
"v_mul_f32 v231, v231, v230\n"
"v_accvgpr_write_b32 a135, v231\n"
"v_sub_f32 v132, v132, v228\n"
"v_sub_f32 v133, v133, v228\n"
"v_sub_f32 v134, v134, v228\n"
"v_sub_f32 v135, v135, v228\n"
"v_sub_f32 v148, v148, v228\n"
"v_sub_f32 v149, v149, v228\n"
"v_sub_f32 v150, v150, v228\n"
"v_sub_f32 v151, v151, v228\n"
"v_exp_f32 v132, v132\n"
"v_exp_f32 v133, v133\n"
"v_exp_f32 v134, v134\n"
"v_exp_f32 v135, v135\n"
"v_exp_f32 v148, v148\n"
"v_exp_f32 v149, v149\n"
"v_exp_f32 v150, v150\n"
"v_exp_f32 v151, v151\n"
"s_nop 0\n"
"v_cvt_pk_bf16_f32 v196, v132, v133\n"
"v_cvt_pk_bf16_f32 v197, v134, v135\n"
"v_cvt_pk_bf16_f32 v198, v148, v149\n"
"v_cvt_pk_bf16_f32 v199, v150, v151\n"
"v_mul_f32 v8, v8, v234\n"
"v_mul_f32 v9, v9, v234\n"
"v_mul_f32 v10, v10, v234\n"
"v_mul_f32 v11, v11, v234\n"
"v_mul_f32 v24, v24, v234\n"
"v_mul_f32 v25, v25, v234\n"
"v_mul_f32 v26, v26, v234\n"
"v_mul_f32 v27, v27, v234\n"
"v_mul_f32 v40, v40, v234\n"
"v_mul_f32 v41, v41, v234\n"
"v_mul_f32 v42, v42, v234\n"
"v_mul_f32 v43, v43, v234\n"
"v_mul_f32 v56, v56, v234\n"
"v_mul_f32 v57, v57, v234\n"
"v_mul_f32 v58, v58, v234\n"
"v_mul_f32 v59, v59, v234\n"
"v_mul_f32 v72, v72, v234\n"
"v_mul_f32 v73, v73, v234\n"
"v_mul_f32 v74, v74, v234\n"
"v_mul_f32 v75, v75, v234\n"
"v_mul_f32 v88, v88, v234\n"
"v_mul_f32 v89, v89, v234\n"
"v_mul_f32 v90, v90, v234\n"
"v_mul_f32 v91, v91, v234\n"
"v_mul_f32 v104, v104, v234\n"
"v_mul_f32 v105, v105, v234\n"
"v_mul_f32 v106, v106, v234\n"
"v_mul_f32 v107, v107, v234\n"
"v_mul_f32 v120, v120, v234\n"
"v_mul_f32 v121, v121, v234\n"
"v_mul_f32 v122, v122, v234\n"
"v_mul_f32 v123, v123, v234\n"
"v_accvgpr_read_b32 v235, a136\n"
"v_mul_f32 v235, v235, v234\n"
"v_accvgpr_write_b32 a136, v235\n"
"v_accvgpr_read_b32 v235, a137\n"
"v_mul_f32 v235, v235, v234\n"
"v_accvgpr_write_b32 a137, v235\n"
"v_accvgpr_read_b32 v235, a138\n"
"v_mul_f32 v235, v235, v234\n"
"v_accvgpr_write_b32 a138, v235\n"
"v_accvgpr_read_b32 v235, a139\n"
"v_mul_f32 v235, v235, v234\n"
"v_accvgpr_write_b32 a139, v235\n"
"v_sub_f32 v136, v136, v232\n"
"v_sub_f32 v137, v137, v232\n"
"v_sub_f32 v138, v138, v232\n"
"v_sub_f32 v139, v139, v232\n"
"v_sub_f32 v152, v152, v232\n"
"v_sub_f32 v153, v153, v232\n"
"v_sub_f32 v154, v154, v232\n"
"v_sub_f32 v155, v155, v232\n"
"v_exp_f32 v136, v136\n"
"v_exp_f32 v137, v137\n"
"v_exp_f32 v138, v138\n"
"v_exp_f32 v139, v139\n"
"v_exp_f32 v152, v152\n"
"v_exp_f32 v153, v153\n"
"v_exp_f32 v154, v154\n"
"v_exp_f32 v155, v155\n"
"s_nop 0\n"
"v_cvt_pk_bf16_f32 v200, v136, v137\n"
"v_cvt_pk_bf16_f32 v201, v138, v139\n"
"v_cvt_pk_bf16_f32 v202, v152, v153\n"
"v_cvt_pk_bf16_f32 v203, v154, v155\n"
"v_mul_f32 v12, v12, v238\n"
"v_mul_f32 v13, v13, v238\n"
"v_mul_f32 v14, v14, v238\n"
"v_mul_f32 v15, v15, v238\n"
"v_mul_f32 v28, v28, v238\n"
"v_mul_f32 v29, v29, v238\n"
"v_mul_f32 v30, v30, v238\n"
"v_mul_f32 v31, v31, v238\n"
"v_mul_f32 v44, v44, v238\n"
"v_mul_f32 v45, v45, v238\n"
"v_mul_f32 v46, v46, v238\n"
"v_mul_f32 v47, v47, v238\n"
"v_mul_f32 v60, v60, v238\n"
"v_mul_f32 v61, v61, v238\n"
"v_mul_f32 v62, v62, v238\n"
"v_mul_f32 v63, v63, v238\n"
"v_mul_f32 v76, v76, v238\n"
"v_mul_f32 v77, v77, v238\n"
"v_mul_f32 v78, v78, v238\n"
"v_mul_f32 v79, v79, v238\n"
"v_mul_f32 v92, v92, v238\n"
"v_mul_f32 v93, v93, v238\n"
"v_mul_f32 v94, v94, v238\n"
"v_mul_f32 v95, v95, v238\n"
"v_mul_f32 v108, v108, v238\n"
"v_mul_f32 v109, v109, v238\n"
"v_mul_f32 v110, v110, v238\n"
"v_mul_f32 v111, v111, v238\n"
"v_mul_f32 v124, v124, v238\n"
"v_mul_f32 v125, v125, v238\n"
"v_mul_f32 v126, v126, v238\n"
"v_mul_f32 v127, v127, v238\n"
"v_accvgpr_read_b32 v239, a140\n"
"v_mul_f32 v239, v239, v238\n"
"v_accvgpr_write_b32 a140, v239\n"
"v_accvgpr_read_b32 v239, a141\n"
"v_mul_f32 v239, v239, v238\n"
"v_accvgpr_write_b32 a141, v239\n"
"v_accvgpr_read_b32 v239, a142\n"
"v_mul_f32 v239, v239, v238\n"
"v_accvgpr_write_b32 a142, v239\n"
"v_accvgpr_read_b32 v239, a143\n"
"v_mul_f32 v239, v239, v238\n"
"v_accvgpr_write_b32 a143, v239\n"
"v_sub_f32 v140, v140, v236\n"
"v_sub_f32 v141, v141, v236\n"
"v_sub_f32 v142, v142, v236\n"
"v_sub_f32 v143, v143, v236\n"
"v_sub_f32 v156, v156, v236\n"
"v_sub_f32 v157, v157, v236\n"
"v_sub_f32 v158, v158, v236\n"
"v_sub_f32 v159, v159, v236\n"
"v_exp_f32 v140, v140\n"
"v_exp_f32 v141, v141\n"
"v_exp_f32 v142, v142\n"
"v_exp_f32 v143, v143\n"
"v_exp_f32 v156, v156\n"
"v_exp_f32 v157, v157\n"
"v_exp_f32 v158, v158\n"
"v_exp_f32 v159, v159\n"
"s_nop 0\n"
"v_cvt_pk_bf16_f32 v204, v140, v141\n"
"v_cvt_pk_bf16_f32 v205, v142, v143\n"
"v_cvt_pk_bf16_f32 v206, v156, v157\n"
"v_cvt_pk_bf16_f32 v207, v158, v159\n"
"s_nop 1\n"
"v_mfma_f32_16x16x32_bf16 v[0:3], a[96:99], v[192:195], v[0:3]\n"
"v_mfma_f32_16x16x32_bf16 v[4:7], a[96:99], v[196:199], v[4:7]\n"
"v_mfma_f32_16x16x32_bf16 v[8:11], a[96:99], v[200:203], v[8:11]\n"
"v_mfma_f32_16x16x32_bf16 v[12:15], a[96:99], v[204:207], v[12:15]\n"
"v_mfma_f32_16x16x32_bf16 v[16:19], a[100:103], v[192:195], v[16:19]\n"
"v_mfma_f32_16x16x32_bf16 v[20:23], a[100:103], v[196:199], v[20:23]\n"
"v_mfma_f32_16x16x32_bf16 v[24:27], a[100:103], v[200:203], v[24:27]\n"
"v_mfma_f32_16x16x32_bf16 v[28:31], a[100:103], v[204:207], v[28:31]\n"
"v_mfma_f32_16x16x32_bf16 v[32:35], a[104:107], v[192:195], v[32:35]\n"
"v_mfma_f32_16x16x32_bf16 v[36:39], a[104:107], v[196:199], v[36:39]\n"
"v_mfma_f32_16x16x32_bf16 v[40:43], a[104:107], v[200:203], v[40:43]\n"
"v_mfma_f32_16x16x32_bf16 v[44:47], a[104:107], v[204:207], v[44:47]\n"
"v_mfma_f32_16x16x32_bf16 v[48:51], a[108:111], v[192:195], v[48:51]\n"
"v_mfma_f32_16x16x32_bf16 v[52:55], a[108:111], v[196:199], v[52:55]\n"
"v_mfma_f32_16x16x32_bf16 v[56:59], a[108:111], v[200:203], v[56:59]\n"
"v_mfma_f32_16x16x32_bf16 v[60:63], a[108:111], v[204:207], v[60:63]\n"
"v_mfma_f32_16x16x32_bf16 v[64:67], a[112:115], v[192:195], v[64:67]\n"
"v_mfma_f32_16x16x32_bf16 v[68:71], a[112:115], v[196:199], v[68:71]\n"
"v_mfma_f32_16x16x32_bf16 v[72:75], a[112:115], v[200:203], v[72:75]\n"
"v_mfma_f32_16x16x32_bf16 v[76:79], a[112:115], v[204:207], v[76:79]\n"
"v_mfma_f32_16x16x32_bf16 v[80:83], a[116:119], v[192:195], v[80:83]\n"
"v_mfma_f32_16x16x32_bf16 v[84:87], a[116:119], v[196:199], v[84:87]\n"
"v_mfma_f32_16x16x32_bf16 v[88:91], a[116:119], v[200:203], v[88:91]\n"
"v_mfma_f32_16x16x32_bf16 v[92:95], a[116:119], v[204:207], v[92:95]\n"
"v_mfma_f32_16x16x32_bf16 v[96:99], a[120:123], v[192:195], v[96:99]\n"
"v_mfma_f32_16x16x32_bf16 v[100:103], a[120:123], v[196:199], v[100:103]\n"
"v_mfma_f32_16x16x32_bf16 v[104:107], a[120:123], v[200:203], v[104:107]\n"
"v_mfma_f32_16x16x32_bf16 v[108:111], a[120:123], v[204:207], v[108:111]\n"
"v_mfma_f32_16x16x32_bf16 v[112:115], a[124:127], v[192:195], v[112:115]\n"
"v_mfma_f32_16x16x32_bf16 v[116:119], a[124:127], v[196:199], v[116:119]\n"
"v_mfma_f32_16x16x32_bf16 v[120:123], a[124:127], v[200:203], v[120:123]\n"
"v_mfma_f32_16x16x32_bf16 v[124:127], a[124:127], v[204:207], v[124:127]\n"
"v_mfma_f32_16x16x32_bf16 a[128:131], a[144:147], v[192:195], a[128:131]\n"
"v_mfma_f32_16x16x32_bf16 a[132:135], a[144:147], v[196:199], a[132:135]\n"
"v_mfma_f32_16x16x32_bf16 a[136:139], a[144:147], v[200:203], a[136:139]\n"
"v_mfma_f32_16x16x32_bf16 a[140:143], a[144:147], v[204:207], a[140:143]\n"
"s_nop 7\n"
"s_barrier\n"
"s_sub_u32 s61, s61, 1\n"
"s_cmp_lg_u32 s61, 0\n"
"s_cbranch_scc1 Lat_sloop\n"
"s_nop 7\n"
"s_mov_b32 s79, 1\n"
"v_mbcnt_lo_u32_b32 v252, -1, 0\n"
"v_mbcnt_hi_u32_b32 v252, -1, v252\n"
"v_lshlrev_b32 v252, 4, v252\n"
"s_barrier\n"
"v_mbcnt_lo_u32_b32 v140, -1, 0\n"
"v_mbcnt_hi_u32_b32 v140, -1, v140\n"
"v_lshrrev_b32 v141, 4, v140\n"
"v_and_b32 v142, 1, v141\n"
"v_lshlrev_b32 v142, 1, v142\n"
"v_and_b32 v143, 3, v140\n"
"v_xor_b32 v142, v142, v143\n"
"v_lshlrev_b32 v142, 4, v142\n"
"v_lshrrev_b32 v143, 5, v140\n"
"v_lshl_add_u32 v142, v143, 6, v142\n"
"v_bfe_u32 v143, v140, 2, 3\n"
"s_mov_b32 s64, 1024\n"
"v_mul_lo_u32 v143, v143, s64\n"
"v_add_u32 v247, v143, v142\n"
"v_add_u32 v246, 896, v247\n"
"s_add_u32 m0, s70, 0\n"
"s_add_u32 s64, s71, 0\n"
"buffer_load_dwordx4 v246, s[44:47], s64 offen lds\n"
"buffer_load_dwordx4 v247, s[44:47], s64 offen offset:1024 lds\n"
"s_add_u32 m0, s70, 8192\n"
"s_add_u32 s64, s71, 32768\n"
"buffer_load_dwordx4 v246, s[44:47], s64 offen lds\n"
"buffer_load_dwordx4 v247, s[44:47], s64 offen offset:1024 lds\n"
"s_add_u32 m0, s70, 16384\n"
"s_add_u32 s64, s71, 65536\n"
"buffer_load_dwordx4 v246, s[44:47], s64 offen lds\n"
"buffer_load_dwordx4 v247, s[44:47], s64 offen offset:1024 lds\n"
"s_add_u32 m0, s70, 24576\n"
"s_add_u32 s64, s71, 98304\n"
"buffer_load_dwordx4 v246, s[44:47], s64 offen lds\n"
"buffer_load_dwordx4 v247, s[44:47], s64 offen offset:1024 lds\n"
"s_add_u32 m0, s70, 32768\n"
"s_add_u32 s64, s71, 131072\n"
"buffer_load_dwordx4 v246, s[44:47], s64 offen lds\n"
"buffer_load_dwordx4 v247, s[44:47], s64 offen offset:1024 lds\n"
"s_add_u32 m0, s70, 40960\n"
"s_add_u32 s64, s71, 163840\n"
"buffer_load_dwordx4 v246, s[44:47], s64 offen lds\n"
"buffer_load_dwordx4 v247, s[44:47], s64 offen offset:1024 lds\n"
"s_add_u32 m0, s70, 49152\n"
"s_add_u32 s64, s71, 196608\n"
"buffer_load_dwordx4 v246, s[44:47], s64 offen lds\n"
"buffer_load_dwordx4 v247, s[44:47], s64 offen offset:1024 lds\n"
"s_add_u32 m0, s70, 57344\n"
"s_add_u32 s64, s71, 229376\n"
"buffer_load_dwordx4 v246, s[44:47], s64 offen lds\n"
"buffer_load_dwordx4 v247, s[44:47], s64 offen offset:1024 lds\n"
"s_branch Lat_store\n"
"Lat_end:\n"
    :: "s"(RFL(kb)), "s"(RFL(kb >> 32)), "s"(RFL(vb)), "s"(RFL(vb >> 32)), "s"(RFL(wb)), "s"(RFL(wb >> 32)), "s"(RFL(hb)), "s"(RFL(hb >> 32)), "s"(RFL(ob)), "s"(RFL(ob >> 32)), "s"(wid), "s"(RFL(db)), "s"(RFL(db >> 32)), "s"(RFL((size_t)modp)), "s"(RFL((size_t)modp >> 32)), "s"(RFL(pfb)), "s"(RFL(pfb >> 32)), "s"(RFL(pfs)), "s"(RFL(wob)), "s"(RFL(wob >> 32)), "s"(RFL((b >> 3) * 4 + wid)), "s"(RFL((size_t)(flags + (pan * 8 + sp) * 64))), "s"(RFL((size_t)(flags + (pan * 8 + sp) * 64) >> 32))
    : "v0", "v1", "v2", "v3", "v4", "v5", "v6", "v7", "v8", "v9", "v10", "v11", "v12", "v13", "v14", "v15", "v16", "v17", "v18", "v19", "v20", "v21", "v22", "v23", "v24", "v25", "v26", "v27", "v28", "v29", "v30", "v31", "v32", "v33", "v34", "v35", "v36", "v37", "v38", "v39", "v40", "v41", "v42", "v43", "v44", "v45", "v46", "v47", "v48", "v49", "v50", "v51", "v52", "v53", "v54", "v55", "v56", "v57", "v58", "v59", "v60", "v61", "v62", "v63", "v64", "v65", "v66", "v67", "v68", "v69", "v70", "v71", "v72", "v73", "v74", "v75", "v76", "v77", "v78", "v79", "v80", "v81", "v82", "v83", "v84", "v85", "v86", "v87", "v88", "v89", "v90", "v91", "v92", "v93", "v94", "v95", "v96", "v97", "v98", "v99", "v100", "v101", "v102", "v103", "v104", "v105", "v106", "v107", "v108", "v109", "v110", "v111", "v112", "v113", "v114", "v115", "v116", "v117", "v118", "v119", "v120", "v121", "v122", "v123", "v124", "v125", "v126", "v127", "v128", "v129", "v130", "v131", "v132", "v133", "v134", "v135", "v136", "v137", "v138", "v139", "v140", "v141", "v142", "v143", "v144", "v145", "v146", "v147", "v148", "v149", "v150", "v151", "v152", "v153", "v154", "v155", "v156", "v157", "v158", "v159", "v160", "v161", "v162", "v163", "v164", "v165", "v166", "v167", "v168", "v169", "v170", "v171", "v172", "v173", "v174", "v175", "v176", "v177", "v178", "v179", "v180", "v181", "v182", "v183", "v184", "v185", "v186", "v187", "v188", "v189", "v190", "v191", "v192", "v193", "v194", "v195", "v196", "v197", "v198", "v199", "v200", "v201", "v202", "v203", "v204", "v205", "v206", "v207", "v208", "v209", "v210", "v211", "v212", "v213", "v214", "v215", "v216", "v217", "v218", "v219", "v220", "v221", "v222", "v223", "v224", "v225", "v226", "v227", "v228", "v229", "v230", "v231", "v232", "v233", "v234", "v235", "v236", "v237", "v238", "v239", "v240", "v241", "v242", "v243", "v244", "v245", "v246", "v247", "v248", "v249", "v250", "v251", "v252", "v253", "v254", "v255", "a0", "a1", "a2", "a3", "a4", "a5", "a6", "a7", "a8", "a9", "a10", "a11", "a12", "a13", "a14", "a15", "a16", "a17", "a18", "a19", "a20", "a21", "a22", "a23", "a24", "a25", "a26", "a27", "a28", "a29", "a30", "a31", "a32", "a33", "a34", "a35", "a36", "a37", "a38", "a39", "a40", "a41", "a42", "a43", "a44", "a45", "a46", "a47", "a48", "a49", "a50", "a51", "a52", "a53", "a54", "a55", "a56", "a57", "a58", "a59", "a60", "a61", "a62", "a63", "a64", "a65", "a66", "a67", "a68", "a69", "a70", "a71", "a72", "a73", "a74", "a75", "a76", "a77", "a78", "a79", "a80", "a81", "a82", "a83", "a84", "a85", "a86", "a87", "a88", "a89", "a90", "a91", "a92", "a93", "a94", "a95", "a96", "a97", "a98", "a99", "a100", "a101", "a102", "a103", "a104", "a105", "a106", "a107", "a108", "a109", "a110", "a111", "a112", "a113", "a114", "a115", "a116", "a117", "a118", "a119", "a120", "a121", "a122", "a123", "a124", "a125", "a126", "a127", "a128", "a129", "a130", "a131", "a132", "a133", "a134", "a135", "a136", "a137", "a138", "a139", "a140", "a141", "a142", "a143", "a144", "a145", "a146", "a147", "a148", "a149", "a150", "a151", "a152", "a153", "a154", "a155", "a156", "a157", "a158", "a159", "a160", "a161", "a162", "a163", "a164", "a165", "a166", "a167", "a168", "a169", "a170", "a171", "a172", "a173", "a174", "a175", "a176", "a177", "a178", "a179", "a180", "a181", "a182", "a183", "a184", "a185", "a186", "a187", "a188", "a189", "a190", "a191", "a192", "a193", "a194", "a195", "a196", "a197", "a198", "a199", "a200", "a201", "a202", "a203", "a204", "a205", "a206", "a207", "a208", "a209", "a210", "a211", "a212", "a213", "a214", "a215", "a216", "a217", "a218", "a219", "a220", "a221", "a222", "a223", "a224", "a225", "a226", "a227", "a228", "a229", "a230", "a231", "a232", "a233", "a234", "a235", "a236", "a237", "a238", "a239", "a240", "a241", "a242", "a243", "a244", "a245", "a246", "a247", "a248", "a249", "a250", "a251", "a252", "a253", "a254", "a255", "s36", "s37", "s38", "s39", "s40", "s41", "s42", "s43", "s44", "s45", "s46", "s47", "s48", "s49", "s50", "s51", "s52", "s53", "s54", "s55", "s56", "s57", "s58", "s59", "s60", "s61", "s62", "s63", "s64", "s65", "s66", "s67", "s68", "s69", "s70", "s71", "s72", "s73", "s74", "s75", "s76", "s77", "s78", "s79", "s80", "s81", "s82", "s83", "vcc", "scc", "memory");

  }
  { ARGS(); signal(flags, 128 + pan * 8 + sp, wid); mod_to_lds(modp, lds, wid); }
  const unsigned r5 = b >> 3, hasu = r5 < 28, pu = (b & 1) * 4 + (hasu ? r5 / 7 : 0), ft = ((b & 7) >> 1) * 7 + (hasu ? r5 % 7 : 0);
  const unsigned pd = MAPD ? (b & 7) : pan, jd = MAPD ? r5 : j, dn = jd >> 2, dh = (jd >> 1) & 1, da = jd & 1;
  { ARGS(); wait_for(flags, pan * 8 + sp, 4, wid);
    const size_t xb = (size_t)o + ((size_t)(pan * 16 + wid * 4) * 128 + sp * 16) * 1024, ob = (size_t)part + (((size_t)sp * 128 + pan * 16 + wid * 4) * 16 + nt * 4) * 1024;
    const size_t w1b = (size_t)w1 + ft * 128, w3b = (size_t)w3 + ft * 128 - 1024;
  asm volatile(
"s_mov_b32 s44, %0\n"
"s_and_b32 s45, %1, 0xffff\n"
"s_mov_b32 s46, 409600\n"
"s_mov_b32 s47, 0x20000\n"
"s_mov_b32 s48, %2\n"
"s_and_b32 s49, %3, 0xffff\n"
"s_mov_b32 s50, 53248\n"
"s_mov_b32 s51, 0x20000\n"
"s_mov_b32 s56, %4\n"
"s_mov_b32 s66, %5\n"
"s_mov_b32 s67, %6\n"
"s_mov_b32 s70, %7\n"
"s_mov_b32 s71, %8\n"
"s_mov_b32 s72, %9\n"
"s_mov_b32 s73, %10\n"
"v_mbcnt_lo_u32_b32 v240, -1, 0\n"
"v_mbcnt_hi_u32_b32 v240, -1, v240\n"
"v_lshrrev_b32 v241, 4, v240\n"
"v_lshlrev_b32 v246, 4, v240\n"
"v_and_b32 v242, 1, v241\n"
"v_lshlrev_b32 v242, 1, v242\n"
"v_and_b32 v243, 3, v240\n"
"v_xor_b32 v242, v242, v243\n"
"v_lshlrev_b32 v242, 4, v242\n"
"v_lshrrev_b32 v243, 5, v240\n"
"v_lshl_add_u32 v242, v243, 6, v242\n"
"v_bfe_u32 v243, v240, 2, 3\n"
"s_mov_b32 s59, 1024\n"
"v_mul_lo_u32 v243, v243, s59\n"
"v_add_u32 v248, v243, v242\n"
"v_add_u32 v247, 0, v248\n"
"v_lshrrev_b32 v242, 1, v241\n"
"v_mul_u32_u24 v242, 2048, v242\n"
"v_and_b32 v243, 1, v241\n"
"v_lshl_add_u32 v242, v243, 8, v242\n"
"v_bfe_u32 v244, v240, 2, 2\n"
"v_lshl_add_u32 v242, v244, 6, v242\n"
"v_and_b32 v244, 3, v240\n"
"v_lshl_add_u32 v245, v244, 3, v242\n"
"v_lshl_add_u32 v249, v243, 5, v245\n"
"v_xor_b32 v244, 1, v243\n"
"v_lshl_add_u32 v250, v244, 5, v245\n"
"v_add_u32 v251, 0x10000, v249\n"
"v_add_u32 v252, 0x10000, v250\n"
"s_mov_b32 s58, 0\n"
"buffer_load_dwordx4 a[0:3], v246, s[44:47], s58 offen offset:0 sc1\n"
"s_mov_b32 s58, 131072\n"
"buffer_load_dwordx4 a[4:7], v246, s[44:47], s58 offen offset:0 sc1\n"
"s_mov_b32 s58, 0\n"
"buffer_load_dwordx4 a[16:19], v246, s[44:47], s58 offen offset:1024 sc1\n"
"s_mov_b32 s58, 131072\n"
"buffer_load_dwordx4 a[20:23], v246, s[44:47], s58 offen offset:1024 sc1\n"
"s_mov_b32 s58, 0\n"
"buffer_load_dwordx4 a[32:35], v246, s[44:47], s58 offen offset:2048 sc1\n"
"s_mov_b32 s58, 131072\n"
"buffer_load_dwordx4 a[36:39], v246, s[44:47], s58 offen offset:2048 sc1\n"
"s_mov_b32 s58, 0\n"
"buffer_load_dwordx4 a[48:51], v246, s[44:47], s58 offen offset:3072 sc1\n"
"s_mov_b32 s58, 131072\n"
"buffer_load_dwordx4 a[52:55], v246, s[44:47], s58 offen offset:3072 sc1\n"
"s_mov_b32 s58, 4096\n"
"buffer_load_dwordx4 a[64:67], v246, s[44:47], s58 offen offset:0 sc1\n"
"s_mov_b32 s58, 135168\n"
"buffer_load_dwordx4 a[68:71], v246, s[44:47], s58 offen offset:0 sc1\n"
"s_mov_b32 s58, 4096\n"
"buffer_load_dwordx4 a[80:83], v246, s[44:47], s58 offen offset:1024 sc1\n"
"s_mov_b32 s58, 135168\n"
"buffer_load_dwordx4 a[84:87], v246, s[44:47], s58 offen offset:1024 sc1\n"
"s_mov_b32 s58, 4096\n"
"buffer_load_dwordx4 a[96:99], v246, s[44:47], s58 offen offset:2048 sc1\n"
"s_mov_b32 s58, 135168\n"
"buffer_load_dwordx4 a[100:103], v246, s[44:47], s58 offen offset:2048 sc1\n"
"s_mov_b32 s58, 4096\n"
"buffer_load_dwordx4 a[112:115], v246, s[44:47], s58 offen offset:3072 sc1\n"
"s_mov_b32 s58, 135168\n"
"buffer_load_dwordx4 a[116:119], v246, s[44:47], s58 offen offset:3072 sc1\n"
"s_mov_b32 s58, 8192\n"
"buffer_load_dwordx4 a[128:131], v246, s[44:47], s58 offen offset:0 sc1\n"
"s_mov_b32 s58, 139264\n"
"buffer_load_dwordx4 a[132:135], v246, s[44:47], s58 offen offset:0 sc1\n"
"s_mov_b32 s58, 8192\n"
"buffer_load_dwordx4 a[144:147], v246, s[44:47], s58 offen offset:1024 sc1\n"
"s_mov_b32 s58, 139264\n"
"buffer_load_dwordx4 a[148:151], v246, s[44:47], s58 offen offset:1024 sc1\n"
"s_mov_b32 s58, 8192\n"
"buffer_load_dwordx4 a[160:163], v246, s[44:47], s58 offen offset:2048 sc1\n"
"s_mov_b32 s58, 139264\n"
"buffer_load_dwordx4 a[164:167], v246, s[44:47], s58 offen offset:2048 sc1\n"
"s_mov_b32 s58, 8192\n"
"buffer_load_dwordx4 a[176:179], v246, s[44:47], s58 offen offset:3072 sc1\n"
"s_mov_b32 s58, 139264\n"
"buffer_load_dwordx4 a[180:183], v246, s[44:47], s58 offen offset:3072 sc1\n"
"s_mov_b32 s58, 12288\n"
"buffer_load_dwordx4 a[192:195], v246, s[44:47], s58 offen offset:0 sc1\n"
"s_mov_b32 s58, 143360\n"
"buffer_load_dwordx4 a[196:199], v246, s[44:47], s58 offen offset:0 sc1\n"
"s_mov_b32 s58, 12288\n"
"buffer_load_dwordx4 a[208:211], v246, s[44:47], s58 offen offset:1024 sc1\n"
"s_mov_b32 s58, 143360\n"
"buffer_load_dwordx4 a[212:215], v246, s[44:47], s58 offen offset:1024 sc1\n"
"s_mov_b32 s58, 12288\n"
"buffer_load_dwordx4 a[224:227], v246, s[44:47], s58 offen offset:2048 sc1\n"
"s_mov_b32 s58, 143360\n"
"buffer_load_dwordx4 a[228:231], v246, s[44:47], s58 offen offset:2048 sc1\n"
"s_mov_b32 s58, 12288\n"
"buffer_load_dwordx4 a[240:243], v246, s[44:47], s58 offen offset:3072 sc1\n"
"s_mov_b32 s58, 143360\n"
"buffer_load_dwordx4 a[244:247], v246, s[44:47], s58 offen offset:3072 sc1\n"
"s_mov_b32 s76, %15\n"
"s_mov_b32 s77, %16\n"
"s_mov_b32 s74, %17\n"
"s_mov_b32 s75, %18\n"
"v_mov_b32 v228, 0\n"
"global_load_dword v229, v228, s[76:77] sc1\n"
"ds_read_b64_tr_b16 v[128:129], v249 offset:0\n"
"ds_read_b64_tr_b16 v[130:131], v249 offset:4096\n"
"ds_read_b64_tr_b16 v[132:133], v250 offset:0\n"
"ds_read_b64_tr_b16 v[134:135], v250 offset:4096\n"
"ds_read_b64_tr_b16 v[136:137], v249 offset:512\n"
"ds_read_b64_tr_b16 v[138:139], v249 offset:4608\n"
"ds_read_b64_tr_b16 v[140:141], v250 offset:512\n"
"ds_read_b64_tr_b16 v[142:143], v250 offset:4608\n"
"ds_read_b64_tr_b16 v[144:145], v249 offset:1024\n"
"ds_read_b64_tr_b16 v[146:147], v249 offset:5120\n"
"ds_read_b64_tr_b16 v[148:149], v250 offset:1024\n"
"ds_read_b64_tr_b16 v[150:151], v250 offset:5120\n"
"ds_read_b64_tr_b16 v[152:153], v249 offset:1536\n"
"ds_read_b64_tr_b16 v[154:155], v249 offset:5632\n"
"ds_read_b64_tr_b16 v[156:157], v250 offset:1536\n"
"ds_read_b64_tr_b16 v[158:159], v250 offset:5632\n"
"s_waitcnt vmcnt(31) lgkmcnt(0)\n"
"v_mfma_f32_16x16x32_bf16 v[0:3], v[128:131], a[0:3], 0\n"
"ds_read_b64_tr_b16 v[160:161], v249 offset:8192\n"
"ds_read_b64_tr_b16 v[162:163], v249 offset:12288\n"
"v_mfma_f32_16x16x32_bf16 v[4:7], v[128:131], a[4:7], 0\n"
"ds_read_b64_tr_b16 v[164:165], v250 offset:8192\n"
"ds_read_b64_tr_b16 v[166:167], v250 offset:12288\n"
"v_mfma_f32_16x16x32_bf16 v[16:19], v[132:135], a[0:3], 0\n"
"ds_read_b64_tr_b16 v[168:169], v249 offset:8704\n"
"ds_read_b64_tr_b16 v[170:171], v249 offset:12800\n"
"v_mfma_f32_16x16x32_bf16 v[20:23], v[132:135], a[4:7], 0\n"
"ds_read_b64_tr_b16 v[172:173], v250 offset:8704\n"
"ds_read_b64_tr_b16 v[174:175], v250 offset:12800\n"
"v_mfma_f32_16x16x32_bf16 v[32:35], v[136:139], a[0:3], 0\n"
"ds_read_b64_tr_b16 v[176:177], v249 offset:9216\n"
"ds_read_b64_tr_b16 v[178:179], v249 offset:13312\n"
"v_mfma_f32_16x16x32_bf16 v[36:39], v[136:139], a[4:7], 0\n"
"ds_read_b64_tr_b16 v[180:181], v250 offset:9216\n"
"ds_read_b64_tr_b16 v[182:183], v250 offset:13312\n"
"v_mfma_f32_16x16x32_bf16 v[48:51], v[140:143], a[0:3], 0\n"
"ds_read_b64_tr_b16 v[184:185], v249 offset:9728\n"
"ds_read_b64_tr_b16 v[186:187], v249 offset:13824\n"
"v_mfma_f32_16x16x32_bf16 v[52:55], v[140:143], a[4:7], 0\n"
"ds_read_b64_tr_b16 v[188:189], v250 offset:9728\n"
"ds_read_b64_tr_b16 v[190:191], v250 offset:13824\n"
"v_mfma_f32_16x16x32_bf16 v[64:67], v[144:147], a[0:3], 0\n"
"v_mfma_f32_16x16x32_bf16 v[68:71], v[144:147], a[4:7], 0\n"
"v_mfma_f32_16x16x32_bf16 v[80:83], v[148:151], a[0:3], 0\n"
"v_mfma_f32_16x16x32_bf16 v[84:87], v[148:151], a[4:7], 0\n"
"v_mfma_f32_16x16x32_bf16 v[96:99], v[152:155], a[0:3], 0\n"
"v_mfma_f32_16x16x32_bf16 v[100:103], v[152:155], a[4:7], 0\n"
"v_mfma_f32_16x16x32_bf16 v[112:115], v[156:159], a[0:3], 0\n"
"v_mfma_f32_16x16x32_bf16 v[116:119], v[156:159], a[4:7], 0\n"
"s_waitcnt vmcnt(29) lgkmcnt(0)\n"
"v_mfma_f32_16x16x32_bf16 v[0:3], v[160:163], a[16:19], v[0:3]\n"
"ds_read_b64_tr_b16 v[128:129], v249 offset:16384\n"
"ds_read_b64_tr_b16 v[130:131], v249 offset:20480\n"
"v_mfma_f32_16x16x32_bf16 v[4:7], v[160:163], a[20:23], v[4:7]\n"
"ds_read_b64_tr_b16 v[132:133], v250 offset:16384\n"
"ds_read_b64_tr_b16 v[134:135], v250 offset:20480\n"
"v_mfma_f32_16x16x32_bf16 v[16:19], v[164:167], a[16:19], v[16:19]\n"
"ds_read_b64_tr_b16 v[136:137], v249 offset:16896\n"
"ds_read_b64_tr_b16 v[138:139], v249 offset:20992\n"
"v_mfma_f32_16x16x32_bf16 v[20:23], v[164:167], a[20:23], v[20:23]\n"
"ds_read_b64_tr_b16 v[140:141], v250 offset:16896\n"
"ds_read_b64_tr_b16 v[142:143], v250 offset:20992\n"
"v_mfma_f32_16x16x32_bf16 v[32:35], v[168:171], a[16:19], v[32:35]\n"
"ds_read_b64_tr_b16 v[144:145], v249 offset:17408\n"
"ds_read_b64_tr_b16 v[146:147], v249 offset:21504\n"
"v_mfma_f32_16x16x32_bf16 v[36:39], v[168:171], a[20:23], v[36:39]\n"
"ds_read_b64_tr_b16 v[148:149], v250 offset:17408\n"
"ds_read_b64_tr_b16 v[150:151], v250 offset:21504\n"
"v_mfma_f32_16x16x32_bf16 v[48:51], v[172:175], a[16:19], v[48:51]\n"
"ds_read_b64_tr_b16 v[152:153], v249 offset:17920\n"
"ds_read_b64_tr_b16 v[154:155], v249 offset:22016\n"
"v_mfma_f32_16x16x32_bf16 v[52:55], v[172:175], a[20:23], v[52:55]\n"
"ds_read_b64_tr_b16 v[156:157], v250 offset:17920\n"
"ds_read_b64_tr_b16 v[158:159], v250 offset:22016\n"
"v_mfma_f32_16x16x32_bf16 v[64:67], v[176:179], a[16:19], v[64:67]\n"
"v_mfma_f32_16x16x32_bf16 v[68:71], v[176:179], a[20:23], v[68:71]\n"
"v_mfma_f32_16x16x32_bf16 v[80:83], v[180:183], a[16:19], v[80:83]\n"
"v_mfma_f32_16x16x32_bf16 v[84:87], v[180:183], a[20:23], v[84:87]\n"
"v_mfma_f32_16x16x32_bf16 v[96:99], v[184:187], a[16:19], v[96:99]\n"
"v_mfma_f32_16x16x32_bf16 v[100:103], v[184:187], a[20:23], v[100:103]\n"
"v_mfma_f32_16x16x32_bf16 v[112:115], v[188:191], a[16:19], v[112:115]\n"
"v_mfma_f32_16x16x32_bf16 v[116:119], v[188:191], a[20:23], v[116:119]\n"
"s_waitcnt vmcnt(27) lgkmcnt(0)\n"
"v_mfma_f32_16x16x32_bf16 v[0:3], v[128:131], a[32:35], v[0:3]\n"
"ds_read_b64_tr_b16 v[160:161], v249 offset:24576\n"
"ds_read_b64_tr_b16 v[162:163], v249 offset:28672\n"
"v_mfma_f32_16x16x32_bf16 v[4:7], v[128:131], a[36:39], v[4:7]\n"
"ds_read_b64_tr_b16 v[164:165], v250 offset:24576\n"
"ds_read_b64_tr_b16 v[166:167], v250 offset:28672\n"
"v_mfma_f32_16x16x32_bf16 v[16:19], v[132:135], a[32:35], v[16:19]\n"
"ds_read_b64_tr_b16 v[168:169], v249 offset:25088\n"
"ds_read_b64_tr_b16 v[170:171], v249 offset:29184\n"
"v_mfma_f32_16x16x32_bf16 v[20:23], v[132:135], a[36:39], v[20:23]\n"
"ds_read_b64_tr_b16 v[172:173], v250 offset:25088\n"
"ds_read_b64_tr_b16 v[174:175], v250 offset:29184\n"
"v_mfma_f32_16x16x32_bf16 v[32:35], v[136:139], a[32:35], v[32:35]\n"
"ds_read_b64_tr_b16 v[176:177], v249 offset:25600\n"
"ds_read_b64_tr_b16 v[178:179], v249 offset:29696\n"
"v_mfma_f32_16x16x32_bf16 v[36:39], v[136:139], a[36:39], v[36:39]\n"
"ds_read_b64_tr_b16 v[180:181], v250 offset:25600\n"
"ds_read_b64_tr_b16 v[182:183], v250 offset:29696\n"
"v_mfma_f32_16x16x32_bf16 v[48:51], v[140:143], a[32:35], v[48:51]\n"
"ds_read_b64_tr_b16 v[184:185], v249 offset:26112\n"
"ds_read_b64_tr_b16 v[186:187], v249 offset:30208\n"
"v_mfma_f32_16x16x32_bf16 v[52:55], v[140:143], a[36:39], v[52:55]\n"
"ds_read_b64_tr_b16 v[188:189], v250 offset:26112\n"
"ds_read_b64_tr_b16 v[190:191], v250 offset:30208\n"
"v_mfma_f32_16x16x32_bf16 v[64:67], v[144:147], a[32:35], v[64:67]\n"
"v_mfma_f32_16x16x32_bf16 v[68:71], v[144:147], a[36:39], v[68:71]\n"
"v_mfma_f32_16x16x32_bf16 v[80:83], v[148:151], a[32:35], v[80:83]\n"
"v_mfma_f32_16x16x32_bf16 v[84:87], v[148:151], a[36:39], v[84:87]\n"
"v_mfma_f32_16x16x32_bf16 v[96:99], v[152:155], a[32:35], v[96:99]\n"
"v_mfma_f32_16x16x32_bf16 v[100:103], v[152:155], a[36:39], v[100:103]\n"
"v_mfma_f32_16x16x32_bf16 v[112:115], v[156:159], a[32:35], v[112:115]\n"
"v_mfma_f32_16x16x32_bf16 v[116:119], v[156:159], a[36:39], v[116:119]\n"
"s_waitcnt vmcnt(25) lgkmcnt(0)\n"
"v_mfma_f32_16x16x32_bf16 v[0:3], v[160:163], a[48:51], v[0:3]\n"
"ds_read_b64_tr_b16 v[128:129], v249 offset:32768\n"
"ds_read_b64_tr_b16 v[130:131], v249 offset:36864\n"
"v_mfma_f32_16x16x32_bf16 v[4:7], v[160:163], a[52:55], v[4:7]\n"
"ds_read_b64_tr_b16 v[132:133], v250 offset:32768\n"
"ds_read_b64_tr_b16 v[134:135], v250 offset:36864\n"
"v_mfma_f32_16x16x32_bf16 v[16:19], v[164:167], a[48:51], v[16:19]\n"
"ds_read_b64_tr_b16 v[136:137], v249 offset:33280\n"
"ds_read_b64_tr_b16 v[138:139], v249 offset:37376\n"
"v_mfma_f32_16x16x32_bf16 v[20:23], v[164:167], a[52:55], v[20:23]\n"
"ds_read_b64_tr_b16 v[140:141], v250 offset:33280\n"
"ds_read_b64_tr_b16 v[142:143], v250 offset:37376\n"
"v_mfma_f32_16x16x32_bf16 v[32:35], v[168:171], a[48:51], v[32:35]\n"
"ds_read_b64_tr_b16 v[144:145], v249 offset:33792\n"
"ds_read_b64_tr_b16 v[146:147], v249 offset:37888\n"
"v_mfma_f32_16x16x32_bf16 v[36:39], v[168:171], a[52:55], v[36:39]\n"
"ds_read_b64_tr_b16 v[148:149], v250 offset:33792\n"
"ds_read_b64_tr_b16 v[150:151], v250 offset:37888\n"
"v_mfma_f32_16x16x32_bf16 v[48:51], v[172:175], a[48:51], v[48:51]\n"
"ds_read_b64_tr_b16 v[152:153], v249 offset:34304\n"
"ds_read_b64_tr_b16 v[154:155], v249 offset:38400\n"
"v_mfma_f32_16x16x32_bf16 v[52:55], v[172:175], a[52:55], v[52:55]\n"
"ds_read_b64_tr_b16 v[156:157], v250 offset:34304\n"
"ds_read_b64_tr_b16 v[158:159], v250 offset:38400\n"
"v_mfma_f32_16x16x32_bf16 v[64:67], v[176:179], a[48:51], v[64:67]\n"
"v_mfma_f32_16x16x32_bf16 v[68:71], v[176:179], a[52:55], v[68:71]\n"
"v_mfma_f32_16x16x32_bf16 v[80:83], v[180:183], a[48:51], v[80:83]\n"
"v_mfma_f32_16x16x32_bf16 v[84:87], v[180:183], a[52:55], v[84:87]\n"
"v_mfma_f32_16x16x32_bf16 v[96:99], v[184:187], a[48:51], v[96:99]\n"
"v_mfma_f32_16x16x32_bf16 v[100:103], v[184:187], a[52:55], v[100:103]\n"
"v_mfma_f32_16x16x32_bf16 v[112:115], v[188:191], a[48:51], v[112:115]\n"
"v_mfma_f32_16x16x32_bf16 v[116:119], v[188:191], a[52:55], v[116:119]\n"
"s_waitcnt vmcnt(23) lgkmcnt(0)\n"
"s_waitcnt vmcnt(0)\n"
"v_readfirstlane_b32 s63, v229\n"
"s_nop 3\n"
"s_cmp_ge_u32 s63, 4\n"
"s_cbranch_scc1 Lpd_ab\n"
"s_mov_b32 s62, 0\n"
"Lp_ab:\n"
"global_load_dword v229, v228, s[76:77] sc1\n"
"s_waitcnt vmcnt(0)\n"
"v_readfirstlane_b32 s63, v229\n"
"s_nop 3\n"
"s_cmp_ge_u32 s63, 4\n"
"s_cbranch_scc1 Lpd_ab\n"
"s_sleep 1\n"
"s_add_u32 s62, s62, 1\n"
"s_cmp_lt_u32 s62, 65536\n"
"s_cbranch_scc1 Lp_ab\n"
"v_mov_b32 v229, 1\n"
"global_store_dword v228, v229, s[74:75] sc1\n"
"Lpd_ab:\n"
"v_mfma_f32_16x16x32_bf16 v[0:3], v[128:131], a[64:67], v[0:3]\n"
"ds_read_b64_tr_b16 v[160:161], v249 offset:40960\n"
"ds_read_b64_tr_b16 v[162:163], v249 offset:45056\n"
"v_mfma_f32_16x16x32_bf16 v[4:7], v[128:131], a[68:71], v[4:7]\n"
"ds_read_b64_tr_b16 v[164:165], v250 offset:40960\n"
"ds_read_b64_tr_b16 v[166:167], v250 offset:45056\n"
"v_mfma_f32_16x16x32_bf16 v[16:19], v[132:135], a[64:67], v[16:19]\n"
"ds_read_b64_tr_b16 v[168:169], v249 offset:41472\n"
"ds_read_b64_tr_b16 v[170:171], v249 offset:45568\n"
"v_mfma_f32_16x16x32_bf16 v[20:23], v[132:135], a[68:71], v[20:23]\n"
"ds_read_b64_tr_b16 v[172:173], v250 offset:41472\n"
"ds_read_b64_tr_b16 v[174:175], v250 offset:45568\n"
"v_mfma_f32_16x16x32_bf16 v[32:35], v[136:139], a[64:67], v[32:35]\n"
"ds_read_b64_tr_b16 v[176:177], v249 offset:41984\n"
"ds_read_b64_tr_b16 v[178:179], v249 offset:46080\n"
"v_mfma_f32_16x16x32_bf16 v[36:39], v[136:139], a[68:71], v[36:39]\n"
"ds_read_b64_tr_b16 v[180:181], v250 offset:41984\n"
"ds_read_b64_tr_b16 v[182:183], v250 offset:46080\n"
"v_mfma_f32_16x16x32_bf16 v[48:51], v[140:143], a[64:67], v[48:51]\n"
"ds_read_b64_tr_b16 v[184:185], v249 offset:42496\n"
"ds_read_b64_tr_b16 v[186:187], v249 offset:46592\n"
"v_mfma_f32_16x16x32_bf16 v[52:55], v[140:143], a[68:71], v[52:55]\n"
"ds_read_b64_tr_b16 v[188:189], v250 offset:42496\n"
"ds_read_b64_tr_b16 v[190:191], v250 offset:46592\n"
"v_mfma_f32_16x16x32_bf16 v[64:67], v[144:147], a[64:67], v[64:67]\n"
"s_mov_b32 s58, 262144\n"
"buffer_load_dwordx4 a[8:11], v246, s[44:47], s58 offen offset:0 sc1\n"
"v_mfma_f32_16x16x32_bf16 v[68:71], v[144:147], a[68:71], v[68:71]\n"
"v_mfma_f32_16x16x32_bf16 v[80:83], v[148:151], a[64:67], v[80:83]\n"
"s_mov_b32 s58, 393216\n"
"buffer_load_dwordx4 a[12:15], v246, s[44:47], s58 offen offset:0 sc1\n"
"v_mfma_f32_16x16x32_bf16 v[84:87], v[148:151], a[68:71], v[84:87]\n"
"v_mfma_f32_16x16x32_bf16 v[96:99], v[152:155], a[64:67], v[96:99]\n"
"s_mov_b32 s58, 262144\n"
"buffer_load_dwordx4 a[24:27], v246, s[44:47], s58 offen offset:1024 sc1\n"
"v_mfma_f32_16x16x32_bf16 v[100:103], v[152:155], a[68:71], v[100:103]\n"
"v_mfma_f32_16x16x32_bf16 v[112:115], v[156:159], a[64:67], v[112:115]\n"
"v_mfma_f32_16x16x32_bf16 v[116:119], v[156:159], a[68:71], v[116:119]\n"
"s_waitcnt vmcnt(24) lgkmcnt(0)\n"
"v_mfma_f32_16x16x32_bf16 v[0:3], v[160:163], a[80:83], v[0:3]\n"
"ds_read_b64_tr_b16 v[128:129], v249 offset:49152\n"
"ds_read_b64_tr_b16 v[130:131], v249 offset:53248\n"
"v_mfma_f32_16x16x32_bf16 v[4:7], v[160:163], a[84:87], v[4:7]\n"
"ds_read_b64_tr_b16 v[132:133], v250 offset:49152\n"
"ds_read_b64_tr_b16 v[134:135], v250 offset:53248\n"
"v_mfma_f32_16x16x32_bf16 v[16:19], v[164:167], a[80:83], v[16:19]\n"
"ds_read_b64_tr_b16 v[136:137], v249 offset:49664\n"
"ds_read_b64_tr_b16 v[138:139], v249 offset:53760\n"
"v_mfma_f32_16x16x32_bf16 v[20:23], v[164:167], a[84:87], v[20:23]\n"
"ds_read_b64_tr_b16 v[140:141], v250 offset:49664\n"
"ds_read_b64_tr_b16 v[142:143], v250 offset:53760\n"
"v_mfma_f32_16x16x32_bf16 v[32:35], v[168:171], a[80:83], v[32:35]\n"
"ds_read_b64_tr_b16 v[144:145], v249 offset:50176\n"
"ds_read_b64_tr_b16 v[146:147], v249 offset:54272\n"
"v_mfma_f32_16x16x32_bf16 v[36:39], v[168:171], a[84:87], v[36:39]\n"
"ds_read_b64_tr_b16 v[148:149], v250 offset:50176\n"
"ds_read_b64_tr_b16 v[150:151], v250 offset:54272\n"
"v_mfma_f32_16x16x32_bf16 v[48:51], v[172:175], a[80:83], v[48:51]\n"
"ds_read_b64_tr_b16 v[152:153], v249 offset:50688\n"
"ds_read_b64_tr_b16 v[154:155], v249 offset:54784\n"
"v_mfma_f32_16x16x32_bf16 v[52:55], v[172:175], a[84:87], v[52:55]\n"
"ds_read_b64_tr_b16 v[156:157], v250 offset:50688\n"
"ds_read_b64_tr_b16 v[158:159], v250 offset:54784\n"
"v_mfma_f32_16x16x32_bf16 v[64:67], v[176:179], a[80:83], v[64:67]\n"
"s_mov_b32 s58, 393216\n"
"buffer_load_dwordx4 a[28:31], v246, s[44:47], s58 offen offset:1024 sc1\n"
"v_mfma_f32_16x16x32_bf16 v[68:71], v[176:179], a[84:87], v[68:71]\n"
"v_mfma_f32_16x16x32_bf16 v[80:83], v[180:183], a[80:83], v[80:83]\n"
"s_mov_b32 s58, 262144\n"
"buffer_load_dwordx4 a[40:43], v246, s[44:47], s58 offen offset:2048 sc1\n"
"v_mfma_f32_16x16x32_bf16 v[84:87], v[180:183], a[84:87], v[84:87]\n"
"v_mfma_f32_16x16x32_bf16 v[96:99], v[184:187], a[80:83], v[96:99]\n"
"s_mov_b32 s58, 393216\n"
"buffer_load_dwordx4 a[44:47], v246, s[44:47], s58 offen offset:2048 sc1\n"
"v_mfma_f32_16x16x32_bf16 v[100:103], v[184:187], a[84:87], v[100:103]\n"
"v_mfma_f32_16x16x32_bf16 v[112:115], v[188:191], a[80:83], v[112:115]\n"
"v_mfma_f32_16x16x32_bf16 v[116:119], v[188:191], a[84:87], v[116:119]\n"
"s_waitcnt vmcnt(25) lgkmcnt(0)\n"
"v_mfma_f32_16x16x32_bf16 v[0:3], v[128:131], a[96:99], v[0:3]\n"
"ds_read_b64_tr_b16 v[160:161], v249 offset:57344\n"
"ds_read_b64_tr_b16 v[162:163], v249 offset:61440\n"
"v_mfma_f32_16x16x32_bf16 v[4:7], v[128:131], a[100:103], v[4:7]\n"
"ds_read_b64_tr_b16 v[164:165], v250 offset:57344\n"
"ds_read_b64_tr_b16 v[166:167], v250 offset:61440\n"
"v_mfma_f32_16x16x32_bf16 v[16:19], v[132:135], a[96:99], v[16:19]\n"
"ds_read_b64_tr_b16 v[168:169], v249 offset:57856\n"
"ds_read_b64_tr_b16 v[170:171], v249 offset:61952\n"
"v_mfma_f32_16x16x32_bf16 v[20:23], v[132:135], a[100:103], v[20:23]\n"
"ds_read_b64_tr_b16 v[172:173], v250 offset:57856\n"
"ds_read_b64_tr_b16 v[174:175], v250 offset:61952\n"
"v_mfma_f32_16x16x32_bf16 v[32:35], v[136:139], a[96:99], v[32:35]\n"
"ds_read_b64_tr_b16 v[176:177], v249 offset:58368\n"
"ds_read_b64_tr_b16 v[178:179], v249 offset:62464\n"
"v_mfma_f32_16x16x32_bf16 v[36:39], v[136:139], a[100:103], v[36:39]\n"
"ds_read_b64_tr_b16 v[180:181], v250 offset:58368\n"
"ds_read_b64_tr_b16 v[182:183], v250 offset:62464\n"
"v_mfma_f32_16x16x32_bf16 v[48:51], v[140:143], a[96:99], v[48:51]\n"
"ds_read_b64_tr_b16 v[184:185], v249 offset:58880\n"
"ds_read_b64_tr_b16 v[186:187], v249 offset:62976\n"
"v_mfma_f32_16x16x32_bf16 v[52:55], v[140:143], a[100:103], v[52:55]\n"
"ds_read_b64_tr_b16 v[188:189], v250 offset:58880\n"
"ds_read_b64_tr_b16 v[190:191], v250 offset:62976\n"
"v_mfma_f32_16x16x32_bf16 v[64:67], v[144:147], a[96:99], v[64:67]\n"
"s_mov_b32 s58, 262144\n"
"buffer_load_dwordx4 a[56:59], v246, s[44:47], s58 offen offset:3072 sc1\n"
"v_mfma_f32_16x16x32_bf16 v[68:71], v[144:147], a[100:103], v[68:71]\n"
"v_mfma_f32_16x16x32_bf16 v[80:83], v[148:151], a[96:99], v[80:83]\n"
"s_mov_b32 s58, 393216\n"
"buffer_load_dwordx4 a[60:63], v246, s[44:47], s58 offen offset:3072 sc1\n"
"v_mfma_f32_16x16x32_bf16 v[84:87], v[148:151], a[100:103], v[84:87]\n"
"v_mfma_f32_16x16x32_bf16 v[96:99], v[152:155], a[96:99], v[96:99]\n"
"s_mov_b32 s58, 266240\n"
"buffer_load_dwordx4 a[72:75], v246, s[44:47], s58 offen offset:0 sc1\n"
"v_mfma_f32_16x16x32_bf16 v[100:103], v[152:155], a[100:103], v[100:103]\n"
"v_mfma_f32_16x16x32_bf16 v[112:115], v[156:159], a[96:99], v[112:115]\n"
"v_mfma_f32_16x16x32_bf16 v[116:119], v[156:159], a[100:103], v[116:119]\n"
"s_waitcnt vmcnt(26) lgkmcnt(0)\n"
"v_mfma_f32_16x16x32_bf16 v[0:3], v[160:163], a[112:115], v[0:3]\n"
"ds_read_b64_tr_b16 v[128:129], v251 offset:0\n"
"ds_read_b64_tr_b16 v[130:131], v251 offset:4096\n"
"v_mfma_f32_16x16x32_bf16 v[4:7], v[160:163], a[116:119], v[4:7]\n"
"ds_read_b64_tr_b16 v[132:133], v252 offset:0\n"
"ds_read_b64_tr_b16 v[134:135], v252 offset:4096\n"
"v_mfma_f32_16x16x32_bf16 v[16:19], v[164:167], a[112:115], v[16:19]\n"
"ds_read_b64_tr_b16 v[136:137], v251 offset:512\n"
"ds_read_b64_tr_b16 v[138:139], v251 offset:4608\n"
"v_mfma_f32_16x16x32_bf16 v[20:23], v[164:167], a[116:119], v[20:23]\n"
"ds_read_b64_tr_b16 v[140:141], v252 offset:512\n"
"ds_read_b64_tr_b16 v[142:143], v252 offset:4608\n"
"v_mfma_f32_16x16x32_bf16 v[32:35], v[168:171], a[112:115], v[32:35]\n"
"ds_read_b64_tr_b16 v[144:145], v251 offset:1024\n"
"ds_read_b64_tr_b16 v[146:147], v251 offset:5120\n"
"v_mfma_f32_16x16x32_bf16 v[36:39], v[168:171], a[116:119], v[36:39]\n"
"ds_read_b64_tr_b16 v[148:149], v252 offset:1024\n"
"ds_read_b64_tr_b16 v[150:151], v252 offset:5120\n"
"v_mfma_f32_16x16x32_bf16 v[48:51], v[172:175], a[112:115], v[48:51]\n"
"ds_read_b64_tr_b16 v[152:153], v251 offset:1536\n"
"ds_read_b64_tr_b16 v[154:155], v251 offset:5632\n"
"v_mfma_f32_16x16x32_bf16 v[52:55], v[172:175], a[116:119], v[52:55]\n"
"ds_read_b64_tr_b16 v[156:157], v252 offset:1536\n"
"ds_read_b64_tr_b16 v[158:159], v252 offset:5632\n"
"v_mfma_f32_16x16x32_bf16 v[64:67], v[176:179], a[112:115], v[64:67]\n"
"s_mov_b32 s58, 397312\n"
"buffer_load_dwordx4 a[76:79], v246, s[44:47], s58 offen offset:0 sc1\n"
"v_mfma_f32_16x16x32_bf16 v[68:71], v[176:179], a[116:119], v[68:71]\n"
"v_mfma_f32_16x16x32_bf16 v[80:83], v[180:183], a[112:115], v[80:83]\n"
"s_mov_b32 s58, 266240\n"
"buffer_load_dwordx4 a[88:91], v246, s[44:47], s58 offen offset:1024 sc1\n"
"v_mfma_f32_16x16x32_bf16 v[84:87], v[180:183], a[116:119], v[84:87]\n"
"v_mfma_f32_16x16x32_bf16 v[96:99], v[184:187], a[112:115], v[96:99]\n"
"s_mov_b32 s58, 397312\n"
"buffer_load_dwordx4 a[92:95], v246, s[44:47], s58 offen offset:1024 sc1\n"
"v_mfma_f32_16x16x32_bf16 v[100:103], v[184:187], a[116:119], v[100:103]\n"
"v_mfma_f32_16x16x32_bf16 v[112:115], v[188:191], a[112:115], v[112:115]\n"
"v_mfma_f32_16x16x32_bf16 v[116:119], v[188:191], a[116:119], v[116:119]\n"
"s_waitcnt vmcnt(27) lgkmcnt(0)\n"
"v_mfma_f32_16x16x32_bf16 v[0:3], v[128:131], a[128:131], v[0:3]\n"
"ds_read_b64_tr_b16 v[160:161], v251 offset:8192\n"
"ds_read_b64_tr_b16 v[162:163], v251 offset:12288\n"
"v_mfma_f32_16x16x32_bf16 v[4:7], v[128:131], a[132:135], v[4:7]\n"
"ds_read_b64_tr_b16 v[164:165], v252 offset:8192\n"
"ds_read_b64_tr_b16 v[166:167], v252 offset:12288\n"
"v_mfma_f32_16x16x32_bf16 v[16:19], v[132:135], a[128:131], v[16:19]\n"
"ds_read_b64_tr_b16 v[168:169], v251 offset:8704\n"
"ds_read_b64_tr_b16 v[170:171], v251 offset:12800\n"
"v_mfma_f32_16x16x32_bf16 v[20:23], v[132:135], a[132:135], v[20:23]\n"
"ds_read_b64_tr_b16 v[172:173], v252 offset:8704\n"
"ds_read_b64_tr_b16 v[174:175], v252 offset:12800\n"
"v_mfma_f32_16x16x32_bf16 v[32:35], v[136:139], a[128:131], v[32:35]\n"
"ds_read_b64_tr_b16 v[176:177], v251 offset:9216\n"
"ds_read_b64_tr_b16 v[178:179], v251 offset:13312\n"
"v_mfma_f32_16x16x32_bf16 v[36:39], v[136:139], a[132:135], v[36:39]\n"
"ds_read_b64_tr_b16 v[180:181], v252 offset:9216\n"
"ds_read_b64_tr_b16 v[182:183], v252 offset:13312\n"
"v_mfma_f32_16x16x32_bf16 v[48:51], v[140:143], a[128:131], v[48:51]\n"
"ds_read_b64_tr_b16 v[184:185], v251 offset:9728\n"
"ds_read_b64_tr_b16 v[186:187], v251 offset:13824\n"
"v_mfma_f32_16x16x32_bf16 v[52:55], v[140:143], a[132:135], v[52:55]\n"
"ds_read_b64_tr_b16 v[188:189], v252 offset:9728\n"
"ds_read_b64_tr_b16 v[190:191], v252 offset:13824\n"
"v_mfma_f32_16x16x32_bf16 v[64:67], v[144:147], a[128:131], v[64:67]\n"
"s_mov_b32 s58, 266240\n"
"buffer_load_dwordx4 a[104:107], v246, s[44:47], s58 offen offset:2048 sc1\n"
"v_mfma_f32_16x16x32_bf16 v[68:71], v[144:147], a[132:135], v[68:71]\n"
"v_mfma_f32_16x16x32_bf16 v[80:83], v[148:151], a[128:131], v[80:83]\n"
"s_mov_b32 s58, 397312\n"
"buffer_load_dwordx4 a[108:111], v246, s[44:47], s58 offen offset:2048 sc1\n"
"v_mfma_f32_16x16x32_bf16 v[84:87], v[148:151], a[132:135], v[84:87]\n"
"v_mfma_f32_16x16x32_bf16 v[96:99], v[152:155], a[128:131], v[96:99]\n"
"s_mov_b32 s58, 266240\n"
"buffer_load_dwordx4 a[120:123], v246, s[44:47], s58 offen offset:3072 sc1\n"
"v_mfma_f32_16x16x32_bf16 v[100:103], v[152:155], a[132:135], v[100:103]\n"
"v_mfma_f32_16x16x32_bf16 v[112:115], v[156:159], a[128:131], v[112:115]\n"
"v_mfma_f32_16x16x32_bf16 v[116:119], v[156:159], a[132:135], v[116:119]\n"
"s_waitcnt vmcnt(28) lgkmcnt(0)\n"
"v_mfma_f32_16x16x32_bf16 v[0:3], v[160:163], a[144:147], v[0:3]\n"
"ds_read_b64_tr_b16 v[128:129], v251 offset:16384\n"
"ds_read_b64_tr_b16 v[130:131], v251 offset:20480\n"
"v_mfma_f32_16x16x32_bf16 v[4:7], v[160:163], a[148:151], v[4:7]\n"
"ds_read_b64_tr_b16 v[132:133], v252 offset:16384\n"
"ds_read_b64_tr_b16 v[134:135], v252 offset:20480\n"
"v_mfma_f32_16x16x32_bf16 v[16:19], v[164:167], a[144:147], v[16:19]\n"
"ds_read_b64_tr_b16 v[136:137], v251 offset:16896\n"
"ds_read_b64_tr_b16 v[138:139], v251 offset:20992\n"
"v_mfma_f32_16x16x32_bf16 v[20:23], v[164:167], a[148:151], v[20:23]\n"
"ds_read_b64_tr_b16 v[140:141], v252 offset:16896\n"
"ds_read_b64_tr_b16 v[142:143], v252 offset:20992\n"
"v_mfma_f32_16x16x32_bf16 v[32:35], v[168:171], a[144:147], v[32:35]\n"
"ds_read_b64_tr_b16 v[144:145], v251 offset:17408\n"
"ds_read_b64_tr_b16 v[146:147], v251 offset:21504\n"
"v_mfma_f32_16x16x32_bf16 v[36:39], v[168:171], a[148:151], v[36:39]\n"
"ds_read_b64_tr_b16 v[148:149], v252 offset:17408\n"
"ds_read_b64_tr_b16 v[150:151], v252 offset:21504\n"
"v_mfma_f32_16x16x32_bf16 v[48:51], v[172:175], a[144:147], v[48:51]\n"
"ds_read_b64_tr_b16 v[152:153], v251 offset:17920\n"
"ds_read_b64_tr_b16 v[154:155], v251 offset:22016\n"
"v_mfma_f32_16x16x32_bf16 v[52:55], v[172:175], a[148:151], v[52:55]\n"
"ds_read_b64_tr_b16 v[156:157], v252 offset:17920\n"
"ds_read_b64_tr_b16 v[158:159], v252 offset:22016\n"
"v_mfma_f32_16x16x32_bf16 v[64:67], v[176:179], a[144:147], v[64:67]\n"
"s_mov_b32 s58, 397312\n"
"buffer_load_dwordx4 a[124:127], v246, s[44:47], s58 offen offset:3072 sc1\n"
"v_mfma_f32_16x16x32_bf16 v[68:71], v[176:179], a[148:151], v[68:71]\n"
"v_mfma_f32_16x16x32_bf16 v[80:83], v[180:183], a[144:147], v[80:83]\n"
"s_mov_b32 s58, 270336\n"
"buffer_load_dwordx4 a[136:139], v246, s[44:47], s58 offen offset:0 sc1\n"
"v_mfma_f32_16x16x32_bf16 v[84:87], v[180:183], a[148:151], v[84:87]\n"
"v_mfma_f32_16x16x32_bf16 v[96:99], v[184:187], a[144:147], v[96:99]\n"
"s_mov_b32 s58, 401408\n"
"buffer_load_dwordx4 a[140:143], v246, s[44:47], s58 offen offset:0 sc1\n"
"v_mfma_f32_16x16x32_bf16 v[100:103], v[184:187], a[148:151], v[100:103]\n"
"v_mfma_f32_16x16x32_bf16 v[112:115], v[188:191], a[144:147], v[112:115]\n"
"v_mfma_f32_16x16x32_bf16 v[116:119], v[188:191], a[148:151], v[116:119]\n"
"s_waitcnt vmcnt(29) lgkmcnt(0)\n"
"v_mfma_f32_16x16x32_bf16 v[0:3], v[128:131], a[160:163], v[0:3]\n"
"ds_read_b64_tr_b16 v[160:161], v251 offset:24576\n"
"ds_read_b64_tr_b16 v[162:163], v251 offset:28672\n"
"v_mfma_f32_16x16x32_bf16 v[4:7], v[128:131], a[164:167], v[4:7]\n"
"ds_read_b64_tr_b16 v[164:165], v252 offset:24576\n"
"ds_read_b64_tr_b16 v[166:167], v252 offset:28672\n"
"v_mfma_f32_16x16x32_bf16 v[16:19], v[132:135], a[160:163], v[16:19]\n"
"ds_read_b64_tr_b16 v[168:169], v251 offset:25088\n"
"ds_read_b64_tr_b16 v[170:171], v251 offset:29184\n"
"v_mfma_f32_16x16x32_bf16 v[20:23], v[132:135], a[164:167], v[20:23]\n"
"ds_read_b64_tr_b16 v[172:173], v252 offset:25088\n"
"ds_read_b64_tr_b16 v[174:175], v252 offset:29184\n"
"v_mfma_f32_16x16x32_bf16 v[32:35], v[136:139], a[160:163], v[32:35]\n"
"ds_read_b64_tr_b16 v[176:177], v251 offset:25600\n"
"ds_read_b64_tr_b16 v[178:179], v251 offset:29696\n"
"v_mfma_f32_16x16x32_bf16 v[36:39], v[136:139], a[164:167], v[36:39]\n"
"ds_read_b64_tr_b16 v[180:181], v252 offset:25600\n"
"ds_read_b64_tr_b16 v[182:183], v252 offset:29696\n"
"v_mfma_f32_16x16x32_bf16 v[48:51], v[140:143], a[160:163], v[48:51]\n"
"ds_read_b64_tr_b16 v[184:185], v251 offset:26112\n"
"ds_read_b64_tr_b16 v[186:187], v251 offset:30208\n"
"v_mfma_f32_16x16x32_bf16 v[52:55], v[140:143], a[164:167], v[52:55]\n"
"ds_read_b64_tr_b16 v[188:189], v252 offset:26112\n"
"ds_read_b64_tr_b16 v[190:191], v252 offset:30208\n"
"v_mfma_f32_16x16x32_bf16 v[64:67], v[144:147], a[160:163], v[64:67]\n"
"s_mov_b32 s58, 270336\n"
"buffer_load_dwordx4 a[152:155], v246, s[44:47], s58 offen offset:1024 sc1\n"
"v_mfma_f32_16x16x32_bf16 v[68:71], v[144:147], a[164:167], v[68:71]\n"
"v_mfma_f32_16x16x32_bf16 v[80:83], v[148:151], a[160:163], v[80:83]\n"
"s_mov_b32 s58, 401408\n"
"buffer_load_dwordx4 a[156:159], v246, s[44:47], s58 offen offset:1024 sc1\n"
"v_mfma_f32_16x16x32_bf16 v[84:87], v[148:151], a[164:167], v[84:87]\n"
"v_mfma_f32_16x16x32_bf16 v[96:99], v[152:155], a[160:163], v[96:99]\n"
"s_mov_b32 s58, 270336\n"
"buffer_load_dwordx4 a[168:171], v246, s[44:47], s58 offen offset:2048 sc1\n"
"v_mfma_f32_16x16x32_bf16 v[100:103], v[152:155], a[164:167], v[100:103]\n"
"v_mfma_f32_16x16x32_bf16 v[112:115], v[156:159], a[160:163], v[112:115]\n"
"v_mfma_f32_16x16x32_bf16 v[116:119], v[156:159], a[164:167], v[116:119]\n"
"s_waitcnt vmcnt(30) lgkmcnt(0)\n"
"v_mfma_f32_16x16x32_bf16 v[0:3], v[160:163], a[176:179], v[0:3]\n"
"ds_read_b64_tr_b16 v[128:129], v251 offset:32768\n"
"ds_read_b64_tr_b16 v[130:131], v251 offset:36864\n"
"v_mfma_f32_16x16x32_bf16 v[4:7], v[160:163], a[180:183], v[4:7]\n"
"ds_read_b64_tr_b16 v[132:133], v252 offset:32768\n"
"ds_read_b64_tr_b16 v[134:135], v252 offset:36864\n"
"v_mfma_f32_16x16x32_bf16 v[16:19], v[164:167], a[176:179], v[16:19]\n"
"ds_read_b64_tr_b16 v[136:137], v251 offset:33280\n"
"ds_read_b64_tr_b16 v[138:139], v251 offset:37376\n"
"v_mfma_f32_16x16x32_bf16 v[20:23], v[164:167], a[180:183], v[20:23]\n"
"ds_read_b64_tr_b16 v[140:141], v252 offset:33280\n"
"ds_read_b64_tr_b16 v[142:143], v252 offset:37376\n"
"v_mfma_f32_16x16x32_bf16 v[32:35], v[168:171], a[176:179], v[32:35]\n"
"ds_read_b64_tr_b16 v[144:145], v251 offset:33792\n"
"ds_read_b64_tr_b16 v[146:147], v251 offset:37888\n"
"v_mfma_f32_16x16x32_bf16 v[36:39], v[168:171], a[180:183], v[36:39]\n"
"ds_read_b64_tr_b16 v[148:149], v252 offset:33792\n"
"ds_read_b64_tr_b16 v[150:151], v252 offset:37888\n"
"v_mfma_f32_16x16x32_bf16 v[48:51], v[172:175], a[176:179], v[48:51]\n"
"ds_read_b64_tr_b16 v[152:153], v251 offset:34304\n"
"ds_read_b64_tr_b16 v[154:155], v251 offset:38400\n"
"v_mfma_f32_16x16x32_bf16 v[52:55], v[172:175], a[180:183], v[52:55]\n"
"ds_read_b64_tr_b16 v[156:157], v252 offset:34304\n"
"ds_read_b64_tr_b16 v[158:159], v252 offset:38400\n"
"v_mfma_f32_16x16x32_bf16 v[64:67], v[176:179], a[176:179], v[64:67]\n"
"s_mov_b32 s58, 401408\n"
"buffer_load_dwordx4 a[172:175], v246, s[44:47], s58 offen offset:2048 sc1\n"
"v_mfma_f32_16x16x32_bf16 v[68:71], v[176:179], a[180:183], v[68:71]\n"
"v_mfma_f32_16x16x32_bf16 v[80:83], v[180:183], a[176:179], v[80:83]\n"
"s_mov_b32 s58, 270336\n"
"buffer_load_dwordx4 a[184:187], v246, s[44:47], s58 offen offset:3072 sc1\n"
"v_mfma_f32_16x16x32_bf16 v[84:87], v[180:183], a[180:183], v[84:87]\n"
"v_mfma_f32_16x16x32_bf16 v[96:99], v[184:187], a[176:179], v[96:99]\n"
"s_mov_b32 s58, 401408\n"
"buffer_load_dwordx4 a[188:191], v246, s[44:47], s58 offen offset:3072 sc1\n"
"v_mfma_f32_16x16x32_bf16 v[100:103], v[184:187], a[180:183], v[100:103]\n"
"v_mfma_f32_16x16x32_bf16 v[112:115], v[188:191], a[176:179], v[112:115]\n"
"v_mfma_f32_16x16x32_bf16 v[116:119], v[188:191], a[180:183], v[116:119]\n"
"s_waitcnt vmcnt(31) lgkmcnt(0)\n"
"v_mfma_f32_16x16x32_bf16 v[0:3], v[128:131], a[192:195], v[0:3]\n"
"ds_read_b64_tr_b16 v[160:161], v251 offset:40960\n"
"ds_read_b64_tr_b16 v[162:163], v251 offset:45056\n"
"v_mfma_f32_16x16x32_bf16 v[4:7], v[128:131], a[196:199], v[4:7]\n"
"ds_read_b64_tr_b16 v[164:165], v252 offset:40960\n"
"ds_read_b64_tr_b16 v[166:167], v252 offset:45056\n"
"v_mfma_f32_16x16x32_bf16 v[16:19], v[132:135], a[192:195], v[16:19]\n"
"ds_read_b64_tr_b16 v[168:169], v251 offset:41472\n"
"ds_read_b64_tr_b16 v[170:171], v251 offset:45568\n"
"v_mfma_f32_16x16x32_bf16 v[20:23], v[132:135], a[196:199], v[20:23]\n"
"ds_read_b64_tr_b16 v[172:173], v252 offset:41472\n"
"ds_read_b64_tr_b16 v[174:175], v252 offset:45568\n"
"v_mfma_f32_16x16x32_bf16 v[32:35], v[136:139], a[192:195], v[32:35]\n"
"ds_read_b64_tr_b16 v[176:177], v251 offset:41984\n"
"ds_read_b64_tr_b16 v[178:179], v251 offset:46080\n"
"v_mfma_f32_16x16x32_bf16 v[36:39], v[136:139], a[196:199], v[36:39]\n"
"ds_read_b64_tr_b16 v[180:181], v252 offset:41984\n"
"ds_read_b64_tr_b16 v[182:183], v252 offset:46080\n"
"v_mfma_f32_16x16x32_bf16 v[48:51], v[140:143], a[192:195], v[48:51]\n"
"ds_read_b64_tr_b16 v[184:185], v251 offset:42496\n"
"ds_read_b64_tr_b16 v[186:187], v251 offset:46592\n"
"v_mfma_f32_16x16x32_bf16 v[52:55], v[140:143], a[196:199], v[52:55]\n"
"ds_read_b64_tr_b16 v[188:189], v252 offset:42496\n"
"ds_read_b64_tr_b16 v[190:191], v252 offset:46592\n"
"v_mfma_f32_16x16x32_bf16 v[64:67], v[144:147], a[192:195], v[64:67]\n"
"s_mov_b32 s58, 274432\n"
"buffer_load_dwordx4 a[200:203], v246, s[44:47], s58 offen offset:0 sc1\n"
"v_mfma_f32_16x16x32_bf16 v[68:71], v[144:147], a[196:199], v[68:71]\n"
"v_mfma_f32_16x16x32_bf16 v[80:83], v[148:151], a[192:195], v[80:83]\n"
"s_mov_b32 s58, 405504\n"
"buffer_load_dwordx4 a[204:207], v246, s[44:47], s58 offen offset:0 sc1\n"
"v_mfma_f32_16x16x32_bf16 v[84:87], v[148:151], a[196:199], v[84:87]\n"
"v_mfma_f32_16x16x32_bf16 v[96:99], v[152:155], a[192:195], v[96:99]\n"
"v_mfma_f32_16x16x32_bf16 v[100:103], v[152:155], a[196:199], v[100:103]\n"
"v_mfma_f32_16x16x32_bf16 v[112:115], v[156:159], a[192:195], v[112:115]\n"
"v_mfma_f32_16x16x32_bf16 v[116:119], v[156:159], a[196:199], v[116:119]\n"
"s_waitcnt vmcnt(31) lgkmcnt(0)\n"
"v_mfma_f32_16x16x32_bf16 v[0:3], v[160:163], a[208:211], v[0:3]\n"
"ds_read_b64_tr_b16 v[128:129], v251 offset:49152\n"
"ds_read_b64_tr_b16 v[130:131], v251 offset:53248\n"
"v_mfma_f32_16x16x32_bf16 v[4:7], v[160:163], a[212:215], v[4:7]\n"
"ds_read_b64_tr_b16 v[132:133], v252 offset:49152\n"
"ds_read_b64_tr_b16 v[134:135], v252 offset:53248\n"
"v_mfma_f32_16x16x32_bf16 v[16:19], v[164:167], a[208:211], v[16:19]\n"
"ds_read_b64_tr_b16 v[136:137], v251 offset:49664\n"
"ds_read_b64_tr_b16 v[138:139], v251 offset:53760\n"
"v_mfma_f32_16x16x32_bf16 v[20:23], v[164:167], a[212:215], v[20:23]\n"
"ds_read_b64_tr_b16 v[140:141], v252 offset:49664\n"
"ds_read_b64_tr_b16 v[142:143], v252 offset:53760\n"
"v_mfma_f32_16x16x32_bf16 v[32:35], v[168:171], a[208:211], v[32:35]\n"
"ds_read_b64_tr_b16 v[144:145], v251 offset:50176\n"
"ds_read_b64_tr_b16 v[146:147], v251 offset:54272\n"
"v_mfma_f32_16x16x32_bf16 v[36:39], v[168:171], a[212:215], v[36:39]\n"
"ds_read_b64_tr_b16 v[148:149], v252 offset:50176\n"
"ds_read_b64_tr_b16 v[150:151], v252 offset:54272\n"
"v_mfma_f32_16x16x32_bf16 v[48:51], v[172:175], a[208:211], v[48:51]\n"
"ds_read_b64_tr_b16 v[152:153], v251 offset:50688\n"
"ds_read_b64_tr_b16 v[154:155], v251 offset:54784\n"
"v_mfma_f32_16x16x32_bf16 v[52:55], v[172:175], a[212:215], v[52:55]\n"
"ds_read_b64_tr_b16 v[156:157], v252 offset:50688\n"
"ds_read_b64_tr_b16 v[158:159], v252 offset:54784\n"
"v_mfma_f32_16x16x32_bf16 v[64:67], v[176:179], a[208:211], v[64:67]\n"
"s_mov_b32 s58, 274432\n"
"buffer_load_dwordx4 a[216:219], v246, s[44:47], s58 offen offset:1024 sc1\n"
"v_mfma_f32_16x16x32_bf16 v[68:71], v[176:179], a[212:215], v[68:71]\n"
"v_mfma_f32_16x16x32_bf16 v[80:83], v[180:183], a[208:211], v[80:83]\n"
"s_mov_b32 s58, 405504\n"
"buffer_load_dwordx4 a[220:223], v246, s[44:47], s58 offen offset:1024 sc1\n"
"v_mfma_f32_16x16x32_bf16 v[84:87], v[180:183], a[212:215], v[84:87]\n"
"v_mfma_f32_16x16x32_bf16 v[96:99], v[184:187], a[208:211], v[96:99]\n"
"v_mfma_f32_16x16x32_bf16 v[100:103], v[184:187], a[212:215], v[100:103]\n"
"v_mfma_f32_16x16x32_bf16 v[112:115], v[188:191], a[208:211], v[112:115]\n"
"v_mfma_f32_16x16x32_bf16 v[116:119], v[188:191], a[212:215], v[116:119]\n"
"s_waitcnt vmcnt(31) lgkmcnt(0)\n"
"v_mfma_f32_16x16x32_bf16 v[0:3], v[128:131], a[224:227], v[0:3]\n"
"ds_read_b64_tr_b16 v[160:161], v251 offset:57344\n"
"ds_read_b64_tr_b16 v[162:163], v251 offset:61440\n"
"v_mfma_f32_16x16x32_bf16 v[4:7], v[128:131], a[228:231], v[4:7]\n"
"ds_read_b64_tr_b16 v[164:165], v252 offset:57344\n"
"ds_read_b64_tr_b16 v[166:167], v252 offset:61440\n"
"v_mfma_f32_16x16x32_bf16 v[16:19], v[132:135], a[224:227], v[16:19]\n"
"ds_read_b64_tr_b16 v[168:169], v251 offset:57856\n"
"ds_read_b64_tr_b16 v[170:171], v251 offset:61952\n"
"v_mfma_f32_16x16x32_bf16 v[20:23], v[132:135], a[228:231], v[20:23]\n"
"ds_read_b64_tr_b16 v[172:173], v252 offset:57856\n"
"ds_read_b64_tr_b16 v[174:175], v252 offset:61952\n"
"v_mfma_f32_16x16x32_bf16 v[32:35], v[136:139], a[224:227], v[32:35]\n"
"ds_read_b64_tr_b16 v[176:177], v251 offset:58368\n"
"ds_read_b64_tr_b16 v[178:179], v251 offset:62464\n"
"v_mfma_f32_16x16x32_bf16 v[36:39], v[136:139], a[228:231], v[36:39]\n"
"ds_read_b64_tr_b16 v[180:181], v252 offset:58368\n"
"ds_read_b64_tr_b16 v[182:183], v252 offset:62464\n"
"v_mfma_f32_16x16x32_bf16 v[48:51], v[140:143], a[224:227], v[48:51]\n"
"ds_read_b64_tr_b16 v[184:185], v251 offset:58880\n"
"ds_read_b64_tr_b16 v[186:187], v251 offset:62976\n"
"v_mfma_f32_16x16x32_bf16 v[52:55], v[140:143], a[228:231], v[52:55]\n"
"ds_read_b64_tr_b16 v[188:189], v252 offset:58880\n"
"ds_read_b64_tr_b16 v[190:191], v252 offset:62976\n"
"v_mfma_f32_16x16x32_bf16 v[64:67], v[144:147], a[224:227], v[64:67]\n"
"s_mov_b32 s58, 274432\n"
"buffer_load_dwordx4 a[232:235], v246, s[44:47], s58 offen offset:2048 sc1\n"
"v_mfma_f32_16x16x32_bf16 v[68:71], v[144:147], a[228:231], v[68:71]\n"
"v_mfma_f32_16x16x32_bf16 v[80:83], v[148:151], a[224:227], v[80:83]\n"
"s_mov_b32 s58, 405504\n"
"buffer_load_dwordx4 a[236:239], v246, s[44:47], s58 offen offset:2048 sc1\n"
"v_mfma_f32_16x16x32_bf16 v[84:87], v[148:151], a[228:231], v[84:87]\n"
"v_mfma_f32_16x16x32_bf16 v[96:99], v[152:155], a[224:227], v[96:99]\n"
"v_mfma_f32_16x16x32_bf16 v[100:103], v[152:155], a[228:231], v[100:103]\n"
"v_mfma_f32_16x16x32_bf16 v[112:115], v[156:159], a[224:227], v[112:115]\n"
"v_mfma_f32_16x16x32_bf16 v[116:119], v[156:159], a[228:231], v[116:119]\n"
"s_waitcnt vmcnt(31) lgkmcnt(0)\n"
"v_mfma_f32_16x16x32_bf16 v[0:3], v[160:163], a[240:243], v[0:3]\n"
"ds_read_b64_tr_b16 v[128:129], v249 offset:0\n"
"ds_read_b64_tr_b16 v[130:131], v249 offset:4096\n"
"v_mfma_f32_16x16x32_bf16 v[4:7], v[160:163], a[244:247], v[4:7]\n"
"ds_read_b64_tr_b16 v[132:133], v250 offset:0\n"
"ds_read_b64_tr_b16 v[134:135], v250 offset:4096\n"
"v_mfma_f32_16x16x32_bf16 v[16:19], v[164:167], a[240:243], v[16:19]\n"
"ds_read_b64_tr_b16 v[136:137], v249 offset:512\n"
"ds_read_b64_tr_b16 v[138:139], v249 offset:4608\n"
"v_mfma_f32_16x16x32_bf16 v[20:23], v[164:167], a[244:247], v[20:23]\n"
"ds_read_b64_tr_b16 v[140:141], v250 offset:512\n"
"ds_read_b64_tr_b16 v[142:143], v250 offset:4608\n"
"v_mfma_f32_16x16x32_bf16 v[32:35], v[168:171], a[240:243], v[32:35]\n"
"ds_read_b64_tr_b16 v[144:145], v249 offset:1024\n"
"ds_read_b64_tr_b16 v[146:147], v249 offset:5120\n"
"v_mfma_f32_16x16x32_bf16 v[36:39], v[168:171], a[244:247], v[36:39]\n"
"ds_read_b64_tr_b16 v[148:149], v250 offset:1024\n"
"ds_read_b64_tr_b16 v[150:151], v250 offset:5120\n"
"v_mfma_f32_16x16x32_bf16 v[48:51], v[172:175], a[240:243], v[48:51]\n"
"ds_read_b64_tr_b16 v[152:153], v249 offset:1536\n"
"ds_read_b64_tr_b16 v[154:155], v249 offset:5632\n"
"v_mfma_f32_16x16x32_bf16 v[52:55], v[172:175], a[244:247], v[52:55]\n"
"ds_read_b64_tr_b16 v[156:157], v250 offset:1536\n"
"ds_read_b64_tr_b16 v[158:159], v250 offset:5632\n"
"v_mfma_f32_16x16x32_bf16 v[64:67], v[176:179], a[240:243], v[64:67]\n"
"s_mov_b32 s58, 274432\n"
"buffer_load_dwordx4 a[248:251], v246, s[44:47], s58 offen offset:3072 sc1\n"
"v_mfma_f32_16x16x32_bf16 v[68:71], v[176:179], a[244:247], v[68:71]\n"
"v_mfma_f32_16x16x32_bf16 v[80:83], v[180:183], a[240:243], v[80:83]\n"
"s_mov_b32 s58, 405504\n"
"buffer_load_dwordx4 a[252:255], v246, s[44:47], s58 offen offset:3072 sc1\n"
"v_mfma_f32_16x16x32_bf16 v[84:87], v[180:183], a[244:247], v[84:87]\n"
"v_mfma_f32_16x16x32_bf16 v[96:99], v[184:187], a[240:243], v[96:99]\n"
"v_mfma_f32_16x16x32_bf16 v[100:103], v[184:187], a[244:247], v[100:103]\n"
"v_mfma_f32_16x16x32_bf16 v[112:115], v[188:191], a[240:243], v[112:115]\n"
"v_mfma_f32_16x16x32_bf16 v[116:119], v[188:191], a[244:247], v[116:119]\n"
"s_nop 7\n"
"s_mov_b32 s59, 0\n"
"v_cvt_pk_bf16_f32 v192, v0, v1\n"
"v_cvt_pk_bf16_f32 v193, v2, v3\n"
"v_cvt_pk_bf16_f32 v194, v16, v17\n"
"v_cvt_pk_bf16_f32 v195, v18, v19\n"
"buffer_store_dwordx4 v[192:195], v246, s[48:51], s59 offen offset:0 sc1\n"
"v_cvt_pk_bf16_f32 v196, v32, v33\n"
"v_cvt_pk_bf16_f32 v197, v34, v35\n"
"v_cvt_pk_bf16_f32 v198, v48, v49\n"
"v_cvt_pk_bf16_f32 v199, v50, v51\n"
"buffer_store_dwordx4 v[196:199], v246, s[48:51], s59 offen offset:1024 sc1\n"
"v_cvt_pk_bf16_f32 v200, v64, v65\n"
"v_cvt_pk_bf16_f32 v201, v66, v67\n"
"v_cvt_pk_bf16_f32 v202, v80, v81\n"
"v_cvt_pk_bf16_f32 v203, v82, v83\n"
"buffer_store_dwordx4 v[200:203], v246, s[48:51], s59 offen offset:2048 sc1\n"
"v_cvt_pk_bf16_f32 v204, v96, v97\n"
"v_cvt_pk_bf16_f32 v205, v98, v99\n"
"v_cvt_pk_bf16_f32 v206, v112, v113\n"
"v_cvt_pk_bf16_f32 v207, v114, v115\n"
"buffer_store_dwordx4 v[204:207], v246, s[48:51], s59 offen offset:3072 sc1\n"
"s_mov_b32 s59, 16384\n"
"v_cvt_pk_bf16_f32 v192, v4, v5\n"
"v_cvt_pk_bf16_f32 v193, v6, v7\n"
"v_cvt_pk_bf16_f32 v194, v20, v21\n"
"v_cvt_pk_bf16_f32 v195, v22, v23\n"
"buffer_store_dwordx4 v[192:195], v246, s[48:51], s59 offen offset:0 sc1\n"
"v_cvt_pk_bf16_f32 v196, v36, v37\n"
"v_cvt_pk_bf16_f32 v197, v38, v39\n"
"v_cvt_pk_bf16_f32 v198, v52, v53\n"
"v_cvt_pk_bf16_f32 v199, v54, v55\n"
"buffer_store_dwordx4 v[196:199], v246, s[48:51], s59 offen offset:1024 sc1\n"
"v_cvt_pk_bf16_f32 v200, v68, v69\n"
"v_cvt_pk_bf16_f32 v201, v70, v71\n"
"v_cvt_pk_bf16_f32 v202, v84, v85\n"
"v_cvt_pk_bf16_f32 v203, v86, v87\n"
"buffer_store_dwordx4 v[200:203], v246, s[48:51], s59 offen offset:2048 sc1\n"
"v_cvt_pk_bf16_f32 v204, v100, v101\n"
"v_cvt_pk_bf16_f32 v205, v102, v103\n"
"v_cvt_pk_bf16_f32 v206, v116, v117\n"
"v_cvt_pk_bf16_f32 v207, v118, v119\n"
"buffer_store_dwordx4 v[204:207], v246, s[48:51], s59 offen offset:3072 sc1\n"
"s_waitcnt vmcnt(38) lgkmcnt(0)\n"
"v_mfma_f32_16x16x32_bf16 v[8:11], v[128:131], a[8:11], 0\n"
"ds_read_b64_tr_b16 v[160:161], v249 offset:8192\n"
"ds_read_b64_tr_b16 v[162:163], v249 offset:12288\n"
"v_mfma_f32_16x16x32_bf16 v[12:15], v[128:131], a[12:15], 0\n"
"ds_read_b64_tr_b16 v[164:165], v250 offset:8192\n"
"ds_read_b64_tr_b16 v[166:167], v250 offset:12288\n"
"v_mfma_f32_16x16x32_bf16 v[24:27], v[132:135], a[8:11], 0\n"
"ds_read_b64_tr_b16 v[168:169], v249 offset:8704\n"
"ds_read_b64_tr_b16 v[170:171], v249 offset:12800\n"
"v_mfma_f32_16x16x32_bf16 v[28:31], v[132:135], a[12:15], 0\n"
"ds_read_b64_tr_b16 v[172:173], v250 offset:8704\n"
"ds_read_b64_tr_b16 v[174:175], v250 offset:12800\n"
"v_mfma_f32_16x16x32_bf16 v[40:43], v[136:139], a[8:11], 0\n"
"ds_read_b64_tr_b16 v[176:177], v249 offset:9216\n"
"ds_read_b64_tr_b16 v[178:179], v249 offset:13312\n"
"v_mfma_f32_16x16x32_bf16 v[44:47], v[136:139], a[12:15], 0\n"
"ds_read_b64_tr_b16 v[180:181], v250 offset:9216\n"
"ds_read_b64_tr_b16 v[182:183], v250 offset:13312\n"
"v_mfma_f32_16x16x32_bf16 v[56:59], v[140:143], a[8:11], 0\n"
"ds_read_b64_tr_b16 v[184:185], v249 offset:9728\n"
"ds_read_b64_tr_b16 v[186:187], v249 offset:13824\n"
"v_mfma_f32_16x16x32_bf16 v[60:63], v[140:143], a[12:15], 0\n"
"ds_read_b64_tr_b16 v[188:189], v250 offset:9728\n"
"ds_read_b64_tr_b16 v[190:191], v250 offset:13824\n"
"v_mfma_f32_16x16x32_bf16 v[72:75], v[144:147], a[8:11], 0\n"
"v_mfma_f32_16x16x32_bf16 v[76:79], v[144:147], a[12:15], 0\n"
"v_mfma_f32_16x16x32_bf16 v[88:91], v[148:151], a[8:11], 0\n"
"v_mfma_f32_16x16x32_bf16 v[92:95], v[148:151], a[12:15], 0\n"
"v_mfma_f32_16x16x32_bf16 v[104:107], v[152:155], a[8:11], 0\n"
"v_mfma_f32_16x16x32_bf16 v[108:111], v[152:155], a[12:15], 0\n"
"v_mfma_f32_16x16x32_bf16 v[120:123], v[156:159], a[8:11], 0\n"
"v_mfma_f32_16x16x32_bf16 v[124:127], v[156:159], a[12:15], 0\n"
"s_waitcnt vmcnt(36) lgkmcnt(0)\n"
"v_mfma_f32_16x16x32_bf16 v[8:11], v[160:163], a[24:27], v[8:11]\n"
"ds_read_b64_tr_b16 v[128:129], v249 offset:16384\n"
"ds_read_b64_tr_b16 v[130:131], v249 offset:20480\n"
"v_mfma_f32_16x16x32_bf16 v[12:15], v[160:163], a[28:31], v[12:15]\n"
"ds_read_b64_tr_b16 v[132:133], v250 offset:16384\n"
"ds_read_b64_tr_b16 v[134:135], v250 offset:20480\n"
"v_mfma_f32_16x16x32_bf16 v[24:27], v[164:167], a[24:27], v[24:27]\n"
"ds_read_b64_tr_b16 v[136:137], v249 offset:16896\n"
"ds_read_b64_tr_b16 v[138:139], v249 offset:20992\n"
"v_mfma_f32_16x16x32_bf16 v[28:31], v[164:167], a[28:31], v[28:31]\n"
"ds_read_b64_tr_b16 v[140:141], v250 offset:16896\n"
"ds_read_b64_tr_b16 v[142:143], v250 offset:20992\n"
"v_mfma_f32_16x16x32_bf16 v[40:43], v[168:171], a[24:27], v[40:43]\n"
"ds_read_b64_tr_b16 v[144:145], v249 offset:17408\n"
"ds_read_b64_tr_b16 v[146:147], v249 offset:21504\n"
"v_mfma_f32_16x16x32_bf16 v[44:47], v[168:171], a[28:31], v[44:47]\n"
"ds_read_b64_tr_b16 v[148:149], v250 offset:17408\n"
"ds_read_b64_tr_b16 v[150:151], v250 offset:21504\n"
"v_mfma_f32_16x16x32_bf16 v[56:59], v[172:175], a[24:27], v[56:59]\n"
"ds_read_b64_tr_b16 v[152:153], v249 offset:17920\n"
"ds_read_b64_tr_b16 v[154:155], v249 offset:22016\n"
"v_mfma_f32_16x16x32_bf16 v[60:63], v[172:175], a[28:31], v[60:63]\n"
"ds_read_b64_tr_b16 v[156:157], v250 offset:17920\n"
"ds_read_b64_tr_b16 v[158:159], v250 offset:22016\n"
"v_mfma_f32_16x16x32_bf16 v[72:75], v[176:179], a[24:27], v[72:75]\n"
"v_mfma_f32_16x16x32_bf16 v[76:79], v[176:179], a[28:31], v[76:79]\n"
"v_mfma_f32_16x16x32_bf16 v[88:91], v[180:183], a[24:27], v[88:91]\n"
"v_mfma_f32_16x16x32_bf16 v[92:95], v[180:183], a[28:31], v[92:95]\n"
"v_mfma_f32_16x16x32_bf16 v[104:107], v[184:187], a[24:27], v[104:107]\n"
"v_mfma_f32_16x16x32_bf16 v[108:111], v[184:187], a[28:31], v[108:111]\n"
"v_mfma_f32_16x16x32_bf16 v[120:123], v[188:191], a[24:27], v[120:123]\n"
"v_mfma_f32_16x16x32_bf16 v[124:127], v[188:191], a[28:31], v[124:127]\n"
"s_waitcnt vmcnt(34) lgkmcnt(0)\n"
"v_mfma_f32_16x16x32_bf16 v[8:11], v[128:131], a[40:43], v[8:11]\n"
"ds_read_b64_tr_b16 v[160:161], v249 offset:24576\n"
"ds_read_b64_tr_b16 v[162:163], v249 offset:28672\n"
"v_mfma_f32_16x16x32_bf16 v[12:15], v[128:131], a[44:47], v[12:15]\n"
"ds_read_b64_tr_b16 v[164:165], v250 offset:24576\n"
"ds_read_b64_tr_b16 v[166:167], v250 offset:28672\n"
"v_mfma_f32_16x16x32_bf16 v[24:27], v[132:135], a[40:43], v[24:27]\n"
"ds_read_b64_tr_b16 v[168:169], v249 offset:25088\n"
"ds_read_b64_tr_b16 v[170:171], v249 offset:29184\n"
"v_mfma_f32_16x16x32_bf16 v[28:31], v[132:135], a[44:47], v[28:31]\n"
"ds_read_b64_tr_b16 v[172:173], v250 offset:25088\n"
"ds_read_b64_tr_b16 v[174:175], v250 offset:29184\n"
"v_mfma_f32_16x16x32_bf16 v[40:43], v[136:139], a[40:43], v[40:43]\n"
"ds_read_b64_tr_b16 v[176:177], v249 offset:25600\n"
"ds_read_b64_tr_b16 v[178:179], v249 offset:29696\n"
"v_mfma_f32_16x16x32_bf16 v[44:47], v[136:139], a[44:47], v[44:47]\n"
"ds_read_b64_tr_b16 v[180:181], v250 offset:25600\n"
"ds_read_b64_tr_b16 v[182:183], v250 offset:29696\n"
"v_mfma_f32_16x16x32_bf16 v[56:59], v[140:143], a[40:43], v[56:59]\n"
"ds_read_b64_tr_b16 v[184:185], v249 offset:26112\n"
"ds_read_b64_tr_b16 v[186:187], v249 offset:30208\n"
"v_mfma_f32_16x16x32_bf16 v[60:63], v[140:143], a[44:47], v[60:63]\n"
"ds_read_b64_tr_b16 v[188:189], v250 offset:26112\n"
"ds_read_b64_tr_b16 v[190:191], v250 offset:30208\n"
"v_mfma_f32_16x16x32_bf16 v[72:75], v[144:147], a[40:43], v[72:75]\n"
"v_mfma_f32_16x16x32_bf16 v[76:79], v[144:147], a[44:47], v[76:79]\n"
"v_mfma_f32_16x16x32_bf16 v[88:91], v[148:151], a[40:43], v[88:91]\n"
"v_mfma_f32_16x16x32_bf16 v[92:95], v[148:151], a[44:47], v[92:95]\n"
"v_mfma_f32_16x16x32_bf16 v[104:107], v[152:155], a[40:43], v[104:107]\n"
"v_mfma_f32_16x16x32_bf16 v[108:111], v[152:155], a[44:47], v[108:111]\n"
"v_mfma_f32_16x16x32_bf16 v[120:123], v[156:159], a[40:43], v[120:123]\n"
"v_mfma_f32_16x16x32_bf16 v[124:127], v[156:159], a[44:47], v[124:127]\n"
"s_waitcnt vmcnt(32) lgkmcnt(0)\n"
"v_mfma_f32_16x16x32_bf16 v[8:11], v[160:163], a[56:59], v[8:11]\n"
"ds_read_b64_tr_b16 v[128:129], v249 offset:32768\n"
"ds_read_b64_tr_b16 v[130:131], v249 offset:36864\n"
"v_mfma_f32_16x16x32_bf16 v[12:15], v[160:163], a[60:63], v[12:15]\n"
"ds_read_b64_tr_b16 v[132:133], v250 offset:32768\n"
"ds_read_b64_tr_b16 v[134:135], v250 offset:36864\n"
"v_mfma_f32_16x16x32_bf16 v[24:27], v[164:167], a[56:59], v[24:27]\n"
"ds_read_b64_tr_b16 v[136:137], v249 offset:33280\n"
"ds_read_b64_tr_b16 v[138:139], v249 offset:37376\n"
"v_mfma_f32_16x16x32_bf16 v[28:31], v[164:167], a[60:63], v[28:31]\n"
"ds_read_b64_tr_b16 v[140:141], v250 offset:33280\n"
"ds_read_b64_tr_b16 v[142:143], v250 offset:37376\n"
"v_mfma_f32_16x16x32_bf16 v[40:43], v[168:171], a[56:59], v[40:43]\n"
"ds_read_b64_tr_b16 v[144:145], v249 offset:33792\n"
"ds_read_b64_tr_b16 v[146:147], v249 offset:37888\n"
"v_mfma_f32_16x16x32_bf16 v[44:47], v[168:171], a[60:63], v[44:47]\n"
"ds_read_b64_tr_b16 v[148:149], v250 offset:33792\n"
"ds_read_b64_tr_b16 v[150:151], v250 offset:37888\n"
"v_mfma_f32_16x16x32_bf16 v[56:59], v[172:175], a[56:59], v[56:59]\n"
"ds_read_b64_tr_b16 v[152:153], v249 offset:34304\n"
"ds_read_b64_tr_b16 v[154:155], v249 offset:38400\n"
"v_mfma_f32_16x16x32_bf16 v[60:63], v[172:175], a[60:63], v[60:63]\n"
"ds_read_b64_tr_b16 v[156:157], v250 offset:34304\n"
"ds_read_b64_tr_b16 v[158:159], v250 offset:38400\n"
"v_mfma_f32_16x16x32_bf16 v[72:75], v[176:179], a[56:59], v[72:75]\n"
"v_mfma_f32_16x16x32_bf16 v[76:79], v[176:179], a[60:63], v[76:79]\n"
"v_mfma_f32_16x16x32_bf16 v[88:91], v[180:183], a[56:59], v[88:91]\n"
"v_mfma_f32_16x16x32_bf16 v[92:95], v[180:183], a[60:63], v[92:95]\n"
"v_mfma_f32_16x16x32_bf16 v[104:107], v[184:187], a[56:59], v[104:107]\n"
"v_mfma_f32_16x16x32_bf16 v[108:111], v[184:187], a[60:63], v[108:111]\n"
"v_mfma_f32_16x16x32_bf16 v[120:123], v[188:191], a[56:59], v[120:123]\n"
"v_mfma_f32_16x16x32_bf16 v[124:127], v[188:191], a[60:63], v[124:127]\n"
"s_waitcnt vmcnt(30) lgkmcnt(0)\n"
"v_mfma_f32_16x16x32_bf16 v[8:11], v[128:131], a[72:75], v[8:11]\n"
"ds_read_b64_tr_b16 v[160:161], v249 offset:40960\n"
"ds_read_b64_tr_b16 v[162:163], v249 offset:45056\n"
"v_mfma_f32_16x16x32_bf16 v[12:15], v[128:131], a[76:79], v[12:15]\n"
"ds_read_b64_tr_b16 v[164:165], v250 offset:40960\n"
"ds_read_b64_tr_b16 v[166:167], v250 offset:45056\n"
"v_mfma_f32_16x16x32_bf16 v[24:27], v[132:135], a[72:75], v[24:27]\n"
"ds_read_b64_tr_b16 v[168:169], v249 offset:41472\n"
"ds_read_b64_tr_b16 v[170:171], v249 offset:45568\n"
"v_mfma_f32_16x16x32_bf16 v[28:31], v[132:135], a[76:79], v[28:31]\n"
"ds_read_b64_tr_b16 v[172:173], v250 offset:41472\n"
"ds_read_b64_tr_b16 v[174:175], v250 offset:45568\n"
"v_mfma_f32_16x16x32_bf16 v[40:43], v[136:139], a[72:75], v[40:43]\n"
"ds_read_b64_tr_b16 v[176:177], v249 offset:41984\n"
"ds_read_b64_tr_b16 v[178:179], v249 offset:46080\n"
"v_mfma_f32_16x16x32_bf16 v[44:47], v[136:139], a[76:79], v[44:47]\n"
"ds_read_b64_tr_b16 v[180:181], v250 offset:41984\n"
"ds_read_b64_tr_b16 v[182:183], v250 offset:46080\n"
"v_mfma_f32_16x16x32_bf16 v[56:59], v[140:143], a[72:75], v[56:59]\n"
"ds_read_b64_tr_b16 v[184:185], v249 offset:42496\n"
"ds_read_b64_tr_b16 v[186:187], v249 offset:46592\n"
"v_mfma_f32_16x16x32_bf16 v[60:63], v[140:143], a[76:79], v[60:63]\n"
"ds_read_b64_tr_b16 v[188:189], v250 offset:42496\n"
"ds_read_b64_tr_b16 v[190:191], v250 offset:46592\n"
"v_mfma_f32_16x16x32_bf16 v[72:75], v[144:147], a[72:75], v[72:75]\n"
"v_mfma_f32_16x16x32_bf16 v[76:79], v[144:147], a[76:79], v[76:79]\n"
"v_mfma_f32_16x16x32_bf16 v[88:91], v[148:151], a[72:75], v[88:91]\n"
"v_mfma_f32_16x16x32_bf16 v[92:95], v[148:151], a[76:79], v[92:95]\n"
"v_mfma_f32_16x16x32_bf16 v[104:107], v[152:155], a[72:75], v[104:107]\n"
"v_mfma_f32_16x16x32_bf16 v[108:111], v[152:155], a[76:79], v[108:111]\n"
"v_mfma_f32_16x16x32_bf16 v[120:123], v[156:159], a[72:75], v[120:123]\n"
"v_mfma_f32_16x16x32_bf16 v[124:127], v[156:159], a[76:79], v[124:127]\n"
"s_waitcnt vmcnt(28) lgkmcnt(0)\n"
"v_mfma_f32_16x16x32_bf16 v[8:11], v[160:163], a[88:91], v[8:11]\n"
"ds_read_b64_tr_b16 v[128:129], v249 offset:49152\n"
"ds_read_b64_tr_b16 v[130:131], v249 offset:53248\n"
"v_mfma_f32_16x16x32_bf16 v[12:15], v[160:163], a[92:95], v[12:15]\n"
"ds_read_b64_tr_b16 v[132:133], v250 offset:49152\n"
"ds_read_b64_tr_b16 v[134:135], v250 offset:53248\n"
"v_mfma_f32_16x16x32_bf16 v[24:27], v[164:167], a[88:91], v[24:27]\n"
"ds_read_b64_tr_b16 v[136:137], v249 offset:49664\n"
"ds_read_b64_tr_b16 v[138:139], v249 offset:53760\n"
"v_mfma_f32_16x16x32_bf16 v[28:31], v[164:167], a[92:95], v[28:31]\n"
"ds_read_b64_tr_b16 v[140:141], v250 offset:49664\n"
"ds_read_b64_tr_b16 v[142:143], v250 offset:53760\n"
"v_mfma_f32_16x16x32_bf16 v[40:43], v[168:171], a[88:91], v[40:43]\n"
"ds_read_b64_tr_b16 v[144:145], v249 offset:50176\n"
"ds_read_b64_tr_b16 v[146:147], v249 offset:54272\n"
"v_mfma_f32_16x16x32_bf16 v[44:47], v[168:171], a[92:95], v[44:47]\n"
"ds_read_b64_tr_b16 v[148:149], v250 offset:50176\n"
"ds_read_b64_tr_b16 v[150:151], v250 offset:54272\n"
"v_mfma_f32_16x16x32_bf16 v[56:59], v[172:175], a[88:91], v[56:59]\n"
"ds_read_b64_tr_b16 v[152:153], v249 offset:50688\n"
"ds_read_b64_tr_b16 v[154:155], v249 offset:54784\n"
"v_mfma_f32_16x16x32_bf16 v[60:63], v[172:175], a[92:95], v[60:63]\n"
"ds_read_b64_tr_b16 v[156:157], v250 offset:50688\n"
"ds_read_b64_tr_b16 v[158:159], v250 offset:54784\n"
"v_mfma_f32_16x16x32_bf16 v[72:75], v[176:179], a[88:91], v[72:75]\n"
"v_mfma_f32_16x16x32_bf16 v[76:79], v[176:179], a[92:95], v[76:79]\n"
"v_mfma_f32_16x16x32_bf16 v[88:91], v[180:183], a[88:91], v[88:91]\n"
"v_mfma_f32_16x16x32_bf16 v[92:95], v[180:183], a[92:95], v[92:95]\n"
"v_mfma_f32_16x16x32_bf16 v[104:107], v[184:187], a[88:91], v[104:107]\n"
"v_mfma_f32_16x16x32_bf16 v[108:111], v[184:187], a[92:95], v[108:111]\n"
"v_mfma_f32_16x16x32_bf16 v[120:123], v[188:191], a[88:91], v[120:123]\n"
"v_mfma_f32_16x16x32_bf16 v[124:127], v[188:191], a[92:95], v[124:127]\n"
"s_waitcnt vmcnt(26) lgkmcnt(0)\n"
"v_mfma_f32_16x16x32_bf16 v[8:11], v[128:131], a[104:107], v[8:11]\n"
"ds_read_b64_tr_b16 v[160:161], v249 offset:57344\n"
"ds_read_b64_tr_b16 v[162:163], v249 offset:61440\n"
"v_mfma_f32_16x16x32_bf16 v[12:15], v[128:131], a[108:111], v[12:15]\n"
"ds_read_b64_tr_b16 v[164:165], v250 offset:57344\n"
"ds_read_b64_tr_b16 v[166:167], v250 offset:61440\n"
"v_mfma_f32_16x16x32_bf16 v[24:27], v[132:135], a[104:107], v[24:27]\n"
"ds_read_b64_tr_b16 v[168:169], v249 offset:57856\n"
"ds_read_b64_tr_b16 v[170:171], v249 offset:61952\n"
"v_mfma_f32_16x16x32_bf16 v[28:31], v[132:135], a[108:111], v[28:31]\n"
"ds_read_b64_tr_b16 v[172:173], v250 offset:57856\n"
"ds_read_b64_tr_b16 v[174:175], v250 offset:61952\n"
"v_mfma_f32_16x16x32_bf16 v[40:43], v[136:139], a[104:107], v[40:43]\n"
"ds_read_b64_tr_b16 v[176:177], v249 offset:58368\n"
"ds_read_b64_tr_b16 v[178:179], v249 offset:62464\n"
"v_mfma_f32_16x16x32_bf16 v[44:47], v[136:139], a[108:111], v[44:47]\n"
"ds_read_b64_tr_b16 v[180:181], v250 offset:58368\n"
"ds_read_b64_tr_b16 v[182:183], v250 offset:62464\n"
"v_mfma_f32_16x16x32_bf16 v[56:59], v[140:143], a[104:107], v[56:59]\n"
"ds_read_b64_tr_b16 v[184:185], v249 offset:58880\n"
"ds_read_b64_tr_b16 v[186:187], v249 offset:62976\n"
"v_mfma_f32_16x16x32_bf16 v[60:63], v[140:143], a[108:111], v[60:63]\n"
"ds_read_b64_tr_b16 v[188:189], v250 offset:58880\n"
"ds_read_b64_tr_b16 v[190:191], v250 offset:62976\n"
"v_mfma_f32_16x16x32_bf16 v[72:75], v[144:147], a[104:107], v[72:75]\n"
"v_mfma_f32_16x16x32_bf16 v[76:79], v[144:147], a[108:111], v[76:79]\n"
"v_mfma_f32_16x16x32_bf16 v[88:91], v[148:151], a[104:107], v[88:91]\n"
"v_mfma_f32_16x16x32_bf16 v[92:95], v[148:151], a[108:111], v[92:95]\n"
"v_mfma_f32_16x16x32_bf16 v[104:107], v[152:155], a[104:107], v[104:107]\n"
"v_mfma_f32_16x16x32_bf16 v[108:111], v[152:155], a[108:111], v[108:111]\n"
"v_mfma_f32_16x16x32_bf16 v[120:123], v[156:159], a[104:107], v[120:123]\n"
"v_mfma_f32_16x16x32_bf16 v[124:127], v[156:159], a[108:111], v[124:127]\n"
"s_waitcnt vmcnt(24) lgkmcnt(0)\n"
"v_mfma_f32_16x16x32_bf16 v[8:11], v[160:163], a[120:123], v[8:11]\n"
"ds_read_b64_tr_b16 v[128:129], v251 offset:0\n"
"ds_read_b64_tr_b16 v[130:131], v251 offset:4096\n"
"v_mfma_f32_16x16x32_bf16 v[12:15], v[160:163], a[124:127], v[12:15]\n"
"ds_read_b64_tr_b16 v[132:133], v252 offset:0\n"
"ds_read_b64_tr_b16 v[134:135], v252 offset:4096\n"
"v_mfma_f32_16x16x32_bf16 v[24:27], v[164:167], a[120:123], v[24:27]\n"
"ds_read_b64_tr_b16 v[136:137], v251 offset:512\n"
"ds_read_b64_tr_b16 v[138:139], v251 offset:4608\n"
"v_mfma_f32_16x16x32_bf16 v[28:31], v[164:167], a[124:127], v[28:31]\n"
"ds_read_b64_tr_b16 v[140:141], v252 offset:512\n"
"ds_read_b64_tr_b16 v[142:143], v252 offset:4608\n"
"v_mfma_f32_16x16x32_bf16 v[40:43], v[168:171], a[120:123], v[40:43]\n"
"ds_read_b64_tr_b16 v[144:145], v251 offset:1024\n"
"ds_read_b64_tr_b16 v[146:147], v251 offset:5120\n"
"v_mfma_f32_16x16x32_bf16 v[44:47], v[168:171], a[124:127], v[44:47]\n"
"ds_read_b64_tr_b16 v[148:149], v252 offset:1024\n"
"ds_read_b64_tr_b16 v[150:151], v252 offset:5120\n"
"v_mfma_f32_16x16x32_bf16 v[56:59], v[172:175], a[120:123], v[56:59]\n"
"ds_read_b64_tr_b16 v[152:153], v251 offset:1536\n"
"ds_read_b64_tr_b16 v[154:155], v251 offset:5632\n"
"v_mfma_f32_16x16x32_bf16 v[60:63], v[172:175], a[124:127], v[60:63]\n"
"ds_read_b64_tr_b16 v[156:157], v252 offset:1536\n"
"ds_read_b64_tr_b16 v[158:159], v252 offset:5632\n"
"v_mfma_f32_16x16x32_bf16 v[72:75], v[176:179], a[120:123], v[72:75]\n"
"v_mfma_f32_16x16x32_bf16 v[76:79], v[176:179], a[124:127], v[76:79]\n"
"v_mfma_f32_16x16x32_bf16 v[88:91], v[180:183], a[120:123], v[88:91]\n"
"v_mfma_f32_16x16x32_bf16 v[92:95], v[180:183], a[124:127], v[92:95]\n"
"v_mfma_f32_16x16x32_bf16 v[104:107], v[184:187], a[120:123], v[104:107]\n"
"v_mfma_f32_16x16x32_bf16 v[108:111], v[184:187], a[124:127], v[108:111]\n"
"v_mfma_f32_16x16x32_bf16 v[120:123], v[188:191], a[120:123], v[120:123]\n"
"v_mfma_f32_16x16x32_bf16 v[124:127], v[188:191], a[124:127], v[124:127]\n"
"s_waitcnt vmcnt(22) lgkmcnt(0)\n"
"v_mfma_f32_16x16x32_bf16 v[8:11], v[128:131], a[136:139], v[8:11]\n"
"ds_read_b64_tr_b16 v[160:161], v251 offset:8192\n"
"ds_read_b64_tr_b16 v[162:163], v251 offset:12288\n"
"v_mfma_f32_16x16x32_bf16 v[12:15], v[128:131], a[140:143], v[12:15]\n"
"ds_read_b64_tr_b16 v[164:165], v252 offset:8192\n"
"ds_read_b64_tr_b16 v[166:167], v252 offset:12288\n"
"v_mfma_f32_16x16x32_bf16 v[24:27], v[132:135], a[136:139], v[24:27]\n"
"ds_read_b64_tr_b16 v[168:169], v251 offset:8704\n"
"ds_read_b64_tr_b16 v[170:171], v251 offset:12800\n"
"v_mfma_f32_16x16x32_bf16 v[28:31], v[132:135], a[140:143], v[28:31]\n"
"ds_read_b64_tr_b16 v[172:173], v252 offset:8704\n"
"ds_read_b64_tr_b16 v[174:175], v252 offset:12800\n"
"v_mfma_f32_16x16x32_bf16 v[40:43], v[136:139], a[136:139], v[40:43]\n"
"ds_read_b64_tr_b16 v[176:177], v251 offset:9216\n"
"ds_read_b64_tr_b16 v[178:179], v251 offset:13312\n"
"v_mfma_f32_16x16x32_bf16 v[44:47], v[136:139], a[140:143], v[44:47]\n"
"ds_read_b64_tr_b16 v[180:181], v252 offset:9216\n"
"ds_read_b64_tr_b16 v[182:183], v252 offset:13312\n"
"v_mfma_f32_16x16x32_bf16 v[56:59], v[140:143], a[136:139], v[56:59]\n"
"ds_read_b64_tr_b16 v[184:185], v251 offset:9728\n"
"ds_read_b64_tr_b16 v[186:187], v251 offset:13824\n"
"v_mfma_f32_16x16x32_bf16 v[60:63], v[140:143], a[140:143], v[60:63]\n"
"ds_read_b64_tr_b16 v[188:189], v252 offset:9728\n"
"ds_read_b64_tr_b16 v[190:191], v252 offset:13824\n"
"v_mfma_f32_16x16x32_bf16 v[72:75], v[144:147], a[136:139], v[72:75]\n"
"v_mfma_f32_16x16x32_bf16 v[76:79], v[144:147], a[140:143], v[76:79]\n"
"v_mfma_f32_16x16x32_bf16 v[88:91], v[148:151], a[136:139], v[88:91]\n"
"v_mfma_f32_16x16x32_bf16 v[92:95], v[148:151], a[140:143], v[92:95]\n"
"v_mfma_f32_16x16x32_bf16 v[104:107], v[152:155], a[136:139], v[104:107]\n"
"v_mfma_f32_16x16x32_bf16 v[108:111], v[152:155], a[140:143], v[108:111]\n"
"v_mfma_f32_16x16x32_bf16 v[120:123], v[156:159], a[136:139], v[120:123]\n"
"v_mfma_f32_16x16x32_bf16 v[124:127], v[156:159], a[140:143], v[124:127]\n"
"s_waitcnt vmcnt(20) lgkmcnt(0)\n"
"s_waitcnt vmcnt(0)\n"
"s_barrier\n"
"s_cmp_eq_u32 s56, 0\n"
"s_cbranch_scc0 Lsig_oa\n"
"s_mov_b64 s[64:65], exec\n"
"s_mov_b64 exec, 1\n"
"v_mov_b32 v238, 0\n"
"v_mov_b32 v239, 1\n"
"global_atomic_add v238, v239, s[70:71]\n"
"s_mov_b64 exec, s[64:65]\n"
"Lsig_oa:\n"
"v_mfma_f32_16x16x32_bf16 v[8:11], v[160:163], a[152:155], v[8:11]\n"
"ds_read_b64_tr_b16 v[128:129], v251 offset:16384\n"
"ds_read_b64_tr_b16 v[130:131], v251 offset:20480\n"
"v_mfma_f32_16x16x32_bf16 v[12:15], v[160:163], a[156:159], v[12:15]\n"
"ds_read_b64_tr_b16 v[132:133], v252 offset:16384\n"
"ds_read_b64_tr_b16 v[134:135], v252 offset:20480\n"
"v_mfma_f32_16x16x32_bf16 v[24:27], v[164:167], a[152:155], v[24:27]\n"
"ds_read_b64_tr_b16 v[136:137], v251 offset:16896\n"
"ds_read_b64_tr_b16 v[138:139], v251 offset:20992\n"
"v_mfma_f32_16x16x32_bf16 v[28:31], v[164:167], a[156:159], v[28:31]\n"
"ds_read_b64_tr_b16 v[140:141], v252 offset:16896\n"
"ds_read_b64_tr_b16 v[142:143], v252 offset:20992\n"
"v_mfma_f32_16x16x32_bf16 v[40:43], v[168:171], a[152:155], v[40:43]\n"
"ds_read_b64_tr_b16 v[144:145], v251 offset:17408\n"
"ds_read_b64_tr_b16 v[146:147], v251 offset:21504\n"
"v_mfma_f32_16x16x32_bf16 v[44:47], v[168:171], a[156:159], v[44:47]\n"
"ds_read_b64_tr_b16 v[148:149], v252 offset:17408\n"
"ds_read_b64_tr_b16 v[150:151], v252 offset:21504\n"
"v_mfma_f32_16x16x32_bf16 v[56:59], v[172:175], a[152:155], v[56:59]\n"
"ds_read_b64_tr_b16 v[152:153], v251 offset:17920\n"
"ds_read_b64_tr_b16 v[154:155], v251 offset:22016\n"
"v_mfma_f32_16x16x32_bf16 v[60:63], v[172:175], a[156:159], v[60:63]\n"
"ds_read_b64_tr_b16 v[156:157], v252 offset:17920\n"
"ds_read_b64_tr_b16 v[158:159], v252 offset:22016\n"
"v_mfma_f32_16x16x32_bf16 v[72:75], v[176:179], a[152:155], v[72:75]\n"
"v_mfma_f32_16x16x32_bf16 v[76:79], v[176:179], a[156:159], v[76:79]\n"
"v_mfma_f32_16x16x32_bf16 v[88:91], v[180:183], a[152:155], v[88:91]\n"
"v_mfma_f32_16x16x32_bf16 v[92:95], v[180:183], a[156:159], v[92:95]\n"
"v_mfma_f32_16x16x32_bf16 v[104:107], v[184:187], a[152:155], v[104:107]\n"
"v_mfma_f32_16x16x32_bf16 v[108:111], v[184:187], a[156:159], v[108:111]\n"
"v_mfma_f32_16x16x32_bf16 v[120:123], v[188:191], a[152:155], v[120:123]\n"
"v_mfma_f32_16x16x32_bf16 v[124:127], v[188:191], a[156:159], v[124:127]\n"
"s_waitcnt vmcnt(18) lgkmcnt(0)\n"
"v_mfma_f32_16x16x32_bf16 v[8:11], v[128:131], a[168:171], v[8:11]\n"
"ds_read_b64_tr_b16 v[160:161], v251 offset:24576\n"
"ds_read_b64_tr_b16 v[162:163], v251 offset:28672\n"
"v_mfma_f32_16x16x32_bf16 v[12:15], v[128:131], a[172:175], v[12:15]\n"
"ds_read_b64_tr_b16 v[164:165], v252 offset:24576\n"
"ds_read_b64_tr_b16 v[166:167], v252 offset:28672\n"
"v_mfma_f32_16x16x32_bf16 v[24:27], v[132:135], a[168:171], v[24:27]\n"
"ds_read_b64_tr_b16 v[168:169], v251 offset:25088\n"
"ds_read_b64_tr_b16 v[170:171], v251 offset:29184\n"
"v_mfma_f32_16x16x32_bf16 v[28:31], v[132:135], a[172:175], v[28:31]\n"
"ds_read_b64_tr_b16 v[172:173], v252 offset:25088\n"
"ds_read_b64_tr_b16 v[174:175], v252 offset:29184\n"
"v_mfma_f32_16x16x32_bf16 v[40:43], v[136:139], a[168:171], v[40:43]\n"
"ds_read_b64_tr_b16 v[176:177], v251 offset:25600\n"
"ds_read_b64_tr_b16 v[178:179], v251 offset:29696\n"
"v_mfma_f32_16x16x32_bf16 v[44:47], v[136:139], a[172:175], v[44:47]\n"
"ds_read_b64_tr_b16 v[180:181], v252 offset:25600\n"
"ds_read_b64_tr_b16 v[182:183], v252 offset:29696\n"
"v_mfma_f32_16x16x32_bf16 v[56:59], v[140:143], a[168:171], v[56:59]\n"
"ds_read_b64_tr_b16 v[184:185], v251 offset:26112\n"
"ds_read_b64_tr_b16 v[186:187], v251 offset:30208\n"
"v_mfma_f32_16x16x32_bf16 v[60:63], v[140:143], a[172:175], v[60:63]\n"
"ds_read_b64_tr_b16 v[188:189], v252 offset:26112\n"
"ds_read_b64_tr_b16 v[190:191], v252 offset:30208\n"
"v_mfma_f32_16x16x32_bf16 v[72:75], v[144:147], a[168:171], v[72:75]\n"
"v_mfma_f32_16x16x32_bf16 v[76:79], v[144:147], a[172:175], v[76:79]\n"
"v_mfma_f32_16x16x32_bf16 v[88:91], v[148:151], a[168:171], v[88:91]\n"
"v_mfma_f32_16x16x32_bf16 v[92:95], v[148:151], a[172:175], v[92:95]\n"
"v_mfma_f32_16x16x32_bf16 v[104:107], v[152:155], a[168:171], v[104:107]\n"
"v_mfma_f32_16x16x32_bf16 v[108:111], v[152:155], a[172:175], v[108:111]\n"
"v_mfma_f32_16x16x32_bf16 v[120:123], v[156:159], a[168:171], v[120:123]\n"
"v_mfma_f32_16x16x32_bf16 v[124:127], v[156:159], a[172:175], v[124:127]\n"
"s_waitcnt vmcnt(16) lgkmcnt(0)\n"
"v_mfma_f32_16x16x32_bf16 v[8:11], v[160:163], a[184:187], v[8:11]\n"
"ds_read_b64_tr_b16 v[128:129], v251 offset:32768\n"
"ds_read_b64_tr_b16 v[130:131], v251 offset:36864\n"
"v_mfma_f32_16x16x32_bf16 v[12:15], v[160:163], a[188:191], v[12:15]\n"
"ds_read_b64_tr_b16 v[132:133], v252 offset:32768\n"
"ds_read_b64_tr_b16 v[134:135], v252 offset:36864\n"
"v_mfma_f32_16x16x32_bf16 v[24:27], v[164:167], a[184:187], v[24:27]\n"
"ds_read_b64_tr_b16 v[136:137], v251 offset:33280\n"
"ds_read_b64_tr_b16 v[138:139], v251 offset:37376\n"
"v_mfma_f32_16x16x32_bf16 v[28:31], v[164:167], a[188:191], v[28:31]\n"
"ds_read_b64_tr_b16 v[140:141], v252 offset:33280\n"
"ds_read_b64_tr_b16 v[142:143], v252 offset:37376\n"
"v_mfma_f32_16x16x32_bf16 v[40:43], v[168:171], a[184:187], v[40:43]\n"
"ds_read_b64_tr_b16 v[144:145], v251 offset:33792\n"
"ds_read_b64_tr_b16 v[146:147], v251 offset:37888\n"
"v_mfma_f32_16x16x32_bf16 v[44:47], v[168:171], a[188:191], v[44:47]\n"
"ds_read_b64_tr_b16 v[148:149], v252 offset:33792\n"
"ds_read_b64_tr_b16 v[150:151], v252 offset:37888\n"
"v_mfma_f32_16x16x32_bf16 v[56:59], v[172:175], a[184:187], v[56:59]\n"
"ds_read_b64_tr_b16 v[152:153], v251 offset:34304\n"
"ds_read_b64_tr_b16 v[154:155], v251 offset:38400\n"
"v_mfma_f32_16x16x32_bf16 v[60:63], v[172:175], a[188:191], v[60:63]\n"
"ds_read_b64_tr_b16 v[156:157], v252 offset:34304\n"
"ds_read_b64_tr_b16 v[158:159], v252 offset:38400\n"
"v_mfma_f32_16x16x32_bf16 v[72:75], v[176:179], a[184:187], v[72:75]\n"
"v_mfma_f32_16x16x32_bf16 v[76:79], v[176:179], a[188:191], v[76:79]\n"
"v_mfma_f32_16x16x32_bf16 v[88:91], v[180:183], a[184:187], v[88:91]\n"
"v_mfma_f32_16x16x32_bf16 v[92:95], v[180:183], a[188:191], v[92:95]\n"
"v_mfma_f32_16x16x32_bf16 v[104:107], v[184:187], a[184:187], v[104:107]\n"
"v_mfma_f32_16x16x32_bf16 v[108:111], v[184:187], a[188:191], v[108:111]\n"
"v_mfma_f32_16x16x32_bf16 v[120:123], v[188:191], a[184:187], v[120:123]\n"
"v_mfma_f32_16x16x32_bf16 v[124:127], v[188:191], a[188:191], v[124:127]\n"
"s_waitcnt vmcnt(14) lgkmcnt(0)\n"
"v_mfma_f32_16x16x32_bf16 v[8:11], v[128:131], a[200:203], v[8:11]\n"
"ds_read_b64_tr_b16 v[160:161], v251 offset:40960\n"
"ds_read_b64_tr_b16 v[162:163], v251 offset:45056\n"
"v_mfma_f32_16x16x32_bf16 v[12:15], v[128:131], a[204:207], v[12:15]\n"
"ds_read_b64_tr_b16 v[164:165], v252 offset:40960\n"
"ds_read_b64_tr_b16 v[166:167], v252 offset:45056\n"
"v_mfma_f32_16x16x32_bf16 v[24:27], v[132:135], a[200:203], v[24:27]\n"
"ds_read_b64_tr_b16 v[168:169], v251 offset:41472\n"
"ds_read_b64_tr_b16 v[170:171], v251 offset:45568\n"
"v_mfma_f32_16x16x32_bf16 v[28:31], v[132:135], a[204:207], v[28:31]\n"
"ds_read_b64_tr_b16 v[172:173], v252 offset:41472\n"
"ds_read_b64_tr_b16 v[174:175], v252 offset:45568\n"
"v_mfma_f32_16x16x32_bf16 v[40:43], v[136:139], a[200:203], v[40:43]\n"
"ds_read_b64_tr_b16 v[176:177], v251 offset:41984\n"
"ds_read_b64_tr_b16 v[178:179], v251 offset:46080\n"
"v_mfma_f32_16x16x32_bf16 v[44:47], v[136:139], a[204:207], v[44:47]\n"
"ds_read_b64_tr_b16 v[180:181], v252 offset:41984\n"
"ds_read_b64_tr_b16 v[182:183], v252 offset:46080\n"
"v_mfma_f32_16x16x32_bf16 v[56:59], v[140:143], a[200:203], v[56:59]\n"
"ds_read_b64_tr_b16 v[184:185], v251 offset:42496\n"
"ds_read_b64_tr_b16 v[186:187], v251 offset:46592\n"
"v_mfma_f32_16x16x32_bf16 v[60:63], v[140:143], a[204:207], v[60:63]\n"
"ds_read_b64_tr_b16 v[188:189], v252 offset:42496\n"
"ds_read_b64_tr_b16 v[190:191], v252 offset:46592\n"
"v_mfma_f32_16x16x32_bf16 v[72:75], v[144:147], a[200:203], v[72:75]\n"
"v_mfma_f32_16x16x32_bf16 v[76:79], v[144:147], a[204:207], v[76:79]\n"
"v_mfma_f32_16x16x32_bf16 v[88:91], v[148:151], a[200:203], v[88:91]\n"
"v_mfma_f32_16x16x32_bf16 v[92:95], v[148:151], a[204:207], v[92:95]\n"
"v_mfma_f32_16x16x32_bf16 v[104:107], v[152:155], a[200:203], v[104:107]\n"
"v_mfma_f32_16x16x32_bf16 v[108:111], v[152:155], a[204:207], v[108:111]\n"
"v_mfma_f32_16x16x32_bf16 v[120:123], v[156:159], a[200:203], v[120:123]\n"
"v_mfma_f32_16x16x32_bf16 v[124:127], v[156:159], a[204:207], v[124:127]\n"
"s_waitcnt vmcnt(12) lgkmcnt(0)\n"
"v_mfma_f32_16x16x32_bf16 v[8:11], v[160:163], a[216:219], v[8:11]\n"
"ds_read_b64_tr_b16 v[128:129], v251 offset:49152\n"
"ds_read_b64_tr_b16 v[130:131], v251 offset:53248\n"
"v_mfma_f32_16x16x32_bf16 v[12:15], v[160:163], a[220:223], v[12:15]\n"
"ds_read_b64_tr_b16 v[132:133], v252 offset:49152\n"
"ds_read_b64_tr_b16 v[134:135], v252 offset:53248\n"
"v_mfma_f32_16x16x32_bf16 v[24:27], v[164:167], a[216:219], v[24:27]\n"
"ds_read_b64_tr_b16 v[136:137], v251 offset:49664\n"
"ds_read_b64_tr_b16 v[138:139], v251 offset:53760\n"
"v_mfma_f32_16x16x32_bf16 v[28:31], v[164:167], a[220:223], v[28:31]\n"
"ds_read_b64_tr_b16 v[140:141], v252 offset:49664\n"
"ds_read_b64_tr_b16 v[142:143], v252 offset:53760\n"
"v_mfma_f32_16x16x32_bf16 v[40:43], v[168:171], a[216:219], v[40:43]\n"
"ds_read_b64_tr_b16 v[144:145], v251 offset:50176\n"
"ds_read_b64_tr_b16 v[146:147], v251 offset:54272\n"
"v_mfma_f32_16x16x32_bf16 v[44:47], v[168:171], a[220:223], v[44:47]\n"
"ds_read_b64_tr_b16 v[148:149], v252 offset:50176\n"
"ds_read_b64_tr_b16 v[150:151], v252 offset:54272\n"
"v_mfma_f32_16x16x32_bf16 v[56:59], v[172:175], a[216:219], v[56:59]\n"
"ds_read_b64_tr_b16 v[152:153], v251 offset:50688\n"
"ds_read_b64_tr_b16 v[154:155], v251 offset:54784\n"
"v_mfma_f32_16x16x32_bf16 v[60:63], v[172:175], a[220:223], v[60:63]\n"
"ds_read_b64_tr_b16 v[156:157], v252 offset:50688\n"
"ds_read_b64_tr_b16 v[158:159], v252 offset:54784\n"
"v_mfma_f32_16x16x32_bf16 v[72:75], v[176:179], a[216:219], v[72:75]\n"
"v_mfma_f32_16x16x32_bf16 v[76:79], v[176:179], a[220:223], v[76:79]\n"
"v_mfma_f32_16x16x32_bf16 v[88:91], v[180:183], a[216:219], v[88:91]\n"
"v_mfma_f32_16x16x32_bf16 v[92:95], v[180:183], a[220:223], v[92:95]\n"
"v_mfma_f32_16x16x32_bf16 v[104:107], v[184:187], a[216:219], v[104:107]\n"
"v_mfma_f32_16x16x32_bf16 v[108:111], v[184:187], a[220:223], v[108:111]\n"
"v_mfma_f32_16x16x32_bf16 v[120:123], v[188:191], a[216:219], v[120:123]\n"
"v_mfma_f32_16x16x32_bf16 v[124:127], v[188:191], a[220:223], v[124:127]\n"
"s_waitcnt vmcnt(10) lgkmcnt(0)\n"
"v_mfma_f32_16x16x32_bf16 v[8:11], v[128:131], a[232:235], v[8:11]\n"
"ds_read_b64_tr_b16 v[160:161], v251 offset:57344\n"
"ds_read_b64_tr_b16 v[162:163], v251 offset:61440\n"
"v_mfma_f32_16x16x32_bf16 v[12:15], v[128:131], a[236:239], v[12:15]\n"
"ds_read_b64_tr_b16 v[164:165], v252 offset:57344\n"
"ds_read_b64_tr_b16 v[166:167], v252 offset:61440\n"
"v_mfma_f32_16x16x32_bf16 v[24:27], v[132:135], a[232:235], v[24:27]\n"
"ds_read_b64_tr_b16 v[168:169], v251 offset:57856\n"
"ds_read_b64_tr_b16 v[170:171], v251 offset:61952\n"
"v_mfma_f32_16x16x32_bf16 v[28:31], v[132:135], a[236:239], v[28:31]\n"
"ds_read_b64_tr_b16 v[172:173], v252 offset:57856\n"
"ds_read_b64_tr_b16 v[174:175], v252 offset:61952\n"
"v_mfma_f32_16x16x32_bf16 v[40:43], v[136:139], a[232:235], v[40:43]\n"
"ds_read_b64_tr_b16 v[176:177], v251 offset:58368\n"
"ds_read_b64_tr_b16 v[178:179], v251 offset:62464\n"
"v_mfma_f32_16x16x32_bf16 v[44:47], v[136:139], a[236:239], v[44:47]\n"
"ds_read_b64_tr_b16 v[180:181], v252 offset:58368\n"
"ds_read_b64_tr_b16 v[182:183], v252 offset:62464\n"
"v_mfma_f32_16x16x32_bf16 v[56:59], v[140:143], a[232:235], v[56:59]\n"
"ds_read_b64_tr_b16 v[184:185], v251 offset:58880\n"
"ds_read_b64_tr_b16 v[186:187], v251 offset:62976\n"
"v_mfma_f32_16x16x32_bf16 v[60:63], v[140:143], a[236:239], v[60:63]\n"
"ds_read_b64_tr_b16 v[188:189], v252 offset:58880\n"
"ds_read_b64_tr_b16 v[190:191], v252 offset:62976\n"
"v_mfma_f32_16x16x32_bf16 v[72:75], v[144:147], a[232:235], v[72:75]\n"
"v_mfma_f32_16x16x32_bf16 v[76:79], v[144:147], a[236:239], v[76:79]\n"
"v_mfma_f32_16x16x32_bf16 v[88:91], v[148:151], a[232:235], v[88:91]\n"
"v_mfma_f32_16x16x32_bf16 v[92:95], v[148:151], a[236:239], v[92:95]\n"
"v_mfma_f32_16x16x32_bf16 v[104:107], v[152:155], a[232:235], v[104:107]\n"
"v_mfma_f32_16x16x32_bf16 v[108:111], v[152:155], a[236:239], v[108:111]\n"
"v_mfma_f32_16x16x32_bf16 v[120:123], v[156:159], a[232:235], v[120:123]\n"
"v_mfma_f32_16x16x32_bf16 v[124:127], v[156:159], a[236:239], v[124:127]\n"
"s_waitcnt vmcnt(8) lgkmcnt(0)\n"
"v_mfma_f32_16x16x32_bf16 v[8:11], v[160:163], a[248:251], v[8:11]\n"
"v_mfma_f32_16x16x32_bf16 v[12:15], v[160:163], a[252:255], v[12:15]\n"
"v_mfma_f32_16x16x32_bf16 v[24:27], v[164:167], a[248:251], v[24:27]\n"
"v_mfma_f32_16x16x32_bf16 v[28:31], v[164:167], a[252:255], v[28:31]\n"
"v_mfma_f32_16x16x32_bf16 v[40:43], v[168:171], a[248:251], v[40:43]\n"
"v_mfma_f32_16x16x32_bf16 v[44:47], v[168:171], a[252:255], v[44:47]\n"
"v_mfma_f32_16x16x32_bf16 v[56:59], v[172:175], a[248:251], v[56:59]\n"
"v_mfma_f32_16x16x32_bf16 v[60:63], v[172:175], a[252:255], v[60:63]\n"
"v_mfma_f32_16x16x32_bf16 v[72:75], v[176:179], a[248:251], v[72:75]\n"
"v_mfma_f32_16x16x32_bf16 v[76:79], v[176:179], a[252:255], v[76:79]\n"
"v_mfma_f32_16x16x32_bf16 v[88:91], v[180:183], a[248:251], v[88:91]\n"
"v_mfma_f32_16x16x32_bf16 v[92:95], v[180:183], a[252:255], v[92:95]\n"
"v_mfma_f32_16x16x32_bf16 v[104:107], v[184:187], a[248:251], v[104:107]\n"
"v_mfma_f32_16x16x32_bf16 v[108:111], v[184:187], a[252:255], v[108:111]\n"
"v_mfma_f32_16x16x32_bf16 v[120:123], v[188:191], a[248:251], v[120:123]\n"
"v_mfma_f32_16x16x32_bf16 v[124:127], v[188:191], a[252:255], v[124:127]\n"
"s_nop 7\n"
"s_mov_b32 s59, 32768\n"
"v_cvt_pk_bf16_f32 v192, v8, v9\n"
"v_cvt_pk_bf16_f32 v193, v10, v11\n"
"v_cvt_pk_bf16_f32 v194, v24, v25\n"
"v_cvt_pk_bf16_f32 v195, v26, v27\n"
"buffer_store_dwordx4 v[192:195], v246, s[48:51], s59 offen offset:0 sc1\n"
"v_cvt_pk_bf16_f32 v196, v40, v41\n"
"v_cvt_pk_bf16_f32 v197, v42, v43\n"
"v_cvt_pk_bf16_f32 v198, v56, v57\n"
"v_cvt_pk_bf16_f32 v199, v58, v59\n"
"buffer_store_dwordx4 v[196:199], v246, s[48:51], s59 offen offset:1024 sc1\n"
"v_cvt_pk_bf16_f32 v200, v72, v73\n"
"v_cvt_pk_bf16_f32 v201, v74, v75\n"
"v_cvt_pk_bf16_f32 v202, v88, v89\n"
"v_cvt_pk_bf16_f32 v203, v90, v91\n"
"buffer_store_dwordx4 v[200:203], v246, s[48:51], s59 offen offset:2048 sc1\n"
"v_cvt_pk_bf16_f32 v204, v104, v105\n"
"v_cvt_pk_bf16_f32 v205, v106, v107\n"
"v_cvt_pk_bf16_f32 v206, v120, v121\n"
"v_cvt_pk_bf16_f32 v207, v122, v123\n"
"buffer_store_dwordx4 v[204:207], v246, s[48:51], s59 offen offset:3072 sc1\n"
"s_mov_b32 s59, 49152\n"
"v_cvt_pk_bf16_f32 v192, v12, v13\n"
"v_cvt_pk_bf16_f32 v193, v14, v15\n"
"v_cvt_pk_bf16_f32 v194, v28, v29\n"
"v_cvt_pk_bf16_f32 v195, v30, v31\n"
"buffer_store_dwordx4 v[192:195], v246, s[48:51], s59 offen offset:0 sc1\n"
"v_cvt_pk_bf16_f32 v196, v44, v45\n"
"v_cvt_pk_bf16_f32 v197, v46, v47\n"
"v_cvt_pk_bf16_f32 v198, v60, v61\n"
"v_cvt_pk_bf16_f32 v199, v62, v63\n"
"buffer_store_dwordx4 v[196:199], v246, s[48:51], s59 offen offset:1024 sc1\n"
"v_cvt_pk_bf16_f32 v200, v76, v77\n"
"v_cvt_pk_bf16_f32 v201, v78, v79\n"
"v_cvt_pk_bf16_f32 v202, v92, v93\n"
"v_cvt_pk_bf16_f32 v203, v94, v95\n"
"buffer_store_dwordx4 v[200:203], v246, s[48:51], s59 offen offset:2048 sc1\n"
"v_cvt_pk_bf16_f32 v204, v108, v109\n"
"v_cvt_pk_bf16_f32 v205, v110, v111\n"
"v_cvt_pk_bf16_f32 v206, v124, v125\n"
"v_cvt_pk_bf16_f32 v207, v126, v127\n"
"buffer_store_dwordx4 v[204:207], v246, s[48:51], s59 offen offset:3072 sc1\n"
"s_waitcnt vmcnt(0)\n"
"s_barrier\n"
"s_cmp_eq_u32 s56, 0\n"
"s_cbranch_scc0 Lsig_ob\n"
"s_mov_b64 s[64:65], exec\n"
"s_mov_b64 exec, 1\n"
"v_mov_b32 v238, 0\n"
"v_mov_b32 v239, 1\n"
"global_atomic_add v238, v239, s[72:73]\n"
"s_mov_b64 exec, s[64:65]\n"
"Lsig_ob:\n"
"s_mov_b32 s36, %11\n"
"s_and_b32 s37, %12, 0xffff\n"
"s_mov_b32 s38, 1835008\n"
"s_mov_b32 s39, 0x20000\n"
"s_mov_b32 s40, %13\n"
"s_and_b32 s41, %14, 0xffff\n"
"s_mov_b32 s42, 1836032\n"
"s_mov_b32 s43, 0x20000\n"
"v_mbcnt_lo_u32_b32 v232, -1, 0\n"
"v_mbcnt_hi_u32_b32 v232, -1, v232\n"
"v_lshrrev_b32 v233, 4, v232\n"
"v_and_b32 v234, 1, v233\n"
"v_lshlrev_b32 v234, 1, v234\n"
"v_and_b32 v235, 3, v232\n"
"v_xor_b32 v234, v234, v235\n"
"v_lshlrev_b32 v234, 4, v234\n"
"v_lshrrev_b32 v235, 5, v232\n"
"v_lshl_add_u32 v234, v235, 6, v234\n"
"v_bfe_u32 v235, v232, 2, 3\n"
"s_mov_b32 s59, 3584\n"
"v_mul_lo_u32 v235, v235, s59\n"
"v_add_u32 v237, v235, v234\n"
"v_add_u32 v236, 0, v237\n"
"s_cmp_eq_u32 s56, 0\n"
"s_cbranch_scc1 Lw3_wu\n"
"s_mul_i32 s58, s56, 2048\n"
"s_mul_i32 s59, s56, 28672\n"
"s_add_u32 m0, s58, 0\n"
"s_add_u32 s57, s59, 0\n"
"buffer_load_dwordx4 v236, s[36:39], s57 offen lds\n"
"buffer_load_dwordx4 v237, s[40:43], s57 offen offset:1024 lds\n"
"s_add_u32 m0, s58, 8192\n"
"s_add_u32 s57, s59, 114688\n"
"buffer_load_dwordx4 v236, s[36:39], s57 offen lds\n"
"buffer_load_dwordx4 v237, s[40:43], s57 offen offset:1024 lds\n"
"s_add_u32 m0, s58, 16384\n"
"s_add_u32 s57, s59, 229376\n"
"buffer_load_dwordx4 v236, s[36:39], s57 offen lds\n"
"buffer_load_dwordx4 v237, s[40:43], s57 offen offset:1024 lds\n"
"s_add_u32 m0, s58, 24576\n"
"s_add_u32 s57, s59, 344064\n"
"buffer_load_dwordx4 v236, s[36:39], s57 offen lds\n"
"buffer_load_dwordx4 v237, s[40:43], s57 offen offset:1024 lds\n"
"s_add_u32 m0, s58, 32768\n"
"s_add_u32 s57, s59, 458752\n"
"buffer_load_dwordx4 v236, s[36:39], s57 offen lds\n"
"buffer_load_dwordx4 v237, s[40:43], s57 offen offset:1024 lds\n"
"s_add_u32 m0, s58, 40960\n"
"s_add_u32 s57, s59, 573440\n"
"buffer_load_dwordx4 v236, s[36:39], s57 offen lds\n"
"buffer_load_dwordx4 v237, s[40:43], s57 offen offset:1024 lds\n"
"s_add_u32 m0, s58, 49152\n"
"s_add_u32 s57, s59, 688128\n"
"buffer_load_dwordx4 v236, s[36:39], s57 offen lds\n"
"buffer_load_dwordx4 v237, s[40:43], s57 offen offset:1024 lds\n"
"s_add_u32 m0, s58, 57344\n"
"s_add_u32 s57, s59, 802816\n"
"buffer_load_dwordx4 v236, s[36:39], s57 offen lds\n"
"buffer_load_dwordx4 v237, s[40:43], s57 offen offset:1024 lds\n"
"s_add_u32 m0, s58, 65536\n"
"s_add_u32 s57, s59, 917504\n"
"buffer_load_dwordx4 v236, s[36:39], s57 offen lds\n"
"buffer_load_dwordx4 v237, s[40:43], s57 offen offset:1024 lds\n"
"s_add_u32 m0, s58, 73728\n"
"s_add_u32 s57, s59, 1032192\n"
"buffer_load_dwordx4 v236, s[36:39], s57 offen lds\n"
"buffer_load_dwordx4 v237, s[40:43], s57 offen offset:1024 lds\n"
"s_add_u32 m0, s58, 81920\n"
"s_add_u32 s57, s59, 1146880\n"
"buffer_load_dwordx4 v236, s[36:39], s57 offen lds\n"
"buffer_load_dwordx4 v237, s[40:43], s57 offen offset:1024 lds\n"
"s_add_u32 m0, s58, 90112\n"
"s_add_u32 s57, s59, 1261568\n"
"buffer_load_dwordx4 v236, s[36:39], s57 offen lds\n"
"buffer_load_dwordx4 v237, s[40:43], s57 offen offset:1024 lds\n"
"s_add_u32 m0, s58, 98304\n"
"s_add_u32 s57, s59, 1376256\n"
"buffer_load_dwordx4 v236, s[36:39], s57 offen lds\n"
"buffer_load_dwordx4 v237, s[40:43], s57 offen offset:1024 lds\n"
"s_add_u32 m0, s58, 106496\n"
"s_add_u32 s57, s59, 1490944\n"
"buffer_load_dwordx4 v236, s[36:39], s57 offen lds\n"
"buffer_load_dwordx4 v237, s[40:43], s57 offen offset:1024 lds\n"
"s_add_u32 m0, s58, 114688\n"
"s_add_u32 s57, s59, 1605632\n"
"buffer_load_dwordx4 v236, s[36:39], s57 offen lds\n"
"buffer_load_dwordx4 v237, s[40:43], s57 offen offset:1024 lds\n"
"s_add_u32 m0, s58, 122880\n"
"s_add_u32 s57, s59, 1720320\n"
"buffer_load_dwordx4 v236, s[36:39], s57 offen lds\n"
"buffer_load_dwordx4 v237, s[40:43], s57 offen offset:1024 lds\n"
"s_sub_u32 s62, s56, 1\n"
"s_mul_i32 s58, s62, 8192\n"
"s_mul_i32 s59, s62, 114688\n"
"s_add_u32 m0, s58, 0\n"
"s_add_u32 s57, s59, 0\n"
"buffer_load_dwordx4 v236, s[36:39], s57 offen lds\n"
"buffer_load_dwordx4 v237, s[40:43], s57 offen offset:1024 lds\n"
"s_add_u32 m0, s58, 24576\n"
"s_add_u32 s57, s59, 344064\n"
"buffer_load_dwordx4 v236, s[36:39], s57 offen lds\n"
"buffer_load_dwordx4 v237, s[40:43], s57 offen offset:1024 lds\n"
"s_add_u32 m0, s58, 49152\n"
"s_add_u32 s57, s59, 688128\n"
"buffer_load_dwordx4 v236, s[36:39], s57 offen lds\n"
"buffer_load_dwordx4 v237, s[40:43], s57 offen offset:1024 lds\n"
"s_add_u32 m0, s58, 73728\n"
"s_add_u32 s57, s59, 1032192\n"
"buffer_load_dwordx4 v236, s[36:39], s57 offen lds\n"
"buffer_load_dwordx4 v237, s[40:43], s57 offen offset:1024 lds\n"
"s_add_u32 m0, s58, 98304\n"
"s_add_u32 s57, s59, 1376256\n"
"buffer_load_dwordx4 v236, s[36:39], s57 offen lds\n"
"buffer_load_dwordx4 v237, s[40:43], s57 offen offset:1024 lds\n"
"s_cmp_ge_u32 s62, 1\n"
"s_cbranch_scc1 Lw3_wu\n"
"s_add_u32 m0, s58, 122880\n"
"s_add_u32 s57, s59, 1720320\n"
"buffer_load_dwordx4 v236, s[36:39], s57 offen lds\n"
"buffer_load_dwordx4 v237, s[40:43], s57 offen offset:1024 lds\n"
"Lw3_wu:\n"
    :: "s"(RFL((size_t)(xb))), "s"(RFL((size_t)(xb) >> 32)), "s"(RFL((size_t)(ob))), "s"(RFL((size_t)(ob) >> 32)), "s"(wid), "s"(RFL((size_t)(dbgb))), "s"(RFL((size_t)(dbgb) >> 32)), "s"(RFL((size_t)((size_t)(flags + (64 + pan) * 64)))), "s"(RFL((size_t)((size_t)(flags + (64 + pan) * 64)) >> 32)), "s"(RFL((size_t)((size_t)(flags + (72 + pan) * 64)))), "s"(RFL((size_t)((size_t)(flags + (72 + pan) * 64)) >> 32)), "s"(RFL((size_t)(w1b))), "s"(RFL((size_t)(w1b) >> 32)), "s"(RFL((size_t)(w3b))), "s"(RFL((size_t)(w3b) >> 32)), "s"(RFL((size_t)((size_t)(flags + (128 + pan * 8 + sp) * 64)))), "s"(RFL((size_t)((size_t)(flags + (128 + pan * 8 + sp) * 64)) >> 32)), "s"(RFL((size_t)((size_t)(flags + (120) * 64)))), "s"(RFL((size_t)((size_t)(flags + (120) * 64)) >> 32))
    : "v0", "v1", "v2", "v3", "v4", "v5", "v6", "v7", "v8", "v9", "v10", "v11", "v12", "v13", "v14", "v15", "v16", "v17", "v18", "v19", "v20", "v21", "v22", "v23", "v24", "v25", "v26", "v27", "v28", "v29", "v30", "v31", "v32", "v33", "v34", "v35", "v36", "v37", "v38", "v39", "v40", "v41", "v42", "v43", "v44", "v45", "v46", "v47", "v48", "v49", "v50", "v51", "v52", "v53", "v54", "v55", "v56", "v57", "v58", "v59", "v60", "v61", "v62", "v63", "v64", "v65", "v66", "v67", "v68", "v69", "v70", "v71", "v72", "v73", "v74", "v75", "v76", "v77", "v78", "v79", "v80", "v81", "v82", "v83", "v84", "v85", "v86", "v87", "v88", "v89", "v90", "v91", "v92", "v93", "v94", "v95", "v96", "v97", "v98", "v99", "v100", "v101", "v102", "v103", "v104", "v105", "v106", "v107", "v108", "v109", "v110", "v111", "v112", "v113", "v114", "v115", "v116", "v117", "v118", "v119", "v120", "v121", "v122", "v123", "v124", "v125", "v126", "v127", "v128", "v129", "v130", "v131", "v132", "v133", "v134", "v135", "v136", "v137", "v138", "v139", "v140", "v141", "v142", "v143", "v144", "v145", "v146", "v147", "v148", "v149", "v150", "v151", "v152", "v153", "v154", "v155", "v156", "v157", "v158", "v159", "v160", "v161", "v162", "v163", "v164", "v165", "v166", "v167", "v168", "v169", "v170", "v171", "v172", "v173", "v174", "v175", "v176", "v177", "v178", "v179", "v180", "v181", "v182", "v183", "v184", "v185", "v186", "v187", "v188", "v189", "v190", "v191", "v192", "v193", "v194", "v195", "v196", "v197", "v198", "v199", "v200", "v201", "v202", "v203", "v204", "v205", "v206", "v207", "v208", "v209", "v210", "v211", "v212", "v213", "v214", "v215", "v216", "v217", "v218", "v219", "v220", "v221", "v222", "v223", "v224", "v225", "v226", "v227", "v228", "v229", "v230", "v231", "v232", "v233", "v234", "v235", "v236", "v237", "v238", "v239", "v240", "v241", "v242", "v243", "v244", "v245", "v246", "v247", "v248", "v249", "v250", "v251", "v252", "v253", "v254", "v255", "a0", "a1", "a2", "a3", "a4", "a5", "a6", "a7", "a8", "a9", "a10", "a11", "a12", "a13", "a14", "a15", "a16", "a17", "a18", "a19", "a20", "a21", "a22", "a23", "a24", "a25", "a26", "a27", "a28", "a29", "a30", "a31", "a32", "a33", "a34", "a35", "a36", "a37", "a38", "a39", "a40", "a41", "a42", "a43", "a44", "a45", "a46", "a47", "a48", "a49", "a50", "a51", "a52", "a53", "a54", "a55", "a56", "a57", "a58", "a59", "a60", "a61", "a62", "a63", "a64", "a65", "a66", "a67", "a68", "a69", "a70", "a71", "a72", "a73", "a74", "a75", "a76", "a77", "a78", "a79", "a80", "a81", "a82", "a83", "a84", "a85", "a86", "a87", "a88", "a89", "a90", "a91", "a92", "a93", "a94", "a95", "a96", "a97", "a98", "a99", "a100", "a101", "a102", "a103", "a104", "a105", "a106", "a107", "a108", "a109", "a110", "a111", "a112", "a113", "a114", "a115", "a116", "a117", "a118", "a119", "a120", "a121", "a122", "a123", "a124", "a125", "a126", "a127", "a128", "a129", "a130", "a131", "a132", "a133", "a134", "a135", "a136", "a137", "a138", "a139", "a140", "a141", "a142", "a143", "a144", "a145", "a146", "a147", "a148", "a149", "a150", "a151", "a152", "a153", "a154", "a155", "a156", "a157", "a158", "a159", "a160", "a161", "a162", "a163", "a164", "a165", "a166", "a167", "a168", "a169", "a170", "a171", "a172", "a173", "a174", "a175", "a176", "a177", "a178", "a179", "a180", "a181", "a182", "a183", "a184", "a185", "a186", "a187", "a188", "a189", "a190", "a191", "a192", "a193", "a194", "a195", "a196", "a197", "a198", "a199", "a200", "a201", "a202", "a203", "a204", "a205", "a206", "a207", "a208", "a209", "a210", "a211", "a212", "a213", "a214", "a215", "a216", "a217", "a218", "a219", "a220", "a221", "a222", "a223", "a224", "a225", "a226", "a227", "a228", "a229", "a230", "a231", "a232", "a233", "a234", "a235", "a236", "a237", "a238", "a239", "a240", "a241", "a242", "a243", "a244", "a245", "a246", "a247", "a248", "a249", "a250", "a251", "a252", "a253", "a254", "a255", "s36", "s37", "s38", "s39", "s40", "s41", "s42", "s43", "s44", "s45", "s46", "s47", "s48", "s49", "s50", "s51", "s52", "s53", "s54", "s55", "s56", "s57", "s58", "s59", "s60", "s61", "s62", "s63", "s64", "s65", "s66", "s67", "s68", "s69", "s70", "s71", "s72", "s73", "s74", "s75", "s76", "s77", "s78", "s79", "s80", "s81", "s82", "s83", "vcc", "scc", "memory");

  }
  { ARGS();
    const unsigned tk = ticket(flags, 112 + pan, lds, wid) & 31, hl = tk >> 4;
    const unsigned lrb = pan * 16 + ((tk >> 2) & 3) * 4 + hl * 2 + ((tk >> 1) & 1);
    const LnX xres = ln2_load_x(x, wid, lrb, tk & 1);
    wait_for(flags, (hl ? 72 : 64) + pan, 32, wid);
    STAMP(5);
    ln2_phase([&]() { publish(flags, (hl ? 88 : 80) + pan, wid); }, xres, part, x1, h2, lds, wid, lrb, tk & 1);
    STAMP(6);
  }
  if (hasu) {
    ARGS(); wait_for(flags, 80 + pu, 16, wid); const size_t w2b = (size_t)w2 + dn * 128;
    const size_t xb = (size_t)h2 + (size_t)(pu * 16 + wid * 4) * 16384, ob = (size_t)act + ((size_t)(pu * 16 + wid * 4) * 56 + ft * 2) * 1024;
  asm volatile(
"s_mov_b32 s44, %0\n"
"s_and_b32 s45, %1, 0xffff\n"
"s_mov_b32 s46, 65536\n"
"s_mov_b32 s47, 0x20000\n"
"s_mov_b32 s48, %2\n"
"s_and_b32 s49, %3, 0xffff\n"
"s_mov_b32 s50, 174080\n"
"s_mov_b32 s51, 0x20000\n"
"s_mov_b32 s56, %6\n"
"s_mov_b32 s66, %7\n"
"s_mov_b32 s67, %8\n"
"s_mov_b32 s70, %9\n"
"s_mov_b32 s71, %10\n"
"s_mov_b32 s72, %11\n"
"s_mov_b32 s73, %12\n"
"s_mov_b32 s74, %13\n"
"s_mov_b32 s75, %14\n"
"s_mov_b32 s76, %15\n"
"s_mov_b32 s77, %16\n"
"s_mov_b32 s78, %17\n"
"s_mov_b32 s79, %18\n"
"v_mbcnt_lo_u32_b32 v240, -1, 0\n"
"v_mbcnt_hi_u32_b32 v240, -1, v240\n"
"v_lshrrev_b32 v241, 4, v240\n"
"v_lshlrev_b32 v246, 4, v240\n"
"v_and_b32 v242, 1, v241\n"
"v_lshlrev_b32 v242, 1, v242\n"
"v_and_b32 v243, 3, v240\n"
"v_xor_b32 v242, v242, v243\n"
"v_lshlrev_b32 v242, 4, v242\n"
"v_lshrrev_b32 v243, 5, v240\n"
"v_lshl_add_u32 v242, v243, 6, v242\n"
"v_bfe_u32 v243, v240, 2, 3\n"
"s_mov_b32 s59, 1024\n"
"v_mul_lo_u32 v243, v243, s59\n"
"v_add_u32 v248, v243, v242\n"
"v_add_u32 v247, 0, v248\n"
"v_lshrrev_b32 v242, 1, v241\n"
"v_mul_u32_u24 v242, 2048, v242\n"
"v_and_b32 v243, 1, v241\n"
"v_lshl_add_u32 v242, v243, 8, v242\n"
"v_bfe_u32 v244, v240, 2, 2\n"
"v_lshl_add_u32 v242, v244, 6, v242\n"
"v_and_b32 v244, 3, v240\n"
"v_lshl_add_u32 v245, v244, 3, v242\n"
"v_lshl_add_u32 v249, v243, 5, v245\n"
"v_xor_b32 v244, 1, v243\n"
"v_lshl_add_u32 v250, v244, 5, v245\n"
"v_add_u32 v251, 0x10000, v249\n"
"v_add_u32 v252, 0x10000, v250\n"
"s_mov_b32 s58, 0\n"
"buffer_load_dwordx4 a[0:3], v246, s[44:47], s58 offen offset:0 sc1\n"
"s_mov_b32 s58, 16384\n"
"buffer_load_dwordx4 a[4:7], v246, s[44:47], s58 offen offset:0 sc1\n"
"s_mov_b32 s58, 0\n"
"buffer_load_dwordx4 a[16:19], v246, s[44:47], s58 offen offset:1024 sc1\n"
"s_mov_b32 s58, 16384\n"
"buffer_load_dwordx4 a[20:23], v246, s[44:47], s58 offen offset:1024 sc1\n"
"s_mov_b32 s58, 0\n"
"buffer_load_dwordx4 a[32:35], v246, s[44:47], s58 offen offset:2048 sc1\n"
"s_mov_b32 s58, 16384\n"
"buffer_load_dwordx4 a[36:39], v246, s[44:47], s58 offen offset:2048 sc1\n"
"s_mov_b32 s58, 0\n"
"buffer_load_dwordx4 a[48:51], v246, s[44:47], s58 offen offset:3072 sc1\n"
"s_mov_b32 s58, 16384\n"
"buffer_load_dwordx4 a[52:55], v246, s[44:47], s58 offen offset:3072 sc1\n"
"s_mov_b32 s58, 4096\n"
"buffer_load_dwordx4 a[64:67], v246, s[44:47], s58 offen offset:0 sc1\n"
"s_mov_b32 s58, 20480\n"
"buffer_load_dwordx4 a[68:71], v246, s[44:47], s58 offen offset:0 sc1\n"
"s_mov_b32 s58, 4096\n"
"buffer_load_dwordx4 a[80:83], v246, s[44:47], s58 offen offset:1024 sc1\n"
"s_mov_b32 s58, 20480\n"
"buffer_load_dwordx4 a[84:87], v246, s[44:47], s58 offen offset:1024 sc1\n"
"s_mov_b32 s58, 4096\n"
"buffer_load_dwordx4 a[96:99], v246, s[44:47], s58 offen offset:2048 sc1\n"
"s_mov_b32 s58, 20480\n"
"buffer_load_dwordx4 a[100:103], v246, s[44:47], s58 offen offset:2048 sc1\n"
"s_mov_b32 s58, 4096\n"
"buffer_load_dwordx4 a[112:115], v246, s[44:47], s58 offen offset:3072 sc1\n"
"s_mov_b32 s58, 20480\n"
"buffer_load_dwordx4 a[116:119], v246, s[44:47], s58 offen offset:3072 sc1\n"
"s_mov_b32 s58, 8192\n"
"buffer_load_dwordx4 a[128:131], v246, s[44:47], s58 offen offset:0 sc1\n"
"s_mov_b32 s58, 24576\n"
"buffer_load_dwordx4 a[132:135], v246, s[44:47], s58 offen offset:0 sc1\n"
"s_mov_b32 s58, 8192\n"
"buffer_load_dwordx4 a[144:147], v246, s[44:47], s58 offen offset:1024 sc1\n"
"s_mov_b32 s58, 24576\n"
"buffer_load_dwordx4 a[148:151], v246, s[44:47], s58 offen offset:1024 sc1\n"
"s_mov_b32 s58, 8192\n"
"buffer_load_dwordx4 a[160:163], v246, s[44:47], s58 offen offset:2048 sc1\n"
"s_mov_b32 s58, 24576\n"
"buffer_load_dwordx4 a[164:167], v246, s[44:47], s58 offen offset:2048 sc1\n"
"s_mov_b32 s58, 8192\n"
"buffer_load_dwordx4 a[176:179], v246, s[44:47], s58 offen offset:3072 sc1\n"
"s_mov_b32 s58, 24576\n"
"buffer_load_dwordx4 a[180:183], v246, s[44:47], s58 offen offset:3072 sc1\n"
"s_mov_b32 s58, 12288\n"
"buffer_load_dwordx4 a[192:195], v246, s[44:47], s58 offen offset:0 sc1\n"
"s_mov_b32 s58, 28672\n"
"buffer_load_dwordx4 a[196:199], v246, s[44:47], s58 offen offset:0 sc1\n"
"s_mov_b32 s58, 12288\n"
"buffer_load_dwordx4 a[208:211], v246, s[44:47], s58 offen offset:1024 sc1\n"
"s_mov_b32 s58, 28672\n"
"buffer_load_dwordx4 a[212:215], v246, s[44:47], s58 offen offset:1024 sc1\n"
"s_mov_b32 s58, 12288\n"
"buffer_load_dwordx4 a[224:227], v246, s[44:47], s58 offen offset:2048 sc1\n"
"s_mov_b32 s58, 28672\n"
"buffer_load_dwordx4 a[228:231], v246, s[44:47], s58 offen offset:2048 sc1\n"
"s_mov_b32 s58, 12288\n"
"buffer_load_dwordx4 a[240:243], v246, s[44:47], s58 offen offset:3072 sc1\n"
"s_mov_b32 s58, 28672\n"
"buffer_load_dwordx4 a[244:247], v246, s[44:47], s58 offen offset:3072 sc1\n"
"v_mov_b32 v228, 0\n"
"global_load_dword v229, v228, s[70:71] sc1\n"
"ds_read_b64_tr_b16 v[128:129], v249 offset:0\n"
"ds_read_b64_tr_b16 v[130:131], v249 offset:4096\n"
"ds_read_b64_tr_b16 v[132:133], v250 offset:0\n"
"ds_read_b64_tr_b16 v[134:135], v250 offset:4096\n"
"ds_read_b64_tr_b16 v[136:137], v249 offset:512\n"
"ds_read_b64_tr_b16 v[138:139], v249 offset:4608\n"
"ds_read_b64_tr_b16 v[140:141], v250 offset:512\n"
"ds_read_b64_tr_b16 v[142:143], v250 offset:4608\n"
"ds_read_b64_tr_b16 v[144:145], v249 offset:1024\n"
"ds_read_b64_tr_b16 v[146:147], v249 offset:5120\n"
"ds_read_b64_tr_b16 v[148:149], v250 offset:1024\n"
"ds_read_b64_tr_b16 v[150:151], v250 offset:5120\n"
"ds_read_b64_tr_b16 v[152:153], v249 offset:1536\n"
"ds_read_b64_tr_b16 v[154:155], v249 offset:5632\n"
"ds_read_b64_tr_b16 v[156:157], v250 offset:1536\n"
"ds_read_b64_tr_b16 v[158:159], v250 offset:5632\n"
"s_waitcnt vmcnt(31) lgkmcnt(0)\n"
"v_mfma_f32_16x16x32_bf16 v[0:3], v[128:131], a[0:3], 0\n"
"ds_read_b64_tr_b16 v[160:161], v249 offset:8192\n"
"ds_read_b64_tr_b16 v[162:163], v249 offset:12288\n"
"v_mfma_f32_16x16x32_bf16 v[4:7], v[128:131], a[4:7], 0\n"
"ds_read_b64_tr_b16 v[164:165], v250 offset:8192\n"
"ds_read_b64_tr_b16 v[166:167], v250 offset:12288\n"
"v_mfma_f32_16x16x32_bf16 v[16:19], v[132:135], a[0:3], 0\n"
"ds_read_b64_tr_b16 v[168:169], v249 offset:8704\n"
"ds_read_b64_tr_b16 v[170:171], v249 offset:12800\n"
"v_mfma_f32_16x16x32_bf16 v[20:23], v[132:135], a[4:7], 0\n"
"ds_read_b64_tr_b16 v[172:173], v250 offset:8704\n"
"ds_read_b64_tr_b16 v[174:175], v250 offset:12800\n"
"v_mfma_f32_16x16x32_bf16 v[32:35], v[136:139], a[0:3], 0\n"
"ds_read_b64_tr_b16 v[176:177], v249 offset:9216\n"
"ds_read_b64_tr_b16 v[178:179], v249 offset:13312\n"
"v_mfma_f32_16x16x32_bf16 v[36:39], v[136:139], a[4:7], 0\n"
"ds_read_b64_tr_b16 v[180:181], v250 offset:9216\n"
"ds_read_b64_tr_b16 v[182:183], v250 offset:13312\n"
"v_mfma_f32_16x16x32_bf16 v[48:51], v[140:143], a[0:3], 0\n"
"ds_read_b64_tr_b16 v[184:185], v249 offset:9728\n"
"ds_read_b64_tr_b16 v[186:187], v249 offset:13824\n"
"v_mfma_f32_16x16x32_bf16 v[52:55], v[140:143], a[4:7], 0\n"
"ds_read_b64_tr_b16 v[188:189], v250 offset:9728\n"
"ds_read_b64_tr_b16 v[190:191], v250 offset:13824\n"
"v_mfma_f32_16x16x32_bf16 v[64:67], v[144:147], a[0:3], 0\n"
"v_mfma_f32_16x16x32_bf16 v[68:71], v[144:147], a[4:7], 0\n"
"v_mfma_f32_16x16x32_bf16 v[80:83], v[148:151], a[0:3], 0\n"
"v_mfma_f32_16x16x32_bf16 v[84:87], v[148:151], a[4:7], 0\n"
"v_mfma_f32_16x16x32_bf16 v[96:99], v[152:155], a[0:3], 0\n"
"v_mfma_f32_16x16x32_bf16 v[100:103], v[152:155], a[4:7], 0\n"
"v_mfma_f32_16x16x32_bf16 v[112:115], v[156:159], a[0:3], 0\n"
"v_mfma_f32_16x16x32_bf16 v[116:119], v[156:159], a[4:7], 0\n"
"s_waitcnt vmcnt(29) lgkmcnt(0)\n"
"v_mfma_f32_16x16x32_bf16 v[0:3], v[160:163], a[16:19], v[0:3]\n"
"ds_read_b64_tr_b16 v[128:129], v249 offset:16384\n"
"ds_read_b64_tr_b16 v[130:131], v249 offset:20480\n"
"v_mfma_f32_16x16x32_bf16 v[4:7], v[160:163], a[20:23], v[4:7]\n"
"ds_read_b64_tr_b16 v[132:133], v250 offset:16384\n"
"ds_read_b64_tr_b16 v[134:135], v250 offset:20480\n"
"v_mfma_f32_16x16x32_bf16 v[16:19], v[164:167], a[16:19], v[16:19]\n"
"ds_read_b64_tr_b16 v[136:137], v249 offset:16896\n"
"ds_read_b64_tr_b16 v[138:139], v249 offset:20992\n"
"v_mfma_f32_16x16x32_bf16 v[20:23], v[164:167], a[20:23], v[20:23]\n"
"ds_read_b64_tr_b16 v[140:141], v250 offset:16896\n"
"ds_read_b64_tr_b16 v[142:143], v250 offset:20992\n"
"v_mfma_f32_16x16x32_bf16 v[32:35], v[168:171], a[16:19], v[32:35]\n"
"ds_read_b64_tr_b16 v[144:145], v249 offset:17408\n"
"ds_read_b64_tr_b16 v[146:147], v249 offset:21504\n"
"v_mfma_f32_16x16x32_bf16 v[36:39], v[168:171], a[20:23], v[36:39]\n"
"ds_read_b64_tr_b16 v[148:149], v250 offset:17408\n"
"ds_read_b64_tr_b16 v[150:151], v250 offset:21504\n"
"v_mfma_f32_16x16x32_bf16 v[48:51], v[172:175], a[16:19], v[48:51]\n"
"ds_read_b64_tr_b16 v[152:153], v249 offset:17920\n"
"ds_read_b64_tr_b16 v[154:155], v249 offset:22016\n"
"v_mfma_f32_16x16x32_bf16 v[52:55], v[172:175], a[20:23], v[52:55]\n"
"ds_read_b64_tr_b16 v[156:157], v250 offset:17920\n"
"ds_read_b64_tr_b16 v[158:159], v250 offset:22016\n"
"v_mfma_f32_16x16x32_bf16 v[64:67], v[176:179], a[16:19], v[64:67]\n"
"v_mfma_f32_16x16x32_bf16 v[68:71], v[176:179], a[20:23], v[68:71]\n"
"v_mfma_f32_16x16x32_bf16 v[80:83], v[180:183], a[16:19], v[80:83]\n"
"v_mfma_f32_16x16x32_bf16 v[84:87], v[180:183], a[20:23], v[84:87]\n"
"v_mfma_f32_16x16x32_bf16 v[96:99], v[184:187], a[16:19], v[96:99]\n"
"v_mfma_f32_16x16x32_bf16 v[100:103], v[184:187], a[20:23], v[100:103]\n"
"v_mfma_f32_16x16x32_bf16 v[112:115], v[188:191], a[16:19], v[112:115]\n"
"v_mfma_f32_16x16x32_bf16 v[116:119], v[188:191], a[20:23], v[116:119]\n"
"s_waitcnt vmcnt(27) lgkmcnt(0)\n"
"v_mfma_f32_16x16x32_bf16 v[0:3], v[128:131], a[32:35], v[0:3]\n"
"ds_read_b64_tr_b16 v[160:161], v249 offset:24576\n"
"ds_read_b64_tr_b16 v[162:163], v249 offset:28672\n"
"v_mfma_f32_16x16x32_bf16 v[4:7], v[128:131], a[36:39], v[4:7]\n"
"ds_read_b64_tr_b16 v[164:165], v250 offset:24576\n"
"ds_read_b64_tr_b16 v[166:167], v250 offset:28672\n"
"v_mfma_f32_16x16x32_bf16 v[16:19], v[132:135], a[32:35], v[16:19]\n"
"ds_read_b64_tr_b16 v[168:169], v249 offset:25088\n"
"ds_read_b64_tr_b16 v[170:171], v249 offset:29184\n"
"v_mfma_f32_16x16x32_bf16 v[20:23], v[132:135], a[36:39], v[20:23]\n"
"ds_read_b64_tr_b16 v[172:173], v250 offset:25088\n"
"ds_read_b64_tr_b16 v[174:175], v250 offset:29184\n"
"v_mfma_f32_16x16x32_bf16 v[32:35], v[136:139], a[32:35], v[32:35]\n"
"ds_read_b64_tr_b16 v[176:177], v249 offset:25600\n"
"ds_read_b64_tr_b16 v[178:179], v249 offset:29696\n"
"v_mfma_f32_16x16x32_bf16 v[36:39], v[136:139], a[36:39], v[36:39]\n"
"ds_read_b64_tr_b16 v[180:181], v250 offset:25600\n"
"ds_read_b64_tr_b16 v[182:183], v250 offset:29696\n"
"v_mfma_f32_16x16x32_bf16 v[48:51], v[140:143], a[32:35], v[48:51]\n"
"ds_read_b64_tr_b16 v[184:185], v249 offset:26112\n"
"ds_read_b64_tr_b16 v[186:187], v249 offset:30208\n"
"v_mfma_f32_16x16x32_bf16 v[52:55], v[140:143], a[36:39], v[52:55]\n"
"ds_read_b64_tr_b16 v[188:189], v250 offset:26112\n"
"ds_read_b64_tr_b16 v[190:191], v250 offset:30208\n"
"v_mfma_f32_16x16x32_bf16 v[64:67], v[144:147], a[32:35], v[64:67]\n"
"v_mfma_f32_16x16x32_bf16 v[68:71], v[144:147], a[36:39], v[68:71]\n"
"v_mfma_f32_16x16x32_bf16 v[80:83], v[148:151], a[32:35], v[80:83]\n"
"v_mfma_f32_16x16x32_bf16 v[84:87], v[148:151], a[36:39], v[84:87]\n"
"v_mfma_f32_16x16x32_bf16 v[96:99], v[152:155], a[32:35], v[96:99]\n"
"v_mfma_f32_16x16x32_bf16 v[100:103], v[152:155], a[36:39], v[100:103]\n"
"v_mfma_f32_16x16x32_bf16 v[112:115], v[156:159], a[32:35], v[112:115]\n"
"v_mfma_f32_16x16x32_bf16 v[116:119], v[156:159], a[36:39], v[116:119]\n"
"s_waitcnt vmcnt(25) lgkmcnt(0)\n"
"v_mfma_f32_16x16x32_bf16 v[0:3], v[160:163], a[48:51], v[0:3]\n"
"ds_read_b64_tr_b16 v[128:129], v249 offset:32768\n"
"ds_read_b64_tr_b16 v[130:131], v249 offset:36864\n"
"v_mfma_f32_16x16x32_bf16 v[4:7], v[160:163], a[52:55], v[4:7]\n"
"ds_read_b64_tr_b16 v[132:133], v250 offset:32768\n"
"ds_read_b64_tr_b16 v[134:135], v250 offset:36864\n"
"v_mfma_f32_16x16x32_bf16 v[16:19], v[164:167], a[48:51], v[16:19]\n"
"ds_read_b64_tr_b16 v[136:137], v249 offset:33280\n"
"ds_read_b64_tr_b16 v[138:139], v249 offset:37376\n"
"v_mfma_f32_16x16x32_bf16 v[20:23], v[164:167], a[52:55], v[20:23]\n"
"ds_read_b64_tr_b16 v[140:141], v250 offset:33280\n"
"ds_read_b64_tr_b16 v[142:143], v250 offset:37376\n"
"v_mfma_f32_16x16x32_bf16 v[32:35], v[168:171], a[48:51], v[32:35]\n"
"ds_read_b64_tr_b16 v[144:145], v249 offset:33792\n"
"ds_read_b64_tr_b16 v[146:147], v249 offset:37888\n"
"v_mfma_f32_16x16x32_bf16 v[36:39], v[168:171], a[52:55], v[36:39]\n"
"ds_read_b64_tr_b16 v[148:149], v250 offset:33792\n"
"ds_read_b64_tr_b16 v[150:151], v250 offset:37888\n"
"v_mfma_f32_16x16x32_bf16 v[48:51], v[172:175], a[48:51], v[48:51]\n"
"ds_read_b64_tr_b16 v[152:153], v249 offset:34304\n"
"ds_read_b64_tr_b16 v[154:155], v249 offset:38400\n"
"v_mfma_f32_16x16x32_bf16 v[52:55], v[172:175], a[52:55], v[52:55]\n"
"ds_read_b64_tr_b16 v[156:157], v250 offset:34304\n"
"ds_read_b64_tr_b16 v[158:159], v250 offset:38400\n"
"v_mfma_f32_16x16x32_bf16 v[64:67], v[176:179], a[48:51], v[64:67]\n"
"v_mfma_f32_16x16x32_bf16 v[68:71], v[176:179], a[52:55], v[68:71]\n"
"v_mfma_f32_16x16x32_bf16 v[80:83], v[180:183], a[48:51], v[80:83]\n"
"v_mfma_f32_16x16x32_bf16 v[84:87], v[180:183], a[52:55], v[84:87]\n"
"v_mfma_f32_16x16x32_bf16 v[96:99], v[184:187], a[48:51], v[96:99]\n"
"v_mfma_f32_16x16x32_bf16 v[100:103], v[184:187], a[52:55], v[100:103]\n"
"v_mfma_f32_16x16x32_bf16 v[112:115], v[188:191], a[48:51], v[112:115]\n"
"v_mfma_f32_16x16x32_bf16 v[116:119], v[188:191], a[52:55], v[116:119]\n"
"s_waitcnt vmcnt(23) lgkmcnt(0)\n"
"v_mfma_f32_16x16x32_bf16 v[0:3], v[128:131], a[64:67], v[0:3]\n"
"ds_read_b64_tr_b16 v[160:161], v249 offset:40960\n"
"ds_read_b64_tr_b16 v[162:163], v249 offset:45056\n"
"v_mfma_f32_16x16x32_bf16 v[4:7], v[128:131], a[68:71], v[4:7]\n"
"ds_read_b64_tr_b16 v[164:165], v250 offset:40960\n"
"ds_read_b64_tr_b16 v[166:167], v250 offset:45056\n"
"v_mfma_f32_16x16x32_bf16 v[16:19], v[132:135], a[64:67], v[16:19]\n"
"ds_read_b64_tr_b16 v[168:169], v249 offset:41472\n"
"ds_read_b64_tr_b16 v[170:171], v249 offset:45568\n"
"v_mfma_f32_16x16x32_bf16 v[20:23], v[132:135], a[68:71], v[20:23]\n"
"ds_read_b64_tr_b16 v[172:173], v250 offset:41472\n"
"ds_read_b64_tr_b16 v[174:175], v250 offset:45568\n"
"v_mfma_f32_16x16x32_bf16 v[32:35], v[136:139], a[64:67], v[32:35]\n"
"ds_read_b64_tr_b16 v[176:177], v249 offset:41984\n"
"ds_read_b64_tr_b16 v[178:179], v249 offset:46080\n"
"v_mfma_f32_16x16x32_bf16 v[36:39], v[136:139], a[68:71], v[36:39]\n"
"ds_read_b64_tr_b16 v[180:181], v250 offset:41984\n"
"ds_read_b64_tr_b16 v[182:183], v250 offset:46080\n"
"v_mfma_f32_16x16x32_bf16 v[48:51], v[140:143], a[64:67], v[48:51]\n"
"ds_read_b64_tr_b16 v[184:185], v249 offset:42496\n"
"ds_read_b64_tr_b16 v[186:187], v249 offset:46592\n"
"v_mfma_f32_16x16x32_bf16 v[52:55], v[140:143], a[68:71], v[52:55]\n"
"ds_read_b64_tr_b16 v[188:189], v250 offset:42496\n"
"ds_read_b64_tr_b16 v[190:191], v250 offset:46592\n"
"v_mfma_f32_16x16x32_bf16 v[64:67], v[144:147], a[64:67], v[64:67]\n"
"v_mfma_f32_16x16x32_bf16 v[68:71], v[144:147], a[68:71], v[68:71]\n"
"v_mfma_f32_16x16x32_bf16 v[80:83], v[148:151], a[64:67], v[80:83]\n"
"v_mfma_f32_16x16x32_bf16 v[84:87], v[148:151], a[68:71], v[84:87]\n"
"v_mfma_f32_16x16x32_bf16 v[96:99], v[152:155], a[64:67], v[96:99]\n"
"v_mfma_f32_16x16x32_bf16 v[100:103], v[152:155], a[68:71], v[100:103]\n"
"v_mfma_f32_16x16x32_bf16 v[112:115], v[156:159], a[64:67], v[112:115]\n"
"v_mfma_f32_16x16x32_bf16 v[116:119], v[156:159], a[68:71], v[116:119]\n"
"s_waitcnt vmcnt(21) lgkmcnt(0)\n"
"v_mfma_f32_16x16x32_bf16 v[0:3], v[160:163], a[80:83], v[0:3]\n"
"ds_read_b64_tr_b16 v[128:129], v249 offset:49152\n"
"ds_read_b64_tr_b16 v[130:131], v249 offset:53248\n"
"v_mfma_f32_16x16x32_bf16 v[4:7], v[160:163], a[84:87], v[4:7]\n"
"ds_read_b64_tr_b16 v[132:133], v250 offset:49152\n"
"ds_read_b64_tr_b16 v[134:135], v250 offset:53248\n"
"v_mfma_f32_16x16x32_bf16 v[16:19], v[164:167], a[80:83], v[16:19]\n"
"ds_read_b64_tr_b16 v[136:137], v249 offset:49664\n"
"ds_read_b64_tr_b16 v[138:139], v249 offset:53760\n"
"v_mfma_f32_16x16x32_bf16 v[20:23], v[164:167], a[84:87], v[20:23]\n"
"ds_read_b64_tr_b16 v[140:141], v250 offset:49664\n"
"ds_read_b64_tr_b16 v[142:143], v250 offset:53760\n"
"v_mfma_f32_16x16x32_bf16 v[32:35], v[168:171], a[80:83], v[32:35]\n"
"ds_read_b64_tr_b16 v[144:145], v249 offset:50176\n"
"ds_read_b64_tr_b16 v[146:147], v249 offset:54272\n"
"v_mfma_f32_16x16x32_bf16 v[36:39], v[168:171], a[84:87], v[36:39]\n"
"ds_read_b64_tr_b16 v[148:149], v250 offset:50176\n"
"ds_read_b64_tr_b16 v[150:151], v250 offset:54272\n"
"v_mfma_f32_16x16x32_bf16 v[48:51], v[172:175], a[80:83], v[48:51]\n"
"ds_read_b64_tr_b16 v[152:153], v249 offset:50688\n"
"ds_read_b64_tr_b16 v[154:155], v249 offset:54784\n"
"v_mfma_f32_16x16x32_bf16 v[52:55], v[172:175], a[84:87], v[52:55]\n"
"ds_read_b64_tr_b16 v[156:157], v250 offset:50688\n"
"ds_read_b64_tr_b16 v[158:159], v250 offset:54784\n"
"v_mfma_f32_16x16x32_bf16 v[64:67], v[176:179], a[80:83], v[64:67]\n"
"v_mfma_f32_16x16x32_bf16 v[68:71], v[176:179], a[84:87], v[68:71]\n"
"v_mfma_f32_16x16x32_bf16 v[80:83], v[180:183], a[80:83], v[80:83]\n"
"v_mfma_f32_16x16x32_bf16 v[84:87], v[180:183], a[84:87], v[84:87]\n"
"v_mfma_f32_16x16x32_bf16 v[96:99], v[184:187], a[80:83], v[96:99]\n"
"v_mfma_f32_16x16x32_bf16 v[100:103], v[184:187], a[84:87], v[100:103]\n"
"v_mfma_f32_16x16x32_bf16 v[112:115], v[188:191], a[80:83], v[112:115]\n"
"v_mfma_f32_16x16x32_bf16 v[116:119], v[188:191], a[84:87], v[116:119]\n"
"s_waitcnt vmcnt(19) lgkmcnt(0)\n"
"v_mfma_f32_16x16x32_bf16 v[0:3], v[128:131], a[96:99], v[0:3]\n"
"ds_read_b64_tr_b16 v[160:161], v249 offset:57344\n"
"ds_read_b64_tr_b16 v[162:163], v249 offset:61440\n"
"v_mfma_f32_16x16x32_bf16 v[4:7], v[128:131], a[100:103], v[4:7]\n"
"ds_read_b64_tr_b16 v[164:165], v250 offset:57344\n"
"ds_read_b64_tr_b16 v[166:167], v250 offset:61440\n"
"v_mfma_f32_16x16x32_bf16 v[16:19], v[132:135], a[96:99], v[16:19]\n"
"ds_read_b64_tr_b16 v[168:169], v249 offset:57856\n"
"ds_read_b64_tr_b16 v[170:171], v249 offset:61952\n"
"v_mfma_f32_16x16x32_bf16 v[20:23], v[132:135], a[100:103], v[20:23]\n"
"ds_read_b64_tr_b16 v[172:173], v250 offset:57856\n"
"ds_read_b64_tr_b16 v[174:175], v250 offset:61952\n"
"v_mfma_f32_16x16x32_bf16 v[32:35], v[136:139], a[96:99], v[32:35]\n"
"ds_read_b64_tr_b16 v[176:177], v249 offset:58368\n"
"ds_read_b64_tr_b16 v[178:179], v249 offset:62464\n"
"v_mfma_f32_16x16x32_bf16 v[36:39], v[136:139], a[100:103], v[36:39]\n"
"ds_read_b64_tr_b16 v[180:181], v250 offset:58368\n"
"ds_read_b64_tr_b16 v[182:183], v250 offset:62464\n"
"v_mfma_f32_16x16x32_bf16 v[48:51], v[140:143], a[96:99], v[48:51]\n"
"ds_read_b64_tr_b16 v[184:185], v249 offset:58880\n"
"ds_read_b64_tr_b16 v[186:187], v249 offset:62976\n"
"v_mfma_f32_16x16x32_bf16 v[52:55], v[140:143], a[100:103], v[52:55]\n"
"ds_read_b64_tr_b16 v[188:189], v250 offset:58880\n"
"ds_read_b64_tr_b16 v[190:191], v250 offset:62976\n"
"v_mfma_f32_16x16x32_bf16 v[64:67], v[144:147], a[96:99], v[64:67]\n"
"v_mfma_f32_16x16x32_bf16 v[68:71], v[144:147], a[100:103], v[68:71]\n"
"v_mfma_f32_16x16x32_bf16 v[80:83], v[148:151], a[96:99], v[80:83]\n"
"v_mfma_f32_16x16x32_bf16 v[84:87], v[148:151], a[100:103], v[84:87]\n"
"v_mfma_f32_16x16x32_bf16 v[96:99], v[152:155], a[96:99], v[96:99]\n"
"v_mfma_f32_16x16x32_bf16 v[100:103], v[152:155], a[100:103], v[100:103]\n"
"v_mfma_f32_16x16x32_bf16 v[112:115], v[156:159], a[96:99], v[112:115]\n"
"v_mfma_f32_16x16x32_bf16 v[116:119], v[156:159], a[100:103], v[116:119]\n"
"s_waitcnt vmcnt(17) lgkmcnt(0)\n"
"v_mfma_f32_16x16x32_bf16 v[0:3], v[160:163], a[112:115], v[0:3]\n"
"ds_read_b64_tr_b16 v[128:129], v251 offset:0\n"
"ds_read_b64_tr_b16 v[130:131], v251 offset:4096\n"
"v_mfma_f32_16x16x32_bf16 v[4:7], v[160:163], a[116:119], v[4:7]\n"
"ds_read_b64_tr_b16 v[132:133], v252 offset:0\n"
"ds_read_b64_tr_b16 v[134:135], v252 offset:4096\n"
"v_mfma_f32_16x16x32_bf16 v[16:19], v[164:167], a[112:115], v[16:19]\n"
"ds_read_b64_tr_b16 v[136:137], v251 offset:512\n"
"ds_read_b64_tr_b16 v[138:139], v251 offset:4608\n"
"v_mfma_f32_16x16x32_bf16 v[20:23], v[164:167], a[116:119], v[20:23]\n"
"ds_read_b64_tr_b16 v[140:141], v252 offset:512\n"
"ds_read_b64_tr_b16 v[142:143], v252 offset:4608\n"
"v_mfma_f32_16x16x32_bf16 v[32:35], v[168:171], a[112:115], v[32:35]\n"
"ds_read_b64_tr_b16 v[144:145], v251 offset:1024\n"
"ds_read_b64_tr_b16 v[146:147], v251 offset:5120\n"
"v_mfma_f32_16x16x32_bf16 v[36:39], v[168:171], a[116:119], v[36:39]\n"
"ds_read_b64_tr_b16 v[148:149], v252 offset:1024\n"
"ds_read_b64_tr_b16 v[150:151], v252 offset:5120\n"
"v_mfma_f32_16x16x32_bf16 v[48:51], v[172:175], a[112:115], v[48:51]\n"
"ds_read_b64_tr_b16 v[152:153], v251 offset:1536\n"
"ds_read_b64_tr_b16 v[154:155], v251 offset:5632\n"
"v_mfma_f32_16x16x32_bf16 v[52:55], v[172:175], a[116:119], v[52:55]\n"
"ds_read_b64_tr_b16 v[156:157], v252 offset:1536\n"
"ds_read_b64_tr_b16 v[158:159], v252 offset:5632\n"
"v_mfma_f32_16x16x32_bf16 v[64:67], v[176:179], a[112:115], v[64:67]\n"
"v_mfma_f32_16x16x32_bf16 v[68:71], v[176:179], a[116:119], v[68:71]\n"
"v_mfma_f32_16x16x32_bf16 v[80:83], v[180:183], a[112:115], v[80:83]\n"
"v_mfma_f32_16x16x32_bf16 v[84:87], v[180:183], a[116:119], v[84:87]\n"
"v_mfma_f32_16x16x32_bf16 v[96:99], v[184:187], a[112:115], v[96:99]\n"
"v_mfma_f32_16x16x32_bf16 v[100:103], v[184:187], a[116:119], v[100:103]\n"
"v_mfma_f32_16x16x32_bf16 v[112:115], v[188:191], a[112:115], v[112:115]\n"
"v_mfma_f32_16x16x32_bf16 v[116:119], v[188:191], a[116:119], v[116:119]\n"
"s_waitcnt vmcnt(15) lgkmcnt(0)\n"
"s_waitcnt vmcnt(0)\n"
"v_readfirstlane_b32 s63, v229\n"
"s_nop 3\n"
"s_cmp_ge_u32 s63, 16\n"
"s_cbranch_scc1 Lpd_lb\n"
"s_mov_b32 s62, 0\n"
"Lp_lb:\n"
"global_load_dword v229, v228, s[70:71] sc1\n"
"s_waitcnt vmcnt(0)\n"
"v_readfirstlane_b32 s63, v229\n"
"s_nop 3\n"
"s_cmp_ge_u32 s63, 16\n"
"s_cbranch_scc1 Lpd_lb\n"
"s_sleep 1\n"
"s_add_u32 s62, s62, 1\n"
"s_cmp_lt_u32 s62, 65536\n"
"s_cbranch_scc1 Lp_lb\n"
"v_mov_b32 v229, 1\n"
"global_store_dword v228, v229, s[74:75] sc1\n"
"Lpd_lb:\n"
"v_mfma_f32_16x16x32_bf16 v[0:3], v[128:131], a[128:131], v[0:3]\n"
"ds_read_b64_tr_b16 v[160:161], v251 offset:8192\n"
"ds_read_b64_tr_b16 v[162:163], v251 offset:12288\n"
"v_mfma_f32_16x16x32_bf16 v[4:7], v[128:131], a[132:135], v[4:7]\n"
"ds_read_b64_tr_b16 v[164:165], v252 offset:8192\n"
"ds_read_b64_tr_b16 v[166:167], v252 offset:12288\n"
"v_mfma_f32_16x16x32_bf16 v[16:19], v[132:135], a[128:131], v[16:19]\n"
"ds_read_b64_tr_b16 v[168:169], v251 offset:8704\n"
"ds_read_b64_tr_b16 v[170:171], v251 offset:12800\n"
"v_mfma_f32_16x16x32_bf16 v[20:23], v[132:135], a[132:135], v[20:23]\n"
"ds_read_b64_tr_b16 v[172:173], v252 offset:8704\n"
"ds_read_b64_tr_b16 v[174:175], v252 offset:12800\n"
"v_mfma_f32_16x16x32_bf16 v[32:35], v[136:139], a[128:131], v[32:35]\n"
"ds_read_b64_tr_b16 v[176:177], v251 offset:9216\n"
"ds_read_b64_tr_b16 v[178:179], v251 offset:13312\n"
"v_mfma_f32_16x16x32_bf16 v[36:39], v[136:139], a[132:135], v[36:39]\n"
"ds_read_b64_tr_b16 v[180:181], v252 offset:9216\n"
"ds_read_b64_tr_b16 v[182:183], v252 offset:13312\n"
"v_mfma_f32_16x16x32_bf16 v[48:51], v[140:143], a[128:131], v[48:51]\n"
"ds_read_b64_tr_b16 v[184:185], v251 offset:9728\n"
"ds_read_b64_tr_b16 v[186:187], v251 offset:13824\n"
"v_mfma_f32_16x16x32_bf16 v[52:55], v[140:143], a[132:135], v[52:55]\n"
"ds_read_b64_tr_b16 v[188:189], v252 offset:9728\n"
"ds_read_b64_tr_b16 v[190:191], v252 offset:13824\n"
"v_mfma_f32_16x16x32_bf16 v[64:67], v[144:147], a[128:131], v[64:67]\n"
"s_mov_b32 s58, 32768\n"
"buffer_load_dwordx4 a[8:11], v246, s[44:47], s58 offen offset:0 sc1\n"
"v_mfma_f32_16x16x32_bf16 v[68:71], v[144:147], a[132:135], v[68:71]\n"
"v_mfma_f32_16x16x32_bf16 v[80:83], v[148:151], a[128:131], v[80:83]\n"
"s_mov_b32 s58, 49152\n"
"buffer_load_dwordx4 a[12:15], v246, s[44:47], s58 offen offset:0 sc1\n"
"v_mfma_f32_16x16x32_bf16 v[84:87], v[148:151], a[132:135], v[84:87]\n"
"v_mfma_f32_16x16x32_bf16 v[96:99], v[152:155], a[128:131], v[96:99]\n"
"s_mov_b32 s58, 32768\n"
"buffer_load_dwordx4 a[24:27], v246, s[44:47], s58 offen offset:1024 sc1\n"
"v_mfma_f32_16x16x32_bf16 v[100:103], v[152:155], a[132:135], v[100:103]\n"
"v_mfma_f32_16x16x32_bf16 v[112:115], v[156:159], a[128:131], v[112:115]\n"
"s_mov_b32 s58, 49152\n"
"buffer_load_dwordx4 a[28:31], v246, s[44:47], s58 offen offset:1024 sc1\n"
"v_mfma_f32_16x16x32_bf16 v[116:119], v[156:159], a[132:135], v[116:119]\n"
"s_waitcnt vmcnt(17) lgkmcnt(0)\n"
"v_mfma_f32_16x16x32_bf16 v[0:3], v[160:163], a[144:147], v[0:3]\n"
"ds_read_b64_tr_b16 v[128:129], v251 offset:16384\n"
"ds_read_b64_tr_b16 v[130:131], v251 offset:20480\n"
"v_mfma_f32_16x16x32_bf16 v[4:7], v[160:163], a[148:151], v[4:7]\n"
"ds_read_b64_tr_b16 v[132:133], v252 offset:16384\n"
"ds_read_b64_tr_b16 v[134:135], v252 offset:20480\n"
"v_mfma_f32_16x16x32_bf16 v[16:19], v[164:167], a[144:147], v[16:19]\n"
"ds_read_b64_tr_b16 v[136:137], v251 offset:16896\n"
"ds_read_b64_tr_b16 v[138:139], v251 offset:20992\n"
"v_mfma_f32_16x16x32_bf16 v[20:23], v[164:167], a[148:151], v[20:23]\n"
"ds_read_b64_tr_b16 v[140:141], v252 offset:16896\n"
"ds_read_b64_tr_b16 v[142:143], v252 offset:20992\n"
"v_mfma_f32_16x16x32_bf16 v[32:35], v[168:171], a[144:147], v[32:35]\n"
"ds_read_b64_tr_b16 v[144:145], v251 offset:17408\n"
"ds_read_b64_tr_b16 v[146:147], v251 offset:21504\n"
"v_mfma_f32_16x16x32_bf16 v[36:39], v[168:171], a[148:151], v[36:39]\n"
"ds_read_b64_tr_b16 v[148:149], v252 offset:17408\n"
"ds_read_b64_tr_b16 v[150:151], v252 offset:21504\n"
"v_mfma_f32_16x16x32_bf16 v[48:51], v[172:175], a[144:147], v[48:51]\n"
"ds_read_b64_tr_b16 v[152:153], v251 offset:17920\n"
"ds_read_b64_tr_b16 v[154:155], v251 offset:22016\n"
"v_mfma_f32_16x16x32_bf16 v[52:55], v[172:175], a[148:151], v[52:55]\n"
"ds_read_b64_tr_b16 v[156:157], v252 offset:17920\n"
"ds_read_b64_tr_b16 v[158:159], v252 offset:22016\n"
"v_mfma_f32_16x16x32_bf16 v[64:67], v[176:179], a[144:147], v[64:67]\n"
"s_mov_b32 s58, 32768\n"
"buffer_load_dwordx4 a[40:43], v246, s[44:47], s58 offen offset:2048 sc1\n"
"v_mfma_f32_16x16x32_bf16 v[68:71], v[176:179], a[148:151], v[68:71]\n"
"v_mfma_f32_16x16x32_bf16 v[80:83], v[180:183], a[144:147], v[80:83]\n"
"s_mov_b32 s58, 49152\n"
"buffer_load_dwordx4 a[44:47], v246, s[44:47], s58 offen offset:2048 sc1\n"
"v_mfma_f32_16x16x32_bf16 v[84:87], v[180:183], a[148:151], v[84:87]\n"
"v_mfma_f32_16x16x32_bf16 v[96:99], v[184:187], a[144:147], v[96:99]\n"
"s_mov_b32 s58, 32768\n"
"buffer_load_dwordx4 a[56:59], v246, s[44:47], s58 offen offset:3072 sc1\n"
"v_mfma_f32_16x16x32_bf16 v[100:103], v[184:187], a[148:151], v[100:103]\n"
"v_mfma_f32_16x16x32_bf16 v[112:115], v[188:191], a[144:147], v[112:115]\n"
"s_mov_b32 s58, 49152\n"
"buffer_load_dwordx4 a[60:63], v246, s[44:47], s58 offen offset:3072 sc1\n"
"v_mfma_f32_16x16x32_bf16 v[116:119], v[188:191], a[148:151], v[116:119]\n"
"s_waitcnt vmcnt(19) lgkmcnt(0)\n"
"v_mfma_f32_16x16x32_bf16 v[0:3], v[128:131], a[160:163], v[0:3]\n"
"ds_read_b64_tr_b16 v[160:161], v251 offset:24576\n"
"ds_read_b64_tr_b16 v[162:163], v251 offset:28672\n"
"v_mfma_f32_16x16x32_bf16 v[4:7], v[128:131], a[164:167], v[4:7]\n"
"ds_read_b64_tr_b16 v[164:165], v252 offset:24576\n"
"ds_read_b64_tr_b16 v[166:167], v252 offset:28672\n"
"v_mfma_f32_16x16x32_bf16 v[16:19], v[132:135], a[160:163], v[16:19]\n"
"ds_read_b64_tr_b16 v[168:169], v251 offset:25088\n"
"ds_read_b64_tr_b16 v[170:171], v251 offset:29184\n"
"v_mfma_f32_16x16x32_bf16 v[20:23], v[132:135], a[164:167], v[20:23]\n"
"ds_read_b64_tr_b16 v[172:173], v252 offset:25088\n"
"ds_read_b64_tr_b16 v[174:175], v252 offset:29184\n"
"v_mfma_f32_16x16x32_bf16 v[32:35], v[136:139], a[160:163], v[32:35]\n"
"ds_read_b64_tr_b16 v[176:177], v251 offset:25600\n"
"ds_read_b64_tr_b16 v[178:179], v251 offset:29696\n"
"v_mfma_f32_16x16x32_bf16 v[36:39], v[136:139], a[164:167], v[36:39]\n"
"ds_read_b64_tr_b16 v[180:181], v252 offset:25600\n"
"ds_read_b64_tr_b16 v[182:183], v252 offset:29696\n"
"v_mfma_f32_16x16x32_bf16 v[48:51], v[140:143], a[160:163], v[48:51]\n"
"ds_read_b64_tr_b16 v[184:185], v251 offset:26112\n"
"ds_read_b64_tr_b16 v[186:187], v251 offset:30208\n"
"v_mfma_f32_16x16x32_bf16 v[52:55], v[140:143], a[164:167], v[52:55]\n"
"ds_read_b64_tr_b16 v[188:189], v252 offset:26112\n"
"ds_read_b64_tr_b16 v[190:191], v252 offset:30208\n"
"v_mfma_f32_16x16x32_bf16 v[64:67], v[144:147], a[160:163], v[64:67]\n"
"s_mov_b32 s58, 36864\n"
"buffer_load_dwordx4 a[72:75], v246, s[44:47], s58 offen offset:0 sc1\n"
"v_mfma_f32_16x16x32_bf16 v[68:71], v[144:147], a[164:167], v[68:71]\n"
"v_mfma_f32_16x16x32_bf16 v[80:83], v[148:151], a[160:163], v[80:83]\n"
"s_mov_b32 s58, 53248\n"
"buffer_load_dwordx4 a[76:79], v246, s[44:47], s58 offen offset:0 sc1\n"
"v_mfma_f32_16x16x32_bf16 v[84:87], v[148:151], a[164:167], v[84:87]\n"
"v_mfma_f32_16x16x32_bf16 v[96:99], v[152:155], a[160:163], v[96:99]\n"
"s_mov_b32 s58, 36864\n"
"buffer_load_dwordx4 a[88:91], v246, s[44:47], s58 offen offset:1024 sc1\n"
"v_mfma_f32_16x16x32_bf16 v[100:103], v[152:155], a[164:167], v[100:103]\n"
"v_mfma_f32_16x16x32_bf16 v[112:115], v[156:159], a[160:163], v[112:115]\n"
"s_mov_b32 s58, 53248\n"
"buffer_load_dwordx4 a[92:95], v246, s[44:47], s58 offen offset:1024 sc1\n"
"v_mfma_f32_16x16x32_bf16 v[116:119], v[156:159], a[164:167], v[116:119]\n"
"s_waitcnt vmcnt(21) lgkmcnt(0)\n"
"v_mfma_f32_16x16x32_bf16 v[0:3], v[160:163], a[176:179], v[0:3]\n"
"ds_read_b64_tr_b16 v[128:129], v251 offset:32768\n"
"ds_read_b64_tr_b16 v[130:131], v251 offset:36864\n"
"v_mfma_f32_16x16x32_bf16 v[4:7], v[160:163], a[180:183], v[4:7]\n"
"ds_read_b64_tr_b16 v[132:133], v252 offset:32768\n"
"ds_read_b64_tr_b16 v[134:135], v252 offset:36864\n"
"v_mfma_f32_16x16x32_bf16 v[16:19], v[164:167], a[176:179], v[16:19]\n"
"ds_read_b64_tr_b16 v[136:137], v251 offset:33280\n"
"ds_read_b64_tr_b16 v[138:139], v251 offset:37376\n"
"v_mfma_f32_16x16x32_bf16 v[20:23], v[164:167], a[180:183], v[20:23]\n"
"ds_read_b64_tr_b16 v[140:141], v252 offset:33280\n"
"ds_read_b64_tr_b16 v[142:143], v252 offset:37376\n"
"v_mfma_f32_16x16x32_bf16 v[32:35], v[168:171], a[176:179], v[32:35]\n"
"ds_read_b64_tr_b16 v[144:145], v251 offset:33792\n"
"ds_read_b64_tr_b16 v[146:147], v251 offset:37888\n"
"v_mfma_f32_16x16x32_bf16 v[36:39], v[168:171], a[180:183], v[36:39]\n"
"ds_read_b64_tr_b16 v[148:149], v252 offset:33792\n"
"ds_read_b64_tr_b16 v[150:151], v252 offset:37888\n"
"v_mfma_f32_16x16x32_bf16 v[48:51], v[172:175], a[176:179], v[48:51]\n"
"ds_read_b64_tr_b16 v[152:153], v251 offset:34304\n"
"ds_read_b64_tr_b16 v[154:155], v251 offset:38400\n"
"v_mfma_f32_16x16x32_bf16 v[52:55], v[172:175], a[180:183], v[52:55]\n"
"ds_read_b64_tr_b16 v[156:157], v252 offset:34304\n"
"ds_read_b64_tr_b16 v[158:159], v252 offset:38400\n"
"v_mfma_f32_16x16x32_bf16 v[64:67], v[176:179], a[176:179], v[64:67]\n"
"s_mov_b32 s58, 36864\n"
"buffer_load_dwordx4 a[104:107], v246, s[44:47], s58 offen offset:2048 sc1\n"
"v_mfma_f32_16x16x32_bf16 v[68:71], v[176:179], a[180:183], v[68:71]\n"
"v_mfma_f32_16x16x32_bf16 v[80:83], v[180:183], a[176:179], v[80:83]\n"
"s_mov_b32 s58, 53248\n"
"buffer_load_dwordx4 a[108:111], v246, s[44:47], s58 offen offset:2048 sc1\n"
"v_mfma_f32_16x16x32_bf16 v[84:87], v[180:183], a[180:183], v[84:87]\n"
"v_mfma_f32_16x16x32_bf16 v[96:99], v[184:187], a[176:179], v[96:99]\n"
"s_mov_b32 s58, 36864\n"
"buffer_load_dwordx4 a[120:123], v246, s[44:47], s58 offen offset:3072 sc1\n"
"v_mfma_f32_16x16x32_bf16 v[100:103], v[184:187], a[180:183], v[100:103]\n"
"v_mfma_f32_16x16x32_bf16 v[112:115], v[188:191], a[176:179], v[112:115]\n"
"s_mov_b32 s58, 53248\n"
"buffer_load_dwordx4 a[124:127], v246, s[44:47], s58 offen offset:3072 sc1\n"
"v_mfma_f32_16x16x32_bf16 v[116:119], v[188:191], a[180:183], v[116:119]\n"
"s_waitcnt vmcnt(23) lgkmcnt(0)\n"
"v_mfma_f32_16x16x32_bf16 v[0:3], v[128:131], a[192:195], v[0:3]\n"
"ds_read_b64_tr_b16 v[160:161], v251 offset:40960\n"
"ds_read_b64_tr_b16 v[162:163], v251 offset:45056\n"
"v_mfma_f32_16x16x32_bf16 v[4:7], v[128:131], a[196:199], v[4:7]\n"
"ds_read_b64_tr_b16 v[164:165], v252 offset:40960\n"
"ds_read_b64_tr_b16 v[166:167], v252 offset:45056\n"
"v_mfma_f32_16x16x32_bf16 v[16:19], v[132:135], a[192:195], v[16:19]\n"
"ds_read_b64_tr_b16 v[168:169], v251 offset:41472\n"
"ds_read_b64_tr_b16 v[170:171], v251 offset:45568\n"
"v_mfma_f32_16x16x32_bf16 v[20:23], v[132:135], a[196:199], v[20:23]\n"
"ds_read_b64_tr_b16 v[172:173], v252 offset:41472\n"
"ds_read_b64_tr_b16 v[174:175], v252 offset:45568\n"
"v_mfma_f32_16x16x32_bf16 v[32:35], v[136:139], a[192:195], v[32:35]\n"
"ds_read_b64_tr_b16 v[176:177], v251 offset:41984\n"
"ds_read_b64_tr_b16 v[178:179], v251 offset:46080\n"
"v_mfma_f32_16x16x32_bf16 v[36:39], v[136:139], a[196:199], v[36:39]\n"
"ds_read_b64_tr_b16 v[180:181], v252 offset:41984\n"
"ds_read_b64_tr_b16 v[182:183], v252 offset:46080\n"
"v_mfma_f32_16x16x32_bf16 v[48:51], v[140:143], a[192:195], v[48:51]\n"
"ds_read_b64_tr_b16 v[184:185], v251 offset:42496\n"
"ds_read_b64_tr_b16 v[186:187], v251 offset:46592\n"
"v_mfma_f32_16x16x32_bf16 v[52:55], v[140:143], a[196:199], v[52:55]\n"
"ds_read_b64_tr_b16 v[188:189], v252 offset:42496\n"
"ds_read_b64_tr_b16 v[190:191], v252 offset:46592\n"
"v_mfma_f32_16x16x32_bf16 v[64:67], v[144:147], a[192:195], v[64:67]\n"
"s_mov_b32 s58, 40960\n"
"buffer_load_dwordx4 a[136:139], v246, s[44:47], s58 offen offset:0 sc1\n"
"v_mfma_f32_16x16x32_bf16 v[68:71], v[144:147], a[196:199], v[68:71]\n"
"v_mfma_f32_16x16x32_bf16 v[80:83], v[148:151], a[192:195], v[80:83]\n"
"s_mov_b32 s58, 57344\n"
"buffer_load_dwordx4 a[140:143], v246, s[44:47], s58 offen offset:0 sc1\n"
"v_mfma_f32_16x16x32_bf16 v[84:87], v[148:151], a[196:199], v[84:87]\n"
"v_mfma_f32_16x16x32_bf16 v[96:99], v[152:155], a[192:195], v[96:99]\n"
"s_mov_b32 s58, 40960\n"
"buffer_load_dwordx4 a[152:155], v246, s[44:47], s58 offen offset:1024 sc1\n"
"v_mfma_f32_16x16x32_bf16 v[100:103], v[152:155], a[196:199], v[100:103]\n"
"v_mfma_f32_16x16x32_bf16 v[112:115], v[156:159], a[192:195], v[112:115]\n"
"s_mov_b32 s58, 57344\n"
"buffer_load_dwordx4 a[156:159], v246, s[44:47], s58 offen offset:1024 sc1\n"
"v_mfma_f32_16x16x32_bf16 v[116:119], v[156:159], a[196:199], v[116:119]\n"
"s_waitcnt vmcnt(25) lgkmcnt(0)\n"
"v_mfma_f32_16x16x32_bf16 v[0:3], v[160:163], a[208:211], v[0:3]\n"
"ds_read_b64_tr_b16 v[128:129], v251 offset:49152\n"
"ds_read_b64_tr_b16 v[130:131], v251 offset:53248\n"
"v_mfma_f32_16x16x32_bf16 v[4:7], v[160:163], a[212:215], v[4:7]\n"
"ds_read_b64_tr_b16 v[132:133], v252 offset:49152\n"
"ds_read_b64_tr_b16 v[134:135], v252 offset:53248\n"
"v_mfma_f32_16x16x32_bf16 v[16:19], v[164:167], a[208:211], v[16:19]\n"
"ds_read_b64_tr_b16 v[136:137], v251 offset:49664\n"
"ds_read_b64_tr_b16 v[138:139], v251 offset:53760\n"
"v_mfma_f32_16x16x32_bf16 v[20:23], v[164:167], a[212:215], v[20:23]\n"
"ds_read_b64_tr_b16 v[140:141], v252 offset:49664\n"
"ds_read_b64_tr_b16 v[142:143], v252 offset:53760\n"
"v_mfma_f32_16x16x32_bf16 v[32:35], v[168:171], a[208:211], v[32:35]\n"
"ds_read_b64_tr_b16 v[144:145], v251 offset:50176\n"
"ds_read_b64_tr_b16 v[146:147], v251 offset:54272\n"
"v_mfma_f32_16x16x32_bf16 v[36:39], v[168:171], a[212:215], v[36:39]\n"
"ds_read_b64_tr_b16 v[148:149], v252 offset:50176\n"
"ds_read_b64_tr_b16 v[150:151], v252 offset:54272\n"
"v_mfma_f32_16x16x32_bf16 v[48:51], v[172:175], a[208:211], v[48:51]\n"
"ds_read_b64_tr_b16 v[152:153], v251 offset:50688\n"
"ds_read_b64_tr_b16 v[154:155], v251 offset:54784\n"
"v_mfma_f32_16x16x32_bf16 v[52:55], v[172:175], a[212:215], v[52:55]\n"
"ds_read_b64_tr_b16 v[156:157], v252 offset:50688\n"
"ds_read_b64_tr_b16 v[158:159], v252 offset:54784\n"
"v_mfma_f32_16x16x32_bf16 v[64:67], v[176:179], a[208:211], v[64:67]\n"
"s_mov_b32 s58, 40960\n"
"buffer_load_dwordx4 a[168:171], v246, s[44:47], s58 offen offset:2048 sc1\n"
"v_mfma_f32_16x16x32_bf16 v[68:71], v[176:179], a[212:215], v[68:71]\n"
"v_mfma_f32_16x16x32_bf16 v[80:83], v[180:183], a[208:211], v[80:83]\n"
"s_mov_b32 s58, 57344\n"
"buffer_load_dwordx4 a[172:175], v246, s[44:47], s58 offen offset:2048 sc1\n"
"v_mfma_f32_16x16x32_bf16 v[84:87], v[180:183], a[212:215], v[84:87]\n"
"v_mfma_f32_16x16x32_bf16 v[96:99], v[184:187], a[208:211], v[96:99]\n"
"s_mov_b32 s58, 40960\n"
"buffer_load_dwordx4 a[184:187], v246, s[44:47], s58 offen offset:3072 sc1\n"
"v_mfma_f32_16x16x32_bf16 v[100:103], v[184:187], a[212:215], v[100:103]\n"
"v_mfma_f32_16x16x32_bf16 v[112:115], v[188:191], a[208:211], v[112:115]\n"
"s_mov_b32 s58, 57344\n"
"buffer_load_dwordx4 a[188:191], v246, s[44:47], s58 offen offset:3072 sc1\n"
"v_mfma_f32_16x16x32_bf16 v[116:119], v[188:191], a[212:215], v[116:119]\n"
"s_waitcnt vmcnt(27) lgkmcnt(0)\n"
"v_mfma_f32_16x16x32_bf16 v[0:3], v[128:131], a[224:227], v[0:3]\n"
"ds_read_b64_tr_b16 v[160:161], v251 offset:57344\n"
"ds_read_b64_tr_b16 v[162:163], v251 offset:61440\n"
"v_mfma_f32_16x16x32_bf16 v[4:7], v[128:131], a[228:231], v[4:7]\n"
"ds_read_b64_tr_b16 v[164:165], v252 offset:57344\n"
"ds_read_b64_tr_b16 v[166:167], v252 offset:61440\n"
"v_mfma_f32_16x16x32_bf16 v[16:19], v[132:135], a[224:227], v[16:19]\n"
"ds_read_b64_tr_b16 v[168:169], v251 offset:57856\n"
"ds_read_b64_tr_b16 v[170:171], v251 offset:61952\n"
"v_mfma_f32_16x16x32_bf16 v[20:23], v[132:135], a[228:231], v[20:23]\n"
"ds_read_b64_tr_b16 v[172:173], v252 offset:57856\n"
"ds_read_b64_tr_b16 v[174:175], v252 offset:61952\n"
"v_mfma_f32_16x16x32_bf16 v[32:35], v[136:139], a[224:227], v[32:35]\n"
"ds_read_b64_tr_b16 v[176:177], v251 offset:58368\n"
"ds_read_b64_tr_b16 v[178:179], v251 offset:62464\n"
"v_mfma_f32_16x16x32_bf16 v[36:39], v[136:139], a[228:231], v[36:39]\n"
"ds_read_b64_tr_b16 v[180:181], v252 offset:58368\n"
"ds_read_b64_tr_b16 v[182:183], v252 offset:62464\n"
"v_mfma_f32_16x16x32_bf16 v[48:51], v[140:143], a[224:227], v[48:51]\n"
"ds_read_b64_tr_b16 v[184:185], v251 offset:58880\n"
"ds_read_b64_tr_b16 v[186:187], v251 offset:62976\n"
"v_mfma_f32_16x16x32_bf16 v[52:55], v[140:143], a[228:231], v[52:55]\n"
"ds_read_b64_tr_b16 v[188:189], v252 offset:58880\n"
"ds_read_b64_tr_b16 v[190:191], v252 offset:62976\n"
"v_mfma_f32_16x16x32_bf16 v[64:67], v[144:147], a[224:227], v[64:67]\n"
"s_mov_b32 s58, 45056\n"
"buffer_load_dwordx4 a[200:203], v246, s[44:47], s58 offen offset:0 sc1\n"
"v_mfma_f32_16x16x32_bf16 v[68:71], v[144:147], a[228:231], v[68:71]\n"
"v_mfma_f32_16x16x32_bf16 v[80:83], v[148:151], a[224:227], v[80:83]\n"
"s_mov_b32 s58, 61440\n"
"buffer_load_dwordx4 a[204:207], v246, s[44:47], s58 offen offset:0 sc1\n"
"v_mfma_f32_16x16x32_bf16 v[84:87], v[148:151], a[228:231], v[84:87]\n"
"v_mfma_f32_16x16x32_bf16 v[96:99], v[152:155], a[224:227], v[96:99]\n"
"s_mov_b32 s58, 45056\n"
"buffer_load_dwordx4 a[216:219], v246, s[44:47], s58 offen offset:1024 sc1\n"
"v_mfma_f32_16x16x32_bf16 v[100:103], v[152:155], a[228:231], v[100:103]\n"
"v_mfma_f32_16x16x32_bf16 v[112:115], v[156:159], a[224:227], v[112:115]\n"
"s_mov_b32 s58, 61440\n"
"buffer_load_dwordx4 a[220:223], v246, s[44:47], s58 offen offset:1024 sc1\n"
"v_mfma_f32_16x16x32_bf16 v[116:119], v[156:159], a[228:231], v[116:119]\n"
"s_waitcnt vmcnt(29) lgkmcnt(0)\n"
"v_mfma_f32_16x16x32_bf16 v[0:3], v[160:163], a[240:243], v[0:3]\n"
"ds_read_b64_tr_b16 v[128:129], v249 offset:0\n"
"ds_read_b64_tr_b16 v[130:131], v249 offset:4096\n"
"v_mfma_f32_16x16x32_bf16 v[4:7], v[160:163], a[244:247], v[4:7]\n"
"ds_read_b64_tr_b16 v[132:133], v250 offset:0\n"
"ds_read_b64_tr_b16 v[134:135], v250 offset:4096\n"
"v_mfma_f32_16x16x32_bf16 v[16:19], v[164:167], a[240:243], v[16:19]\n"
"ds_read_b64_tr_b16 v[136:137], v249 offset:512\n"
"ds_read_b64_tr_b16 v[138:139], v249 offset:4608\n"
"v_mfma_f32_16x16x32_bf16 v[20:23], v[164:167], a[244:247], v[20:23]\n"
"ds_read_b64_tr_b16 v[140:141], v250 offset:512\n"
"ds_read_b64_tr_b16 v[142:143], v250 offset:4608\n"
"v_mfma_f32_16x16x32_bf16 v[32:35], v[168:171], a[240:243], v[32:35]\n"
"ds_read_b64_tr_b16 v[144:145], v249 offset:1024\n"
"ds_read_b64_tr_b16 v[146:147], v249 offset:5120\n"
"v_mfma_f32_16x16x32_bf16 v[36:39], v[168:171], a[244:247], v[36:39]\n"
"ds_read_b64_tr_b16 v[148:149], v250 offset:1024\n"
"ds_read_b64_tr_b16 v[150:151], v250 offset:5120\n"
"v_mfma_f32_16x16x32_bf16 v[48:51], v[172:175], a[240:243], v[48:51]\n"
"ds_read_b64_tr_b16 v[152:153], v249 offset:1536\n"
"ds_read_b64_tr_b16 v[154:155], v249 offset:5632\n"
"v_mfma_f32_16x16x32_bf16 v[52:55], v[172:175], a[244:247], v[52:55]\n"
"ds_read_b64_tr_b16 v[156:157], v250 offset:1536\n"
"ds_read_b64_tr_b16 v[158:159], v250 offset:5632\n"
"v_mfma_f32_16x16x32_bf16 v[64:67], v[176:179], a[240:243], v[64:67]\n"
"s_mov_b32 s58, 45056\n"
"buffer_load_dwordx4 a[232:235], v246, s[44:47], s58 offen offset:2048 sc1\n"
"v_mfma_f32_16x16x32_bf16 v[68:71], v[176:179], a[244:247], v[68:71]\n"
"v_mfma_f32_16x16x32_bf16 v[80:83], v[180:183], a[240:243], v[80:83]\n"
"s_mov_b32 s58, 61440\n"
"buffer_load_dwordx4 a[236:239], v246, s[44:47], s58 offen offset:2048 sc1\n"
"v_mfma_f32_16x16x32_bf16 v[84:87], v[180:183], a[244:247], v[84:87]\n"
"v_mfma_f32_16x16x32_bf16 v[96:99], v[184:187], a[240:243], v[96:99]\n"
"s_mov_b32 s58, 45056\n"
"buffer_load_dwordx4 a[248:251], v246, s[44:47], s58 offen offset:3072 sc1\n"
"v_mfma_f32_16x16x32_bf16 v[100:103], v[184:187], a[244:247], v[100:103]\n"
"v_mfma_f32_16x16x32_bf16 v[112:115], v[188:191], a[240:243], v[112:115]\n"
"s_mov_b32 s58, 61440\n"
"buffer_load_dwordx4 a[252:255], v246, s[44:47], s58 offen offset:3072 sc1\n"
"v_mfma_f32_16x16x32_bf16 v[116:119], v[188:191], a[244:247], v[116:119]\n"
"s_nop 7\n"
"v_mov_b32 v230, 0xbfb8aa3b\n"
"s_mov_b32 s59, 0\n"
"v_mul_f32 v200, v230, v0\n"
"v_mul_f32 v201, v230, v1\n"
"v_mul_f32 v202, v230, v2\n"
"v_mul_f32 v203, v230, v3\n"
"v_mul_f32 v204, v230, v16\n"
"v_mul_f32 v205, v230, v17\n"
"v_mul_f32 v206, v230, v18\n"
"v_mul_f32 v207, v230, v19\n"
"v_exp_f32 v200, v200\n"
"v_exp_f32 v201, v201\n"
"v_exp_f32 v202, v202\n"
"v_exp_f32 v203, v203\n"
"v_exp_f32 v204, v204\n"
"v_exp_f32 v205, v205\n"
"v_exp_f32 v206, v206\n"
"v_exp_f32 v207, v207\n"
"v_mul_f32 v0, v0, v64\n"
"v_mul_f32 v1, v1, v65\n"
"v_mul_f32 v2, v2, v66\n"
"v_mul_f32 v3, v3, v67\n"
"v_mul_f32 v16, v16, v80\n"
"v_mul_f32 v17, v17, v81\n"
"v_mul_f32 v18, v18, v82\n"
"v_mul_f32 v19, v19, v83\n"
"v_add_f32 v200, 1.0, v200\n"
"v_add_f32 v201, 1.0, v201\n"
"v_add_f32 v202, 1.0, v202\n"
"v_add_f32 v203, 1.0, v203\n"
"v_add_f32 v204, 1.0, v204\n"
"v_add_f32 v205, 1.0, v205\n"
"v_add_f32 v206, 1.0, v206\n"
"v_add_f32 v207, 1.0, v207\n"
"v_rcp_f32 v200, v200\n"
"v_rcp_f32 v201, v201\n"
"v_rcp_f32 v202, v202\n"
"v_rcp_f32 v203, v203\n"
"v_rcp_f32 v204, v204\n"
"v_rcp_f32 v205, v205\n"
"v_rcp_f32 v206, v206\n"
"v_rcp_f32 v207, v207\n"
"s_nop 0\n"
"v_mul_f32 v0, v0, v200\n"
"v_mul_f32 v1, v1, v201\n"
"v_mul_f32 v2, v2, v202\n"
"v_mul_f32 v3, v3, v203\n"
"v_mul_f32 v16, v16, v204\n"
"v_mul_f32 v17, v17, v205\n"
"v_mul_f32 v18, v18, v206\n"
"v_mul_f32 v19, v19, v207\n"
"v_cvt_pk_bf16_f32 v192, v0, v1\n"
"v_cvt_pk_bf16_f32 v193, v2, v3\n"
"v_cvt_pk_bf16_f32 v194, v16, v17\n"
"v_cvt_pk_bf16_f32 v195, v18, v19\n"
"buffer_store_dwordx4 v[192:195], v246, s[48:51], s59 offen offset:0 sc1\n"
"v_mul_f32 v208, v230, v32\n"
"v_mul_f32 v209, v230, v33\n"
"v_mul_f32 v210, v230, v34\n"
"v_mul_f32 v211, v230, v35\n"
"v_mul_f32 v212, v230, v48\n"
"v_mul_f32 v213, v230, v49\n"
"v_mul_f32 v214, v230, v50\n"
"v_mul_f32 v215, v230, v51\n"
"v_exp_f32 v208, v208\n"
"v_exp_f32 v209, v209\n"
"v_exp_f32 v210, v210\n"
"v_exp_f32 v211, v211\n"
"v_exp_f32 v212, v212\n"
"v_exp_f32 v213, v213\n"
"v_exp_f32 v214, v214\n"
"v_exp_f32 v215, v215\n"
"v_mul_f32 v32, v32, v96\n"
"v_mul_f32 v33, v33, v97\n"
"v_mul_f32 v34, v34, v98\n"
"v_mul_f32 v35, v35, v99\n"
"v_mul_f32 v48, v48, v112\n"
"v_mul_f32 v49, v49, v113\n"
"v_mul_f32 v50, v50, v114\n"
"v_mul_f32 v51, v51, v115\n"
"v_add_f32 v208, 1.0, v208\n"
"v_add_f32 v209, 1.0, v209\n"
"v_add_f32 v210, 1.0, v210\n"
"v_add_f32 v211, 1.0, v211\n"
"v_add_f32 v212, 1.0, v212\n"
"v_add_f32 v213, 1.0, v213\n"
"v_add_f32 v214, 1.0, v214\n"
"v_add_f32 v215, 1.0, v215\n"
"v_rcp_f32 v208, v208\n"
"v_rcp_f32 v209, v209\n"
"v_rcp_f32 v210, v210\n"
"v_rcp_f32 v211, v211\n"
"v_rcp_f32 v212, v212\n"
"v_rcp_f32 v213, v213\n"
"v_rcp_f32 v214, v214\n"
"v_rcp_f32 v215, v215\n"
"s_nop 0\n"
"v_mul_f32 v32, v32, v208\n"
"v_mul_f32 v33, v33, v209\n"
"v_mul_f32 v34, v34, v210\n"
"v_mul_f32 v35, v35, v211\n"
"v_mul_f32 v48, v48, v212\n"
"v_mul_f32 v49, v49, v213\n"
"v_mul_f32 v50, v50, v214\n"
"v_mul_f32 v51, v51, v215\n"
"v_cvt_pk_bf16_f32 v196, v32, v33\n"
"v_cvt_pk_bf16_f32 v197, v34, v35\n"
"v_cvt_pk_bf16_f32 v198, v48, v49\n"
"v_cvt_pk_bf16_f32 v199, v50, v51\n"
"buffer_store_dwordx4 v[196:199], v246, s[48:51], s59 offen offset:1024 sc1\n"
"s_mov_b32 s59, 57344\n"
"v_mul_f32 v200, v230, v4\n"
"v_mul_f32 v201, v230, v5\n"
"v_mul_f32 v202, v230, v6\n"
"v_mul_f32 v203, v230, v7\n"
"v_mul_f32 v204, v230, v20\n"
"v_mul_f32 v205, v230, v21\n"
"v_mul_f32 v206, v230, v22\n"
"v_mul_f32 v207, v230, v23\n"
"v_exp_f32 v200, v200\n"
"v_exp_f32 v201, v201\n"
"v_exp_f32 v202, v202\n"
"v_exp_f32 v203, v203\n"
"v_exp_f32 v204, v204\n"
"v_exp_f32 v205, v205\n"
"v_exp_f32 v206, v206\n"
"v_exp_f32 v207, v207\n"
"v_mul_f32 v4, v4, v68\n"
"v_mul_f32 v5, v5, v69\n"
"v_mul_f32 v6, v6, v70\n"
"v_mul_f32 v7, v7, v71\n"
"v_mul_f32 v20, v20, v84\n"
"v_mul_f32 v21, v21, v85\n"
"v_mul_f32 v22, v22, v86\n"
"v_mul_f32 v23, v23, v87\n"
"v_add_f32 v200, 1.0, v200\n"
"v_add_f32 v201, 1.0, v201\n"
"v_add_f32 v202, 1.0, v202\n"
"v_add_f32 v203, 1.0, v203\n"
"v_add_f32 v204, 1.0, v204\n"
"v_add_f32 v205, 1.0, v205\n"
"v_add_f32 v206, 1.0, v206\n"
"v_add_f32 v207, 1.0, v207\n"
"v_rcp_f32 v200, v200\n"
"v_rcp_f32 v201, v201\n"
"v_rcp_f32 v202, v202\n"
"v_rcp_f32 v203, v203\n"
"v_rcp_f32 v204, v204\n"
"v_rcp_f32 v205, v205\n"
"v_rcp_f32 v206, v206\n"
"v_rcp_f32 v207, v207\n"
"s_nop 0\n"
"v_mul_f32 v4, v4, v200\n"
"v_mul_f32 v5, v5, v201\n"
"v_mul_f32 v6, v6, v202\n"
"v_mul_f32 v7, v7, v203\n"
"v_mul_f32 v20, v20, v204\n"
"v_mul_f32 v21, v21, v205\n"
"v_mul_f32 v22, v22, v206\n"
"v_mul_f32 v23, v23, v207\n"
"v_cvt_pk_bf16_f32 v192, v4, v5\n"
"v_cvt_pk_bf16_f32 v193, v6, v7\n"
"v_cvt_pk_bf16_f32 v194, v20, v21\n"
"v_cvt_pk_bf16_f32 v195, v22, v23\n"
"buffer_store_dwordx4 v[192:195], v246, s[48:51], s59 offen offset:0 sc1\n"
"v_mul_f32 v208, v230, v36\n"
"v_mul_f32 v209, v230, v37\n"
"v_mul_f32 v210, v230, v38\n"
"v_mul_f32 v211, v230, v39\n"
"v_mul_f32 v212, v230, v52\n"
"v_mul_f32 v213, v230, v53\n"
"v_mul_f32 v214, v230, v54\n"
"v_mul_f32 v215, v230, v55\n"
"v_exp_f32 v208, v208\n"
"v_exp_f32 v209, v209\n"
"v_exp_f32 v210, v210\n"
"v_exp_f32 v211, v211\n"
"v_exp_f32 v212, v212\n"
"v_exp_f32 v213, v213\n"
"v_exp_f32 v214, v214\n"
"v_exp_f32 v215, v215\n"
"v_mul_f32 v36, v36, v100\n"
"v_mul_f32 v37, v37, v101\n"
"v_mul_f32 v38, v38, v102\n"
"v_mul_f32 v39, v39, v103\n"
"v_mul_f32 v52, v52, v116\n"
"v_mul_f32 v53, v53, v117\n"
"v_mul_f32 v54, v54, v118\n"
"v_mul_f32 v55, v55, v119\n"
"v_add_f32 v208, 1.0, v208\n"
"v_add_f32 v209, 1.0, v209\n"
"v_add_f32 v210, 1.0, v210\n"
"v_add_f32 v211, 1.0, v211\n"
"v_add_f32 v212, 1.0, v212\n"
"v_add_f32 v213, 1.0, v213\n"
"v_add_f32 v214, 1.0, v214\n"
"v_add_f32 v215, 1.0, v215\n"
"v_rcp_f32 v208, v208\n"
"v_rcp_f32 v209, v209\n"
"v_rcp_f32 v210, v210\n"
"v_rcp_f32 v211, v211\n"
"v_rcp_f32 v212, v212\n"
"v_rcp_f32 v213, v213\n"
"v_rcp_f32 v214, v214\n"
"v_rcp_f32 v215, v215\n"
"s_nop 0\n"
"v_mul_f32 v36, v36, v208\n"
"v_mul_f32 v37, v37, v209\n"
"v_mul_f32 v38, v38, v210\n"
"v_mul_f32 v39, v39, v211\n"
"v_mul_f32 v52, v52, v212\n"
"v_mul_f32 v53, v53, v213\n"
"v_mul_f32 v54, v54, v214\n"
"v_mul_f32 v55, v55, v215\n"
"v_cvt_pk_bf16_f32 v196, v36, v37\n"
"v_cvt_pk_bf16_f32 v197, v38, v39\n"
"v_cvt_pk_bf16_f32 v198, v52, v53\n"
"v_cvt_pk_bf16_f32 v199, v54, v55\n"
"buffer_store_dwordx4 v[196:199], v246, s[48:51], s59 offen offset:1024 sc1\n"
"s_waitcnt vmcnt(34) lgkmcnt(0)\n"
"v_mfma_f32_16x16x32_bf16 v[8:11], v[128:131], a[8:11], 0\n"
"ds_read_b64_tr_b16 v[160:161], v249 offset:8192\n"
"ds_read_b64_tr_b16 v[162:163], v249 offset:12288\n"
"v_mfma_f32_16x16x32_bf16 v[12:15], v[128:131], a[12:15], 0\n"
"ds_read_b64_tr_b16 v[164:165], v250 offset:8192\n"
"ds_read_b64_tr_b16 v[166:167], v250 offset:12288\n"
"v_mfma_f32_16x16x32_bf16 v[24:27], v[132:135], a[8:11], 0\n"
"ds_read_b64_tr_b16 v[168:169], v249 offset:8704\n"
"ds_read_b64_tr_b16 v[170:171], v249 offset:12800\n"
"v_mfma_f32_16x16x32_bf16 v[28:31], v[132:135], a[12:15], 0\n"
"ds_read_b64_tr_b16 v[172:173], v250 offset:8704\n"
"ds_read_b64_tr_b16 v[174:175], v250 offset:12800\n"
"v_mfma_f32_16x16x32_bf16 v[40:43], v[136:139], a[8:11], 0\n"
"ds_read_b64_tr_b16 v[176:177], v249 offset:9216\n"
"ds_read_b64_tr_b16 v[178:179], v249 offset:13312\n"
"v_mfma_f32_16x16x32_bf16 v[44:47], v[136:139], a[12:15], 0\n"
"ds_read_b64_tr_b16 v[180:181], v250 offset:9216\n"
"ds_read_b64_tr_b16 v[182:183], v250 offset:13312\n"
"v_mfma_f32_16x16x32_bf16 v[56:59], v[140:143], a[8:11], 0\n"
"ds_read_b64_tr_b16 v[184:185], v249 offset:9728\n"
"ds_read_b64_tr_b16 v[186:187], v249 offset:13824\n"
"v_mfma_f32_16x16x32_bf16 v[60:63], v[140:143], a[12:15], 0\n"
"ds_read_b64_tr_b16 v[188:189], v250 offset:9728\n"
"ds_read_b64_tr_b16 v[190:191], v250 offset:13824\n"
"v_mfma_f32_16x16x32_bf16 v[72:75], v[144:147], a[8:11], 0\n"
"v_mfma_f32_16x16x32_bf16 v[76:79], v[144:147], a[12:15], 0\n"
"v_mfma_f32_16x16x32_bf16 v[88:91], v[148:151], a[8:11], 0\n"
"v_mfma_f32_16x16x32_bf16 v[92:95], v[148:151], a[12:15], 0\n"
"v_mfma_f32_16x16x32_bf16 v[104:107], v[152:155], a[8:11], 0\n"
"v_mfma_f32_16x16x32_bf16 v[108:111], v[152:155], a[12:15], 0\n"
"v_mfma_f32_16x16x32_bf16 v[120:123], v[156:159], a[8:11], 0\n"
"v_mfma_f32_16x16x32_bf16 v[124:127], v[156:159], a[12:15], 0\n"
"s_waitcnt vmcnt(32) lgkmcnt(0)\n"
"v_mfma_f32_16x16x32_bf16 v[8:11], v[160:163], a[24:27], v[8:11]\n"
"ds_read_b64_tr_b16 v[128:129], v249 offset:16384\n"
"ds_read_b64_tr_b16 v[130:131], v249 offset:20480\n"
"v_mfma_f32_16x16x32_bf16 v[12:15], v[160:163], a[28:31], v[12:15]\n"
"ds_read_b64_tr_b16 v[132:133], v250 offset:16384\n"
"ds_read_b64_tr_b16 v[134:135], v250 offset:20480\n"
"v_mfma_f32_16x16x32_bf16 v[24:27], v[164:167], a[24:27], v[24:27]\n"
"ds_read_b64_tr_b16 v[136:137], v249 offset:16896\n"
"ds_read_b64_tr_b16 v[138:139], v249 offset:20992\n"
"v_mfma_f32_16x16x32_bf16 v[28:31], v[164:167], a[28:31], v[28:31]\n"
"ds_read_b64_tr_b16 v[140:141], v250 offset:16896\n"
"ds_read_b64_tr_b16 v[142:143], v250 offset:20992\n"
"v_mfma_f32_16x16x32_bf16 v[40:43], v[168:171], a[24:27], v[40:43]\n"
"ds_read_b64_tr_b16 v[144:145], v249 offset:17408\n"
"ds_read_b64_tr_b16 v[146:147], v249 offset:21504\n"
"v_mfma_f32_16x16x32_bf16 v[44:47], v[168:171], a[28:31], v[44:47]\n"
"ds_read_b64_tr_b16 v[148:149], v250 offset:17408\n"
"ds_read_b64_tr_b16 v[150:151], v250 offset:21504\n"
"v_mfma_f32_16x16x32_bf16 v[56:59], v[172:175], a[24:27], v[56:59]\n"
"ds_read_b64_tr_b16 v[152:153], v249 offset:17920\n"
"ds_read_b64_tr_b16 v[154:155], v249 offset:22016\n"
"v_mfma_f32_16x16x32_bf16 v[60:63], v[172:175], a[28:31], v[60:63]\n"
"ds_read_b64_tr_b16 v[156:157], v250 offset:17920\n"
"ds_read_b64_tr_b16 v[158:159], v250 offset:22016\n"
"v_mfma_f32_16x16x32_bf16 v[72:75], v[176:179], a[24:27], v[72:75]\n"
"v_mfma_f32_16x16x32_bf16 v[76:79], v[176:179], a[28:31], v[76:79]\n"
"v_mfma_f32_16x16x32_bf16 v[88:91], v[180:183], a[24:27], v[88:91]\n"
"v_mfma_f32_16x16x32_bf16 v[92:95], v[180:183], a[28:31], v[92:95]\n"
"v_mfma_f32_16x16x32_bf16 v[104:107], v[184:187], a[24:27], v[104:107]\n"
"v_mfma_f32_16x16x32_bf16 v[108:111], v[184:187], a[28:31], v[108:111]\n"
"v_mfma_f32_16x16x32_bf16 v[120:123], v[188:191], a[24:27], v[120:123]\n"
"v_mfma_f32_16x16x32_bf16 v[124:127], v[188:191], a[28:31], v[124:127]\n"
"s_waitcnt vmcnt(30) lgkmcnt(0)\n"
"v_mfma_f32_16x16x32_bf16 v[8:11], v[128:131], a[40:43], v[8:11]\n"
"ds_read_b64_tr_b16 v[160:161], v249 offset:24576\n"
"ds_read_b64_tr_b16 v[162:163], v249 offset:28672\n"
"v_mfma_f32_16x16x32_bf16 v[12:15], v[128:131], a[44:47], v[12:15]\n"
"ds_read_b64_tr_b16 v[164:165], v250 offset:24576\n"
"ds_read_b64_tr_b16 v[166:167], v250 offset:28672\n"
"v_mfma_f32_16x16x32_bf16 v[24:27], v[132:135], a[40:43], v[24:27]\n"
"ds_read_b64_tr_b16 v[168:169], v249 offset:25088\n"
"ds_read_b64_tr_b16 v[170:171], v249 offset:29184\n"
"v_mfma_f32_16x16x32_bf16 v[28:31], v[132:135], a[44:47], v[28:31]\n"
"ds_read_b64_tr_b16 v[172:173], v250 offset:25088\n"
"ds_read_b64_tr_b16 v[174:175], v250 offset:29184\n"
"v_mfma_f32_16x16x32_bf16 v[40:43], v[136:139], a[40:43], v[40:43]\n"
"ds_read_b64_tr_b16 v[176:177], v249 offset:25600\n"
"ds_read_b64_tr_b16 v[178:179], v249 offset:29696\n"
"v_mfma_f32_16x16x32_bf16 v[44:47], v[136:139], a[44:47], v[44:47]\n"
"ds_read_b64_tr_b16 v[180:181], v250 offset:25600\n"
"ds_read_b64_tr_b16 v[182:183], v250 offset:29696\n"
"v_mfma_f32_16x16x32_bf16 v[56:59], v[140:143], a[40:43], v[56:59]\n"
"ds_read_b64_tr_b16 v[184:185], v249 offset:26112\n"
"ds_read_b64_tr_b16 v[186:187], v249 offset:30208\n"
"v_mfma_f32_16x16x32_bf16 v[60:63], v[140:143], a[44:47], v[60:63]\n"
"ds_read_b64_tr_b16 v[188:189], v250 offset:26112\n"
"ds_read_b64_tr_b16 v[190:191], v250 offset:30208\n"
"v_mfma_f32_16x16x32_bf16 v[72:75], v[144:147], a[40:43], v[72:75]\n"
"v_mfma_f32_16x16x32_bf16 v[76:79], v[144:147], a[44:47], v[76:79]\n"
"v_mfma_f32_16x16x32_bf16 v[88:91], v[148:151], a[40:43], v[88:91]\n"
"v_mfma_f32_16x16x32_bf16 v[92:95], v[148:151], a[44:47], v[92:95]\n"
"v_mfma_f32_16x16x32_bf16 v[104:107], v[152:155], a[40:43], v[104:107]\n"
"v_mfma_f32_16x16x32_bf16 v[108:111], v[152:155], a[44:47], v[108:111]\n"
"v_mfma_f32_16x16x32_bf16 v[120:123], v[156:159], a[40:43], v[120:123]\n"
"v_mfma_f32_16x16x32_bf16 v[124:127], v[156:159], a[44:47], v[124:127]\n"
"s_waitcnt vmcnt(28) lgkmcnt(0)\n"
"v_mfma_f32_16x16x32_bf16 v[8:11], v[160:163], a[56:59], v[8:11]\n"
"ds_read_b64_tr_b16 v[128:129], v249 offset:32768\n"
"ds_read_b64_tr_b16 v[130:131], v249 offset:36864\n"
"v_mfma_f32_16x16x32_bf16 v[12:15], v[160:163], a[60:63], v[12:15]\n"
"ds_read_b64_tr_b16 v[132:133], v250 offset:32768\n"
"ds_read_b64_tr_b16 v[134:135], v250 offset:36864\n"
"v_mfma_f32_16x16x32_bf16 v[24:27], v[164:167], a[56:59], v[24:27]\n"
"ds_read_b64_tr_b16 v[136:137], v249 offset:33280\n"
"ds_read_b64_tr_b16 v[138:139], v249 offset:37376\n"
"v_mfma_f32_16x16x32_bf16 v[28:31], v[164:167], a[60:63], v[28:31]\n"
"ds_read_b64_tr_b16 v[140:141], v250 offset:33280\n"
"ds_read_b64_tr_b16 v[142:143], v250 offset:37376\n"
"v_mfma_f32_16x16x32_bf16 v[40:43], v[168:171], a[56:59], v[40:43]\n"
"ds_read_b64_tr_b16 v[144:145], v249 offset:33792\n"
"ds_read_b64_tr_b16 v[146:147], v249 offset:37888\n"
"v_mfma_f32_16x16x32_bf16 v[44:47], v[168:171], a[60:63], v[44:47]\n"
"ds_read_b64_tr_b16 v[148:149], v250 offset:33792\n"
"ds_read_b64_tr_b16 v[150:151], v250 offset:37888\n"
"v_mfma_f32_16x16x32_bf16 v[56:59], v[172:175], a[56:59], v[56:59]\n"
"ds_read_b64_tr_b16 v[152:153], v249 offset:34304\n"
"ds_read_b64_tr_b16 v[154:155], v249 offset:38400\n"
"v_mfma_f32_16x16x32_bf16 v[60:63], v[172:175], a[60:63], v[60:63]\n"
"ds_read_b64_tr_b16 v[156:157], v250 offset:34304\n"
"ds_read_b64_tr_b16 v[158:159], v250 offset:38400\n"
"v_mfma_f32_16x16x32_bf16 v[72:75], v[176:179], a[56:59], v[72:75]\n"
"v_mfma_f32_16x16x32_bf16 v[76:79], v[176:179], a[60:63], v[76:79]\n"
"v_mfma_f32_16x16x32_bf16 v[88:91], v[180:183], a[56:59], v[88:91]\n"
"v_mfma_f32_16x16x32_bf16 v[92:95], v[180:183], a[60:63], v[92:95]\n"
"v_mfma_f32_16x16x32_bf16 v[104:107], v[184:187], a[56:59], v[104:107]\n"
"v_mfma_f32_16x16x32_bf16 v[108:111], v[184:187], a[60:63], v[108:111]\n"
"v_mfma_f32_16x16x32_bf16 v[120:123], v[188:191], a[56:59], v[120:123]\n"
"v_mfma_f32_16x16x32_bf16 v[124:127], v[188:191], a[60:63], v[124:127]\n"
"s_waitcnt vmcnt(26) lgkmcnt(0)\n"
"v_mfma_f32_16x16x32_bf16 v[8:11], v[128:131], a[72:75], v[8:11]\n"
"ds_read_b64_tr_b16 v[160:161], v249 offset:40960\n"
"ds_read_b64_tr_b16 v[162:163], v249 offset:45056\n"
"v_mfma_f32_16x16x32_bf16 v[12:15], v[128:131], a[76:79], v[12:15]\n"
"ds_read_b64_tr_b16 v[164:165], v250 offset:40960\n"
"ds_read_b64_tr_b16 v[166:167], v250 offset:45056\n"
"v_mfma_f32_16x16x32_bf16 v[24:27], v[132:135], a[72:75], v[24:27]\n"
"ds_read_b64_tr_b16 v[168:169], v249 offset:41472\n"
"ds_read_b64_tr_b16 v[170:171], v249 offset:45568\n"
"v_mfma_f32_16x16x32_bf16 v[28:31], v[132:135], a[76:79], v[28:31]\n"
"ds_read_b64_tr_b16 v[172:173], v250 offset:41472\n"
"ds_read_b64_tr_b16 v[174:175], v250 offset:45568\n"
"v_mfma_f32_16x16x32_bf16 v[40:43], v[136:139], a[72:75], v[40:43]\n"
"ds_read_b64_tr_b16 v[176:177], v249 offset:41984\n"
"ds_read_b64_tr_b16 v[178:179], v249 offset:46080\n"
"v_mfma_f32_16x16x32_bf16 v[44:47], v[136:139], a[76:79], v[44:47]\n"
"ds_read_b64_tr_b16 v[180:181], v250 offset:41984\n"
"ds_read_b64_tr_b16 v[182:183], v250 offset:46080\n"
"v_mfma_f32_16x16x32_bf16 v[56:59], v[140:143], a[72:75], v[56:59]\n"
"ds_read_b64_tr_b16 v[184:185], v249 offset:42496\n"
"ds_read_b64_tr_b16 v[186:187], v249 offset:46592\n"
"v_mfma_f32_16x16x32_bf16 v[60:63], v[140:143], a[76:79], v[60:63]\n"
"ds_read_b64_tr_b16 v[188:189], v250 offset:42496\n"
"ds_read_b64_tr_b16 v[190:191], v250 offset:46592\n"
"v_mfma_f32_16x16x32_bf16 v[72:75], v[144:147], a[72:75], v[72:75]\n"
"v_mfma_f32_16x16x32_bf16 v[76:79], v[144:147], a[76:79], v[76:79]\n"
"v_mfma_f32_16x16x32_bf16 v[88:91], v[148:151], a[72:75], v[88:91]\n"
"v_mfma_f32_16x16x32_bf16 v[92:95], v[148:151], a[76:79], v[92:95]\n"
"v_mfma_f32_16x16x32_bf16 v[104:107], v[152:155], a[72:75], v[104:107]\n"
"v_mfma_f32_16x16x32_bf16 v[108:111], v[152:155], a[76:79], v[108:111]\n"
"v_mfma_f32_16x16x32_bf16 v[120:123], v[156:159], a[72:75], v[120:123]\n"
"v_mfma_f32_16x16x32_bf16 v[124:127], v[156:159], a[76:79], v[124:127]\n"
"s_waitcnt vmcnt(24) lgkmcnt(0)\n"
"v_mfma_f32_16x16x32_bf16 v[8:11], v[160:163], a[88:91], v[8:11]\n"
"ds_read_b64_tr_b16 v[128:129], v249 offset:49152\n"
"ds_read_b64_tr_b16 v[130:131], v249 offset:53248\n"
"v_mfma_f32_16x16x32_bf16 v[12:15], v[160:163], a[92:95], v[12:15]\n"
"ds_read_b64_tr_b16 v[132:133], v250 offset:49152\n"
"ds_read_b64_tr_b16 v[134:135], v250 offset:53248\n"
"v_mfma_f32_16x16x32_bf16 v[24:27], v[164:167], a[88:91], v[24:27]\n"
"ds_read_b64_tr_b16 v[136:137], v249 offset:49664\n"
"ds_read_b64_tr_b16 v[138:139], v249 offset:53760\n"
"v_mfma_f32_16x16x32_bf16 v[28:31], v[164:167], a[92:95], v[28:31]\n"
"ds_read_b64_tr_b16 v[140:141], v250 offset:49664\n"
"ds_read_b64_tr_b16 v[142:143], v250 offset:53760\n"
"v_mfma_f32_16x16x32_bf16 v[40:43], v[168:171], a[88:91], v[40:43]\n"
"ds_read_b64_tr_b16 v[144:145], v249 offset:50176\n"
"ds_read_b64_tr_b16 v[146:147], v249 offset:54272\n"
"v_mfma_f32_16x16x32_bf16 v[44:47], v[168:171], a[92:95], v[44:47]\n"
"ds_read_b64_tr_b16 v[148:149], v250 offset:50176\n"
"ds_read_b64_tr_b16 v[150:151], v250 offset:54272\n"
"v_mfma_f32_16x16x32_bf16 v[56:59], v[172:175], a[88:91], v[56:59]\n"
"ds_read_b64_tr_b16 v[152:153], v249 offset:50688\n"
"ds_read_b64_tr_b16 v[154:155], v249 offset:54784\n"
"v_mfma_f32_16x16x32_bf16 v[60:63], v[172:175], a[92:95], v[60:63]\n"
"ds_read_b64_tr_b16 v[156:157], v250 offset:50688\n"
"ds_read_b64_tr_b16 v[158:159], v250 offset:54784\n"
"v_mfma_f32_16x16x32_bf16 v[72:75], v[176:179], a[88:91], v[72:75]\n"
"v_mfma_f32_16x16x32_bf16 v[76:79], v[176:179], a[92:95], v[76:79]\n"
"v_mfma_f32_16x16x32_bf16 v[88:91], v[180:183], a[88:91], v[88:91]\n"
"v_mfma_f32_16x16x32_bf16 v[92:95], v[180:183], a[92:95], v[92:95]\n"
"v_mfma_f32_16x16x32_bf16 v[104:107], v[184:187], a[88:91], v[104:107]\n"
"v_mfma_f32_16x16x32_bf16 v[108:111], v[184:187], a[92:95], v[108:111]\n"
"v_mfma_f32_16x16x32_bf16 v[120:123], v[188:191], a[88:91], v[120:123]\n"
"v_mfma_f32_16x16x32_bf16 v[124:127], v[188:191], a[92:95], v[124:127]\n"
"s_waitcnt vmcnt(22) lgkmcnt(0)\n"
"v_mfma_f32_16x16x32_bf16 v[8:11], v[128:131], a[104:107], v[8:11]\n"
"ds_read_b64_tr_b16 v[160:161], v249 offset:57344\n"
"ds_read_b64_tr_b16 v[162:163], v249 offset:61440\n"
"v_mfma_f32_16x16x32_bf16 v[12:15], v[128:131], a[108:111], v[12:15]\n"
"ds_read_b64_tr_b16 v[164:165], v250 offset:57344\n"
"ds_read_b64_tr_b16 v[166:167], v250 offset:61440\n"
"v_mfma_f32_16x16x32_bf16 v[24:27], v[132:135], a[104:107], v[24:27]\n"
"ds_read_b64_tr_b16 v[168:169], v249 offset:57856\n"
"ds_read_b64_tr_b16 v[170:171], v249 offset:61952\n"
"v_mfma_f32_16x16x32_bf16 v[28:31], v[132:135], a[108:111], v[28:31]\n"
"ds_read_b64_tr_b16 v[172:173], v250 offset:57856\n"
"ds_read_b64_tr_b16 v[174:175], v250 offset:61952\n"
"v_mfma_f32_16x16x32_bf16 v[40:43], v[136:139], a[104:107], v[40:43]\n"
"ds_read_b64_tr_b16 v[176:177], v249 offset:58368\n"
"ds_read_b64_tr_b16 v[178:179], v249 offset:62464\n"
"v_mfma_f32_16x16x32_bf16 v[44:47], v[136:139], a[108:111], v[44:47]\n"
"ds_read_b64_tr_b16 v[180:181], v250 offset:58368\n"
"ds_read_b64_tr_b16 v[182:183], v250 offset:62464\n"
"v_mfma_f32_16x16x32_bf16 v[56:59], v[140:143], a[104:107], v[56:59]\n"
"ds_read_b64_tr_b16 v[184:185], v249 offset:58880\n"
"ds_read_b64_tr_b16 v[186:187], v249 offset:62976\n"
"v_mfma_f32_16x16x32_bf16 v[60:63], v[140:143], a[108:111], v[60:63]\n"
"ds_read_b64_tr_b16 v[188:189], v250 offset:58880\n"
"ds_read_b64_tr_b16 v[190:191], v250 offset:62976\n"
"v_mfma_f32_16x16x32_bf16 v[72:75], v[144:147], a[104:107], v[72:75]\n"
"v_mfma_f32_16x16x32_bf16 v[76:79], v[144:147], a[108:111], v[76:79]\n"
"v_mfma_f32_16x16x32_bf16 v[88:91], v[148:151], a[104:107], v[88:91]\n"
"v_mfma_f32_16x16x32_bf16 v[92:95], v[148:151], a[108:111], v[92:95]\n"
"v_mfma_f32_16x16x32_bf16 v[104:107], v[152:155], a[104:107], v[104:107]\n"
"v_mfma_f32_16x16x32_bf16 v[108:111], v[152:155], a[108:111], v[108:111]\n"
"v_mfma_f32_16x16x32_bf16 v[120:123], v[156:159], a[104:107], v[120:123]\n"
"v_mfma_f32_16x16x32_bf16 v[124:127], v[156:159], a[108:111], v[124:127]\n"
"s_waitcnt vmcnt(20) lgkmcnt(0)\n"
"v_mfma_f32_16x16x32_bf16 v[8:11], v[160:163], a[120:123], v[8:11]\n"
"ds_read_b64_tr_b16 v[128:129], v251 offset:0\n"
"ds_read_b64_tr_b16 v[130:131], v251 offset:4096\n"
"v_mfma_f32_16x16x32_bf16 v[12:15], v[160:163], a[124:127], v[12:15]\n"
"ds_read_b64_tr_b16 v[132:133], v252 offset:0\n"
"ds_read_b64_tr_b16 v[134:135], v252 offset:4096\n"
"v_mfma_f32_16x16x32_bf16 v[24:27], v[164:167], a[120:123], v[24:27]\n"
"ds_read_b64_tr_b16 v[136:137], v251 offset:512\n"
"ds_read_b64_tr_b16 v[138:139], v251 offset:4608\n"
"v_mfma_f32_16x16x32_bf16 v[28:31], v[164:167], a[124:127], v[28:31]\n"
"ds_read_b64_tr_b16 v[140:141], v252 offset:512\n"
"ds_read_b64_tr_b16 v[142:143], v252 offset:4608\n"
"v_mfma_f32_16x16x32_bf16 v[40:43], v[168:171], a[120:123], v[40:43]\n"
"ds_read_b64_tr_b16 v[144:145], v251 offset:1024\n"
"ds_read_b64_tr_b16 v[146:147], v251 offset:5120\n"
"v_mfma_f32_16x16x32_bf16 v[44:47], v[168:171], a[124:127], v[44:47]\n"
"ds_read_b64_tr_b16 v[148:149], v252 offset:1024\n"
"ds_read_b64_tr_b16 v[150:151], v252 offset:5120\n"
"v_mfma_f32_16x16x32_bf16 v[56:59], v[172:175], a[120:123], v[56:59]\n"
"ds_read_b64_tr_b16 v[152:153], v251 offset:1536\n"
"ds_read_b64_tr_b16 v[154:155], v251 offset:5632\n"
"v_mfma_f32_16x16x32_bf16 v[60:63], v[172:175], a[124:127], v[60:63]\n"
"ds_read_b64_tr_b16 v[156:157], v252 offset:1536\n"
"ds_read_b64_tr_b16 v[158:159], v252 offset:5632\n"
"v_mfma_f32_16x16x32_bf16 v[72:75], v[176:179], a[120:123], v[72:75]\n"
"v_mfma_f32_16x16x32_bf16 v[76:79], v[176:179], a[124:127], v[76:79]\n"
"v_mfma_f32_16x16x32_bf16 v[88:91], v[180:183], a[120:123], v[88:91]\n"
"v_mfma_f32_16x16x32_bf16 v[92:95], v[180:183], a[124:127], v[92:95]\n"
"v_mfma_f32_16x16x32_bf16 v[104:107], v[184:187], a[120:123], v[104:107]\n"
"v_mfma_f32_16x16x32_bf16 v[108:111], v[184:187], a[124:127], v[108:111]\n"
"v_mfma_f32_16x16x32_bf16 v[120:123], v[188:191], a[120:123], v[120:123]\n"
"v_mfma_f32_16x16x32_bf16 v[124:127], v[188:191], a[124:127], v[124:127]\n"
"s_waitcnt vmcnt(18) lgkmcnt(0)\n"
"v_mfma_f32_16x16x32_bf16 v[8:11], v[128:131], a[136:139], v[8:11]\n"
"ds_read_b64_tr_b16 v[160:161], v251 offset:8192\n"
"ds_read_b64_tr_b16 v[162:163], v251 offset:12288\n"
"v_mfma_f32_16x16x32_bf16 v[12:15], v[128:131], a[140:143], v[12:15]\n"
"ds_read_b64_tr_b16 v[164:165], v252 offset:8192\n"
"ds_read_b64_tr_b16 v[166:167], v252 offset:12288\n"
"v_mfma_f32_16x16x32_bf16 v[24:27], v[132:135], a[136:139], v[24:27]\n"
"ds_read_b64_tr_b16 v[168:169], v251 offset:8704\n"
"ds_read_b64_tr_b16 v[170:171], v251 offset:12800\n"
"v_mfma_f32_16x16x32_bf16 v[28:31], v[132:135], a[140:143], v[28:31]\n"
"ds_read_b64_tr_b16 v[172:173], v252 offset:8704\n"
"ds_read_b64_tr_b16 v[174:175], v252 offset:12800\n"
"v_mfma_f32_16x16x32_bf16 v[40:43], v[136:139], a[136:139], v[40:43]\n"
"ds_read_b64_tr_b16 v[176:177], v251 offset:9216\n"
"ds_read_b64_tr_b16 v[178:179], v251 offset:13312\n"
"v_mfma_f32_16x16x32_bf16 v[44:47], v[136:139], a[140:143], v[44:47]\n"
"ds_read_b64_tr_b16 v[180:181], v252 offset:9216\n"
"ds_read_b64_tr_b16 v[182:183], v252 offset:13312\n"
"v_mfma_f32_16x16x32_bf16 v[56:59], v[140:143], a[136:139], v[56:59]\n"
"ds_read_b64_tr_b16 v[184:185], v251 offset:9728\n"
"ds_read_b64_tr_b16 v[186:187], v251 offset:13824\n"
"v_mfma_f32_16x16x32_bf16 v[60:63], v[140:143], a[140:143], v[60:63]\n"
"ds_read_b64_tr_b16 v[188:189], v252 offset:9728\n"
"ds_read_b64_tr_b16 v[190:191], v252 offset:13824\n"
"v_mfma_f32_16x16x32_bf16 v[72:75], v[144:147], a[136:139], v[72:75]\n"
"v_mfma_f32_16x16x32_bf16 v[76:79], v[144:147], a[140:143], v[76:79]\n"
"v_mfma_f32_16x16x32_bf16 v[88:91], v[148:151], a[136:139], v[88:91]\n"
"v_mfma_f32_16x16x32_bf16 v[92:95], v[148:151], a[140:143], v[92:95]\n"
"v_mfma_f32_16x16x32_bf16 v[104:107], v[152:155], a[136:139], v[104:107]\n"
"v_mfma_f32_16x16x32_bf16 v[108:111], v[152:155], a[140:143], v[108:111]\n"
"v_mfma_f32_16x16x32_bf16 v[120:123], v[156:159], a[136:139], v[120:123]\n"
"v_mfma_f32_16x16x32_bf16 v[124:127], v[156:159], a[140:143], v[124:127]\n"
"s_waitcnt vmcnt(16) lgkmcnt(0)\n"
"s_waitcnt vmcnt(0)\n"
"s_barrier\n"
"s_cmp_eq_u32 s56, 0\n"
"s_cbranch_scc0 Lsig_ua\n"
"s_mov_b64 s[64:65], exec\n"
"s_mov_b64 exec, 1\n"
"v_mov_b32 v238, 0\n"
"v_mov_b32 v239, 1\n"
"global_atomic_add v238, v239, s[72:73]\n"
"s_mov_b64 exec, s[64:65]\n"
"Lsig_ua:\n"
"s_cmp_eq_u32 s56, 0\n"
"s_cbranch_scc0 Lsig_x1\n"
"s_mov_b64 s[64:65], exec\n"
"s_mov_b64 exec, 1\n"
"v_mov_b32 v238, 0\n"
"v_mov_b32 v239, 1\n"
"global_atomic_add v238, v239, s[78:79]\n"
"s_mov_b64 exec, s[64:65]\n"
"Lsig_x1:\n"
"v_mfma_f32_16x16x32_bf16 v[8:11], v[160:163], a[152:155], v[8:11]\n"
"ds_read_b64_tr_b16 v[128:129], v251 offset:16384\n"
"ds_read_b64_tr_b16 v[130:131], v251 offset:20480\n"
"v_mfma_f32_16x16x32_bf16 v[12:15], v[160:163], a[156:159], v[12:15]\n"
"ds_read_b64_tr_b16 v[132:133], v252 offset:16384\n"
"ds_read_b64_tr_b16 v[134:135], v252 offset:20480\n"
"v_mfma_f32_16x16x32_bf16 v[24:27], v[164:167], a[152:155], v[24:27]\n"
"ds_read_b64_tr_b16 v[136:137], v251 offset:16896\n"
"ds_read_b64_tr_b16 v[138:139], v251 offset:20992\n"
"v_mfma_f32_16x16x32_bf16 v[28:31], v[164:167], a[156:159], v[28:31]\n"
"ds_read_b64_tr_b16 v[140:141], v252 offset:16896\n"
"ds_read_b64_tr_b16 v[142:143], v252 offset:20992\n"
"v_mfma_f32_16x16x32_bf16 v[40:43], v[168:171], a[152:155], v[40:43]\n"
"ds_read_b64_tr_b16 v[144:145], v251 offset:17408\n"
"ds_read_b64_tr_b16 v[146:147], v251 offset:21504\n"
"v_mfma_f32_16x16x32_bf16 v[44:47], v[168:171], a[156:159], v[44:47]\n"
"ds_read_b64_tr_b16 v[148:149], v252 offset:17408\n"
"ds_read_b64_tr_b16 v[150:151], v252 offset:21504\n"
"v_mfma_f32_16x16x32_bf16 v[56:59], v[172:175], a[152:155], v[56:59]\n"
"ds_read_b64_tr_b16 v[152:153], v251 offset:17920\n"
"ds_read_b64_tr_b16 v[154:155], v251 offset:22016\n"
"v_mfma_f32_16x16x32_bf16 v[60:63], v[172:175], a[156:159], v[60:63]\n"
"ds_read_b64_tr_b16 v[156:157], v252 offset:17920\n"
"ds_read_b64_tr_b16 v[158:159], v252 offset:22016\n"
"v_mfma_f32_16x16x32_bf16 v[72:75], v[176:179], a[152:155], v[72:75]\n"
"v_mfma_f32_16x16x32_bf16 v[76:79], v[176:179], a[156:159], v[76:79]\n"
"v_mfma_f32_16x16x32_bf16 v[88:91], v[180:183], a[152:155], v[88:91]\n"
"v_mfma_f32_16x16x32_bf16 v[92:95], v[180:183], a[156:159], v[92:95]\n"
"v_mfma_f32_16x16x32_bf16 v[104:107], v[184:187], a[152:155], v[104:107]\n"
"v_mfma_f32_16x16x32_bf16 v[108:111], v[184:187], a[156:159], v[108:111]\n"
"v_mfma_f32_16x16x32_bf16 v[120:123], v[188:191], a[152:155], v[120:123]\n"
"v_mfma_f32_16x16x32_bf16 v[124:127], v[188:191], a[156:159], v[124:127]\n"
"s_waitcnt vmcnt(14) lgkmcnt(0)\n"
"v_mfma_f32_16x16x32_bf16 v[8:11], v[128:131], a[168:171], v[8:11]\n"
"ds_read_b64_tr_b16 v[160:161], v251 offset:24576\n"
"ds_read_b64_tr_b16 v[162:163], v251 offset:28672\n"
"v_mfma_f32_16x16x32_bf16 v[12:15], v[128:131], a[172:175], v[12:15]\n"
"ds_read_b64_tr_b16 v[164:165], v252 offset:24576\n"
"ds_read_b64_tr_b16 v[166:167], v252 offset:28672\n"
"v_mfma_f32_16x16x32_bf16 v[24:27], v[132:135], a[168:171], v[24:27]\n"
"ds_read_b64_tr_b16 v[168:169], v251 offset:25088\n"
"ds_read_b64_tr_b16 v[170:171], v251 offset:29184\n"
"v_mfma_f32_16x16x32_bf16 v[28:31], v[132:135], a[172:175], v[28:31]\n"
"ds_read_b64_tr_b16 v[172:173], v252 offset:25088\n"
"ds_read_b64_tr_b16 v[174:175], v252 offset:29184\n"
"v_mfma_f32_16x16x32_bf16 v[40:43], v[136:139], a[168:171], v[40:43]\n"
"ds_read_b64_tr_b16 v[176:177], v251 offset:25600\n"
"ds_read_b64_tr_b16 v[178:179], v251 offset:29696\n"
"v_mfma_f32_16x16x32_bf16 v[44:47], v[136:139], a[172:175], v[44:47]\n"
"ds_read_b64_tr_b16 v[180:181], v252 offset:25600\n"
"ds_read_b64_tr_b16 v[182:183], v252 offset:29696\n"
"v_mfma_f32_16x16x32_bf16 v[56:59], v[140:143], a[168:171], v[56:59]\n"
"ds_read_b64_tr_b16 v[184:185], v251 offset:26112\n"
"ds_read_b64_tr_b16 v[186:187], v251 offset:30208\n"
"v_mfma_f32_16x16x32_bf16 v[60:63], v[140:143], a[172:175], v[60:63]\n"
"ds_read_b64_tr_b16 v[188:189], v252 offset:26112\n"
"ds_read_b64_tr_b16 v[190:191], v252 offset:30208\n"
"v_mfma_f32_16x16x32_bf16 v[72:75], v[144:147], a[168:171], v[72:75]\n"
"v_mfma_f32_16x16x32_bf16 v[76:79], v[144:147], a[172:175], v[76:79]\n"
"v_mfma_f32_16x16x32_bf16 v[88:91], v[148:151], a[168:171], v[88:91]\n"
"v_mfma_f32_16x16x32_bf16 v[92:95], v[148:151], a[172:175], v[92:95]\n"
"v_mfma_f32_16x16x32_bf16 v[104:107], v[152:155], a[168:171], v[104:107]\n"
"v_mfma_f32_16x16x32_bf16 v[108:111], v[152:155], a[172:175], v[108:111]\n"
"v_mfma_f32_16x16x32_bf16 v[120:123], v[156:159], a[168:171], v[120:123]\n"
"v_mfma_f32_16x16x32_bf16 v[124:127], v[156:159], a[172:175], v[124:127]\n"
"s_waitcnt vmcnt(12) lgkmcnt(0)\n"
"v_mfma_f32_16x16x32_bf16 v[8:11], v[160:163], a[184:187], v[8:11]\n"
"ds_read_b64_tr_b16 v[128:129], v251 offset:32768\n"
"ds_read_b64_tr_b16 v[130:131], v251 offset:36864\n"
"v_mfma_f32_16x16x32_bf16 v[12:15], v[160:163], a[188:191], v[12:15]\n"
"ds_read_b64_tr_b16 v[132:133], v252 offset:32768\n"
"ds_read_b64_tr_b16 v[134:135], v252 offset:36864\n"
"v_mfma_f32_16x16x32_bf16 v[24:27], v[164:167], a[184:187], v[24:27]\n"
"ds_read_b64_tr_b16 v[136:137], v251 offset:33280\n"
"ds_read_b64_tr_b16 v[138:139], v251 offset:37376\n"
"v_mfma_f32_16x16x32_bf16 v[28:31], v[164:167], a[188:191], v[28:31]\n"
"ds_read_b64_tr_b16 v[140:141], v252 offset:33280\n"
"ds_read_b64_tr_b16 v[142:143], v252 offset:37376\n"
"v_mfma_f32_16x16x32_bf16 v[40:43], v[168:171], a[184:187], v[40:43]\n"
"ds_read_b64_tr_b16 v[144:145], v251 offset:33792\n"
"ds_read_b64_tr_b16 v[146:147], v251 offset:37888\n"
"v_mfma_f32_16x16x32_bf16 v[44:47], v[168:171], a[188:191], v[44:47]\n"
"ds_read_b64_tr_b16 v[148:149], v252 offset:33792\n"
"ds_read_b64_tr_b16 v[150:151], v252 offset:37888\n"
"v_mfma_f32_16x16x32_bf16 v[56:59], v[172:175], a[184:187], v[56:59]\n"
"ds_read_b64_tr_b16 v[152:153], v251 offset:34304\n"
"ds_read_b64_tr_b16 v[154:155], v251 offset:38400\n"
"v_mfma_f32_16x16x32_bf16 v[60:63], v[172:175], a[188:191], v[60:63]\n"
"ds_read_b64_tr_b16 v[156:157], v252 offset:34304\n"
"ds_read_b64_tr_b16 v[158:159], v252 offset:38400\n"
"v_mfma_f32_16x16x32_bf16 v[72:75], v[176:179], a[184:187], v[72:75]\n"
"v_mfma_f32_16x16x32_bf16 v[76:79], v[176:179], a[188:191], v[76:79]\n"
"v_mfma_f32_16x16x32_bf16 v[88:91], v[180:183], a[184:187], v[88:91]\n"
"v_mfma_f32_16x16x32_bf16 v[92:95], v[180:183], a[188:191], v[92:95]\n"
"v_mfma_f32_16x16x32_bf16 v[104:107], v[184:187], a[184:187], v[104:107]\n"
"v_mfma_f32_16x16x32_bf16 v[108:111], v[184:187], a[188:191], v[108:111]\n"
"v_mfma_f32_16x16x32_bf16 v[120:123], v[188:191], a[184:187], v[120:123]\n"
"v_mfma_f32_16x16x32_bf16 v[124:127], v[188:191], a[188:191], v[124:127]\n"
"s_waitcnt vmcnt(10) lgkmcnt(0)\n"
"v_mfma_f32_16x16x32_bf16 v[8:11], v[128:131], a[200:203], v[8:11]\n"
"ds_read_b64_tr_b16 v[160:161], v251 offset:40960\n"
"ds_read_b64_tr_b16 v[162:163], v251 offset:45056\n"
"v_mfma_f32_16x16x32_bf16 v[12:15], v[128:131], a[204:207], v[12:15]\n"
"ds_read_b64_tr_b16 v[164:165], v252 offset:40960\n"
"ds_read_b64_tr_b16 v[166:167], v252 offset:45056\n"
"v_mfma_f32_16x16x32_bf16 v[24:27], v[132:135], a[200:203], v[24:27]\n"
"ds_read_b64_tr_b16 v[168:169], v251 offset:41472\n"
"ds_read_b64_tr_b16 v[170:171], v251 offset:45568\n"
"v_mfma_f32_16x16x32_bf16 v[28:31], v[132:135], a[204:207], v[28:31]\n"
"ds_read_b64_tr_b16 v[172:173], v252 offset:41472\n"
"ds_read_b64_tr_b16 v[174:175], v252 offset:45568\n"
"v_mfma_f32_16x16x32_bf16 v[40:43], v[136:139], a[200:203], v[40:43]\n"
"ds_read_b64_tr_b16 v[176:177], v251 offset:41984\n"
"ds_read_b64_tr_b16 v[178:179], v251 offset:46080\n"
"v_mfma_f32_16x16x32_bf16 v[44:47], v[136:139], a[204:207], v[44:47]\n"
"ds_read_b64_tr_b16 v[180:181], v252 offset:41984\n"
"ds_read_b64_tr_b16 v[182:183], v252 offset:46080\n"
"v_mfma_f32_16x16x32_bf16 v[56:59], v[140:143], a[200:203], v[56:59]\n"
"ds_read_b64_tr_b16 v[184:185], v251 offset:42496\n"
"ds_read_b64_tr_b16 v[186:187], v251 offset:46592\n"
"v_mfma_f32_16x16x32_bf16 v[60:63], v[140:143], a[204:207], v[60:63]\n"
"ds_read_b64_tr_b16 v[188:189], v252 offset:42496\n"
"ds_read_b64_tr_b16 v[190:191], v252 offset:46592\n"
"v_mfma_f32_16x16x32_bf16 v[72:75], v[144:147], a[200:203], v[72:75]\n"
"v_mfma_f32_16x16x32_bf16 v[76:79], v[144:147], a[204:207], v[76:79]\n"
"v_mfma_f32_16x16x32_bf16 v[88:91], v[148:151], a[200:203], v[88:91]\n"
"v_mfma_f32_16x16x32_bf16 v[92:95], v[148:151], a[204:207], v[92:95]\n"
"v_mfma_f32_16x16x32_bf16 v[104:107], v[152:155], a[200:203], v[104:107]\n"
"v_mfma_f32_16x16x32_bf16 v[108:111], v[152:155], a[204:207], v[108:111]\n"
"v_mfma_f32_16x16x32_bf16 v[120:123], v[156:159], a[200:203], v[120:123]\n"
"v_mfma_f32_16x16x32_bf16 v[124:127], v[156:159], a[204:207], v[124:127]\n"
"s_waitcnt vmcnt(8) lgkmcnt(0)\n"
"v_mfma_f32_16x16x32_bf16 v[8:11], v[160:163], a[216:219], v[8:11]\n"
"ds_read_b64_tr_b16 v[128:129], v251 offset:49152\n"
"ds_read_b64_tr_b16 v[130:131], v251 offset:53248\n"
"v_mfma_f32_16x16x32_bf16 v[12:15], v[160:163], a[220:223], v[12:15]\n"
"ds_read_b64_tr_b16 v[132:133], v252 offset:49152\n"
"ds_read_b64_tr_b16 v[134:135], v252 offset:53248\n"
"v_mfma_f32_16x16x32_bf16 v[24:27], v[164:167], a[216:219], v[24:27]\n"
"ds_read_b64_tr_b16 v[136:137], v251 offset:49664\n"
"ds_read_b64_tr_b16 v[138:139], v251 offset:53760\n"
"v_mfma_f32_16x16x32_bf16 v[28:31], v[164:167], a[220:223], v[28:31]\n"
"ds_read_b64_tr_b16 v[140:141], v252 offset:49664\n"
"ds_read_b64_tr_b16 v[142:143], v252 offset:53760\n"
"v_mfma_f32_16x16x32_bf16 v[40:43], v[168:171], a[216:219], v[40:43]\n"
"ds_read_b64_tr_b16 v[144:145], v251 offset:50176\n"
"ds_read_b64_tr_b16 v[146:147], v251 offset:54272\n"
"v_mfma_f32_16x16x32_bf16 v[44:47], v[168:171], a[220:223], v[44:47]\n"
"ds_read_b64_tr_b16 v[148:149], v252 offset:50176\n"
"ds_read_b64_tr_b16 v[150:151], v252 offset:54272\n"
"v_mfma_f32_16x16x32_bf16 v[56:59], v[172:175], a[216:219], v[56:59]\n"
"ds_read_b64_tr_b16 v[152:153], v251 offset:50688\n"
"ds_read_b64_tr_b16 v[154:155], v251 offset:54784\n"
"v_mfma_f32_16x16x32_bf16 v[60:63], v[172:175], a[220:223], v[60:63]\n"
"ds_read_b64_tr_b16 v[156:157], v252 offset:50688\n"
"ds_read_b64_tr_b16 v[158:159], v252 offset:54784\n"
"v_mfma_f32_16x16x32_bf16 v[72:75], v[176:179], a[216:219], v[72:75]\n"
"v_mfma_f32_16x16x32_bf16 v[76:79], v[176:179], a[220:223], v[76:79]\n"
"v_mfma_f32_16x16x32_bf16 v[88:91], v[180:183], a[216:219], v[88:91]\n"
"v_mfma_f32_16x16x32_bf16 v[92:95], v[180:183], a[220:223], v[92:95]\n"
"v_mfma_f32_16x16x32_bf16 v[104:107], v[184:187], a[216:219], v[104:107]\n"
"v_mfma_f32_16x16x32_bf16 v[108:111], v[184:187], a[220:223], v[108:111]\n"
"v_mfma_f32_16x16x32_bf16 v[120:123], v[188:191], a[216:219], v[120:123]\n"
"v_mfma_f32_16x16x32_bf16 v[124:127], v[188:191], a[220:223], v[124:127]\n"
"s_waitcnt vmcnt(6) lgkmcnt(0)\n"
"v_mfma_f32_16x16x32_bf16 v[8:11], v[128:131], a[232:235], v[8:11]\n"
"ds_read_b64_tr_b16 v[160:161], v251 offset:57344\n"
"ds_read_b64_tr_b16 v[162:163], v251 offset:61440\n"
"v_mfma_f32_16x16x32_bf16 v[12:15], v[128:131], a[236:239], v[12:15]\n"
"ds_read_b64_tr_b16 v[164:165], v252 offset:57344\n"
"ds_read_b64_tr_b16 v[166:167], v252 offset:61440\n"
"v_mfma_f32_16x16x32_bf16 v[24:27], v[132:135], a[232:235], v[24:27]\n"
"ds_read_b64_tr_b16 v[168:169], v251 offset:57856\n"
"ds_read_b64_tr_b16 v[170:171], v251 offset:61952\n"
"v_mfma_f32_16x16x32_bf16 v[28:31], v[132:135], a[236:239], v[28:31]\n"
"ds_read_b64_tr_b16 v[172:173], v252 offset:57856\n"
"ds_read_b64_tr_b16 v[174:175], v252 offset:61952\n"
"v_mfma_f32_16x16x32_bf16 v[40:43], v[136:139], a[232:235], v[40:43]\n"
"ds_read_b64_tr_b16 v[176:177], v251 offset:58368\n"
"ds_read_b64_tr_b16 v[178:179], v251 offset:62464\n"
"v_mfma_f32_16x16x32_bf16 v[44:47], v[136:139], a[236:239], v[44:47]\n"
"ds_read_b64_tr_b16 v[180:181], v252 offset:58368\n"
"ds_read_b64_tr_b16 v[182:183], v252 offset:62464\n"
"v_mfma_f32_16x16x32_bf16 v[56:59], v[140:143], a[232:235], v[56:59]\n"
"ds_read_b64_tr_b16 v[184:185], v251 offset:58880\n"
"ds_read_b64_tr_b16 v[186:187], v251 offset:62976\n"
"v_mfma_f32_16x16x32_bf16 v[60:63], v[140:143], a[236:239], v[60:63]\n"
"ds_read_b64_tr_b16 v[188:189], v252 offset:58880\n"
"ds_read_b64_tr_b16 v[190:191], v252 offset:62976\n"
"v_mfma_f32_16x16x32_bf16 v[72:75], v[144:147], a[232:235], v[72:75]\n"
"v_mfma_f32_16x16x32_bf16 v[76:79], v[144:147], a[236:239], v[76:79]\n"
"v_mfma_f32_16x16x32_bf16 v[88:91], v[148:151], a[232:235], v[88:91]\n"
"v_mfma_f32_16x16x32_bf16 v[92:95], v[148:151], a[236:239], v[92:95]\n"
"v_mfma_f32_16x16x32_bf16 v[104:107], v[152:155], a[232:235], v[104:107]\n"
"v_mfma_f32_16x16x32_bf16 v[108:111], v[152:155], a[236:239], v[108:111]\n"
"v_mfma_f32_16x16x32_bf16 v[120:123], v[156:159], a[232:235], v[120:123]\n"
"v_mfma_f32_16x16x32_bf16 v[124:127], v[156:159], a[236:239], v[124:127]\n"
"s_waitcnt vmcnt(4) lgkmcnt(0)\n"
"v_mfma_f32_16x16x32_bf16 v[8:11], v[160:163], a[248:251], v[8:11]\n"
"v_mfma_f32_16x16x32_bf16 v[12:15], v[160:163], a[252:255], v[12:15]\n"
"v_mfma_f32_16x16x32_bf16 v[24:27], v[164:167], a[248:251], v[24:27]\n"
"v_mfma_f32_16x16x32_bf16 v[28:31], v[164:167], a[252:255], v[28:31]\n"
"v_mfma_f32_16x16x32_bf16 v[40:43], v[168:171], a[248:251], v[40:43]\n"
"v_mfma_f32_16x16x32_bf16 v[44:47], v[168:171], a[252:255], v[44:47]\n"
"v_mfma_f32_16x16x32_bf16 v[56:59], v[172:175], a[248:251], v[56:59]\n"
"v_mfma_f32_16x16x32_bf16 v[60:63], v[172:175], a[252:255], v[60:63]\n"
"v_mfma_f32_16x16x32_bf16 v[72:75], v[176:179], a[248:251], v[72:75]\n"
"v_mfma_f32_16x16x32_bf16 v[76:79], v[176:179], a[252:255], v[76:79]\n"
"v_mfma_f32_16x16x32_bf16 v[88:91], v[180:183], a[248:251], v[88:91]\n"
"v_mfma_f32_16x16x32_bf16 v[92:95], v[180:183], a[252:255], v[92:95]\n"
"v_mfma_f32_16x16x32_bf16 v[104:107], v[184:187], a[248:251], v[104:107]\n"
"v_mfma_f32_16x16x32_bf16 v[108:111], v[184:187], a[252:255], v[108:111]\n"
"v_mfma_f32_16x16x32_bf16 v[120:123], v[188:191], a[248:251], v[120:123]\n"
"v_mfma_f32_16x16x32_bf16 v[124:127], v[188:191], a[252:255], v[124:127]\n"
"s_nop 7\n"
"v_mov_b32 v230, 0xbfb8aa3b\n"
"s_mov_b32 s59, 114688\n"
"v_mul_f32 v200, v230, v8\n"
"v_mul_f32 v201, v230, v9\n"
"v_mul_f32 v202, v230, v10\n"
"v_mul_f32 v203, v230, v11\n"
"v_mul_f32 v204, v230, v24\n"
"v_mul_f32 v205, v230, v25\n"
"v_mul_f32 v206, v230, v26\n"
"v_mul_f32 v207, v230, v27\n"
"v_exp_f32 v200, v200\n"
"v_exp_f32 v201, v201\n"
"v_exp_f32 v202, v202\n"
"v_exp_f32 v203, v203\n"
"v_exp_f32 v204, v204\n"
"v_exp_f32 v205, v205\n"
"v_exp_f32 v206, v206\n"
"v_exp_f32 v207, v207\n"
"v_mul_f32 v8, v8, v72\n"
"v_mul_f32 v9, v9, v73\n"
"v_mul_f32 v10, v10, v74\n"
"v_mul_f32 v11, v11, v75\n"
"v_mul_f32 v24, v24, v88\n"
"v_mul_f32 v25, v25, v89\n"
"v_mul_f32 v26, v26, v90\n"
"v_mul_f32 v27, v27, v91\n"
"v_add_f32 v200, 1.0, v200\n"
"v_add_f32 v201, 1.0, v201\n"
"v_add_f32 v202, 1.0, v202\n"
"v_add_f32 v203, 1.0, v203\n"
"v_add_f32 v204, 1.0, v204\n"
"v_add_f32 v205, 1.0, v205\n"
"v_add_f32 v206, 1.0, v206\n"
"v_add_f32 v207, 1.0, v207\n"
"v_rcp_f32 v200, v200\n"
"v_rcp_f32 v201, v201\n"
"v_rcp_f32 v202, v202\n"
"v_rcp_f32 v203, v203\n"
"v_rcp_f32 v204, v204\n"
"v_rcp_f32 v205, v205\n"
"v_rcp_f32 v206, v206\n"
"v_rcp_f32 v207, v207\n"
"s_nop 0\n"
"v_mul_f32 v8, v8, v200\n"
"v_mul_f32 v9, v9, v201\n"
"v_mul_f32 v10, v10, v202\n"
"v_mul_f32 v11, v11, v203\n"
"v_mul_f32 v24, v24, v204\n"
"v_mul_f32 v25, v25, v205\n"
"v_mul_f32 v26, v26, v206\n"
"v_mul_f32 v27, v27, v207\n"
"v_cvt_pk_bf16_f32 v192, v8, v9\n"
"v_cvt_pk_bf16_f32 v193, v10, v11\n"
"v_cvt_pk_bf16_f32 v194, v24, v25\n"
"v_cvt_pk_bf16_f32 v195, v26, v27\n"
"buffer_store_dwordx4 v[192:195], v246, s[48:51], s59 offen offset:0 sc1\n"
"v_mul_f32 v208, v230, v40\n"
"v_mul_f32 v209, v230, v41\n"
"v_mul_f32 v210, v230, v42\n"
"v_mul_f32 v211, v230, v43\n"
"v_mul_f32 v212, v230, v56\n"
"v_mul_f32 v213, v230, v57\n"
"v_mul_f32 v214, v230, v58\n"
"v_mul_f32 v215, v230, v59\n"
"v_exp_f32 v208, v208\n"
"v_exp_f32 v209, v209\n"
"v_exp_f32 v210, v210\n"
"v_exp_f32 v211, v211\n"
"v_exp_f32 v212, v212\n"
"v_exp_f32 v213, v213\n"
"v_exp_f32 v214, v214\n"
"v_exp_f32 v215, v215\n"
"v_mul_f32 v40, v40, v104\n"
"v_mul_f32 v41, v41, v105\n"
"v_mul_f32 v42, v42, v106\n"
"v_mul_f32 v43, v43, v107\n"
"v_mul_f32 v56, v56, v120\n"
"v_mul_f32 v57, v57, v121\n"
"v_mul_f32 v58, v58, v122\n"
"v_mul_f32 v59, v59, v123\n"
"v_add_f32 v208, 1.0, v208\n"
"v_add_f32 v209, 1.0, v209\n"
"v_add_f32 v210, 1.0, v210\n"
"v_add_f32 v211, 1.0, v211\n"
"v_add_f32 v212, 1.0, v212\n"
"v_add_f32 v213, 1.0, v213\n"
"v_add_f32 v214, 1.0, v214\n"
"v_add_f32 v215, 1.0, v215\n"
"v_rcp_f32 v208, v208\n"
"v_rcp_f32 v209, v209\n"
"v_rcp_f32 v210, v210\n"
"v_rcp_f32 v211, v211\n"
"v_rcp_f32 v212, v212\n"
"v_rcp_f32 v213, v213\n"
"v_rcp_f32 v214, v214\n"
"v_rcp_f32 v215, v215\n"
"s_nop 0\n"
"v_mul_f32 v40, v40, v208\n"
"v_mul_f32 v41, v41, v209\n"
"v_mul_f32 v42, v42, v210\n"
"v_mul_f32 v43, v43, v211\n"
"v_mul_f32 v56, v56, v212\n"
"v_mul_f32 v57, v57, v213\n"
"v_mul_f32 v58, v58, v214\n"
"v_mul_f32 v59, v59, v215\n"
"v_cvt_pk_bf16_f32 v196, v40, v41\n"
"v_cvt_pk_bf16_f32 v197, v42, v43\n"
"v_cvt_pk_bf16_f32 v198, v56, v57\n"
"v_cvt_pk_bf16_f32 v199, v58, v59\n"
"buffer_store_dwordx4 v[196:199], v246, s[48:51], s59 offen offset:1024 sc1\n"
"s_mov_b32 s59, 172032\n"
"v_mul_f32 v200, v230, v12\n"
"v_mul_f32 v201, v230, v13\n"
"v_mul_f32 v202, v230, v14\n"
"v_mul_f32 v203, v230, v15\n"
"v_mul_f32 v204, v230, v28\n"
"v_mul_f32 v205, v230, v29\n"
"v_mul_f32 v206, v230, v30\n"
"v_mul_f32 v207, v230, v31\n"
"v_exp_f32 v200, v200\n"
"v_exp_f32 v201, v201\n"
"v_exp_f32 v202, v202\n"
"v_exp_f32 v203, v203\n"
"v_exp_f32 v204, v204\n"
"v_exp_f32 v205, v205\n"
"v_exp_f32 v206, v206\n"
"v_exp_f32 v207, v207\n"
"v_mul_f32 v12, v12, v76\n"
"v_mul_f32 v13, v13, v77\n"
"v_mul_f32 v14, v14, v78\n"
"v_mul_f32 v15, v15, v79\n"
"v_mul_f32 v28, v28, v92\n"
"v_mul_f32 v29, v29, v93\n"
"v_mul_f32 v30, v30, v94\n"
"v_mul_f32 v31, v31, v95\n"
"v_add_f32 v200, 1.0, v200\n"
"v_add_f32 v201, 1.0, v201\n"
"v_add_f32 v202, 1.0, v202\n"
"v_add_f32 v203, 1.0, v203\n"
"v_add_f32 v204, 1.0, v204\n"
"v_add_f32 v205, 1.0, v205\n"
"v_add_f32 v206, 1.0, v206\n"
"v_add_f32 v207, 1.0, v207\n"
"v_rcp_f32 v200, v200\n"
"v_rcp_f32 v201, v201\n"
"v_rcp_f32 v202, v202\n"
"v_rcp_f32 v203, v203\n"
"v_rcp_f32 v204, v204\n"
"v_rcp_f32 v205, v205\n"
"v_rcp_f32 v206, v206\n"
"v_rcp_f32 v207, v207\n"
"s_nop 0\n"
"v_mul_f32 v12, v12, v200\n"
"v_mul_f32 v13, v13, v201\n"
"v_mul_f32 v14, v14, v202\n"
"v_mul_f32 v15, v15, v203\n"
"v_mul_f32 v28, v28, v204\n"
"v_mul_f32 v29, v29, v205\n"
"v_mul_f32 v30, v30, v206\n"
"v_mul_f32 v31, v31, v207\n"
"v_cvt_pk_bf16_f32 v192, v12, v13\n"
"v_cvt_pk_bf16_f32 v193, v14, v15\n"
"v_cvt_pk_bf16_f32 v194, v28, v29\n"
"v_cvt_pk_bf16_f32 v195, v30, v31\n"
"buffer_store_dwordx4 v[192:195], v246, s[48:51], s59 offen offset:0 sc1\n"
"v_mul_f32 v208, v230, v44\n"
"v_mul_f32 v209, v230, v45\n"
"v_mul_f32 v210, v230, v46\n"
"v_mul_f32 v211, v230, v47\n"
"v_mul_f32 v212, v230, v60\n"
"v_mul_f32 v213, v230, v61\n"
"v_mul_f32 v214, v230, v62\n"
"v_mul_f32 v215, v230, v63\n"
"v_exp_f32 v208, v208\n"
"v_exp_f32 v209, v209\n"
"v_exp_f32 v210, v210\n"
"v_exp_f32 v211, v211\n"
"v_exp_f32 v212, v212\n"
"v_exp_f32 v213, v213\n"
"v_exp_f32 v214, v214\n"
"v_exp_f32 v215, v215\n"
"v_mul_f32 v44, v44, v108\n"
"v_mul_f32 v45, v45, v109\n"
"v_mul_f32 v46, v46, v110\n"
"v_mul_f32 v47, v47, v111\n"
"v_mul_f32 v60, v60, v124\n"
"v_mul_f32 v61, v61, v125\n"
"v_mul_f32 v62, v62, v126\n"
"v_mul_f32 v63, v63, v127\n"
"v_add_f32 v208, 1.0, v208\n"
"v_add_f32 v209, 1.0, v209\n"
"v_add_f32 v210, 1.0, v210\n"
"v_add_f32 v211, 1.0, v211\n"
"v_add_f32 v212, 1.0, v212\n"
"v_add_f32 v213, 1.0, v213\n"
"v_add_f32 v214, 1.0, v214\n"
"v_add_f32 v215, 1.0, v215\n"
"v_rcp_f32 v208, v208\n"
"v_rcp_f32 v209, v209\n"
"v_rcp_f32 v210, v210\n"
"v_rcp_f32 v211, v211\n"
"v_rcp_f32 v212, v212\n"
"v_rcp_f32 v213, v213\n"
"v_rcp_f32 v214, v214\n"
"v_rcp_f32 v215, v215\n"
"s_nop 0\n"
"v_mul_f32 v44, v44, v208\n"
"v_mul_f32 v45, v45, v209\n"
"v_mul_f32 v46, v46, v210\n"
"v_mul_f32 v47, v47, v211\n"
"v_mul_f32 v60, v60, v212\n"
"v_mul_f32 v61, v61, v213\n"
"v_mul_f32 v62, v62, v214\n"
"v_mul_f32 v63, v63, v215\n"
"v_cvt_pk_bf16_f32 v196, v44, v45\n"
"v_cvt_pk_bf16_f32 v197, v46, v47\n"
"v_cvt_pk_bf16_f32 v198, v60, v61\n"
"v_cvt_pk_bf16_f32 v199, v62, v63\n"
"buffer_store_dwordx4 v[196:199], v246, s[48:51], s59 offen offset:1024 sc1\n"
"s_waitcnt vmcnt(0)\n"
"s_barrier\n"
"s_cmp_eq_u32 s56, 0\n"
"s_cbranch_scc0 Lsig_ub\n"
"s_mov_b64 s[64:65], exec\n"
"s_mov_b64 exec, 1\n"
"v_mov_b32 v238, 0\n"
"v_mov_b32 v239, 1\n"
"global_atomic_add v238, v239, s[76:77]\n"
"s_mov_b64 exec, s[64:65]\n"
"Lsig_ub:\n"
"s_mov_b32 s52, %4\n"
"s_and_b32 s53, %5, 0xffff\n"
"s_mov_b32 s54, 1835008\n"
"s_mov_b32 s55, 0x20000\n"
"v_mbcnt_lo_u32_b32 v232, -1, 0\n"
"v_mbcnt_hi_u32_b32 v232, -1, v232\n"
"v_lshrrev_b32 v233, 4, v232\n"
"v_and_b32 v234, 1, v233\n"
"v_lshlrev_b32 v234, 1, v234\n"
"v_and_b32 v235, 3, v232\n"
"v_xor_b32 v234, v234, v235\n"
"v_lshlrev_b32 v234, 4, v234\n"
"v_lshrrev_b32 v235, 5, v232\n"
"v_lshl_add_u32 v234, v235, 6, v234\n"
"v_bfe_u32 v235, v232, 2, 3\n"
"s_mov_b32 s59, 1024\n"
"v_mul_lo_u32 v235, v235, s59\n"
"v_add_u32 v237, v235, v234\n"
"v_add_u32 v236, 0, v237\n"
"s_cmp_eq_u32 s56, 0\n"
"s_cbranch_scc1 Lw3_wd1\n"
"s_mul_i32 s58, s56, 1024\n"
"s_mul_i32 s59, s56, 8192\n"
"s_add_u32 m0, s58, 0\n"
"s_add_u32 s57, s59, 0\n"
"buffer_load_dwordx4 v236, s[52:55], s57 offen lds\n"
"s_add_u32 m0, s58, 4096\n"
"s_add_u32 s57, s59, 32768\n"
"buffer_load_dwordx4 v236, s[52:55], s57 offen lds\n"
"s_add_u32 m0, s58, 8192\n"
"s_add_u32 s57, s59, 65536\n"
"buffer_load_dwordx4 v236, s[52:55], s57 offen lds\n"
"s_add_u32 m0, s58, 12288\n"
"s_add_u32 s57, s59, 98304\n"
"buffer_load_dwordx4 v236, s[52:55], s57 offen lds\n"
"s_add_u32 m0, s58, 16384\n"
"s_add_u32 s57, s59, 131072\n"
"buffer_load_dwordx4 v236, s[52:55], s57 offen lds\n"
"s_add_u32 m0, s58, 20480\n"
"s_add_u32 s57, s59, 163840\n"
"buffer_load_dwordx4 v236, s[52:55], s57 offen lds\n"
"s_add_u32 m0, s58, 24576\n"
"s_add_u32 s57, s59, 196608\n"
"buffer_load_dwordx4 v236, s[52:55], s57 offen lds\n"
"s_add_u32 m0, s58, 28672\n"
"s_add_u32 s57, s59, 229376\n"
"buffer_load_dwordx4 v236, s[52:55], s57 offen lds\n"
"s_add_u32 m0, s58, 32768\n"
"s_add_u32 s57, s59, 262144\n"
"buffer_load_dwordx4 v236, s[52:55], s57 offen lds\n"
"s_add_u32 m0, s58, 36864\n"
"s_add_u32 s57, s59, 294912\n"
"buffer_load_dwordx4 v236, s[52:55], s57 offen lds\n"
"s_add_u32 m0, s58, 40960\n"
"s_add_u32 s57, s59, 327680\n"
"buffer_load_dwordx4 v236, s[52:55], s57 offen lds\n"
"s_add_u32 m0, s58, 45056\n"
"s_add_u32 s57, s59, 360448\n"
"buffer_load_dwordx4 v236, s[52:55], s57 offen lds\n"
"s_add_u32 m0, s58, 49152\n"
"s_add_u32 s57, s59, 393216\n"
"buffer_load_dwordx4 v236, s[52:55], s57 offen lds\n"
"s_add_u32 m0, s58, 53248\n"
"s_add_u32 s57, s59, 425984\n"
"buffer_load_dwordx4 v236, s[52:55], s57 offen lds\n"
"s_add_u32 m0, s58, 57344\n"
"s_add_u32 s57, s59, 458752\n"
"buffer_load_dwordx4 v236, s[52:55], s57 offen lds\n"
"s_add_u32 m0, s58, 61440\n"
"s_add_u32 s57, s59, 491520\n"
"buffer_load_dwordx4 v236, s[52:55], s57 offen lds\n"
"s_add_u32 m0, s58, 65536\n"
"s_add_u32 s57, s59, 524288\n"
"buffer_load_dwordx4 v236, s[52:55], s57 offen lds\n"
"s_add_u32 m0, s58, 69632\n"
"s_add_u32 s57, s59, 557056\n"
"buffer_load_dwordx4 v236, s[52:55], s57 offen lds\n"
"s_add_u32 m0, s58, 73728\n"
"s_add_u32 s57, s59, 589824\n"
"buffer_load_dwordx4 v236, s[52:55], s57 offen lds\n"
"s_add_u32 m0, s58, 77824\n"
"s_add_u32 s57, s59, 622592\n"
"buffer_load_dwordx4 v236, s[52:55], s57 offen lds\n"
"s_add_u32 m0, s58, 81920\n"
"s_add_u32 s57, s59, 655360\n"
"buffer_load_dwordx4 v236, s[52:55], s57 offen lds\n"
"s_add_u32 m0, s58, 86016\n"
"s_add_u32 s57, s59, 688128\n"
"buffer_load_dwordx4 v236, s[52:55], s57 offen lds\n"
"s_add_u32 m0, s58, 90112\n"
"s_add_u32 s57, s59, 720896\n"
"buffer_load_dwordx4 v236, s[52:55], s57 offen lds\n"
"s_add_u32 m0, s58, 94208\n"
"s_add_u32 s57, s59, 753664\n"
"buffer_load_dwordx4 v236, s[52:55], s57 offen lds\n"
"s_add_u32 m0, s58, 98304\n"
"s_add_u32 s57, s59, 786432\n"
"buffer_load_dwordx4 v236, s[52:55], s57 offen lds\n"
"s_add_u32 m0, s58, 102400\n"
"s_add_u32 s57, s59, 819200\n"
"buffer_load_dwordx4 v236, s[52:55], s57 offen lds\n"
"s_add_u32 m0, s58, 106496\n"
"s_add_u32 s57, s59, 851968\n"
"buffer_load_dwordx4 v236, s[52:55], s57 offen lds\n"
"s_add_u32 m0, s58, 110592\n"
"s_add_u32 s57, s59, 884736\n"
"buffer_load_dwordx4 v236, s[52:55], s57 offen lds\n"
"s_add_u32 m0, s58, 114688\n"
"s_add_u32 s57, s59, 917504\n"
"buffer_load_dwordx4 v236, s[52:55], s57 offen lds\n"
"s_add_u32 m0, s58, 118784\n"
"s_add_u32 s57, s59, 950272\n"
"buffer_load_dwordx4 v236, s[52:55], s57 offen lds\n"
"s_add_u32 m0, s58, 122880\n"
"s_add_u32 s57, s59, 983040\n"
"buffer_load_dwordx4 v236, s[52:55], s57 offen lds\n"
"s_add_u32 m0, s58, 126976\n"
"s_add_u32 s57, s59, 1015808\n"
"buffer_load_dwordx4 v236, s[52:55], s57 offen lds\n"
"s_sub_u32 s62, s56, 1\n"
"s_mul_i32 s58, s62, 4096\n"
"s_mul_i32 s59, s62, 32768\n"
"s_add_u32 m0, s58, 0\n"
"s_add_u32 s57, s59, 0\n"
"buffer_load_dwordx4 v236, s[52:55], s57 offen lds\n"
"s_add_u32 m0, s58, 12288\n"
"s_add_u32 s57, s59, 98304\n"
"buffer_load_dwordx4 v236, s[52:55], s57 offen lds\n"
"s_add_u32 m0, s58, 24576\n"
"s_add_u32 s57, s59, 196608\n"
"buffer_load_dwordx4 v236, s[52:55], s57 offen lds\n"
"s_add_u32 m0, s58, 36864\n"
"s_add_u32 s57, s59, 294912\n"
"buffer_load_dwordx4 v236, s[52:55], s57 offen lds\n"
"s_add_u32 m0, s58, 49152\n"
"s_add_u32 s57, s59, 393216\n"
"buffer_load_dwordx4 v236, s[52:55], s57 offen lds\n"
"s_add_u32 m0, s58, 61440\n"
"s_add_u32 s57, s59, 491520\n"
"buffer_load_dwordx4 v236, s[52:55], s57 offen lds\n"
"s_add_u32 m0, s58, 73728\n"
"s_add_u32 s57, s59, 589824\n"
"buffer_load_dwordx4 v236, s[52:55], s57 offen lds\n"
"s_add_u32 m0, s58, 86016\n"
"s_add_u32 s57, s59, 688128\n"
"buffer_load_dwordx4 v236, s[52:55], s57 offen lds\n"
"s_add_u32 m0, s58, 98304\n"
"s_add_u32 s57, s59, 786432\n"
"buffer_load_dwordx4 v236, s[52:55], s57 offen lds\n"
"s_add_u32 m0, s58, 110592\n"
"s_add_u32 s57, s59, 884736\n"
"buffer_load_dwordx4 v236, s[52:55], s57 offen lds\n"
"s_cmp_ge_u32 s62, 2\n"
"s_cbranch_scc1 Lw3_wd1\n"
"s_add_u32 m0, s58, 122880\n"
"s_add_u32 s57, s59, 983040\n"
"buffer_load_dwordx4 v236, s[52:55], s57 offen lds\n"
"Lw3_wd1:\n"
    :: "s"(RFL((size_t)(xb))), "s"(RFL((size_t)(xb) >> 32)), "s"(RFL((size_t)(ob))), "s"(RFL((size_t)(ob) >> 32)), "s"(RFL((size_t)(w2b))), "s"(RFL((size_t)(w2b) >> 32)), "s"(wid), "s"(RFL((size_t)(dbgb))), "s"(RFL((size_t)(dbgb) >> 32)), "s"(RFL((size_t)((size_t)(flags + (88 + pu) * 64)))), "s"(RFL((size_t)((size_t)(flags + (88 + pu) * 64)) >> 32)), "s"(RFL((size_t)((size_t)(flags + (96 + pu) * 64)))), "s"(RFL((size_t)((size_t)(flags + (96 + pu) * 64)) >> 32)), "s"(RFL((size_t)((size_t)(flags + (120) * 64)))), "s"(RFL((size_t)((size_t)(flags + (120) * 64)) >> 32)), "s"(RFL((size_t)((size_t)(flags + (104 + pu) * 64)))), "s"(RFL((size_t)((size_t)(flags + (104 + pu) * 64)) >> 32)), "s"(RFL((size_t)((size_t)(flags + (200 + pan) * 64)))), "s"(RFL((size_t)((size_t)(flags + (200 + pan) * 64)) >> 32))
    : "v0", "v1", "v2", "v3", "v4", "v5", "v6", "v7", "v8", "v9", "v10", "v11", "v12", "v13", "v14", "v15", "v16", "v17", "v18", "v19", "v20", "v21", "v22", "v23", "v24", "v25", "v26", "v27", "v28", "v29", "v30", "v31", "v32", "v33", "v34", "v35", "v36", "v37", "v38", "v39", "v40", "v41", "v42", "v43", "v44", "v45", "v46", "v47", "v48", "v49", "v50", "v51", "v52", "v53", "v54", "v55", "v56", "v57", "v58", "v59", "v60", "v61", "v62", "v63", "v64", "v65", "v66", "v67", "v68", "v69", "v70", "v71", "v72", "v73", "v74", "v75", "v76", "v77", "v78", "v79", "v80", "v81", "v82", "v83", "v84", "v85", "v86", "v87", "v88", "v89", "v90", "v91", "v92", "v93", "v94", "v95", "v96", "v97", "v98", "v99", "v100", "v101", "v102", "v103", "v104", "v105", "v106", "v107", "v108", "v109", "v110", "v111", "v112", "v113", "v114", "v115", "v116", "v117", "v118", "v119", "v120", "v121", "v122", "v123", "v124", "v125", "v126", "v127", "v128", "v129", "v130", "v131", "v132", "v133", "v134", "v135", "v136", "v137", "v138", "v139", "v140", "v141", "v142", "v143", "v144", "v145", "v146", "v147", "v148", "v149", "v150", "v151", "v152", "v153", "v154", "v155", "v156", "v157", "v158", "v159", "v160", "v161", "v162", "v163", "v164", "v165", "v166", "v167", "v168", "v169", "v170", "v171", "v172", "v173", "v174", "v175", "v176", "v177", "v178", "v179", "v180", "v181", "v182", "v183", "v184", "v185", "v186", "v187", "v188", "v189", "v190", "v191", "v192", "v193", "v194", "v195", "v196", "v197", "v198", "v199", "v200", "v201", "v202", "v203", "v204", "v205", "v206", "v207", "v208", "v209", "v210", "v211", "v212", "v213", "v214", "v215", "v216", "v217", "v218", "v219", "v220", "v221", "v222", "v223", "v224", "v225", "v226", "v227", "v228", "v229", "v230", "v231", "v232", "v233", "v234", "v235", "v236", "v237", "v238", "v239", "v240", "v241", "v242", "v243", "v244", "v245", "v246", "v247", "v248", "v249", "v250", "v251", "v252", "v253", "v254", "v255", "a0", "a1", "a2", "a3", "a4", "a5", "a6", "a7", "a8", "a9", "a10", "a11", "a12", "a13", "a14", "a15", "a16", "a17", "a18", "a19", "a20", "a21", "a22", "a23", "a24", "a25", "a26", "a27", "a28", "a29", "a30", "a31", "a32", "a33", "a34", "a35", "a36", "a37", "a38", "a39", "a40", "a41", "a42", "a43", "a44", "a45", "a46", "a47", "a48", "a49", "a50", "a51", "a52", "a53", "a54", "a55", "a56", "a57", "a58", "a59", "a60", "a61", "a62", "a63", "a64", "a65", "a66", "a67", "a68", "a69", "a70", "a71", "a72", "a73", "a74", "a75", "a76", "a77", "a78", "a79", "a80", "a81", "a82", "a83", "a84", "a85", "a86", "a87", "a88", "a89", "a90", "a91", "a92", "a93", "a94", "a95", "a96", "a97", "a98", "a99", "a100", "a101", "a102", "a103", "a104", "a105", "a106", "a107", "a108", "a109", "a110", "a111", "a112", "a113", "a114", "a115", "a116", "a117", "a118", "a119", "a120", "a121", "a122", "a123", "a124", "a125", "a126", "a127", "a128", "a129", "a130", "a131", "a132", "a133", "a134", "a135", "a136", "a137", "a138", "a139", "a140", "a141", "a142", "a143", "a144", "a145", "a146", "a147", "a148", "a149", "a150", "a151", "a152", "a153", "a154", "a155", "a156", "a157", "a158", "a159", "a160", "a161", "a162", "a163", "a164", "a165", "a166", "a167", "a168", "a169", "a170", "a171", "a172", "a173", "a174", "a175", "a176", "a177", "a178", "a179", "a180", "a181", "a182", "a183", "a184", "a185", "a186", "a187", "a188", "a189", "a190", "a191", "a192", "a193", "a194", "a195", "a196", "a197", "a198", "a199", "a200", "a201", "a202", "a203", "a204", "a205", "a206", "a207", "a208", "a209", "a210", "a211", "a212", "a213", "a214", "a215", "a216", "a217", "a218", "a219", "a220", "a221", "a222", "a223", "a224", "a225", "a226", "a227", "a228", "a229", "a230", "a231", "a232", "a233", "a234", "a235", "a236", "a237", "a238", "a239", "a240", "a241", "a242", "a243", "a244", "a245", "a246", "a247", "a248", "a249", "a250", "a251", "a252", "a253", "a254", "a255", "s36", "s37", "s38", "s39", "s40", "s41", "s42", "s43", "s44", "s45", "s46", "s47", "s48", "s49", "s50", "s51", "s52", "s53", "s54", "s55", "s56", "s57", "s58", "s59", "s60", "s61", "s62", "s63", "s64", "s65", "s66", "s67", "s68", "s69", "s70", "s71", "s72", "s73", "s74", "s75", "s76", "s77", "s78", "s79", "s80", "s81", "s82", "s83", "vcc", "scc", "memory");

  } else {
    ARGS(); const size_t w2b = (size_t)w2 + dn * 128;
    publish(flags, 200 + pan, wid);
  asm volatile(
"s_mov_b32 s56, %2\n"
"s_mov_b32 s52, %0\n"
"s_and_b32 s53, %1, 0xffff\n"
"s_mov_b32 s54, 1835008\n"
"s_mov_b32 s55, 0x20000\n"
"v_mbcnt_lo_u32_b32 v232, -1, 0\n"
"v_mbcnt_hi_u32_b32 v232, -1, v232\n"
"v_lshrrev_b32 v233, 4, v232\n"
"v_and_b32 v234, 1, v233\n"
"v_lshlrev_b32 v234, 1, v234\n"
"v_and_b32 v235, 3, v232\n"
"v_xor_b32 v234, v234, v235\n"
"v_lshlrev_b32 v234, 4, v234\n"
"v_lshrrev_b32 v235, 5, v232\n"
"v_lshl_add_u32 v234, v235, 6, v234\n"
"v_bfe_u32 v235, v232, 2, 3\n"
"s_mov_b32 s59, 1024\n"
"v_mul_lo_u32 v235, v235, s59\n"
"v_add_u32 v237, v235, v234\n"
"v_add_u32 v236, 0, v237\n"
"s_cmp_eq_u32 s56, 0\n"
"s_cbranch_scc1 Lw3_wd2\n"
"s_mul_i32 s58, s56, 1024\n"
"s_mul_i32 s59, s56, 8192\n"
"s_add_u32 m0, s58, 0\n"
"s_add_u32 s57, s59, 0\n"
"buffer_load_dwordx4 v236, s[52:55], s57 offen lds\n"
"s_add_u32 m0, s58, 4096\n"
"s_add_u32 s57, s59, 32768\n"
"buffer_load_dwordx4 v236, s[52:55], s57 offen lds\n"
"s_add_u32 m0, s58, 8192\n"
"s_add_u32 s57, s59, 65536\n"
"buffer_load_dwordx4 v236, s[52:55], s57 offen lds\n"
"s_add_u32 m0, s58, 12288\n"
"s_add_u32 s57, s59, 98304\n"
"buffer_load_dwordx4 v236, s[52:55], s57 offen lds\n"
"s_add_u32 m0, s58, 16384\n"
"s_add_u32 s57, s59, 131072\n"
"buffer_load_dwordx4 v236, s[52:55], s57 offen lds\n"
"s_add_u32 m0, s58, 20480\n"
"s_add_u32 s57, s59, 163840\n"
"buffer_load_dwordx4 v236, s[52:55], s57 offen lds\n"
"s_add_u32 m0, s58, 24576\n"
"s_add_u32 s57, s59, 196608\n"
"buffer_load_dwordx4 v236, s[52:55], s57 offen lds\n"
"s_add_u32 m0, s58, 28672\n"
"s_add_u32 s57, s59, 229376\n"
"buffer_load_dwordx4 v236, s[52:55], s57 offen lds\n"
"s_add_u32 m0, s58, 32768\n"
"s_add_u32 s57, s59, 262144\n"
"buffer_load_dwordx4 v236, s[52:55], s57 offen lds\n"
"s_add_u32 m0, s58, 36864\n"
"s_add_u32 s57, s59, 294912\n"
"buffer_load_dwordx4 v236, s[52:55], s57 offen lds\n"
"s_add_u32 m0, s58, 40960\n"
"s_add_u32 s57, s59, 327680\n"
"buffer_load_dwordx4 v236, s[52:55], s57 offen lds\n"
"s_add_u32 m0, s58, 45056\n"
"s_add_u32 s57, s59, 360448\n"
"buffer_load_dwordx4 v236, s[52:55], s57 offen lds\n"
"s_add_u32 m0, s58, 49152\n"
"s_add_u32 s57, s59, 393216\n"
"buffer_load_dwordx4 v236, s[52:55], s57 offen lds\n"
"s_add_u32 m0, s58, 53248\n"
"s_add_u32 s57, s59, 425984\n"
"buffer_load_dwordx4 v236, s[52:55], s57 offen lds\n"
"s_add_u32 m0, s58, 57344\n"
"s_add_u32 s57, s59, 458752\n"
"buffer_load_dwordx4 v236, s[52:55], s57 offen lds\n"
"s_add_u32 m0, s58, 61440\n"
"s_add_u32 s57, s59, 491520\n"
"buffer_load_dwordx4 v236, s[52:55], s57 offen lds\n"
"s_add_u32 m0, s58, 65536\n"
"s_add_u32 s57, s59, 524288\n"
"buffer_load_dwordx4 v236, s[52:55], s57 offen lds\n"
"s_add_u32 m0, s58, 69632\n"
"s_add_u32 s57, s59, 557056\n"
"buffer_load_dwordx4 v236, s[52:55], s57 offen lds\n"
"s_add_u32 m0, s58, 73728\n"
"s_add_u32 s57, s59, 589824\n"
"buffer_load_dwordx4 v236, s[52:55], s57 offen lds\n"
"s_add_u32 m0, s58, 77824\n"
"s_add_u32 s57, s59, 622592\n"
"buffer_load_dwordx4 v236, s[52:55], s57 offen lds\n"
"s_add_u32 m0, s58, 81920\n"
"s_add_u32 s57, s59, 655360\n"
"buffer_load_dwordx4 v236, s[52:55], s57 offen lds\n"
"s_add_u32 m0, s58, 86016\n"
"s_add_u32 s57, s59, 688128\n"
"buffer_load_dwordx4 v236, s[52:55], s57 offen lds\n"
"s_add_u32 m0, s58, 90112\n"
"s_add_u32 s57, s59, 720896\n"
"buffer_load_dwordx4 v236, s[52:55], s57 offen lds\n"
"s_add_u32 m0, s58, 94208\n"
"s_add_u32 s57, s59, 753664\n"
"buffer_load_dwordx4 v236, s[52:55], s57 offen lds\n"
"s_add_u32 m0, s58, 98304\n"
"s_add_u32 s57, s59, 786432\n"
"buffer_load_dwordx4 v236, s[52:55], s57 offen lds\n"
"s_add_u32 m0, s58, 102400\n"
"s_add_u32 s57, s59, 819200\n"
"buffer_load_dwordx4 v236, s[52:55], s57 offen lds\n"
"s_add_u32 m0, s58, 106496\n"
"s_add_u32 s57, s59, 851968\n"
"buffer_load_dwordx4 v236, s[52:55], s57 offen lds\n"
"s_add_u32 m0, s58, 110592\n"
"s_add_u32 s57, s59, 884736\n"
"buffer_load_dwordx4 v236, s[52:55], s57 offen lds\n"
"s_add_u32 m0, s58, 114688\n"
"s_add_u32 s57, s59, 917504\n"
"buffer_load_dwordx4 v236, s[52:55], s57 offen lds\n"
"s_add_u32 m0, s58, 118784\n"
"s_add_u32 s57, s59, 950272\n"
"buffer_load_dwordx4 v236, s[52:55], s57 offen lds\n"
"s_add_u32 m0, s58, 122880\n"
"s_add_u32 s57, s59, 983040\n"
"buffer_load_dwordx4 v236, s[52:55], s57 offen lds\n"
"s_add_u32 m0, s58, 126976\n"
"s_add_u32 s57, s59, 1015808\n"
"buffer_load_dwordx4 v236, s[52:55], s57 offen lds\n"
"s_sub_u32 s62, s56, 1\n"
"s_mul_i32 s58, s62, 4096\n"
"s_mul_i32 s59, s62, 32768\n"
"s_add_u32 m0, s58, 0\n"
"s_add_u32 s57, s59, 0\n"
"buffer_load_dwordx4 v236, s[52:55], s57 offen lds\n"
"s_add_u32 m0, s58, 12288\n"
"s_add_u32 s57, s59, 98304\n"
"buffer_load_dwordx4 v236, s[52:55], s57 offen lds\n"
"s_add_u32 m0, s58, 24576\n"
"s_add_u32 s57, s59, 196608\n"
"buffer_load_dwordx4 v236, s[52:55], s57 offen lds\n"
"s_add_u32 m0, s58, 36864\n"
"s_add_u32 s57, s59, 294912\n"
"buffer_load_dwordx4 v236, s[52:55], s57 offen lds\n"
"s_add_u32 m0, s58, 49152\n"
"s_add_u32 s57, s59, 393216\n"
"buffer_load_dwordx4 v236, s[52:55], s57 offen lds\n"
"s_add_u32 m0, s58, 61440\n"
"s_add_u32 s57, s59, 491520\n"
"buffer_load_dwordx4 v236, s[52:55], s57 offen lds\n"
"s_add_u32 m0, s58, 73728\n"
"s_add_u32 s57, s59, 589824\n"
"buffer_load_dwordx4 v236, s[52:55], s57 offen lds\n"
"s_add_u32 m0, s58, 86016\n"
"s_add_u32 s57, s59, 688128\n"
"buffer_load_dwordx4 v236, s[52:55], s57 offen lds\n"
"s_add_u32 m0, s58, 98304\n"
"s_add_u32 s57, s59, 786432\n"
"buffer_load_dwordx4 v236, s[52:55], s57 offen lds\n"
"s_add_u32 m0, s58, 110592\n"
"s_add_u32 s57, s59, 884736\n"
"buffer_load_dwordx4 v236, s[52:55], s57 offen lds\n"
"s_cmp_ge_u32 s62, 2\n"
"s_cbranch_scc1 Lw3_wd2\n"
"s_add_u32 m0, s58, 122880\n"
"s_add_u32 s57, s59, 983040\n"
"buffer_load_dwordx4 v236, s[52:55], s57 offen lds\n"
"Lw3_wd2:\n"
    :: "s"(RFL((size_t)(w2b))), "s"(RFL((size_t)(w2b) >> 32)), "s"(wid)
    : "v232", "v233", "v234", "v235", "v236", "v237", "s36", "s37", "s38", "s39", "s40", "s41", "s42", "s43", "s52", "s53", "s54", "s55", "s56", "s57", "s58", "s59", "s60", "s61", "s62", "scc", "memory");

  }
  { ARGS(); wait_for2(flags, 200 + pd, 32, (dh ? 104 : 96) + pd, 28, wid);
    const unsigned rb = pd * 16 + (2 * da + (wid >> 1)) * 4 + 2 * dh + (wid & 1); const size_t w2b = (size_t)w2 + dn * 128, xb = (size_t)act + (size_t)rb * 57344, ob = (size_t)out + (size_t)rb * 16384 + dn * 128, x1b = (size_t)x1 + ((size_t)rb * 16 + dn * 2) * 1024;
  asm volatile(
"s_mov_b32 s36, %0\n"
"s_and_b32 s37, %1, 0xffff\n"
"s_mov_b32 s38, 1835008\n"
"s_mov_b32 s39, 0x20000\n"
"s_mov_b32 s44, %2\n"
"s_and_b32 s45, %3, 0xffff\n"
"s_mov_b32 s46, 57344\n"
"s_mov_b32 s47, 0x20000\n"
"s_mov_b32 s48, %4\n"
"s_and_b32 s49, %5, 0xffff\n"
"s_mov_b32 s50, 16384\n"
"s_mov_b32 s51, 0x20000\n"
"s_mov_b32 s52, %6\n"
"s_and_b32 s53, %7, 0xffff\n"
"s_mov_b32 s54, 2048\n"
"s_mov_b32 s55, 0x20000\n"
"s_mov_b32 s56, %8\n"
"s_mov_b32 s62, %9\n"
"s_mov_b32 s66, %10\n"
"s_mov_b32 s67, %11\n"
"v_mbcnt_lo_u32_b32 v240, -1, 0\n"
"v_mbcnt_hi_u32_b32 v240, -1, v240\n"
"v_lshrrev_b32 v241, 4, v240\n"
"v_lshlrev_b32 v246, 4, v240\n"
"v_and_b32 v242, 1, v241\n"
"v_lshlrev_b32 v242, 1, v242\n"
"v_and_b32 v243, 3, v240\n"
"v_xor_b32 v242, v242, v243\n"
"v_lshlrev_b32 v242, 4, v242\n"
"v_lshrrev_b32 v243, 5, v240\n"
"v_lshl_add_u32 v242, v243, 6, v242\n"
"v_bfe_u32 v243, v240, 2, 3\n"
"s_mov_b32 s59, 1024\n"
"v_mul_lo_u32 v243, v243, s59\n"
"v_add_u32 v248, v243, v242\n"
"v_add_u32 v247, 0, v248\n"
"v_lshrrev_b32 v242, 1, v241\n"
"v_mul_u32_u24 v242, 1024, v242\n"
"v_and_b32 v243, 1, v241\n"
"v_lshl_add_u32 v242, v243, 8, v242\n"
"v_bfe_u32 v244, v240, 2, 2\n"
"v_lshl_add_u32 v242, v244, 6, v242\n"
"v_and_b32 v244, 3, v240\n"
"v_lshl_add_u32 v245, v244, 3, v242\n"
"v_lshl_add_u32 v249, v243, 5, v245\n"
"v_xor_b32 v244, 1, v243\n"
"v_lshl_add_u32 v250, v244, 5, v245\n"
"v_add_u32 v251, 0x10000, v249\n"
"v_add_u32 v252, 0x10000, v250\n"
"buffer_load_dwordx4 v[152:155], v246, s[52:55], 0 offen offset:0 sc1\n"
"buffer_load_dwordx4 v[156:159], v246, s[52:55], 0 offen offset:1024 sc1\n"
"v_lshlrev_b32 v244, 4, v241\n"
"v_add_u32 v244, s62, v244\n"
"v_add_u32 v244, 161792, v244\n"
"ds_read_b128 v[176:179], v244 offset:0\n"
"ds_read_b128 v[180:183], v244 offset:64\n"
"ds_read_b128 v[184:187], v244 offset:128\n"
"ds_read_b128 v[188:191], v244 offset:192\n"
"s_mul_i32 s60, s56, 1024\n"
"s_mul_i32 s61, s56, 8192\n"
"s_mov_b32 s58, 0\n"
"buffer_load_dwordx4 a[0:3], v246, s[44:47], s58 offen offset:0 sc1\n"
"s_mov_b32 s58, 0\n"
"buffer_load_dwordx4 a[4:7], v246, s[44:47], s58 offen offset:1024 sc1\n"
"s_mov_b32 s58, 0\n"
"buffer_load_dwordx4 a[8:11], v246, s[44:47], s58 offen offset:2048 sc1\n"
"s_mov_b32 s58, 0\n"
"buffer_load_dwordx4 a[12:15], v246, s[44:47], s58 offen offset:3072 sc1\n"
"s_mov_b32 s58, 4096\n"
"buffer_load_dwordx4 a[16:19], v246, s[44:47], s58 offen offset:0 sc1\n"
"s_mov_b32 s58, 4096\n"
"buffer_load_dwordx4 a[20:23], v246, s[44:47], s58 offen offset:1024 sc1\n"
"s_mov_b32 s58, 4096\n"
"buffer_load_dwordx4 a[24:27], v246, s[44:47], s58 offen offset:2048 sc1\n"
"s_mov_b32 s58, 4096\n"
"buffer_load_dwordx4 a[28:31], v246, s[44:47], s58 offen offset:3072 sc1\n"
"s_mov_b32 s58, 8192\n"
"buffer_load_dwordx4 a[32:35], v246, s[44:47], s58 offen offset:0 sc1\n"
"s_mov_b32 s58, 8192\n"
"buffer_load_dwordx4 a[36:39], v246, s[44:47], s58 offen offset:1024 sc1\n"
"s_mov_b32 s58, 8192\n"
"buffer_load_dwordx4 a[40:43], v246, s[44:47], s58 offen offset:2048 sc1\n"
"s_mov_b32 s58, 8192\n"
"buffer_load_dwordx4 a[44:47], v246, s[44:47], s58 offen offset:3072 sc1\n"
"s_mov_b32 s58, 12288\n"
"buffer_load_dwordx4 a[48:51], v246, s[44:47], s58 offen offset:0 sc1\n"
"s_mov_b32 s58, 12288\n"
"buffer_load_dwordx4 a[52:55], v246, s[44:47], s58 offen offset:1024 sc1\n"
"s_mov_b32 s58, 12288\n"
"buffer_load_dwordx4 a[56:59], v246, s[44:47], s58 offen offset:2048 sc1\n"
"s_mov_b32 s58, 12288\n"
"buffer_load_dwordx4 a[60:63], v246, s[44:47], s58 offen offset:3072 sc1\n"
"s_mov_b32 s58, 16384\n"
"buffer_load_dwordx4 a[64:67], v246, s[44:47], s58 offen offset:0 sc1\n"
"s_mov_b32 s58, 16384\n"
"buffer_load_dwordx4 a[68:71], v246, s[44:47], s58 offen offset:1024 sc1\n"
"s_mov_b32 s58, 16384\n"
"buffer_load_dwordx4 a[72:75], v246, s[44:47], s58 offen offset:2048 sc1\n"
"s_mov_b32 s58, 16384\n"
"buffer_load_dwordx4 a[76:79], v246, s[44:47], s58 offen offset:3072 sc1\n"
"s_mov_b32 s58, 20480\n"
"buffer_load_dwordx4 a[80:83], v246, s[44:47], s58 offen offset:0 sc1\n"
"s_mov_b32 s58, 20480\n"
"buffer_load_dwordx4 a[84:87], v246, s[44:47], s58 offen offset:1024 sc1\n"
"s_mov_b32 s58, 20480\n"
"buffer_load_dwordx4 a[88:91], v246, s[44:47], s58 offen offset:2048 sc1\n"
"s_mov_b32 s58, 20480\n"
"buffer_load_dwordx4 a[92:95], v246, s[44:47], s58 offen offset:3072 sc1\n"
"s_mov_b32 s58, 24576\n"
"buffer_load_dwordx4 a[96:99], v246, s[44:47], s58 offen offset:0 sc1\n"
"s_mov_b32 s58, 24576\n"
"buffer_load_dwordx4 a[100:103], v246, s[44:47], s58 offen offset:1024 sc1\n"
"s_mov_b32 s58, 24576\n"
"buffer_load_dwordx4 a[104:107], v246, s[44:47], s58 offen offset:2048 sc1\n"
"s_mov_b32 s58, 24576\n"
"buffer_load_dwordx4 a[108:111], v246, s[44:47], s58 offen offset:3072 sc1\n"
"s_mov_b32 s58, 28672\n"
"buffer_load_dwordx4 a[112:115], v246, s[44:47], s58 offen offset:0 sc1\n"
"s_mov_b32 s58, 28672\n"
"buffer_load_dwordx4 a[116:119], v246, s[44:47], s58 offen offset:1024 sc1\n"
"s_mov_b32 s58, 28672\n"
"buffer_load_dwordx4 a[120:123], v246, s[44:47], s58 offen offset:2048 sc1\n"
"s_mov_b32 s58, 28672\n"
"buffer_load_dwordx4 a[124:127], v246, s[44:47], s58 offen offset:3072 sc1\n"
"s_mov_b32 s58, 32768\n"
"buffer_load_dwordx4 a[128:131], v246, s[44:47], s58 offen offset:0 sc1\n"
"s_mov_b32 s58, 32768\n"
"buffer_load_dwordx4 a[132:135], v246, s[44:47], s58 offen offset:1024 sc1\n"
"s_mov_b32 s58, 32768\n"
"buffer_load_dwordx4 a[136:139], v246, s[44:47], s58 offen offset:2048 sc1\n"
"s_mov_b32 s58, 32768\n"
"buffer_load_dwordx4 a[140:143], v246, s[44:47], s58 offen offset:3072 sc1\n"
"s_waitcnt vmcnt(38)\n"
"s_barrier\n"
"ds_read_b64_tr_b16 v[64:65], v249 offset:0\n"
"ds_read_b64_tr_b16 v[66:67], v249 offset:2048\n"
"ds_read_b64_tr_b16 v[68:69], v250 offset:0\n"
"ds_read_b64_tr_b16 v[70:71], v250 offset:2048\n"
"ds_read_b64_tr_b16 v[72:73], v249 offset:512\n"
"ds_read_b64_tr_b16 v[74:75], v249 offset:2560\n"
"ds_read_b64_tr_b16 v[76:77], v250 offset:512\n"
"ds_read_b64_tr_b16 v[78:79], v250 offset:2560\n"
"ds_read_b64_tr_b16 v[80:81], v249 offset:4096\n"
"ds_read_b64_tr_b16 v[82:83], v249 offset:6144\n"
"ds_read_b64_tr_b16 v[84:85], v250 offset:4096\n"
"ds_read_b64_tr_b16 v[86:87], v250 offset:6144\n"
"ds_read_b64_tr_b16 v[88:89], v249 offset:4608\n"
"ds_read_b64_tr_b16 v[90:91], v249 offset:6656\n"
"ds_read_b64_tr_b16 v[92:93], v250 offset:4608\n"
"ds_read_b64_tr_b16 v[94:95], v250 offset:6656\n"
"s_waitcnt vmcnt(35) lgkmcnt(8)\n"
"v_mfma_f32_16x16x32_bf16 v[0:3], v[64:67], a[0:3], 0\n"
"ds_read_b64_tr_b16 v[96:97], v249 offset:8192\n"
"ds_read_b64_tr_b16 v[98:99], v249 offset:10240\n"
"s_mov_b32 s58, 36864\n"
"buffer_load_dwordx4 a[144:147], v246, s[44:47], s58 offen offset:0 sc1\n"
"v_mfma_f32_16x16x32_bf16 v[4:7], v[68:71], a[0:3], 0\n"
"ds_read_b64_tr_b16 v[100:101], v250 offset:8192\n"
"ds_read_b64_tr_b16 v[102:103], v250 offset:10240\n"
"v_mfma_f32_16x16x32_bf16 v[8:11], v[72:75], a[0:3], 0\n"
"ds_read_b64_tr_b16 v[104:105], v249 offset:8704\n"
"s_mov_b32 s58, 36864\n"
"buffer_load_dwordx4 a[148:151], v246, s[44:47], s58 offen offset:1024 sc1\n"
"v_mfma_f32_16x16x32_bf16 v[12:15], v[76:79], a[0:3], 0\n"
"ds_read_b64_tr_b16 v[106:107], v249 offset:10752\n"
"ds_read_b64_tr_b16 v[108:109], v250 offset:8704\n"
"ds_read_b64_tr_b16 v[110:111], v250 offset:10752\n"
"s_waitcnt vmcnt(36) lgkmcnt(8)\n"
"v_mfma_f32_16x16x32_bf16 v[0:3], v[80:83], a[4:7], v[0:3]\n"
"ds_read_b64_tr_b16 v[64:65], v249 offset:12288\n"
"ds_read_b64_tr_b16 v[66:67], v249 offset:14336\n"
"s_mov_b32 s58, 36864\n"
"buffer_load_dwordx4 a[152:155], v246, s[44:47], s58 offen offset:2048 sc1\n"
"v_mfma_f32_16x16x32_bf16 v[4:7], v[84:87], a[4:7], v[4:7]\n"
"ds_read_b64_tr_b16 v[68:69], v250 offset:12288\n"
"ds_read_b64_tr_b16 v[70:71], v250 offset:14336\n"
"v_mfma_f32_16x16x32_bf16 v[8:11], v[88:91], a[4:7], v[8:11]\n"
"ds_read_b64_tr_b16 v[72:73], v249 offset:12800\n"
"s_mov_b32 s58, 36864\n"
"buffer_load_dwordx4 a[156:159], v246, s[44:47], s58 offen offset:3072 sc1\n"
"v_mfma_f32_16x16x32_bf16 v[12:15], v[92:95], a[4:7], v[12:15]\n"
"ds_read_b64_tr_b16 v[74:75], v249 offset:14848\n"
"ds_read_b64_tr_b16 v[76:77], v250 offset:12800\n"
"ds_read_b64_tr_b16 v[78:79], v250 offset:14848\n"
"s_waitcnt vmcnt(37) lgkmcnt(8)\n"
"v_mfma_f32_16x16x32_bf16 v[0:3], v[96:99], a[8:11], v[0:3]\n"
"ds_read_b64_tr_b16 v[80:81], v249 offset:16384\n"
"ds_read_b64_tr_b16 v[82:83], v249 offset:18432\n"
"s_mov_b32 s58, 40960\n"
"buffer_load_dwordx4 a[160:163], v246, s[44:47], s58 offen offset:0 sc1\n"
"v_mfma_f32_16x16x32_bf16 v[4:7], v[100:103], a[8:11], v[4:7]\n"
"ds_read_b64_tr_b16 v[84:85], v250 offset:16384\n"
"ds_read_b64_tr_b16 v[86:87], v250 offset:18432\n"
"v_mfma_f32_16x16x32_bf16 v[8:11], v[104:107], a[8:11], v[8:11]\n"
"ds_read_b64_tr_b16 v[88:89], v249 offset:16896\n"
"s_mov_b32 s58, 40960\n"
"buffer_load_dwordx4 a[164:167], v246, s[44:47], s58 offen offset:1024 sc1\n"
"v_mfma_f32_16x16x32_bf16 v[12:15], v[108:111], a[8:11], v[12:15]\n"
"ds_read_b64_tr_b16 v[90:91], v249 offset:18944\n"
"ds_read_b64_tr_b16 v[92:93], v250 offset:16896\n"
"ds_read_b64_tr_b16 v[94:95], v250 offset:18944\n"
"s_waitcnt vmcnt(38) lgkmcnt(8)\n"
"v_mfma_f32_16x16x32_bf16 v[0:3], v[64:67], a[12:15], v[0:3]\n"
"ds_read_b64_tr_b16 v[96:97], v249 offset:20480\n"
"ds_read_b64_tr_b16 v[98:99], v249 offset:22528\n"
"s_mov_b32 s58, 40960\n"
"buffer_load_dwordx4 a[168:171], v246, s[44:47], s58 offen offset:2048 sc1\n"
"v_mfma_f32_16x16x32_bf16 v[4:7], v[68:71], a[12:15], v[4:7]\n"
"ds_read_b64_tr_b16 v[100:101], v250 offset:20480\n"
"ds_read_b64_tr_b16 v[102:103], v250 offset:22528\n"
"v_mfma_f32_16x16x32_bf16 v[8:11], v[72:75], a[12:15], v[8:11]\n"
"ds_read_b64_tr_b16 v[104:105], v249 offset:20992\n"
"s_mov_b32 s58, 40960\n"
"buffer_load_dwordx4 a[172:175], v246, s[44:47], s58 offen offset:3072 sc1\n"
"v_mfma_f32_16x16x32_bf16 v[12:15], v[76:79], a[12:15], v[12:15]\n"
"ds_read_b64_tr_b16 v[106:107], v249 offset:23040\n"
"ds_read_b64_tr_b16 v[108:109], v250 offset:20992\n"
"ds_read_b64_tr_b16 v[110:111], v250 offset:23040\n"
"s_waitcnt vmcnt(39) lgkmcnt(8)\n"
"v_mfma_f32_16x16x32_bf16 v[0:3], v[80:83], a[16:19], v[0:3]\n"
"ds_read_b64_tr_b16 v[64:65], v249 offset:24576\n"
"ds_read_b64_tr_b16 v[66:67], v249 offset:26624\n"
"s_mov_b32 s58, 45056\n"
"buffer_load_dwordx4 a[176:179], v246, s[44:47], s58 offen offset:0 sc1\n"
"v_mfma_f32_16x16x32_bf16 v[4:7], v[84:87], a[16:19], v[4:7]\n"
"ds_read_b64_tr_b16 v[68:69], v250 offset:24576\n"
"ds_read_b64_tr_b16 v[70:71], v250 offset:26624\n"
"v_mfma_f32_16x16x32_bf16 v[8:11], v[88:91], a[16:19], v[8:11]\n"
"ds_read_b64_tr_b16 v[72:73], v249 offset:25088\n"
"s_mov_b32 s58, 45056\n"
"buffer_load_dwordx4 a[180:183], v246, s[44:47], s58 offen offset:1024 sc1\n"
"v_mfma_f32_16x16x32_bf16 v[12:15], v[92:95], a[16:19], v[12:15]\n"
"ds_read_b64_tr_b16 v[74:75], v249 offset:27136\n"
"ds_read_b64_tr_b16 v[76:77], v250 offset:25088\n"
"ds_read_b64_tr_b16 v[78:79], v250 offset:27136\n"
"s_waitcnt vmcnt(40) lgkmcnt(8)\n"
"v_mfma_f32_16x16x32_bf16 v[0:3], v[96:99], a[20:23], v[0:3]\n"
"ds_read_b64_tr_b16 v[80:81], v249 offset:28672\n"
"ds_read_b64_tr_b16 v[82:83], v249 offset:30720\n"
"s_mov_b32 s58, 45056\n"
"buffer_load_dwordx4 a[184:187], v246, s[44:47], s58 offen offset:2048 sc1\n"
"v_mfma_f32_16x16x32_bf16 v[4:7], v[100:103], a[20:23], v[4:7]\n"
"ds_read_b64_tr_b16 v[84:85], v250 offset:28672\n"
"ds_read_b64_tr_b16 v[86:87], v250 offset:30720\n"
"v_mfma_f32_16x16x32_bf16 v[8:11], v[104:107], a[20:23], v[8:11]\n"
"ds_read_b64_tr_b16 v[88:89], v249 offset:29184\n"
"s_mov_b32 s58, 45056\n"
"buffer_load_dwordx4 a[188:191], v246, s[44:47], s58 offen offset:3072 sc1\n"
"v_mfma_f32_16x16x32_bf16 v[12:15], v[108:111], a[20:23], v[12:15]\n"
"ds_read_b64_tr_b16 v[90:91], v249 offset:31232\n"
"ds_read_b64_tr_b16 v[92:93], v250 offset:29184\n"
"ds_read_b64_tr_b16 v[94:95], v250 offset:31232\n"
"s_waitcnt vmcnt(41) lgkmcnt(0)\n"
"s_barrier\n"
"v_mfma_f32_16x16x32_bf16 v[0:3], v[64:67], a[24:27], v[0:3]\n"
"ds_read_b64_tr_b16 v[96:97], v249 offset:32768\n"
"ds_read_b64_tr_b16 v[98:99], v249 offset:34816\n"
"s_mov_b32 s58, 49152\n"
"buffer_load_dwordx4 a[0:3], v246, s[44:47], s58 offen offset:0 sc1\n"
"v_mfma_f32_16x16x32_bf16 v[4:7], v[68:71], a[24:27], v[4:7]\n"
"ds_read_b64_tr_b16 v[100:101], v250 offset:32768\n"
"ds_read_b64_tr_b16 v[102:103], v250 offset:34816\n"
"s_mov_b32 s58, 49152\n"
"buffer_load_dwordx4 a[4:7], v246, s[44:47], s58 offen offset:1024 sc1\n"
"v_mfma_f32_16x16x32_bf16 v[8:11], v[72:75], a[24:27], v[8:11]\n"
"ds_read_b64_tr_b16 v[104:105], v249 offset:33280\n"
"s_add_u32 m0, s60, 0\n"
"s_add_u32 s57, s61, 1048576\n"
"buffer_load_dwordx4 v247, s[36:39], s57 offen lds\n"
"v_mfma_f32_16x16x32_bf16 v[12:15], v[76:79], a[24:27], v[12:15]\n"
"ds_read_b64_tr_b16 v[106:107], v249 offset:35328\n"
"ds_read_b64_tr_b16 v[108:109], v250 offset:33280\n"
"ds_read_b64_tr_b16 v[110:111], v250 offset:35328\n"
"s_waitcnt vmcnt(43) lgkmcnt(8)\n"
"v_mfma_f32_16x16x32_bf16 v[0:3], v[80:83], a[28:31], v[0:3]\n"
"ds_read_b64_tr_b16 v[64:65], v249 offset:36864\n"
"ds_read_b64_tr_b16 v[66:67], v249 offset:38912\n"
"s_mov_b32 s58, 49152\n"
"buffer_load_dwordx4 a[8:11], v246, s[44:47], s58 offen offset:2048 sc1\n"
"v_mfma_f32_16x16x32_bf16 v[4:7], v[84:87], a[28:31], v[4:7]\n"
"ds_read_b64_tr_b16 v[68:69], v250 offset:36864\n"
"ds_read_b64_tr_b16 v[70:71], v250 offset:38912\n"
"v_mfma_f32_16x16x32_bf16 v[8:11], v[88:91], a[28:31], v[8:11]\n"
"ds_read_b64_tr_b16 v[72:73], v249 offset:37376\n"
"s_add_u32 m0, s60, 4096\n"
"s_add_u32 s57, s61, 1081344\n"
"buffer_load_dwordx4 v247, s[36:39], s57 offen lds\n"
"v_mfma_f32_16x16x32_bf16 v[12:15], v[92:95], a[28:31], v[12:15]\n"
"ds_read_b64_tr_b16 v[74:75], v249 offset:39424\n"
"ds_read_b64_tr_b16 v[76:77], v250 offset:37376\n"
"ds_read_b64_tr_b16 v[78:79], v250 offset:39424\n"
"s_waitcnt vmcnt(44) lgkmcnt(8)\n"
"v_mfma_f32_16x16x32_bf16 v[0:3], v[96:99], a[32:35], v[0:3]\n"
"ds_read_b64_tr_b16 v[80:81], v249 offset:40960\n"
"ds_read_b64_tr_b16 v[82:83], v249 offset:43008\n"
"s_mov_b32 s58, 49152\n"
"buffer_load_dwordx4 a[12:15], v246, s[44:47], s58 offen offset:3072 sc1\n"
"v_mfma_f32_16x16x32_bf16 v[4:7], v[100:103], a[32:35], v[4:7]\n"
"ds_read_b64_tr_b16 v[84:85], v250 offset:40960\n"
"ds_read_b64_tr_b16 v[86:87], v250 offset:43008\n"
"v_mfma_f32_16x16x32_bf16 v[8:11], v[104:107], a[32:35], v[8:11]\n"
"ds_read_b64_tr_b16 v[88:89], v249 offset:41472\n"
"s_add_u32 m0, s60, 8192\n"
"s_add_u32 s57, s61, 1114112\n"
"buffer_load_dwordx4 v247, s[36:39], s57 offen lds\n"
"v_mfma_f32_16x16x32_bf16 v[12:15], v[108:111], a[32:35], v[12:15]\n"
"ds_read_b64_tr_b16 v[90:91], v249 offset:43520\n"
"ds_read_b64_tr_b16 v[92:93], v250 offset:41472\n"
"ds_read_b64_tr_b16 v[94:95], v250 offset:43520\n"
"s_waitcnt vmcnt(45) lgkmcnt(8)\n"
"v_mfma_f32_16x16x32_bf16 v[0:3], v[64:67], a[36:39], v[0:3]\n"
"ds_read_b64_tr_b16 v[96:97], v249 offset:45056\n"
"ds_read_b64_tr_b16 v[98:99], v249 offset:47104\n"
"s_mov_b32 s58, 53248\n"
"buffer_load_dwordx4 a[16:19], v246, s[44:47], s58 offen offset:0 sc1\n"
"v_mfma_f32_16x16x32_bf16 v[4:7], v[68:71], a[36:39], v[4:7]\n"
"ds_read_b64_tr_b16 v[100:101], v250 offset:45056\n"
"ds_read_b64_tr_b16 v[102:103], v250 offset:47104\n"
"v_mfma_f32_16x16x32_bf16 v[8:11], v[72:75], a[36:39], v[8:11]\n"
"ds_read_b64_tr_b16 v[104:105], v249 offset:45568\n"
"s_add_u32 m0, s60, 12288\n"
"s_add_u32 s57, s61, 1146880\n"
"buffer_load_dwordx4 v247, s[36:39], s57 offen lds\n"
"v_mfma_f32_16x16x32_bf16 v[12:15], v[76:79], a[36:39], v[12:15]\n"
"ds_read_b64_tr_b16 v[106:107], v249 offset:47616\n"
"ds_read_b64_tr_b16 v[108:109], v250 offset:45568\n"
"ds_read_b64_tr_b16 v[110:111], v250 offset:47616\n"
"s_waitcnt vmcnt(46) lgkmcnt(8)\n"
"v_mfma_f32_16x16x32_bf16 v[0:3], v[80:83], a[40:43], v[0:3]\n"
"ds_read_b64_tr_b16 v[64:65], v249 offset:49152\n"
"ds_read_b64_tr_b16 v[66:67], v249 offset:51200\n"
"s_mov_b32 s58, 53248\n"
"buffer_load_dwordx4 a[20:23], v246, s[44:47], s58 offen offset:1024 sc1\n"
"v_mfma_f32_16x16x32_bf16 v[4:7], v[84:87], a[40:43], v[4:7]\n"
"ds_read_b64_tr_b16 v[68:69], v250 offset:49152\n"
"ds_read_b64_tr_b16 v[70:71], v250 offset:51200\n"
"v_mfma_f32_16x16x32_bf16 v[8:11], v[88:91], a[40:43], v[8:11]\n"
"ds_read_b64_tr_b16 v[72:73], v249 offset:49664\n"
"s_add_u32 m0, s60, 16384\n"
"s_add_u32 s57, s61, 1179648\n"
"buffer_load_dwordx4 v247, s[36:39], s57 offen lds\n"
"v_mfma_f32_16x16x32_bf16 v[12:15], v[92:95], a[40:43], v[12:15]\n"
"ds_read_b64_tr_b16 v[74:75], v249 offset:51712\n"
"ds_read_b64_tr_b16 v[76:77], v250 offset:49664\n"
"ds_read_b64_tr_b16 v[78:79], v250 offset:51712\n"
"s_waitcnt vmcnt(47) lgkmcnt(8)\n"
"v_mfma_f32_16x16x32_bf16 v[0:3], v[96:99], a[44:47], v[0:3]\n"
"ds_read_b64_tr_b16 v[80:81], v249 offset:53248\n"
"ds_read_b64_tr_b16 v[82:83], v249 offset:55296\n"
"s_mov_b32 s58, 53248\n"
"buffer_load_dwordx4 a[24:27], v246, s[44:47], s58 offen offset:2048 sc1\n"
"v_mfma_f32_16x16x32_bf16 v[4:7], v[100:103], a[44:47], v[4:7]\n"
"ds_read_b64_tr_b16 v[84:85], v250 offset:53248\n"
"ds_read_b64_tr_b16 v[86:87], v250 offset:55296\n"
"v_mfma_f32_16x16x32_bf16 v[8:11], v[104:107], a[44:47], v[8:11]\n"
"ds_read_b64_tr_b16 v[88:89], v249 offset:53760\n"
"s_add_u32 m0, s60, 20480\n"
"s_add_u32 s57, s61, 1212416\n"
"buffer_load_dwordx4 v247, s[36:39], s57 offen lds\n"
"v_mfma_f32_16x16x32_bf16 v[12:15], v[108:111], a[44:47], v[12:15]\n"
"ds_read_b64_tr_b16 v[90:91], v249 offset:55808\n"
"ds_read_b64_tr_b16 v[92:93], v250 offset:53760\n"
"ds_read_b64_tr_b16 v[94:95], v250 offset:55808\n"
"s_waitcnt vmcnt(48) lgkmcnt(8)\n"
"v_mfma_f32_16x16x32_bf16 v[0:3], v[64:67], a[48:51], v[0:3]\n"
"ds_read_b64_tr_b16 v[96:97], v249 offset:57344\n"
"ds_read_b64_tr_b16 v[98:99], v249 offset:59392\n"
"s_mov_b32 s58, 53248\n"
"buffer_load_dwordx4 a[28:31], v246, s[44:47], s58 offen offset:3072 sc1\n"
"v_mfma_f32_16x16x32_bf16 v[4:7], v[68:71], a[48:51], v[4:7]\n"
"ds_read_b64_tr_b16 v[100:101], v250 offset:57344\n"
"ds_read_b64_tr_b16 v[102:103], v250 offset:59392\n"
"v_mfma_f32_16x16x32_bf16 v[8:11], v[72:75], a[48:51], v[8:11]\n"
"ds_read_b64_tr_b16 v[104:105], v249 offset:57856\n"
"s_add_u32 m0, s60, 24576\n"
"s_add_u32 s57, s61, 1245184\n"
"buffer_load_dwordx4 v247, s[36:39], s57 offen lds\n"
"v_mfma_f32_16x16x32_bf16 v[12:15], v[76:79], a[48:51], v[12:15]\n"
"ds_read_b64_tr_b16 v[106:107], v249 offset:59904\n"
"ds_read_b64_tr_b16 v[108:109], v250 offset:57856\n"
"ds_read_b64_tr_b16 v[110:111], v250 offset:59904\n"
"s_waitcnt vmcnt(49) lgkmcnt(8)\n"
"v_mfma_f32_16x16x32_bf16 v[0:3], v[80:83], a[52:55], v[0:3]\n"
"ds_read_b64_tr_b16 v[64:65], v249 offset:61440\n"
"ds_read_b64_tr_b16 v[66:67], v249 offset:63488\n"
"s_add_u32 m0, s60, 28672\n"
"s_add_u32 s57, s61, 1277952\n"
"buffer_load_dwordx4 v247, s[36:39], s57 offen lds\n"
"v_mfma_f32_16x16x32_bf16 v[4:7], v[84:87], a[52:55], v[4:7]\n"
"ds_read_b64_tr_b16 v[68:69], v250 offset:61440\n"
"ds_read_b64_tr_b16 v[70:71], v250 offset:63488\n"
"v_mfma_f32_16x16x32_bf16 v[8:11], v[88:91], a[52:55], v[8:11]\n"
"ds_read_b64_tr_b16 v[72:73], v249 offset:61952\n"
"v_mfma_f32_16x16x32_bf16 v[12:15], v[92:95], a[52:55], v[12:15]\n"
"ds_read_b64_tr_b16 v[74:75], v249 offset:64000\n"
"ds_read_b64_tr_b16 v[76:77], v250 offset:61952\n"
"ds_read_b64_tr_b16 v[78:79], v250 offset:64000\n"
"s_waitcnt vmcnt(49) lgkmcnt(0)\n"
"s_barrier\n"
"v_mfma_f32_16x16x32_bf16 v[0:3], v[96:99], a[56:59], v[0:3]\n"
"ds_read_b64_tr_b16 v[80:81], v251 offset:0\n"
"ds_read_b64_tr_b16 v[82:83], v251 offset:2048\n"
"s_add_u32 m0, s60, 32768\n"
"s_add_u32 s57, s61, 1310720\n"
"buffer_load_dwordx4 v247, s[36:39], s57 offen lds\n"
"v_mfma_f32_16x16x32_bf16 v[4:7], v[100:103], a[56:59], v[4:7]\n"
"ds_read_b64_tr_b16 v[84:85], v252 offset:0\n"
"ds_read_b64_tr_b16 v[86:87], v252 offset:2048\n"
"v_mfma_f32_16x16x32_bf16 v[8:11], v[104:107], a[56:59], v[8:11]\n"
"ds_read_b64_tr_b16 v[88:89], v251 offset:512\n"
"v_mfma_f32_16x16x32_bf16 v[12:15], v[108:111], a[56:59], v[12:15]\n"
"ds_read_b64_tr_b16 v[90:91], v251 offset:2560\n"
"ds_read_b64_tr_b16 v[92:93], v252 offset:512\n"
"ds_read_b64_tr_b16 v[94:95], v252 offset:2560\n"
"s_waitcnt vmcnt(49) lgkmcnt(8)\n"
"v_mfma_f32_16x16x32_bf16 v[0:3], v[64:67], a[60:63], v[0:3]\n"
"ds_read_b64_tr_b16 v[96:97], v251 offset:4096\n"
"ds_read_b64_tr_b16 v[98:99], v251 offset:6144\n"
"s_add_u32 m0, s60, 36864\n"
"s_add_u32 s57, s61, 1343488\n"
"buffer_load_dwordx4 v247, s[36:39], s57 offen lds\n"
"v_mfma_f32_16x16x32_bf16 v[4:7], v[68:71], a[60:63], v[4:7]\n"
"ds_read_b64_tr_b16 v[100:101], v252 offset:4096\n"
"ds_read_b64_tr_b16 v[102:103], v252 offset:6144\n"
"v_mfma_f32_16x16x32_bf16 v[8:11], v[72:75], a[60:63], v[8:11]\n"
"ds_read_b64_tr_b16 v[104:105], v251 offset:4608\n"
"v_mfma_f32_16x16x32_bf16 v[12:15], v[76:79], a[60:63], v[12:15]\n"
"ds_read_b64_tr_b16 v[106:107], v251 offset:6656\n"
"ds_read_b64_tr_b16 v[108:109], v252 offset:4608\n"
"ds_read_b64_tr_b16 v[110:111], v252 offset:6656\n"
"s_waitcnt vmcnt(49) lgkmcnt(8)\n"
"v_mfma_f32_16x16x32_bf16 v[0:3], v[80:83], a[64:67], v[0:3]\n"
"ds_read_b64_tr_b16 v[64:65], v251 offset:8192\n"
"ds_read_b64_tr_b16 v[66:67], v251 offset:10240\n"
"s_add_u32 m0, s60, 40960\n"
"s_add_u32 s57, s61, 1376256\n"
"buffer_load_dwordx4 v247, s[36:39], s57 offen lds\n"
"v_mfma_f32_16x16x32_bf16 v[4:7], v[84:87], a[64:67], v[4:7]\n"
"ds_read_b64_tr_b16 v[68:69], v252 offset:8192\n"
"ds_read_b64_tr_b16 v[70:71], v252 offset:10240\n"
"v_mfma_f32_16x16x32_bf16 v[8:11], v[88:91], a[64:67], v[8:11]\n"
"ds_read_b64_tr_b16 v[72:73], v251 offset:8704\n"
"v_mfma_f32_16x16x32_bf16 v[12:15], v[92:95], a[64:67], v[12:15]\n"
"ds_read_b64_tr_b16 v[74:75], v251 offset:10752\n"
"ds_read_b64_tr_b16 v[76:77], v252 offset:8704\n"
"ds_read_b64_tr_b16 v[78:79], v252 offset:10752\n"
"s_waitcnt vmcnt(49) lgkmcnt(8)\n"
"v_mfma_f32_16x16x32_bf16 v[0:3], v[96:99], a[68:71], v[0:3]\n"
"ds_read_b64_tr_b16 v[80:81], v251 offset:12288\n"
"ds_read_b64_tr_b16 v[82:83], v251 offset:14336\n"
"s_add_u32 m0, s60, 45056\n"
"s_add_u32 s57, s61, 1409024\n"
"buffer_load_dwordx4 v247, s[36:39], s57 offen lds\n"
"v_mfma_f32_16x16x32_bf16 v[4:7], v[100:103], a[68:71], v[4:7]\n"
"ds_read_b64_tr_b16 v[84:85], v252 offset:12288\n"
"ds_read_b64_tr_b16 v[86:87], v252 offset:14336\n"
"v_mfma_f32_16x16x32_bf16 v[8:11], v[104:107], a[68:71], v[8:11]\n"
"ds_read_b64_tr_b16 v[88:89], v251 offset:12800\n"
"v_mfma_f32_16x16x32_bf16 v[12:15], v[108:111], a[68:71], v[12:15]\n"
"ds_read_b64_tr_b16 v[90:91], v251 offset:14848\n"
"ds_read_b64_tr_b16 v[92:93], v252 offset:12800\n"
"ds_read_b64_tr_b16 v[94:95], v252 offset:14848\n"
"s_waitcnt vmcnt(49) lgkmcnt(8)\n"
"v_mfma_f32_16x16x32_bf16 v[0:3], v[64:67], a[72:75], v[0:3]\n"
"ds_read_b64_tr_b16 v[96:97], v251 offset:16384\n"
"ds_read_b64_tr_b16 v[98:99], v251 offset:18432\n"
"s_add_u32 m0, s60, 49152\n"
"s_add_u32 s57, s61, 1441792\n"
"buffer_load_dwordx4 v247, s[36:39], s57 offen lds\n"
"v_mfma_f32_16x16x32_bf16 v[4:7], v[68:71], a[72:75], v[4:7]\n"
"ds_read_b64_tr_b16 v[100:101], v252 offset:16384\n"
"ds_read_b64_tr_b16 v[102:103], v252 offset:18432\n"
"v_mfma_f32_16x16x32_bf16 v[8:11], v[72:75], a[72:75], v[8:11]\n"
"ds_read_b64_tr_b16 v[104:105], v251 offset:16896\n"
"v_mfma_f32_16x16x32_bf16 v[12:15], v[76:79], a[72:75], v[12:15]\n"
"ds_read_b64_tr_b16 v[106:107], v251 offset:18944\n"
"ds_read_b64_tr_b16 v[108:109], v252 offset:16896\n"
"ds_read_b64_tr_b16 v[110:111], v252 offset:18944\n"
"s_waitcnt vmcnt(49) lgkmcnt(8)\n"
"v_mfma_f32_16x16x32_bf16 v[0:3], v[80:83], a[76:79], v[0:3]\n"
"ds_read_b64_tr_b16 v[64:65], v251 offset:20480\n"
"ds_read_b64_tr_b16 v[66:67], v251 offset:22528\n"
"s_add_u32 m0, s60, 53248\n"
"s_add_u32 s57, s61, 1474560\n"
"buffer_load_dwordx4 v247, s[36:39], s57 offen lds\n"
"v_mfma_f32_16x16x32_bf16 v[4:7], v[84:87], a[76:79], v[4:7]\n"
"ds_read_b64_tr_b16 v[68:69], v252 offset:20480\n"
"ds_read_b64_tr_b16 v[70:71], v252 offset:22528\n"
"v_mfma_f32_16x16x32_bf16 v[8:11], v[88:91], a[76:79], v[8:11]\n"
"ds_read_b64_tr_b16 v[72:73], v251 offset:20992\n"
"v_mfma_f32_16x16x32_bf16 v[12:15], v[92:95], a[76:79], v[12:15]\n"
"ds_read_b64_tr_b16 v[74:75], v251 offset:23040\n"
"ds_read_b64_tr_b16 v[76:77], v252 offset:20992\n"
"ds_read_b64_tr_b16 v[78:79], v252 offset:23040\n"
"s_waitcnt vmcnt(49) lgkmcnt(8)\n"
"v_mfma_f32_16x16x32_bf16 v[0:3], v[96:99], a[80:83], v[0:3]\n"
"ds_read_b64_tr_b16 v[80:81], v251 offset:24576\n"
"ds_read_b64_tr_b16 v[82:83], v251 offset:26624\n"
"s_add_u32 m0, s60, 57344\n"
"s_add_u32 s57, s61, 1507328\n"
"buffer_load_dwordx4 v247, s[36:39], s57 offen lds\n"
"v_mfma_f32_16x16x32_bf16 v[4:7], v[100:103], a[80:83], v[4:7]\n"
"ds_read_b64_tr_b16 v[84:85], v252 offset:24576\n"
"ds_read_b64_tr_b16 v[86:87], v252 offset:26624\n"
"v_mfma_f32_16x16x32_bf16 v[8:11], v[104:107], a[80:83], v[8:11]\n"
"ds_read_b64_tr_b16 v[88:89], v251 offset:25088\n"
"v_mfma_f32_16x16x32_bf16 v[12:15], v[108:111], a[80:83], v[12:15]\n"
"ds_read_b64_tr_b16 v[90:91], v251 offset:27136\n"
"ds_read_b64_tr_b16 v[92:93], v252 offset:25088\n"
"ds_read_b64_tr_b16 v[94:95], v252 offset:27136\n"
"s_waitcnt vmcnt(49) lgkmcnt(8)\n"
"v_mfma_f32_16x16x32_bf16 v[0:3], v[64:67], a[84:87], v[0:3]\n"
"ds_read_b64_tr_b16 v[96:97], v251 offset:28672\n"
"ds_read_b64_tr_b16 v[98:99], v251 offset:30720\n"
"s_add_u32 m0, s60, 61440\n"
"s_add_u32 s57, s61, 1540096\n"
"buffer_load_dwordx4 v247, s[36:39], s57 offen lds\n"
"v_mfma_f32_16x16x32_bf16 v[4:7], v[68:71], a[84:87], v[4:7]\n"
"ds_read_b64_tr_b16 v[100:101], v252 offset:28672\n"
"ds_read_b64_tr_b16 v[102:103], v252 offset:30720\n"
"v_mfma_f32_16x16x32_bf16 v[8:11], v[72:75], a[84:87], v[8:11]\n"
"ds_read_b64_tr_b16 v[104:105], v251 offset:29184\n"
"v_mfma_f32_16x16x32_bf16 v[12:15], v[76:79], a[84:87], v[12:15]\n"
"ds_read_b64_tr_b16 v[106:107], v251 offset:31232\n"
"ds_read_b64_tr_b16 v[108:109], v252 offset:29184\n"
"ds_read_b64_tr_b16 v[110:111], v252 offset:31232\n"
"s_waitcnt vmcnt(49) lgkmcnt(0)\n"
"s_barrier\n"
"v_mfma_f32_16x16x32_bf16 v[0:3], v[80:83], a[88:91], v[0:3]\n"
"ds_read_b64_tr_b16 v[64:65], v251 offset:32768\n"
"ds_read_b64_tr_b16 v[66:67], v251 offset:34816\n"
"s_add_u32 m0, s60, 65536\n"
"s_add_u32 s57, s61, 1572864\n"
"buffer_load_dwordx4 v247, s[36:39], s57 offen lds\n"
"v_mfma_f32_16x16x32_bf16 v[4:7], v[84:87], a[88:91], v[4:7]\n"
"ds_read_b64_tr_b16 v[68:69], v252 offset:32768\n"
"ds_read_b64_tr_b16 v[70:71], v252 offset:34816\n"
"v_mfma_f32_16x16x32_bf16 v[8:11], v[88:91], a[88:91], v[8:11]\n"
"ds_read_b64_tr_b16 v[72:73], v251 offset:33280\n"
"v_mfma_f32_16x16x32_bf16 v[12:15], v[92:95], a[88:91], v[12:15]\n"
"ds_read_b64_tr_b16 v[74:75], v251 offset:35328\n"
"ds_read_b64_tr_b16 v[76:77], v252 offset:33280\n"
"ds_read_b64_tr_b16 v[78:79], v252 offset:35328\n"
"s_waitcnt vmcnt(49) lgkmcnt(8)\n"
"v_mfma_f32_16x16x32_bf16 v[0:3], v[96:99], a[92:95], v[0:3]\n"
"ds_read_b64_tr_b16 v[80:81], v251 offset:36864\n"
"ds_read_b64_tr_b16 v[82:83], v251 offset:38912\n"
"s_add_u32 m0, s60, 69632\n"
"s_add_u32 s57, s61, 1605632\n"
"buffer_load_dwordx4 v247, s[36:39], s57 offen lds\n"
"v_mfma_f32_16x16x32_bf16 v[4:7], v[100:103], a[92:95], v[4:7]\n"
"ds_read_b64_tr_b16 v[84:85], v252 offset:36864\n"
"ds_read_b64_tr_b16 v[86:87], v252 offset:38912\n"
"v_mfma_f32_16x16x32_bf16 v[8:11], v[104:107], a[92:95], v[8:11]\n"
"ds_read_b64_tr_b16 v[88:89], v251 offset:37376\n"
"v_mfma_f32_16x16x32_bf16 v[12:15], v[108:111], a[92:95], v[12:15]\n"
"ds_read_b64_tr_b16 v[90:91], v251 offset:39424\n"
"ds_read_b64_tr_b16 v[92:93], v252 offset:37376\n"
"ds_read_b64_tr_b16 v[94:95], v252 offset:39424\n"
"s_waitcnt vmcnt(49) lgkmcnt(8)\n"
"v_mfma_f32_16x16x32_bf16 v[0:3], v[64:67], a[96:99], v[0:3]\n"
"ds_read_b64_tr_b16 v[96:97], v251 offset:40960\n"
"ds_read_b64_tr_b16 v[98:99], v251 offset:43008\n"
"s_add_u32 m0, s60, 73728\n"
"s_add_u32 s57, s61, 1638400\n"
"buffer_load_dwordx4 v247, s[36:39], s57 offen lds\n"
"v_mfma_f32_16x16x32_bf16 v[4:7], v[68:71], a[96:99], v[4:7]\n"
"ds_read_b64_tr_b16 v[100:101], v252 offset:40960\n"
"ds_read_b64_tr_b16 v[102:103], v252 offset:43008\n"
"v_mfma_f32_16x16x32_bf16 v[8:11], v[72:75], a[96:99], v[8:11]\n"
"ds_read_b64_tr_b16 v[104:105], v251 offset:41472\n"
"v_mfma_f32_16x16x32_bf16 v[12:15], v[76:79], a[96:99], v[12:15]\n"
"ds_read_b64_tr_b16 v[106:107], v251 offset:43520\n"
"ds_read_b64_tr_b16 v[108:109], v252 offset:41472\n"
"ds_read_b64_tr_b16 v[110:111], v252 offset:43520\n"
"s_waitcnt vmcnt(49) lgkmcnt(8)\n"
"v_mfma_f32_16x16x32_bf16 v[0:3], v[80:83], a[100:103], v[0:3]\n"
"ds_read_b64_tr_b16 v[64:65], v251 offset:45056\n"
"ds_read_b64_tr_b16 v[66:67], v251 offset:47104\n"
"s_add_u32 m0, s60, 77824\n"
"s_add_u32 s57, s61, 1671168\n"
"buffer_load_dwordx4 v247, s[36:39], s57 offen lds\n"
"v_mfma_f32_16x16x32_bf16 v[4:7], v[84:87], a[100:103], v[4:7]\n"
"ds_read_b64_tr_b16 v[68:69], v252 offset:45056\n"
"ds_read_b64_tr_b16 v[70:71], v252 offset:47104\n"
"v_mfma_f32_16x16x32_bf16 v[8:11], v[88:91], a[100:103], v[8:11]\n"
"ds_read_b64_tr_b16 v[72:73], v251 offset:45568\n"
"v_mfma_f32_16x16x32_bf16 v[12:15], v[92:95], a[100:103], v[12:15]\n"
"ds_read_b64_tr_b16 v[74:75], v251 offset:47616\n"
"ds_read_b64_tr_b16 v[76:77], v252 offset:45568\n"
"ds_read_b64_tr_b16 v[78:79], v252 offset:47616\n"
"s_waitcnt vmcnt(49) lgkmcnt(8)\n"
"v_mfma_f32_16x16x32_bf16 v[0:3], v[96:99], a[104:107], v[0:3]\n"
"ds_read_b64_tr_b16 v[80:81], v251 offset:49152\n"
"ds_read_b64_tr_b16 v[82:83], v251 offset:51200\n"
"s_add_u32 m0, s60, 81920\n"
"s_add_u32 s57, s61, 1703936\n"
"buffer_load_dwordx4 v247, s[36:39], s57 offen lds\n"
"v_mfma_f32_16x16x32_bf16 v[4:7], v[100:103], a[104:107], v[4:7]\n"
"ds_read_b64_tr_b16 v[84:85], v252 offset:49152\n"
"ds_read_b64_tr_b16 v[86:87], v252 offset:51200\n"
"v_mfma_f32_16x16x32_bf16 v[8:11], v[104:107], a[104:107], v[8:11]\n"
"ds_read_b64_tr_b16 v[88:89], v251 offset:49664\n"
"v_mfma_f32_16x16x32_bf16 v[12:15], v[108:111], a[104:107], v[12:15]\n"
"ds_read_b64_tr_b16 v[90:91], v251 offset:51712\n"
"ds_read_b64_tr_b16 v[92:93], v252 offset:49664\n"
"ds_read_b64_tr_b16 v[94:95], v252 offset:51712\n"
"s_waitcnt vmcnt(49) lgkmcnt(8)\n"
"v_mfma_f32_16x16x32_bf16 v[0:3], v[64:67], a[108:111], v[0:3]\n"
"ds_read_b64_tr_b16 v[96:97], v251 offset:53248\n"
"ds_read_b64_tr_b16 v[98:99], v251 offset:55296\n"
"s_add_u32 m0, s60, 86016\n"
"s_add_u32 s57, s61, 1736704\n"
"buffer_load_dwordx4 v247, s[36:39], s57 offen lds\n"
"v_mfma_f32_16x16x32_bf16 v[4:7], v[68:71], a[108:111], v[4:7]\n"
"ds_read_b64_tr_b16 v[100:101], v252 offset:53248\n"
"ds_read_b64_tr_b16 v[102:103], v252 offset:55296\n"
"v_mfma_f32_16x16x32_bf16 v[8:11], v[72:75], a[108:111], v[8:11]\n"
"ds_read_b64_tr_b16 v[104:105], v251 offset:53760\n"
"v_mfma_f32_16x16x32_bf16 v[12:15], v[76:79], a[108:111], v[12:15]\n"
"ds_read_b64_tr_b16 v[106:107], v251 offset:55808\n"
"ds_read_b64_tr_b16 v[108:109], v252 offset:53760\n"
"ds_read_b64_tr_b16 v[110:111], v252 offset:55808\n"
"s_waitcnt vmcnt(49) lgkmcnt(8)\n"
"v_mfma_f32_16x16x32_bf16 v[0:3], v[80:83], a[112:115], v[0:3]\n"
"ds_read_b64_tr_b16 v[64:65], v251 offset:57344\n"
"ds_read_b64_tr_b16 v[66:67], v251 offset:59392\n"
"s_add_u32 m0, s60, 90112\n"
"s_add_u32 s57, s61, 1769472\n"
"buffer_load_dwordx4 v247, s[36:39], s57 offen lds\n"
"v_mfma_f32_16x16x32_bf16 v[4:7], v[84:87], a[112:115], v[4:7]\n"
"ds_read_b64_tr_b16 v[68:69], v252 offset:57344\n"
"ds_read_b64_tr_b16 v[70:71], v252 offset:59392\n"
"v_mfma_f32_16x16x32_bf16 v[8:11], v[88:91], a[112:115], v[8:11]\n"
"ds_read_b64_tr_b16 v[72:73], v251 offset:57856\n"
"v_mfma_f32_16x16x32_bf16 v[12:15], v[92:95], a[112:115], v[12:15]\n"
"ds_read_b64_tr_b16 v[74:75], v251 offset:59904\n"
"ds_read_b64_tr_b16 v[76:77], v252 offset:57856\n"
"ds_read_b64_tr_b16 v[78:79], v252 offset:59904\n"
"s_waitcnt vmcnt(49) lgkmcnt(8)\n"
"v_mfma_f32_16x16x32_bf16 v[0:3], v[96:99], a[116:119], v[0:3]\n"
"ds_read_b64_tr_b16 v[80:81], v251 offset:61440\n"
"ds_read_b64_tr_b16 v[82:83], v251 offset:63488\n"
"s_add_u32 m0, s60, 94208\n"
"s_add_u32 s57, s61, 1802240\n"
"buffer_load_dwordx4 v247, s[36:39], s57 offen lds\n"
"v_mfma_f32_16x16x32_bf16 v[4:7], v[100:103], a[116:119], v[4:7]\n"
"ds_read_b64_tr_b16 v[84:85], v252 offset:61440\n"
"ds_read_b64_tr_b16 v[86:87], v252 offset:63488\n"
"v_mfma_f32_16x16x32_bf16 v[8:11], v[104:107], a[116:119], v[8:11]\n"
"ds_read_b64_tr_b16 v[88:89], v251 offset:61952\n"
"v_mfma_f32_16x16x32_bf16 v[12:15], v[108:111], a[116:119], v[12:15]\n"
"ds_read_b64_tr_b16 v[90:91], v251 offset:64000\n"
"ds_read_b64_tr_b16 v[92:93], v252 offset:61952\n"
"ds_read_b64_tr_b16 v[94:95], v252 offset:64000\n"
"s_waitcnt vmcnt(16) lgkmcnt(0)\n"
"s_barrier\n"
"v_mfma_f32_16x16x32_bf16 v[0:3], v[64:67], a[120:123], v[0:3]\n"
"ds_read_b64_tr_b16 v[96:97], v249 offset:0\n"
"ds_read_b64_tr_b16 v[98:99], v249 offset:2048\n"
"v_mfma_f32_16x16x32_bf16 v[4:7], v[68:71], a[120:123], v[4:7]\n"
"ds_read_b64_tr_b16 v[100:101], v250 offset:0\n"
"ds_read_b64_tr_b16 v[102:103], v250 offset:2048\n"
"v_mfma_f32_16x16x32_bf16 v[8:11], v[72:75], a[120:123], v[8:11]\n"
"ds_read_b64_tr_b16 v[104:105], v249 offset:512\n"
"v_mfma_f32_16x16x32_bf16 v[12:15], v[76:79], a[120:123], v[12:15]\n"
"ds_read_b64_tr_b16 v[106:107], v249 offset:2560\n"
"ds_read_b64_tr_b16 v[108:109], v250 offset:512\n"
"ds_read_b64_tr_b16 v[110:111], v250 offset:2560\n"
"s_waitcnt vmcnt(48) lgkmcnt(8)\n"
"v_mfma_f32_16x16x32_bf16 v[0:3], v[80:83], a[124:127], v[0:3]\n"
"ds_read_b64_tr_b16 v[64:65], v249 offset:4096\n"
"ds_read_b64_tr_b16 v[66:67], v249 offset:6144\n"
"v_mfma_f32_16x16x32_bf16 v[4:7], v[84:87], a[124:127], v[4:7]\n"
"ds_read_b64_tr_b16 v[68:69], v250 offset:4096\n"
"ds_read_b64_tr_b16 v[70:71], v250 offset:6144\n"
"v_mfma_f32_16x16x32_bf16 v[8:11], v[88:91], a[124:127], v[8:11]\n"
"ds_read_b64_tr_b16 v[72:73], v249 offset:4608\n"
"v_mfma_f32_16x16x32_bf16 v[12:15], v[92:95], a[124:127], v[12:15]\n"
"ds_read_b64_tr_b16 v[74:75], v249 offset:6656\n"
"ds_read_b64_tr_b16 v[76:77], v250 offset:4608\n"
"ds_read_b64_tr_b16 v[78:79], v250 offset:6656\n"
"s_waitcnt vmcnt(47) lgkmcnt(8)\n"
"v_mfma_f32_16x16x32_bf16 v[0:3], v[96:99], a[128:131], v[0:3]\n"
"ds_read_b64_tr_b16 v[80:81], v249 offset:8192\n"
"ds_read_b64_tr_b16 v[82:83], v249 offset:10240\n"
"v_mfma_f32_16x16x32_bf16 v[4:7], v[100:103], a[128:131], v[4:7]\n"
"ds_read_b64_tr_b16 v[84:85], v250 offset:8192\n"
"ds_read_b64_tr_b16 v[86:87], v250 offset:10240\n"
"v_mfma_f32_16x16x32_bf16 v[8:11], v[104:107], a[128:131], v[8:11]\n"
"ds_read_b64_tr_b16 v[88:89], v249 offset:8704\n"
"v_mfma_f32_16x16x32_bf16 v[12:15], v[108:111], a[128:131], v[12:15]\n"
"ds_read_b64_tr_b16 v[90:91], v249 offset:10752\n"
"ds_read_b64_tr_b16 v[92:93], v250 offset:8704\n"
"ds_read_b64_tr_b16 v[94:95], v250 offset:10752\n"
"s_waitcnt vmcnt(46) lgkmcnt(8)\n"
"v_mfma_f32_16x16x32_bf16 v[0:3], v[64:67], a[132:135], v[0:3]\n"
"ds_read_b64_tr_b16 v[96:97], v249 offset:12288\n"
"ds_read_b64_tr_b16 v[98:99], v249 offset:14336\n"
"v_mfma_f32_16x16x32_bf16 v[4:7], v[68:71], a[132:135], v[4:7]\n"
"ds_read_b64_tr_b16 v[100:101], v250 offset:12288\n"
"ds_read_b64_tr_b16 v[102:103], v250 offset:14336\n"
"v_mfma_f32_16x16x32_bf16 v[8:11], v[72:75], a[132:135], v[8:11]\n"
"ds_read_b64_tr_b16 v[104:105], v249 offset:12800\n"
"v_mfma_f32_16x16x32_bf16 v[12:15], v[76:79], a[132:135], v[12:15]\n"
"ds_read_b64_tr_b16 v[106:107], v249 offset:14848\n"
"ds_read_b64_tr_b16 v[108:109], v250 offset:12800\n"
"ds_read_b64_tr_b16 v[110:111], v250 offset:14848\n"
"s_waitcnt vmcnt(45) lgkmcnt(8)\n"
"v_mfma_f32_16x16x32_bf16 v[0:3], v[80:83], a[136:139], v[0:3]\n"
"ds_read_b64_tr_b16 v[64:65], v249 offset:16384\n"
"ds_read_b64_tr_b16 v[66:67], v249 offset:18432\n"
"v_mfma_f32_16x16x32_bf16 v[4:7], v[84:87], a[136:139], v[4:7]\n"
"ds_read_b64_tr_b16 v[68:69], v250 offset:16384\n"
"ds_read_b64_tr_b16 v[70:71], v250 offset:18432\n"
"v_mfma_f32_16x16x32_bf16 v[8:11], v[88:91], a[136:139], v[8:11]\n"
"ds_read_b64_tr_b16 v[72:73], v249 offset:16896\n"
"v_mfma_f32_16x16x32_bf16 v[12:15], v[92:95], a[136:139], v[12:15]\n"
"ds_read_b64_tr_b16 v[74:75], v249 offset:18944\n"
"ds_read_b64_tr_b16 v[76:77], v250 offset:16896\n"
"ds_read_b64_tr_b16 v[78:79], v250 offset:18944\n"
"s_waitcnt vmcnt(44) lgkmcnt(8)\n"
"v_mfma_f32_16x16x32_bf16 v[0:3], v[96:99], a[140:143], v[0:3]\n"
"ds_read_b64_tr_b16 v[80:81], v249 offset:20480\n"
"ds_read_b64_tr_b16 v[82:83], v249 offset:22528\n"
"v_mfma_f32_16x16x32_bf16 v[4:7], v[100:103], a[140:143], v[4:7]\n"
"ds_read_b64_tr_b16 v[84:85], v250 offset:20480\n"
"ds_read_b64_tr_b16 v[86:87], v250 offset:22528\n"
"v_mfma_f32_16x16x32_bf16 v[8:11], v[104:107], a[140:143], v[8:11]\n"
"ds_read_b64_tr_b16 v[88:89], v249 offset:20992\n"
"v_mfma_f32_16x16x32_bf16 v[12:15], v[108:111], a[140:143], v[12:15]\n"
"ds_read_b64_tr_b16 v[90:91], v249 offset:23040\n"
"ds_read_b64_tr_b16 v[92:93], v250 offset:20992\n"
"ds_read_b64_tr_b16 v[94:95], v250 offset:23040\n"
"s_waitcnt vmcnt(43) lgkmcnt(8)\n"
"v_mfma_f32_16x16x32_bf16 v[0:3], v[64:67], a[144:147], v[0:3]\n"
"ds_read_b64_tr_b16 v[96:97], v249 offset:24576\n"
"ds_read_b64_tr_b16 v[98:99], v249 offset:26624\n"
"v_mfma_f32_16x16x32_bf16 v[4:7], v[68:71], a[144:147], v[4:7]\n"
"ds_read_b64_tr_b16 v[100:101], v250 offset:24576\n"
"ds_read_b64_tr_b16 v[102:103], v250 offset:26624\n"
"v_mfma_f32_16x16x32_bf16 v[8:11], v[72:75], a[144:147], v[8:11]\n"
"ds_read_b64_tr_b16 v[104:105], v249 offset:25088\n"
"v_mfma_f32_16x16x32_bf16 v[12:15], v[76:79], a[144:147], v[12:15]\n"
"ds_read_b64_tr_b16 v[106:107], v249 offset:27136\n"
"ds_read_b64_tr_b16 v[108:109], v250 offset:25088\n"
"ds_read_b64_tr_b16 v[110:111], v250 offset:27136\n"
"s_waitcnt vmcnt(42) lgkmcnt(8)\n"
"v_mfma_f32_16x16x32_bf16 v[0:3], v[80:83], a[148:151], v[0:3]\n"
"ds_read_b64_tr_b16 v[64:65], v249 offset:28672\n"
"ds_read_b64_tr_b16 v[66:67], v249 offset:30720\n"
"v_mfma_f32_16x16x32_bf16 v[4:7], v[84:87], a[148:151], v[4:7]\n"
"ds_read_b64_tr_b16 v[68:69], v250 offset:28672\n"
"ds_read_b64_tr_b16 v[70:71], v250 offset:30720\n"
"v_mfma_f32_16x16x32_bf16 v[8:11], v[88:91], a[148:151], v[8:11]\n"
"ds_read_b64_tr_b16 v[72:73], v249 offset:29184\n"
"v_mfma_f32_16x16x32_bf16 v[12:15], v[92:95], a[148:151], v[12:15]\n"
"ds_read_b64_tr_b16 v[74:75], v249 offset:31232\n"
"ds_read_b64_tr_b16 v[76:77], v250 offset:29184\n"
"ds_read_b64_tr_b16 v[78:79], v250 offset:31232\n"
"s_waitcnt vmcnt(8) lgkmcnt(0)\n"
"s_barrier\n"
"v_mfma_f32_16x16x32_bf16 v[0:3], v[96:99], a[152:155], v[0:3]\n"
"ds_read_b64_tr_b16 v[80:81], v249 offset:32768\n"
"ds_read_b64_tr_b16 v[82:83], v249 offset:34816\n"
"v_mfma_f32_16x16x32_bf16 v[4:7], v[100:103], a[152:155], v[4:7]\n"
"ds_read_b64_tr_b16 v[84:85], v250 offset:32768\n"
"ds_read_b64_tr_b16 v[86:87], v250 offset:34816\n"
"v_mfma_f32_16x16x32_bf16 v[8:11], v[104:107], a[152:155], v[8:11]\n"
"ds_read_b64_tr_b16 v[88:89], v249 offset:33280\n"
"v_mfma_f32_16x16x32_bf16 v[12:15], v[108:111], a[152:155], v[12:15]\n"
"ds_read_b64_tr_b16 v[90:91], v249 offset:35328\n"
"ds_read_b64_tr_b16 v[92:93], v250 offset:33280\n"
"ds_read_b64_tr_b16 v[94:95], v250 offset:35328\n"
"s_waitcnt vmcnt(40) lgkmcnt(8)\n"
"v_mfma_f32_16x16x32_bf16 v[0:3], v[64:67], a[156:159], v[0:3]\n"
"ds_read_b64_tr_b16 v[96:97], v249 offset:36864\n"
"ds_read_b64_tr_b16 v[98:99], v249 offset:38912\n"
"v_mfma_f32_16x16x32_bf16 v[4:7], v[68:71], a[156:159], v[4:7]\n"
"ds_read_b64_tr_b16 v[100:101], v250 offset:36864\n"
"ds_read_b64_tr_b16 v[102:103], v250 offset:38912\n"
"v_mfma_f32_16x16x32_bf16 v[8:11], v[72:75], a[156:159], v[8:11]\n"
"ds_read_b64_tr_b16 v[104:105], v249 offset:37376\n"
"v_mfma_f32_16x16x32_bf16 v[12:15], v[76:79], a[156:159], v[12:15]\n"
"ds_read_b64_tr_b16 v[106:107], v249 offset:39424\n"
"ds_read_b64_tr_b16 v[108:109], v250 offset:37376\n"
"ds_read_b64_tr_b16 v[110:111], v250 offset:39424\n"
"s_waitcnt vmcnt(39) lgkmcnt(8)\n"
"v_mfma_f32_16x16x32_bf16 v[0:3], v[80:83], a[160:163], v[0:3]\n"
"ds_read_b64_tr_b16 v[64:65], v249 offset:40960\n"
"ds_read_b64_tr_b16 v[66:67], v249 offset:43008\n"
"v_mfma_f32_16x16x32_bf16 v[4:7], v[84:87], a[160:163], v[4:7]\n"
"ds_read_b64_tr_b16 v[68:69], v250 offset:40960\n"
"ds_read_b64_tr_b16 v[70:71], v250 offset:43008\n"
"v_mfma_f32_16x16x32_bf16 v[8:11], v[88:91], a[160:163], v[8:11]\n"
"ds_read_b64_tr_b16 v[72:73], v249 offset:41472\n"
"v_mfma_f32_16x16x32_bf16 v[12:15], v[92:95], a[160:163], v[12:15]\n"
"ds_read_b64_tr_b16 v[74:75], v249 offset:43520\n"
"ds_read_b64_tr_b16 v[76:77], v250 offset:41472\n"
"ds_read_b64_tr_b16 v[78:79], v250 offset:43520\n"
"s_waitcnt vmcnt(38) lgkmcnt(8)\n"
"v_mfma_f32_16x16x32_bf16 v[0:3], v[96:99], a[164:167], v[0:3]\n"
"ds_read_b64_tr_b16 v[80:81], v249 offset:45056\n"
"ds_read_b64_tr_b16 v[82:83], v249 offset:47104\n"
"v_mfma_f32_16x16x32_bf16 v[4:7], v[100:103], a[164:167], v[4:7]\n"
"ds_read_b64_tr_b16 v[84:85], v250 offset:45056\n"
"ds_read_b64_tr_b16 v[86:87], v250 offset:47104\n"
"v_mfma_f32_16x16x32_bf16 v[8:11], v[104:107], a[164:167], v[8:11]\n"
"ds_read_b64_tr_b16 v[88:89], v249 offset:45568\n"
"v_mfma_f32_16x16x32_bf16 v[12:15], v[108:111], a[164:167], v[12:15]\n"
"ds_read_b64_tr_b16 v[90:91], v249 offset:47616\n"
"ds_read_b64_tr_b16 v[92:93], v250 offset:45568\n"
"ds_read_b64_tr_b16 v[94:95], v250 offset:47616\n"
"s_waitcnt vmcnt(37) lgkmcnt(8)\n"
"v_mfma_f32_16x16x32_bf16 v[0:3], v[64:67], a[168:171], v[0:3]\n"
"ds_read_b64_tr_b16 v[96:97], v249 offset:49152\n"
"ds_read_b64_tr_b16 v[98:99], v249 offset:51200\n"
"v_mfma_f32_16x16x32_bf16 v[4:7], v[68:71], a[168:171], v[4:7]\n"
"ds_read_b64_tr_b16 v[100:101], v250 offset:49152\n"
"ds_read_b64_tr_b16 v[102:103], v250 offset:51200\n"
"v_mfma_f32_16x16x32_bf16 v[8:11], v[72:75], a[168:171], v[8:11]\n"
"ds_read_b64_tr_b16 v[104:105], v249 offset:49664\n"
"v_mfma_f32_16x16x32_bf16 v[12:15], v[76:79], a[168:171], v[12:15]\n"
"ds_read_b64_tr_b16 v[106:107], v249 offset:51712\n"
"ds_read_b64_tr_b16 v[108:109], v250 offset:49664\n"
"ds_read_b64_tr_b16 v[110:111], v250 offset:51712\n"
"s_waitcnt vmcnt(36) lgkmcnt(8)\n"
"v_mfma_f32_16x16x32_bf16 v[0:3], v[80:83], a[172:175], v[0:3]\n"
"ds_read_b64_tr_b16 v[64:65], v249 offset:53248\n"
"ds_read_b64_tr_b16 v[66:67], v249 offset:55296\n"
"v_mfma_f32_16x16x32_bf16 v[4:7], v[84:87], a[172:175], v[4:7]\n"
"ds_read_b64_tr_b16 v[68:69], v250 offset:53248\n"
"ds_read_b64_tr_b16 v[70:71], v250 offset:55296\n"
"v_mfma_f32_16x16x32_bf16 v[8:11], v[88:91], a[172:175], v[8:11]\n"
"ds_read_b64_tr_b16 v[72:73], v249 offset:53760\n"
"v_mfma_f32_16x16x32_bf16 v[12:15], v[92:95], a[172:175], v[12:15]\n"
"ds_read_b64_tr_b16 v[74:75], v249 offset:55808\n"
"ds_read_b64_tr_b16 v[76:77], v250 offset:53760\n"
"ds_read_b64_tr_b16 v[78:79], v250 offset:55808\n"
"s_waitcnt vmcnt(35) lgkmcnt(8)\n"
"v_mfma_f32_16x16x32_bf16 v[0:3], v[96:99], a[176:179], v[0:3]\n"
"ds_read_b64_tr_b16 v[80:81], v249 offset:57344\n"
"ds_read_b64_tr_b16 v[82:83], v249 offset:59392\n"
"v_mfma_f32_16x16x32_bf16 v[4:7], v[100:103], a[176:179], v[4:7]\n"
"ds_read_b64_tr_b16 v[84:85], v250 offset:57344\n"
"ds_read_b64_tr_b16 v[86:87], v250 offset:59392\n"
"v_mfma_f32_16x16x32_bf16 v[8:11], v[104:107], a[176:179], v[8:11]\n"
"ds_read_b64_tr_b16 v[88:89], v249 offset:57856\n"
"v_mfma_f32_16x16x32_bf16 v[12:15], v[108:111], a[176:179], v[12:15]\n"
"ds_read_b64_tr_b16 v[90:91], v249 offset:59904\n"
"ds_read_b64_tr_b16 v[92:93], v250 offset:57856\n"
"ds_read_b64_tr_b16 v[94:95], v250 offset:59904\n"
"s_waitcnt vmcnt(34) lgkmcnt(8)\n"
"v_mfma_f32_16x16x32_bf16 v[0:3], v[64:67], a[180:183], v[0:3]\n"
"ds_read_b64_tr_b16 v[96:97], v249 offset:61440\n"
"ds_read_b64_tr_b16 v[98:99], v249 offset:63488\n"
"v_mfma_f32_16x16x32_bf16 v[4:7], v[68:71], a[180:183], v[4:7]\n"
"ds_read_b64_tr_b16 v[100:101], v250 offset:61440\n"
"ds_read_b64_tr_b16 v[102:103], v250 offset:63488\n"
"v_mfma_f32_16x16x32_bf16 v[8:11], v[72:75], a[180:183], v[8:11]\n"
"ds_read_b64_tr_b16 v[104:105], v249 offset:61952\n"
"v_mfma_f32_16x16x32_bf16 v[12:15], v[76:79], a[180:183], v[12:15]\n"
"ds_read_b64_tr_b16 v[106:107], v249 offset:64000\n"
"ds_read_b64_tr_b16 v[108:109], v250 offset:61952\n"
"ds_read_b64_tr_b16 v[110:111], v250 offset:64000\n"
"s_waitcnt vmcnt(0) lgkmcnt(0)\n"
"s_barrier\n"
"v_mfma_f32_16x16x32_bf16 v[0:3], v[80:83], a[184:187], v[0:3]\n"
"ds_read_b64_tr_b16 v[64:65], v251 offset:0\n"
"ds_read_b64_tr_b16 v[66:67], v251 offset:2048\n"
"v_mfma_f32_16x16x32_bf16 v[4:7], v[84:87], a[184:187], v[4:7]\n"
"ds_read_b64_tr_b16 v[68:69], v252 offset:0\n"
"ds_read_b64_tr_b16 v[70:71], v252 offset:2048\n"
"v_mfma_f32_16x16x32_bf16 v[8:11], v[88:91], a[184:187], v[8:11]\n"
"ds_read_b64_tr_b16 v[72:73], v251 offset:512\n"
"v_mfma_f32_16x16x32_bf16 v[12:15], v[92:95], a[184:187], v[12:15]\n"
"ds_read_b64_tr_b16 v[74:75], v251 offset:2560\n"
"ds_read_b64_tr_b16 v[76:77], v252 offset:512\n"
"ds_read_b64_tr_b16 v[78:79], v252 offset:2560\n"
"s_waitcnt vmcnt(32) lgkmcnt(8)\n"
"v_mfma_f32_16x16x32_bf16 v[0:3], v[96:99], a[188:191], v[0:3]\n"
"ds_read_b64_tr_b16 v[80:81], v251 offset:4096\n"
"ds_read_b64_tr_b16 v[82:83], v251 offset:6144\n"
"v_mfma_f32_16x16x32_bf16 v[4:7], v[100:103], a[188:191], v[4:7]\n"
"ds_read_b64_tr_b16 v[84:85], v252 offset:4096\n"
"ds_read_b64_tr_b16 v[86:87], v252 offset:6144\n"
"v_mfma_f32_16x16x32_bf16 v[8:11], v[104:107], a[188:191], v[8:11]\n"
"ds_read_b64_tr_b16 v[88:89], v251 offset:4608\n"
"v_mfma_f32_16x16x32_bf16 v[12:15], v[108:111], a[188:191], v[12:15]\n"
"ds_read_b64_tr_b16 v[90:91], v251 offset:6656\n"
"ds_read_b64_tr_b16 v[92:93], v252 offset:4608\n"
"ds_read_b64_tr_b16 v[94:95], v252 offset:6656\n"
"s_waitcnt vmcnt(31) lgkmcnt(8)\n"
"v_mfma_f32_16x16x32_bf16 v[0:3], v[64:67], a[0:3], v[0:3]\n"
"ds_read_b64_tr_b16 v[96:97], v251 offset:8192\n"
"ds_read_b64_tr_b16 v[98:99], v251 offset:10240\n"
"v_mfma_f32_16x16x32_bf16 v[4:7], v[68:71], a[0:3], v[4:7]\n"
"ds_read_b64_tr_b16 v[100:101], v252 offset:8192\n"
"ds_read_b64_tr_b16 v[102:103], v252 offset:10240\n"
"v_mfma_f32_16x16x32_bf16 v[8:11], v[72:75], a[0:3], v[8:11]\n"
"ds_read_b64_tr_b16 v[104:105], v251 offset:8704\n"
"v_mfma_f32_16x16x32_bf16 v[12:15], v[76:79], a[0:3], v[12:15]\n"
"ds_read_b64_tr_b16 v[106:107], v251 offset:10752\n"
"ds_read_b64_tr_b16 v[108:109], v252 offset:8704\n"
"ds_read_b64_tr_b16 v[110:111], v252 offset:10752\n"
"s_waitcnt vmcnt(30) lgkmcnt(8)\n"
"v_mfma_f32_16x16x32_bf16 v[0:3], v[80:83], a[4:7], v[0:3]\n"
"ds_read_b64_tr_b16 v[64:65], v251 offset:12288\n"
"ds_read_b64_tr_b16 v[66:67], v251 offset:14336\n"
"v_mfma_f32_16x16x32_bf16 v[4:7], v[84:87], a[4:7], v[4:7]\n"
"ds_read_b64_tr_b16 v[68:69], v252 offset:12288\n"
"ds_read_b64_tr_b16 v[70:71], v252 offset:14336\n"
"v_mfma_f32_16x16x32_bf16 v[8:11], v[88:91], a[4:7], v[8:11]\n"
"ds_read_b64_tr_b16 v[72:73], v251 offset:12800\n"
"v_mfma_f32_16x16x32_bf16 v[12:15], v[92:95], a[4:7], v[12:15]\n"
"ds_read_b64_tr_b16 v[74:75], v251 offset:14848\n"
"ds_read_b64_tr_b16 v[76:77], v252 offset:12800\n"
"ds_read_b64_tr_b16 v[78:79], v252 offset:14848\n"
"s_waitcnt vmcnt(28) lgkmcnt(8)\n"
"v_mfma_f32_16x16x32_bf16 v[0:3], v[96:99], a[8:11], v[0:3]\n"
"ds_read_b64_tr_b16 v[80:81], v251 offset:16384\n"
"ds_read_b64_tr_b16 v[82:83], v251 offset:18432\n"
"v_mfma_f32_16x16x32_bf16 v[4:7], v[100:103], a[8:11], v[4:7]\n"
"ds_read_b64_tr_b16 v[84:85], v252 offset:16384\n"
"ds_read_b64_tr_b16 v[86:87], v252 offset:18432\n"
"v_mfma_f32_16x16x32_bf16 v[8:11], v[104:107], a[8:11], v[8:11]\n"
"ds_read_b64_tr_b16 v[88:89], v251 offset:16896\n"
"v_mfma_f32_16x16x32_bf16 v[12:15], v[108:111], a[8:11], v[12:15]\n"
"ds_read_b64_tr_b16 v[90:91], v251 offset:18944\n"
"ds_read_b64_tr_b16 v[92:93], v252 offset:16896\n"
"ds_read_b64_tr_b16 v[94:95], v252 offset:18944\n"
"s_waitcnt vmcnt(26) lgkmcnt(8)\n"
"v_mfma_f32_16x16x32_bf16 v[0:3], v[64:67], a[12:15], v[0:3]\n"
"ds_read_b64_tr_b16 v[96:97], v251 offset:20480\n"
"ds_read_b64_tr_b16 v[98:99], v251 offset:22528\n"
"v_mfma_f32_16x16x32_bf16 v[4:7], v[68:71], a[12:15], v[4:7]\n"
"ds_read_b64_tr_b16 v[100:101], v252 offset:20480\n"
"ds_read_b64_tr_b16 v[102:103], v252 offset:22528\n"
"v_mfma_f32_16x16x32_bf16 v[8:11], v[72:75], a[12:15], v[8:11]\n"
"ds_read_b64_tr_b16 v[104:105], v251 offset:20992\n"
"v_mfma_f32_16x16x32_bf16 v[12:15], v[76:79], a[12:15], v[12:15]\n"
"ds_read_b64_tr_b16 v[106:107], v251 offset:23040\n"
"ds_read_b64_tr_b16 v[108:109], v252 offset:20992\n"
"ds_read_b64_tr_b16 v[110:111], v252 offset:23040\n"
"s_waitcnt vmcnt(24) lgkmcnt(8)\n"
"v_mfma_f32_16x16x32_bf16 v[0:3], v[80:83], a[16:19], v[0:3]\n"
"ds_read_b64_tr_b16 v[64:65], v251 offset:24576\n"
"ds_read_b64_tr_b16 v[66:67], v251 offset:26624\n"
"v_mfma_f32_16x16x32_bf16 v[4:7], v[84:87], a[16:19], v[4:7]\n"
"ds_read_b64_tr_b16 v[68:69], v252 offset:24576\n"
"ds_read_b64_tr_b16 v[70:71], v252 offset:26624\n"
"v_mfma_f32_16x16x32_bf16 v[8:11], v[88:91], a[16:19], v[8:11]\n"
"ds_read_b64_tr_b16 v[72:73], v251 offset:25088\n"
"v_mfma_f32_16x16x32_bf16 v[12:15], v[92:95], a[16:19], v[12:15]\n"
"ds_read_b64_tr_b16 v[74:75], v251 offset:27136\n"
"ds_read_b64_tr_b16 v[76:77], v252 offset:25088\n"
"ds_read_b64_tr_b16 v[78:79], v252 offset:27136\n"
"s_waitcnt vmcnt(22) lgkmcnt(8)\n"
"v_mfma_f32_16x16x32_bf16 v[0:3], v[96:99], a[20:23], v[0:3]\n"
"ds_read_b64_tr_b16 v[80:81], v251 offset:28672\n"
"ds_read_b64_tr_b16 v[82:83], v251 offset:30720\n"
"v_mfma_f32_16x16x32_bf16 v[4:7], v[100:103], a[20:23], v[4:7]\n"
"ds_read_b64_tr_b16 v[84:85], v252 offset:28672\n"
"ds_read_b64_tr_b16 v[86:87], v252 offset:30720\n"
"v_mfma_f32_16x16x32_bf16 v[8:11], v[104:107], a[20:23], v[8:11]\n"
"ds_read_b64_tr_b16 v[88:89], v251 offset:29184\n"
"v_mfma_f32_16x16x32_bf16 v[12:15], v[108:111], a[20:23], v[12:15]\n"
"ds_read_b64_tr_b16 v[90:91], v251 offset:31232\n"
"ds_read_b64_tr_b16 v[92:93], v252 offset:29184\n"
"ds_read_b64_tr_b16 v[94:95], v252 offset:31232\n"
"s_waitcnt vmcnt(20) lgkmcnt(8)\n"
"v_mfma_f32_16x16x32_bf16 v[0:3], v[64:67], a[24:27], v[0:3]\n"
"v_mfma_f32_16x16x32_bf16 v[4:7], v[68:71], a[24:27], v[4:7]\n"
"v_mfma_f32_16x16x32_bf16 v[8:11], v[72:75], a[24:27], v[8:11]\n"
"v_mfma_f32_16x16x32_bf16 v[12:15], v[76:79], a[24:27], v[12:15]\n"
"s_waitcnt vmcnt(18) lgkmcnt(0)\n"
"v_mfma_f32_16x16x32_bf16 v[0:3], v[80:83], a[28:31], v[0:3]\n"
"v_mfma_f32_16x16x32_bf16 v[4:7], v[84:87], a[28:31], v[4:7]\n"
"v_mfma_f32_16x16x32_bf16 v[8:11], v[88:91], a[28:31], v[8:11]\n"
"v_mfma_f32_16x16x32_bf16 v[12:15], v[92:95], a[28:31], v[12:15]\n"
"s_waitcnt vmcnt(0) lgkmcnt(0)\n"
"s_nop 7\n"
"v_and_b32 v244, 15, v240\n"
"v_lshlrev_b32 v244, 10, v244\n"
"v_lshl_add_u32 v244, v241, 3, v244\n"
"v_lshlrev_b32 v160, 16, v152\n"
"v_and_b32 v161, 0xffff0000, v152\n"
"v_lshlrev_b32 v162, 16, v153\n"
"v_and_b32 v163, 0xffff0000, v153\n"
"v_lshlrev_b32 v164, 16, v154\n"
"v_and_b32 v165, 0xffff0000, v154\n"
"v_lshlrev_b32 v166, 16, v155\n"
"v_and_b32 v167, 0xffff0000, v155\n"
"v_lshlrev_b32 v168, 16, v156\n"
"v_and_b32 v169, 0xffff0000, v156\n"
"v_lshlrev_b32 v170, 16, v157\n"
"v_and_b32 v171, 0xffff0000, v157\n"
"v_lshlrev_b32 v172, 16, v158\n"
"v_and_b32 v173, 0xffff0000, v158\n"
"v_lshlrev_b32 v174, 16, v159\n"
"v_and_b32 v175, 0xffff0000, v159\n"
"v_fma_f32 v160, v0, v176, v160\n"
"v_fma_f32 v161, v1, v177, v161\n"
"v_fma_f32 v162, v2, v178, v162\n"
"v_fma_f32 v163, v3, v179, v163\n"
"v_cvt_pk_bf16_f32 v100, v160, v161\n"
"v_cvt_pk_bf16_f32 v101, v162, v163\n"
"buffer_store_dwordx2 v[100:101], v244, s[48:51], 0 offen offset:0\n"
"v_fma_f32 v164, v4, v180, v164\n"
"v_fma_f32 v165, v5, v181, v165\n"
"v_fma_f32 v166, v6, v182, v166\n"
"v_fma_f32 v167, v7, v183, v167\n"
"v_cvt_pk_bf16_f32 v102, v164, v165\n"
"v_cvt_pk_bf16_f32 v103, v166, v167\n"
"buffer_store_dwordx2 v[102:103], v244, s[48:51], 0 offen offset:32\n"
"v_fma_f32 v168, v8, v184, v168\n"
"v_fma_f32 v169, v9, v185, v169\n"
"v_fma_f32 v170, v10, v186, v170\n"
"v_fma_f32 v171, v11, v187, v171\n"
"v_cvt_pk_bf16_f32 v104, v168, v169\n"
"v_cvt_pk_bf16_f32 v105, v170, v171\n"
"buffer_store_dwordx2 v[104:105], v244, s[48:51], 0 offen offset:64\n"
"v_fma_f32 v172, v12, v188, v172\n"
"v_fma_f32 v173, v13, v189, v173\n"
"v_fma_f32 v174, v14, v190, v174\n"
"v_fma_f32 v175, v15, v191, v175\n"
"v_cvt_pk_bf16_f32 v106, v172, v173\n"
"v_cvt_pk_bf16_f32 v107, v174, v175\n"
"buffer_store_dwordx2 v[106:107], v244, s[48:51], 0 offen offset:96\n"
"Lcode_end:\n"
    :: "s"(RFL((size_t)(w2b))), "s"(RFL((size_t)(w2b) >> 32)), "s"(RFL((size_t)(xb))), "s"(RFL((size_t)(xb) >> 32)), "s"(RFL((size_t)(ob))), "s"(RFL((size_t)(ob) >> 32)), "s"(RFL((size_t)(x1b))), "s"(RFL((size_t)(x1b) >> 32)), "s"(wid), "s"(RFL(dn * 256)), "s"(RFL((size_t)(dbgb))), "s"(RFL((size_t)(dbgb) >> 32))
    : "v0", "v1", "v2", "v3", "v4", "v5", "v6", "v7", "v8", "v9", "v10", "v11", "v12", "v13", "v14", "v15", "v16", "v17", "v18", "v19", "v20", "v21", "v22", "v23", "v24", "v25", "v26", "v27", "v28", "v29", "v30", "v31", "v32", "v33", "v34", "v35", "v36", "v37", "v38", "v39", "v40", "v41", "v42", "v43", "v44", "v45", "v46", "v47", "v48", "v49", "v50", "v51", "v52", "v53", "v54", "v55", "v56", "v57", "v58", "v59", "v60", "v61", "v62", "v63", "v64", "v65", "v66", "v67", "v68", "v69", "v70", "v71", "v72", "v73", "v74", "v75", "v76", "v77", "v78", "v79", "v80", "v81", "v82", "v83", "v84", "v85", "v86", "v87", "v88", "v89", "v90", "v91", "v92", "v93", "v94", "v95", "v96", "v97", "v98", "v99", "v100", "v101", "v102", "v103", "v104", "v105", "v106", "v107", "v108", "v109", "v110", "v111", "v112", "v113", "v114", "v115", "v116", "v117", "v118", "v119", "v120", "v121", "v122", "v123", "v124", "v125", "v126", "v127", "v128", "v129", "v130", "v131", "v132", "v133", "v134", "v135", "v136", "v137", "v138", "v139", "v140", "v141", "v142", "v143", "v144", "v145", "v146", "v147", "v148", "v149", "v150", "v151", "v152", "v153", "v154", "v155", "v156", "v157", "v158", "v159", "v160", "v161", "v162", "v163", "v164", "v165", "v166", "v167", "v168", "v169", "v170", "v171", "v172", "v173", "v174", "v175", "v176", "v177", "v178", "v179", "v180", "v181", "v182", "v183", "v184", "v185", "v186", "v187", "v188", "v189", "v190", "v191", "v192", "v193", "v194", "v195", "v196", "v197", "v198", "v199", "v200", "v201", "v202", "v203", "v204", "v205", "v206", "v207", "v208", "v209", "v210", "v211", "v212", "v213", "v214", "v215", "v216", "v217", "v218", "v219", "v220", "v221", "v222", "v223", "v224", "v225", "v226", "v227", "v228", "v229", "v230", "v231", "v232", "v233", "v234", "v235", "v236", "v237", "v238", "v239", "v240", "v241", "v242", "v243", "v244", "v245", "v246", "v247", "v248", "v249", "v250", "v251", "v252", "v253", "v254", "v255", "a0", "a1", "a2", "a3", "a4", "a5", "a6", "a7", "a8", "a9", "a10", "a11", "a12", "a13", "a14", "a15", "a16", "a17", "a18", "a19", "a20", "a21", "a22", "a23", "a24", "a25", "a26", "a27", "a28", "a29", "a30", "a31", "a32", "a33", "a34", "a35", "a36", "a37", "a38", "a39", "a40", "a41", "a42", "a43", "a44", "a45", "a46", "a47", "a48", "a49", "a50", "a51", "a52", "a53", "a54", "a55", "a56", "a57", "a58", "a59", "a60", "a61", "a62", "a63", "a64", "a65", "a66", "a67", "a68", "a69", "a70", "a71", "a72", "a73", "a74", "a75", "a76", "a77", "a78", "a79", "a80", "a81", "a82", "a83", "a84", "a85", "a86", "a87", "a88", "a89", "a90", "a91", "a92", "a93", "a94", "a95", "a96", "a97", "a98", "a99", "a100", "a101", "a102", "a103", "a104", "a105", "a106", "a107", "a108", "a109", "a110", "a111", "a112", "a113", "a114", "a115", "a116", "a117", "a118", "a119", "a120", "a121", "a122", "a123", "a124", "a125", "a126", "a127", "a128", "a129", "a130", "a131", "a132", "a133", "a134", "a135", "a136", "a137", "a138", "a139", "a140", "a141", "a142", "a143", "a144", "a145", "a146", "a147", "a148", "a149", "a150", "a151", "a152", "a153", "a154", "a155", "a156", "a157", "a158", "a159", "a160", "a161", "a162", "a163", "a164", "a165", "a166", "a167", "a168", "a169", "a170", "a171", "a172", "a173", "a174", "a175", "a176", "a177", "a178", "a179", "a180", "a181", "a182", "a183", "a184", "a185", "a186", "a187", "a188", "a189", "a190", "a191", "a192", "a193", "a194", "a195", "a196", "a197", "a198", "a199", "a200", "a201", "a202", "a203", "a204", "a205", "a206", "a207", "a208", "a209", "a210", "a211", "a212", "a213", "a214", "a215", "a216", "a217", "a218", "a219", "a220", "a221", "a222", "a223", "a224", "a225", "a226", "a227", "a228", "a229", "a230", "a231", "a232", "a233", "a234", "a235", "a236", "a237", "a238", "a239", "a240", "a241", "a242", "a243", "a244", "a245", "a246", "a247", "a248", "a249", "a250", "a251", "a252", "a253", "a254", "a255", "s36", "s37", "s38", "s39", "s40", "s41", "s42", "s43", "s44", "s45", "s46", "s47", "s48", "s49", "s50", "s51", "s52", "s53", "s54", "s55", "s56", "s57", "s58", "s59", "s60", "s61", "s62", "s63", "s64", "s65", "s66", "s67", "s68", "s69", "s70", "s71", "s72", "s73", "s74", "s75", "s76", "s77", "s78", "s79", "s80", "s81", "s82", "s83", "vcc", "scc", "memory");

  }
}
extern "C" void kernel_launch(void* const* d_in, const int* in_sizes, int n_in, void* d_out, int out_size, void* d_ws, size_t ws_size, hipStream_t stream) {
  const u16* x = (const u16*)d_in[0]; const u16* kc = (const u16*)d_in[1]; const u16* vc = (const u16*)d_in[2]; const u16* wq = (const u16*)d_in[3]; const u16* wo = (const u16*)d_in[4];
  const u16* w1 = (const u16*)d_in[6]; const u16* w3 = (const u16*)d_in[7]; const u16* w2 = (const u16*)d_in[8]; const u16* c = (const u16*)d_in[9]; const u16* wmod = (const u16*)d_in[10];
  char* ws = (char*)d_ws;
  float* modp = (float*)(ws + WS_MODP); u16* h1 = (u16*)(ws + WS_H1);
  unsigned* flags = (unsigned*)(ws + WS_FLAGS);
  (void)hipFuncSetAttribute((const void*)k_main, hipFuncAttributeMaxDynamicSharedMemorySize, 163840);
  k_pre<<<384, 256, 0, stream>>>(c, wmod, modp, x, h1, (const char*)wq, (const char*)kc, (const char*)vc, flags);
  MainArgs a; a.x = x; a.wq = wq; a.kc = kc; a.vc = vc; a.wo = wo; a.w1 = w1; a.w3 = w3; a.w2 = w2; a.out = (u16*)d_out; a.ws = ws;
  k_main<<<256, 256, 163840, stream>>>(a);
}
extern "C" void launch_attn(void* const* d_in, void* d_ws, hipStream_t stream) {}
```
